```python
import torch

def _softmax(x, axis=-1):
    return torch.softmax(x, dim=axis)
def _gelu(x):
    return torch.nn.functional.gelu(x, approximate="tanh")
def _silu(x):
    return torch.nn.functional.silu(x)
def _sigmoid(x):
    return torch.sigmoid(x)
def _layernorm(x, axis=-1, eps=1e-5):
    m = x.mean(dim=axis, keepdim=True)
    v = x.var(dim=axis, keepdim=True, unbiased=False)
    return (x - m) / torch.sqrt(v + eps)
def _scatter_add(tgt, idx, src, axis=0):
    return tgt.clone().index_add_(axis, idx, src)
def _segment_sum(src, seg, n):
    out = torch.zeros((n,) + src.shape[1:], dtype=src.dtype)
    return out.index_add_(0, seg, src)
def _pad(x, pw):
    flat = tuple(v for pair in reversed(pw) for v in pair)
    return torch.nn.functional.pad(x, flat)

kernel_name = 'concat_heads__linear_kernel_hedgehog__sliding_local_dilated__prompt'

def setup_inputs():

    _w = (lambda a: (lambda t: t.to(torch.bfloat16) if t.is_floating_point() else t)(torch.as_tensor(a)))
    def _A_setup():
        B, S, D, H = 1, 2048, 576, 32
        Dh = D // H
        return {
            "x": _w((lambda *s: torch.randn(*s, dtype=torch.bfloat16) * 0.2)(B, S, D)),
            "wqkv": _w((lambda *s: torch.randn(*s, dtype=torch.bfloat16) * 0.8)(D, 3 * D)),
            "w_phi": _w((lambda *s: torch.randn(*s, dtype=torch.bfloat16) * 0.8)(H, Dh, Dh) * (Dh ** -0.5)),
            "wo": _w((lambda *s: torch.randn(*s, dtype=torch.bfloat16) * 0.2)(D, D)),
        }
    _A_in = _A_setup()

    def _B_setup():
        B, S, D = 1, 2048, 576
        return {
            "x": _w((lambda *s: torch.randn(*s, dtype=torch.bfloat16) * 0.2)(B, S, D)),
            "wqkv": _w((lambda *s: torch.randn(*s, dtype=torch.bfloat16) * 0.2)(D, 3 * D)),
            "wo": _w((lambda *s: torch.randn(*s, dtype=torch.bfloat16) * 0.05)(D, D)),
        }
    _B_in = _B_setup()

    return {
        "A_x": _A_in["x"],
        "A_wqkv": _A_in["wqkv"],
        "A_w_phi": _A_in["w_phi"],
        "A_wo": _A_in["wo"],
        "B_x": _B_in["x"],
        "B_wqkv": _B_in["wqkv"],
        "B_wo": _B_in["wo"],
    }

def reference(A_x, A_wqkv, A_w_phi, A_wo, B_x, B_wqkv, B_wo):
    def _A_ref(x, wqkv, w_phi, wo):
        B, S, D = x.shape
        H = 32; Dh = D // H
        qkv = (lambda x, *d: x.permute(*d))((x @ wqkv).reshape(B, S, 3, H, Dh), 2, 0, 3, 1, 4)
        q, k, v = qkv[0], qkv[1], qkv[2]
        pq = torch.einsum("bhsd,hdf->bhsf", q, w_phi)
        pk = torch.einsum("bhsd,hdf->bhsf", k, w_phi)
        phi_q = torch.concatenate([_softmax(pq, axis=-1), _softmax(-pq, axis=-1)], axis=-1)
        phi_k = torch.concatenate([_softmax(pk, axis=-1), _softmax(-pk, axis=-1)], axis=-1)
        kv = torch.einsum("bhsf,bhsd->bhfd", phi_k, v)
        z  = torch.einsum("bhsf->bhf", phi_k)
        num = torch.einsum("bhqf,bhfd->bhqd", phi_q, kv)
        den = torch.einsum("bhqf,bhf->bhq", phi_q, z)[..., None] + 1e-6
        o = num / den
        return (lambda x, *d: x.permute(*d))(o, 0, 2, 1, 3).reshape(B, S, D) @ wo

    def _B_ref(x, wqkv, wo):
        B, S, D = x.shape
        H = 32; Dh = D // H
        W, dil = 8, 2
        qkv = (lambda x, *d: x.permute(*d))((x @ wqkv).reshape(B, S, 3, H, Dh), 2, 0, 3, 1, 4)
        q, k, v = qkv[0], qkv[1], qkv[2]
        scale = Dh ** -0.5
        pos = torch.arange(S)
        off = torch.arange(W) * dil
        idx = pos[:, None] - off[None, :]
        mask = idx >= 0
        idx_c = torch.where(mask, idx, 0).reshape(-1)
        kg = k[:, :, idx_c, :].reshape(B, H, S, W, Dh)
        vg = v[:, :, idx_c, :].reshape(B, H, S, W, Dh)
        logits = torch.einsum("bhsd,bhswd->bhsw", q, kg) * scale
        logits = torch.where(mask[None, None, :, :], logits, -1e9)
        attn = _softmax(logits, axis=-1)
        o = torch.einsum("bhsw,bhswd->bhsd", attn, vg)
        return (lambda x, *d: x.permute(*d))(o, 0, 2, 1, 3).reshape(B, S, D) @ wo

    a = _A_ref(A_x, A_wqkv, A_w_phi, A_wo)
    b = _B_ref(B_x, B_wqkv, B_wo)
    return torch.concatenate([a, b], axis=-1)
```

```cpp
#include <hip/hip_runtime.h>
#include <hip/hip_bf16.h>
#include <stdint.h>

namespace naive {
typedef unsigned short u16;
__device__ __forceinline__ float bf2f(u16 v) { return __uint_as_float(((unsigned)v) << 16); }
__device__ __forceinline__ u16 f2bf(float f) { unsigned u = __float_as_uint(f); u += 0x7fffu + ((u >> 16) & 1u); return (u16)(u >> 16); }
__device__ __forceinline__ float rbf(float f) { return bf2f(f2bf(f)); }
constexpr int S = 2048, D = 576, H = 32, Dh = 18;

__global__ void gemm(const u16* A, const u16* B, u16* C, int M, int N, int K, int ldc) {
  int n = blockIdx.x * 64 + (threadIdx.x & 63);
  int m = blockIdx.y * 4 + (threadIdx.x >> 6);
  if (n >= N || m >= M) return;
  float acc = 0.f;
  for (int k = 0; k < K; ++k) acc += bf2f(A[m * K + k]) * bf2f(B[k * N + n]);
  C[m * ldc + n] = f2bf(acc);
}
__global__ __launch_bounds__(256) void phi_kernel(const u16* qkv, const u16* wphi, u16* phi) {
  int idx = blockIdx.x * 256 + threadIdx.x;
  if (idx >= 2 * H * S) return;
  int s = idx % S, h = (idx / S) % H, which = idx / (S * H);
  const u16* q = qkv + s * 3 * D + which * D + h * Dh;
  float p[Dh];
#pragma unroll
  for (int f = 0; f < Dh; ++f) {
    float a = 0.f;
#pragma unroll
    for (int d = 0; d < Dh; ++d) a += bf2f(q[d]) * bf2f(wphi[(h * Dh + d) * Dh + f]);
    p[f] = rbf(a);
  }
  float mx = p[0], mn = p[0];
#pragma unroll
  for (int f = 1; f < Dh; ++f) { mx = fmaxf(mx, p[f]); mn = fminf(mn, p[f]); }
  float sp = 0.f, sn = 0.f;
#pragma unroll
  for (int f = 0; f < Dh; ++f) { sp += __expf(p[f] - mx); sn += __expf(mn - p[f]); }
  u16* o = phi + (size_t)idx * 36;
#pragma unroll
  for (int f = 0; f < Dh; ++f) { o[f] = f2bf(__expf(p[f] - mx) / sp); o[Dh + f] = f2bf(__expf(mn - p[f]) / sn); }
}
__global__ void kv_kernel(const u16* qkv, const u16* phi, float* kvz) {
  int h = blockIdx.x;
  int t = threadIdx.x;
  if (t >= 36 * 19) return;
  int f = t / 19, d = t % 19;
  const u16* pk = phi + (size_t)(H * S + h * S) * 36;
  float acc = 0.f;
  for (int s = 0; s < S; ++s) {
    float v = d < Dh ? bf2f(qkv[s * 3 * D + 2 * D + h * Dh + d]) : 1.f;
    acc += bf2f(pk[s * 36 + f]) * v;
  }
  kvz[h * 684 + t] = rbf(acc);
}
__global__ void oa_kernel(const u16* phi, const float* kvz, u16* o) {
  int idx = blockIdx.x * 256 + threadIdx.x;
  if (idx >= H * S) return;
  int s = idx % S, h = idx / S;
  const u16* pq = phi + (size_t)idx * 36;
  float den = 0.f;
  for (int f = 0; f < 36; ++f) den += bf2f(pq[f]) * kvz[h * 684 + f * 19 + 18];
  den = rbf(rbf(den) + 1e-6f);
  for (int d = 0; d < Dh; ++d) {
    float num = 0.f;
    for (int f = 0; f < 36; ++f) num += bf2f(pq[f]) * kvz[h * 684 + f * 19 + d];
    o[s * D + h * Dh + d] = f2bf(rbf(num) / den);
  }
}
__global__ void ob_kernel(const u16* qkv, u16* o) {
  int idx = blockIdx.x * 256 + threadIdx.x;
  if (idx >= H * S) return;
  int s = idx % S, h = idx / S;
  const u16* q = qkv + s * 3 * D + h * Dh;
  float lg[8];
  const float scale = 0.23570226039551584f;
#pragma unroll
  for (int w = 0; w < 8; ++w) {
    int j = s - 2 * w;
    if (j >= 0) {
      const u16* k = qkv + j * 3 * D + D + h * Dh;
      float a = 0.f;
      for (int d = 0; d < Dh; ++d) a += bf2f(q[d]) * bf2f(k[d]);
      lg[w] = rbf(rbf(a) * scale);
    } else lg[w] = -1e9f;
  }
  float mx = lg[0];
#pragma unroll
  for (int w = 1; w < 8; ++w) mx = fmaxf(mx, lg[w]);
  float sum = 0.f;
#pragma unroll
  for (int w = 0; w < 8; ++w) { lg[w] = __expf(lg[w] - mx); sum += lg[w]; }
#pragma unroll
  for (int w = 0; w < 8; ++w) lg[w] = rbf(lg[w] / sum);
  for (int d = 0; d < Dh; ++d) {
    float a = 0.f;
#pragma unroll
    for (int w = 0; w < 8; ++w) {
      int j = s - 2 * w; if (j < 0) j = 0;
      a += lg[w] * bf2f(qkv[j * 3 * D + 2 * D + h * Dh + d]);
    }
    o[s * D + h * Dh + d] = f2bf(a);
  }
}

inline void launch(void* const* d_in, void* d_out, void* d_ws, hipStream_t stream) {
  const u16* Ax = (const u16*)d_in[0]; const u16* Aw = (const u16*)d_in[1]; const u16* Aphi = (const u16*)d_in[2];
  const u16* Awo = (const u16*)d_in[3]; const u16* Bx = (const u16*)d_in[4]; const u16* Bw = (const u16*)d_in[5];
  const u16* Bwo = (const u16*)d_in[6];
  u16* out = (u16*)d_out;
  char* ws = (char*)d_ws;
  u16* qkvA = (u16*)ws; ws += (size_t)S * 3 * D * 2;
  u16* qkvB = (u16*)ws; ws += (size_t)S * 3 * D * 2;
  u16* phi = (u16*)ws; ws += (size_t)2 * H * S * 36 * 2;
  float* kvz = (float*)ws; ws += (size_t)H * 684 * 4 + 256;
  u16* oA = (u16*)ws; ws += (size_t)S * D * 2;
  u16* oB = (u16*)ws; ws += (size_t)S * D * 2;
  gemm<<<dim3(27, S / 4), 256, 0, stream>>>(Ax, Aw, qkvA, S, 3 * D, D, 3 * D);
  gemm<<<dim3(27, S / 4), 256, 0, stream>>>(Bx, Bw, qkvB, S, 3 * D, D, 3 * D);
  phi_kernel<<<2 * H * S / 256, 256, 0, stream>>>(qkvA, Aphi, phi);
  kv_kernel<<<H, 704, 0, stream>>>(qkvA, phi, kvz);
  oa_kernel<<<H * S / 256, 256, 0, stream>>>(phi, kvz, oA);
  ob_kernel<<<H * S / 256, 256, 0, stream>>>(qkvB, oB);
  gemm<<<dim3(9, S / 4), 256, 0, stream>>>(oA, Awo, out, S, D, D, 2 * D);
  gemm<<<dim3(9, S / 4), 256, 0, stream>>>(oB, Bwo, out + D, S, D, D, 2 * D);
}
}

extern "C" void kernel_launch(void* const* d_in, const int* in_sizes, int n_in, void* d_out, int out_size, void* d_ws,
                              size_t ws_size, hipStream_t stream) {
  naive::launch(d_in, d_out, d_ws, stream);
}
```

```cpp
#include <hip/hip_runtime.h>
#include <stdint.h>

namespace hk {
typedef unsigned int u32;
typedef unsigned long long u64;
typedef unsigned short u16;
typedef float f32x4 __attribute__((ext_vector_type(4)));
typedef float f32x2 __attribute__((ext_vector_type(2)));
typedef u32 u32x4 __attribute__((ext_vector_type(4)));
typedef u32 u32x2 __attribute__((ext_vector_type(2)));
typedef __bf16 bf16x8 __attribute__((ext_vector_type(8)));
typedef __bf16 bf16x2 __attribute__((ext_vector_type(2)));
typedef short s16x4 __attribute__((ext_vector_type(4)));
#define LDS_AS __attribute__((address_space(3)))
#define GLB_AS __attribute__((address_space(1)))
#define DEVI __device__ __forceinline__

constexpr int XROW = 1152, WROW = 3456;
constexpr size_t WS_OF = 0;
constexpr size_t WS_SLAB = 5u << 20;
constexpr size_t WS_FLAG = 6u << 20;
constexpr size_t WS_EPOCH = (6u << 20) + 65536;
constexpr int SLAB_UNITS = 368;
constexpr int SLOT = 36864, SLOT0 = 2048, WOFF = 18432;
constexpr int LQ = SLOT0 + SLOT;
constexpr int LF = LQ + 9 * 9216;
constexpr int LPK = 1024, PKS = 296;
constexpr int LVI = LF, VIS = 168;
constexpr int LKX = LF + 144 * VIS;
constexpr int LKVF = LF + 128 * VIS;
constexpr int LPHF = 149504;
constexpr int LPHR = 157696;
constexpr int LQX = 160512;
constexpr int LDS_BYTES = 163840;

DEVI u32x2 tr_read(u32 a) { return __builtin_bit_cast(u32x2, __builtin_amdgcn_ds_read_tr16_b64_v4i16((LDS_AS s16x4*)a)); }
DEVI u32x4 frag_tr(u32 a0, u32 a1) { u32x2 x = tr_read(a0), y = tr_read(a1); return u32x4{x[0], x[1], y[0], y[1]}; }
DEVI u32x4 lds_r128(u32 a) { return *(LDS_AS u32x4*)a; }
DEVI u32x2 lds_r64(u32 a) { return *(LDS_AS u32x2*)a; }
DEVI u32 lds_r32(u32 a) { return *(LDS_AS u32*)a; }
DEVI u32 lds_r16(u32 a) { return *(LDS_AS u16*)a; }
DEVI void lds_w128(u32 a, u32x4 v) { *(LDS_AS u32x4*)a = v; }
DEVI void lds_w64(u32 a, u32x2 v) { *(LDS_AS u32x2*)a = v; }
DEVI void lds_w32(u32 a, u32 v) { *(LDS_AS u32*)a = v; }
DEVI f32x4 mfma32(u32x4 a, u32x4 b, f32x4 c) {
  return __builtin_amdgcn_mfma_f32_16x16x32_bf16(__builtin_bit_cast(bf16x8, a), __builtin_bit_cast(bf16x8, b), c, 0, 0, 0);
}
DEVI f32x4 mfma16(u32x2 a, u32x2 b, f32x4 c) {
  return __builtin_amdgcn_mfma_f32_16x16x16bf16_1k(__builtin_bit_cast(s16x4, a), __builtin_bit_cast(s16x4, b), c, 0, 0, 0);
}
DEVI u32 pk(float a, float b) { f32x2 v = {a, b}; return __builtin_bit_cast(u32, __builtin_convertvector(v, bf16x2)); }
DEVI u32x2 pk4(f32x4 v) { return u32x2{pk(v[0], v[1]), pk(v[2], v[3])}; }

DEVI void dma_full(u32 lds, u32 voff, __amdgpu_buffer_rsrc_t srd, u32 soff) {
  asm volatile("s_mov_b32 m0, %0\n\ts_nop 0\n\tbuffer_load_dwordx4 %1, %2, %3 offen lds" ::"s"(lds), "v"(voff), "s"(srd), "s"(soff) : "memory");
}
DEVI void dma_mask(u32 lds, u32 voff, __amdgpu_buffer_rsrc_t srd, u32 soff, u64 mask) {
  asm volatile("s_mov_b32 m0, %0\n\ts_mov_b64 exec, %4\n\tbuffer_load_dwordx4 %1, %2, %3 offen lds\n\ts_mov_b64 exec, -1" ::"s"(lds), "v"(voff), "s"(srd),
               "s"(soff), "s"(mask)
               : "memory");
}
template <int N>
DEVI void wait_bar() { asm volatile("s_waitcnt vmcnt(%0) lgkmcnt(0)\n\ts_barrier" ::"n"(N) : "memory"); }
DEVI void bar_lds() { asm volatile("s_waitcnt lgkmcnt(0)\n\ts_barrier" ::: "memory"); }

template <int OP>
DEVI float rop(float a, float b) { return OP == 0 ? fmaxf(a, b) : (OP == 1 ? fminf(a, b) : a + b); }
template <int OP>
DEVI float red4(float a, float b, float c, float d) {
  auto r1 = __builtin_amdgcn_permlane32_swap(__float_as_uint(a), __float_as_uint(b), false, false);
  auto r2 = __builtin_amdgcn_permlane32_swap(__float_as_uint(c), __float_as_uint(d), false, false);
  u32 r10 = r1[0], r11 = r1[1], r20 = r2[0], r21 = r2[1];
  float ab = rop<OP>(__uint_as_float(r10), __uint_as_float(r11));
  float cd = rop<OP>(__uint_as_float(r20), __uint_as_float(r21));
  auto r3 = __builtin_amdgcn_permlane16_swap(__float_as_uint(ab), __float_as_uint(cd), false, false);
  u32 r30 = r3[0], r31 = r3[1];
  return rop<OP>(__uint_as_float(r30), __uint_as_float(r31));
}
DEVI void bcast4(float t, float& a, float& b, float& c, float& d) {
  u32 tu = __float_as_uint(t);
  auto s1 = __builtin_amdgcn_permlane16_swap(tu, tu, false, false);
  u32 x = s1[0], y = s1[1];
  auto s2 = __builtin_amdgcn_permlane32_swap(x, x, false, false);
  auto s3 = __builtin_amdgcn_permlane32_swap(y, y, false, false);
  u32 s20 = s2[0], s21 = s2[1], s30 = s3[0], s31 = s3[1];
  a = __uint_as_float(s20); b = __uint_as_float(s21); c = __uint_as_float(s30); d = __uint_as_float(s31);
}

constexpr float LOG2E = 1.4426950408889634f;

struct Phi { u32x2 p[2][2], m[2][2]; u32 l[2]; };

DEVI void hedgehog(const u32x2 (&mainv)[2][2], const u32x2 (&left)[2], u32 phf, int g, Phi& o) {
  u32x4 fM0 = lds_r128(phf), fM1 = lds_r128(phf + 1024), fL0 = lds_r128(phf + 2048), fL1 = lds_r128(phf + 3072);
  f32x4 T[2][2], TL[2];
  const f32x4 z = {0.f, 0.f, 0.f, 0.f};
#pragma unroll
  for (int sb = 0; sb < 2; ++sb) {
    u32x4 b0 = {mainv[0][sb][0], mainv[0][sb][1], left[sb][0], left[sb][1]};
    u32x4 b1 = {mainv[1][sb][0], mainv[1][sb][1], left[sb][0], left[sb][1]};
    T[0][sb] = mfma32(fM0, b0, z);
    T[1][sb] = mfma32(fM1, b1, z);
    TL[sb] = mfma32(fL1, b1, mfma32(fL0, b0, z));
  }
  float mx[2][2], mn[2][2];
#pragma unroll
  for (int h = 0; h < 2; ++h)
#pragma unroll
    for (int sb = 0; sb < 2; ++sb) {
      f32x4 t = T[h][sb]; float l = TL[sb][h];
      mx[h][sb] = fmaxf(fmaxf(fmaxf(t[0], t[1]), fmaxf(t[2], t[3])), l);
      mn[h][sb] = fminf(fminf(fminf(t[0], t[1]), fminf(t[2], t[3])), l);
    }
  float tmx = red4<0>(mx[0][0], mx[0][1], mx[1][0], mx[1][1]) * (-LOG2E);
  float tmn = red4<1>(mn[0][0], mn[0][1], mn[1][0], mn[1][1]) * LOG2E;
  bcast4(tmx, mx[0][0], mx[0][1], mx[1][0], mx[1][1]);
  bcast4(tmn, mn[0][0], mn[0][1], mn[1][0], mn[1][1]);
  const bool lo = g < 2;
  const float sg = lo ? LOG2E : -LOG2E;
  f32x4 ep[2][2], em[2][2]; float el[2][2], sp[2][2], sm[2][2];
#pragma unroll
  for (int h = 0; h < 2; ++h)
#pragma unroll
    for (int sb = 0; sb < 2; ++sb) {
#pragma unroll
      for (int r = 0; r < 4; ++r) {
        ep[h][sb][r] = __builtin_amdgcn_exp2f(fmaf(T[h][sb][r], LOG2E, mx[h][sb]));
        em[h][sb][r] = __builtin_amdgcn_exp2f(fmaf(T[h][sb][r], -LOG2E, mn[h][sb]));
      }
      float e = __builtin_amdgcn_exp2f(fmaf(TL[sb][h], sg, lo ? mx[h][sb] : mn[h][sb]));
      el[h][sb] = e;
      sp[h][sb] = (ep[h][sb][0] + ep[h][sb][1]) + (ep[h][sb][2] + ep[h][sb][3]) + (lo ? e : 0.f);
      sm[h][sb] = (em[h][sb][0] + em[h][sb][1]) + (em[h][sb][2] + em[h][sb][3]) + (lo ? 0.f : e);
    }
  float tsp = __builtin_amdgcn_rcpf(red4<2>(sp[0][0], sp[0][1], sp[1][0], sp[1][1]));
  float tsm = __builtin_amdgcn_rcpf(red4<2>(sm[0][0], sm[0][1], sm[1][0], sm[1][1]));
  bcast4(tsp, sp[0][0], sp[0][1], sp[1][0], sp[1][1]);
  bcast4(tsm, sm[0][0], sm[0][1], sm[1][0], sm[1][1]);
#pragma unroll
  for (int sb = 0; sb < 2; ++sb) {
#pragma unroll
    for (int h = 0; h < 2; ++h) {
      o.p[h][sb] = pk4(ep[h][sb] * sp[h][sb]);
      o.m[h][sb] = pk4(em[h][sb] * sm[h][sb]);
    }
    o.l[sb] = pk(el[0][sb] * (lo ? sp[0][sb] : sm[0][sb]), el[1][sb] * (lo ? sp[1][sb] : sm[1][sb]));
  }
}

template <int BR>
DEVI void k1_body(const char* __restrict__ X, const char* __restrict__ W, const char* __restrict__ PHI, char* ws, int rb, int hq, u32 lb) {
  const int tid = threadIdx.x, l = tid & 63;
  const int w = __builtin_amdgcn_readfirstlane(tid >> 6);
  const int rg = w & 3, hpl = w >> 2;
  const int c = l & 15, g = l >> 4, q = c >> 2, p = c & 3, g0 = g & 1, g1 = g >> 1;
  __amdgpu_buffer_rsrc_t srdX = __builtin_amdgcn_make_buffer_rsrc((void*)X, (short)0, 2048 * XROW, 0x00020000);
  __amdgpu_buffer_rsrc_t srdW = __builtin_amdgcn_make_buffer_rsrc((void*)W, (short)0, 576 * WROW, 0x00020000);
  const int rot = (rb + 2 * hq) % 9;
  constexpr int NA = BR ? 6 : 5;

  u32x2 ep = {0, 0};
  if (BR == 0) {
    const char* ea = ws + WS_EPOCH + hq * 256;
    asm volatile("global_load_dwordx2 %0, %1, off sc1" : "=v"(ep) : "v"(ea) : "memory");
    __amdgpu_buffer_rsrc_t srdP = __builtin_amdgcn_make_buffer_rsrc((void*)PHI, (short)0, 32 * 648, 0x00020000);
    if (w < 3) dma_mask(lb + LPHR + w * 864, (u32)(hq * 2592 + (w * 54 + l) * 16), srdP, 0, (1ull << 54) - 1);
  }
  u32 vx[2], vw[3], vq[2], vh = 0;
#pragma unroll
  for (int i = 0; i < 2; ++i) {
    int row = 16 * w + 8 * i + (l >> 3);
    vx[i] = (u32)((rb * 128 + row) * XROW + (((l & 7) ^ ((row >> 1) & 7)) << 4));
  }
#pragma unroll
  for (int i = 0; i < 3; ++i) {
    int U = (i * 8 + w) * 48 + l;
    int rho = U / 18, un = U - rho * 18;
    int k = (rho & ~12) | ((rho & 8) >> 1) | ((rho & 4) << 1);
    vw[i] = (u32)(k * WROW + (un < 9 ? 1152 : 2304 - 144) + hq * 144 + un * 16);
  }
#pragma unroll
  for (int i = 0; i < 2; ++i) {
    int U = (2 * w + i) * 36 + l;
    int rho = U / 9, un = U - rho * 9;
    int k = (rho & 0x38) | ((rho & 1) << 2) | ((rho >> 1) & 3);
    vq[i] = (u32)(k * WROW + hq * 144 + un * 16);
  }
  if (BR) {
    int row = 2 * w + ((l >> 3) & 1);
    int grow = rb * 128 - 16 + row; grow = grow < 0 ? 0 : grow;
    vh = (u32)(grow * XROW + (((l & 7) ^ ((row >> 1) & 7)) << 4));
  }
  auto kc_of = [&](int cc) { int k = cc + rot; return k >= 9 ? k - 9 : k; };
  auto issue_chunk = [&](int cc) {
    const int kc = kc_of(cc);
    const u32 sl = lb + SLOT0 + (cc & 3) * SLOT;
    dma_full(sl + w * 2048, vx[0], srdX, (u32)(kc * 128));
    dma_full(sl + w * 2048 + 1024, vx[1], srdX, (u32)(kc * 128));
#pragma unroll
    for (int i = 0; i < 3; ++i) dma_mask(sl + WOFF + (i * 8 + w) * 768, vw[i], srdW, (u32)(kc * 64 * WROW), (1ull << 48) - 1);
    if (BR) dma_mask(sl + 16384 + w * 256, vh, srdX, (u32)(kc * 128), 0xffffull);
  };
  auto issue_q = [&](int cc) {
    const int kc = kc_of(cc);
#pragma unroll
    for (int i = 0; i < 2; ++i) dma_mask(lb + LQ + cc * 9216 + (2 * w + i) * 576, vq[i], srdW, (u32)(kc * 64 * WROW), (1ull << 36) - 1);
  };
  issue_chunk(0); issue_chunk(1); issue_chunk(2); issue_chunk(3);

  const u32 xa0 = lb + (rg * 32 + c) * 128 + ((g ^ ((c >> 1) & 7)) << 4);
  const u32 xa1 = xa0 ^ 64;
  const u32 xh0 = lb + (128 + c) * 128 + ((g ^ ((c >> 1) & 7)) << 4);
  const u32 xh1 = xh0 ^ 64;
  const u32 wrow = lb + WOFF + (16 * g1 + 4 * g0 + q) * 288 + hpl * 72;
  const u32 wa = wrow + 8 * p;
  const u32 wl = wrow + 32 + (p & 1) * 144;
  const u32 wh = wa + (rg & 1) * 40 + (rg >> 1) * 144;

  u32x4 xf[18][2];
  const f32x4 z4 = {0.f, 0.f, 0.f, 0.f};
  f32x4 aK0[2] = {z4, z4}, aK1[2] = {z4, z4}, aV0[2] = {z4, z4}, aV1[2] = {z4, z4}, aL[2] = {z4, z4}, aH = z4, aHL = z4;

#pragma unroll
  for (int cc = 0; cc < 9; ++cc) {
    if (cc == 0) { wait_bar<3 * NA>(); asm volatile("" : "+v"(ep)); }
    else if (cc <= 6) wait_bar<2 * NA>();
    else if (cc == 7) wait_bar<NA + 8>();
    else wait_bar<16>();
    if (cc >= 1 && cc <= 5) issue_chunk(cc + 3);
    if (cc == 6) { issue_q(0); issue_q(1); issue_q(2); issue_q(3); }
    if (cc == 7) { issue_q(4); issue_q(5); issue_q(6); issue_q(7); }
    if (cc == 8) issue_q(8);
    const u32 sl = SLOT0 + (cc & 3) * SLOT;
#pragma unroll
    for (int ks = 0; ks < 2; ++ks) {
      const int t = cc * 2 + ks;
      xf[t][0] = lds_r128((ks ? xa1 : xa0) + sl);
      xf[t][1] = lds_r128((ks ? xa1 : xa0) + sl + 2048);
      const u32 o0 = sl + ks * 32 * 288, o1 = o0 + 8 * 288;
      u32x4 fK0 = frag_tr(wa + o0, wa + o1);
      u32x4 fK1 = frag_tr(wa + o0 + 40, wa + o1 + 40);
      u32x4 fV0 = frag_tr(wa + o0 + 144, wa + o1 + 144);
      u32x4 fV1 = frag_tr(wa + o0 + 184, wa + o1 + 184);
      u32x4 fL = frag_tr(wl + o0, wl + o1);
#pragma unroll
      for (int sb = 0; sb < 2; ++sb) {
        aK0[sb] = mfma32(fK0, xf[t][sb], aK0[sb]);
        aK1[sb] = mfma32(fK1, xf[t][sb], aK1[sb]);
        aV0[sb] = mfma32(fV0, xf[t][sb], aV0[sb]);
        aV1[sb] = mfma32(fV1, xf[t][sb], aV1[sb]);
        aL[sb] = mfma32(fL, xf[t][sb], aL[sb]);
      }
      if (BR) {
        u32x4 xh = lds_r128((ks ? xh1 : xh0) + sl);
        u32x4 fH = frag_tr(wh + o0, wh + o1);
        aH = mfma32(fH, xh, aH);
        aHL = mfma32(fL, xh, aHL);
      }
    }
    if (BR == 0 && cc == 1) {
      const int h = rg & 1, lf = rg >> 1;
      const int f = lf ? 16 + (q & 1) : c;
      const bool rowok = lf ? (p == h) : true;
      const u32 pb = lb + LPHR + (hpl * 2 + h) * 648 + f * 2;
      const int d0 = 2 * h + 4 * g;
      u32 e0 = lds_r16(pb + (d0 + 0) * 36), e1 = lds_r16(pb + (d0 + 1) * 36), e2 = lds_r16(pb + (d0 + 2) * 36), e3 = lds_r16(pb + (d0 + 3) * 36);
      const int dl = h ? 0 : 16;
      u32 l0 = lds_r16(pb + dl * 36), l1 = lds_r16(pb + (dl + 1) * 36);
      u32 lw = (g == 0) ? (l0 | (l1 << 16)) : 0u;
      u32x4 fr = {e0 | (e1 << 16), e2 | (e3 << 16), h ? 0u : lw, h ? lw : 0u};
      if (!rowok) fr = u32x4{0, 0, 0, 0};
      lds_w128(lb + LPHF + (hpl * 4 + rg) * 1024 + l * 16, fr);
    }
  }

  u32x2 kM[2][2], kL[2];
  constexpr int VROW0 = BR ? 16 : 0;
  const u32 ONES = 0x3f803f80u;
#pragma unroll
  for (int sb = 0; sb < 2; ++sb) {
    kM[0][sb] = pk4(aK0[sb]); kM[1][sb] = pk4(aK1[sb]); kL[sb] = pk4(aL[sb]);
    const u32 va = lb + LVI + (VROW0 + rg * 32 + sb * 16 + c) * VIS + hpl * 80;
    lds_w64(va + 8 * g, pk4(aV0[sb]));
    lds_w64(va + 32 + 8 * g, pk4(aV1[sb]));
    if (g0) lds_w64(va + 64 + 8 * g1, u32x2{g1 ? kL[sb][1] : kL[sb][0], ONES});
  }
  const u32 qrow = lb + LQ + (16 * g1 + 8 * g0 + 2 * q) * 144;
  const u32 qa = qrow + hpl * 72 + 8 * p;
  const u32 ql = qrow + 32 + (p & 1) * 72;
  f32x4 aQ0[2] = {z4, z4}, aQ1[2] = {z4, z4}, aQL = z4;
  auto qsteps = [&](int t0, int t1) {
#pragma unroll
    for (int t = t0; t < t1; ++t) {
      const u32 o0 = t * 32 * 144, o1 = o0 + 144;
      u32x4 f0 = frag_tr(qa + o0, qa + o1), f1 = frag_tr(qa + o0 + 40, qa + o1 + 40), fl = frag_tr(ql + o0, ql + o1);
      aQ0[0] = mfma32(f0, xf[t][0], aQ0[0]);
      aQ0[1] = mfma32(f0, xf[t][1], aQ0[1]);
      aQ1[0] = mfma32(f1, xf[t][0], aQ1[0]);
      aQ1[1] = mfma32(f1, xf[t][1], aQ1[1]);
      u32x4 xs;
#pragma unroll
      for (int e = 0; e < 4; ++e) xs[e] = hpl ? xf[t][1][e] : xf[t][0][e];
      aQL = mfma32(fl, xs, aQL);
    }
  };
  char* ofp = ws + WS_OF + ((size_t)(BR * 128 + rb * 8 + rg * 2) * 18) * 1024;
  const int hp = hq * 2 + hpl;
  auto store_o = [&](int sb, f32x4 o0, f32x4 o1, float l0, float l1, float r0, float r1) {
    u32x2 a = pk4(o0 * r0), b = pk4(o1 * r1);
    *(u32x4*)(ofp + (sb * 18 + hp) * 1024 + l * 16) = u32x4{a[0], a[1], b[0], b[1]};
    float rr = g0 ? r1 : r0;
    if (g < 2) *(u32*)(ofp + (sb * 18 + 16 + (hq >> 2)) * 1024 + (c + 16 * (hq & 3)) * 16 + 8 * hpl + 4 * g) = pk(l0 * rr, l1 * rr);
  };

  if (BR == 0) {
    const u32 phf = lb + LPHF + hpl * 4096 + l * 16;
    Phi fk;
    hedgehog(kM, kL, phf, g, fk);
    bar_lds();
#pragma unroll
    for (int sb = 0; sb < 2; ++sb) {
      const u32 pa = lb + LPK + (rg * 32 + sb * 16 + c) * PKS + hpl * 144;
      lds_w64(pa + 8 * g, fk.p[0][sb]);
      lds_w64(pa + 32 + 8 * g, fk.m[0][sb]);
      lds_w64(pa + 64 + 8 * g, fk.p[1][sb]);
      lds_w64(pa + 96 + 8 * g, fk.m[1][sb]);
      lds_w32(pa + 128 + 4 * g, fk.l[sb]);
    }
    bar_lds();
    __amdgpu_buffer_rsrc_t srdS = __builtin_amdgcn_make_buffer_rsrc((void*)(ws + WS_SLAB), (short)0, 128 * 6144, 0x00020000);
    const u32 slab = (u32)((hq * 16 + rb) * 6144);
    const int rr = 4 * (4 * g0 + q) + 2 * g1;
    const u32 prow = lb + LPK + rr * PKS + hpl * 144, vrow = lb + LVI + rr * VIS + hpl * 80;
    const bool h0set = (0x47 >> (c & 7)) & 1;
    if (rg < 2) {
      f32x4 dpm = z4, dmm = z4, dpl = z4, dml = z4;
      const u32 pa = prow + rg * 64 + 8 * p, va = vrow + rg * 32 + 8 * p, vl = vrow + 64 + 8 * (p & 1);
#pragma unroll
      for (int t = 0; t < 4; ++t) {
        u32x4 ap = frag_tr(pa + t * 32 * PKS, pa + t * 32 * PKS + PKS);
        u32x4 am = frag_tr(pa + 32 + t * 32 * PKS, pa + 32 + t * 32 * PKS + PKS);
        u32x4 bv = frag_tr(va + t * 32 * VIS, va + t * 32 * VIS + VIS);
        u32x4 bl = frag_tr(vl + t * 32 * VIS, vl + t * 32 * VIS + VIS);
        dpm = mfma32(ap, bv, dpm); dmm = mfma32(am, bv, dmm);
        dpl = mfma32(ap, bl, dpl); dml = mfma32(am, bl, dml);
      }
      u32x2 a = pk4(dpm), b = pk4(dmm);
      __builtin_amdgcn_raw_buffer_store_b128(u32x4{a[0], a[1], b[0], b[1]}, srdS, slab + (hpl * 2 + rg) * 1024 + l * 16, 0, 16);
      a = pk4(dpl); b = pk4(dml);
      if (((c >> 3) == hpl) && (h0set == (rg == 0)))
        __builtin_amdgcn_raw_buffer_store_b128(u32x4{a[0], a[1], b[0], b[1]}, srdS, slab + 4096 + l * 16, 0, 16);
    } else if (rg == 2) {
      f32x4 d0 = z4, d1 = z4, dl = z4;
      const u32 pa = prow + 128 + 8 * (p >> 1), va = vrow + 8 * p, vl = vrow + 64 + 8 * (p & 1);
#pragma unroll
      for (int t = 0; t < 4; ++t) {
        u32x4 af = frag_tr(pa + t * 32 * PKS, pa + t * 32 * PKS + PKS);
        u32x4 b0 = frag_tr(va + t * 32 * VIS, va + t * 32 * VIS + VIS);
        u32x4 b1 = frag_tr(va + 32 + t * 32 * VIS, va + 32 + t * 32 * VIS + VIS);
        u32x4 bl = frag_tr(vl + t * 32 * VIS, vl + t * 32 * VIS + VIS);
        d0 = mfma32(af, b0, d0); d1 = mfma32(af, b1, d1); dl = mfma32(af, bl, dl);
      }
      u32 alm = pk(g0 ? d0[2] : d0[0], g0 ? d1[3] : d1[1]);
      float x0 = g0 ? dl[2] : dl[0], x1 = g0 ? dl[3] : dl[1];
      u32 all = pk(h0set ? x0 : 0.f, h0set ? 0.f : x1);
      __builtin_amdgcn_raw_buffer_store_b32(alm, srdS, slab + 5120 + hpl * 256 + l * 4, 0, 16);
      if ((c >> 3) == hpl) __builtin_amdgcn_raw_buffer_store_b32(all, srdS, slab + 5632 + l * 4, 0, 16);
    }
    wait_bar<0>();
    const u64 E = ((u64)(u32)__builtin_amdgcn_readfirstlane(ep[1]) << 32) | (u32)__builtin_amdgcn_readfirstlane(ep[0]);
    const u64 tag = E * 6364136223846793005ull + 1442695040888963407ull;
    GLB_AS u64* flags = (GLB_AS u64*)(ws + WS_FLAG + hq * 256);
    if (tid == 0) __hip_atomic_store(flags + rb, tag, __ATOMIC_RELAXED, __HIP_MEMORY_SCOPE_AGENT);
    qsteps(0, 18);
    if (g < 2) lds_w64(lb + LQX + ((2 * rg + hpl) * 2 + g) * 128 + c * 8, pk4(aQL));
    bar_lds();
    u32x2 qM[2][2], qL[2];
#pragma unroll
    for (int sb = 0; sb < 2; ++sb) {
      qM[0][sb] = pk4(aQ0[sb]); qM[1][sb] = pk4(aQ1[sb]);
      qL[sb] = lds_r64(lb + LQX + ((2 * rg + sb) * 2 + hpl) * 128 + c * 8);
    }
    u32x4 gv[16];
    if (w < 6) {
      for (int spin = 0; spin < (1 << 22); ++spin) {
        u64 f = __hip_atomic_load(flags + c, __ATOMIC_RELAXED, __HIP_MEMORY_SCOPE_AGENT);
        if (__all(f == tag)) break;
        __builtin_amdgcn_s_sleep(1);
      }
      __builtin_amdgcn_fence(__ATOMIC_ACQUIRE, "wavefront");
      if (rb == 0 && tid == 0)
        __hip_atomic_store((GLB_AS u64*)(ws + WS_EPOCH + hq * 256), tag, __ATOMIC_RELAXED, __HIP_MEMORY_SCOPE_AGENT);
      const u32 go = (u32)(hq * 16 * 6144 + (tid < SLAB_UNITS ? tid : 0) * 16);
#pragma unroll
      for (int i = 0; i < 16; ++i) gv[i] = __builtin_amdgcn_raw_buffer_load_b128(srdS, go, i * 6144, 16);
    }
    Phi fq;
    hedgehog(qM, qL, phf, g, fq);
    if (w < 6) {
      float s[8] = {0.f, 0.f, 0.f, 0.f, 0.f, 0.f, 0.f, 0.f};
#pragma unroll
      for (int i = 0; i < 16; ++i)
#pragma unroll
        for (int e = 0; e < 4; ++e) {
          u32 v = gv[i][e];
          s[2 * e] += __uint_as_float(v << 16);
          s[2 * e + 1] += __uint_as_float(v & 0xffff0000u);
        }
      if (tid < SLAB_UNITS) lds_w128(lb + LKVF + tid * 16, u32x4{pk(s[0], s[1]), pk(s[2], s[3]), pk(s[4], s[5]), pk(s[6], s[7])});
    }
    bar_lds();
    const u32 kf = lb + LKVF;
    u32x4 AM0 = lds_r128(kf + (hpl * 2) * 1024 + l * 16), AM1 = lds_r128(kf + (hpl * 2 + 1) * 1024 + l * 16);
    const int l8 = g * 16 + (c & 7) + 8 * hpl;
    u32x4 AMLm = lds_r128(kf + 4096 + l8 * 16);
    u32 ALm = lds_r32(kf + 5120 + hpl * 256 + l * 4), ALL = lds_r32(kf + 5632 + l8 * 4);
    u32x4 AML0, AML1;
#pragma unroll
    for (int e = 0; e < 4; ++e) { AML0[e] = h0set ? AMLm[e] : 0u; AML1[e] = h0set ? 0u : AMLm[e]; }
#pragma unroll
    for (int sb = 0; sb < 2; ++sb) {
      u32x4 b0 = {fq.p[0][sb][0], fq.p[0][sb][1], fq.m[0][sb][0], fq.m[0][sb][1]};
      u32x4 b1 = {fq.p[1][sb][0], fq.p[1][sb][1], fq.m[1][sb][0], fq.m[1][sb][1]};
      u32x4 bl = {fq.l[sb], 0u, 0u, 0u};
      f32x4 O0 = mfma32(u32x4{ALm & 0xffffu, 0u, 0u, 0u}, bl, mfma32(AM0, b0, z4));
      f32x4 O1 = mfma32(u32x4{ALm & 0xffff0000u, 0u, 0u, 0u}, bl, mfma32(AM1, b1, z4));
      f32x4 OL = mfma32(u32x4{ALL, 0u, 0u, 0u}, bl, mfma32(AML1, b1, mfma32(AML0, b0, z4)));
      float r0 = __builtin_amdgcn_rcpf(OL[2] + 1e-6f), r1 = __builtin_amdgcn_rcpf(OL[3] + 1e-6f);
      store_o(sb, O0, O1, OL[0], OL[1], r0, r1);
    }
  } else {
    {
      const u32 kx = lb + LKX + l * 8;
      if (rg < 3) {
        const u32 b = kx + (((rg + 1) * 2 + hpl) * 3) * 512;
        lds_w64(b, kM[0][1]); lds_w64(b + 512, kM[1][1]); lds_w64(b + 1024, kL[1]);
      }
      u32x2 hv = pk4(aH);
      const u32 va = lb + LVI + c * VIS + hpl * 80;
      if (rg < 2) lds_w64(kx + (hpl * 3 + rg) * 512, hv);
      else lds_w64(va + (rg - 2) * 32 + 8 * g, hv);
      if (rg == 0) {
        u32x2 hl = pk4(aHL);
        lds_w64(kx + (hpl * 3 + 2) * 512, hl);
        if (g0) lds_w64(va + 64 + 8 * g1, u32x2{g1 ? hl[1] : hl[0], ONES});
      }
    }
    wait_bar<10>();
    qsteps(0, 8);
    wait_bar<2>();
    qsteps(8, 16);
    wait_bar<0>();
    qsteps(16, 18);
    if (g < 2) lds_w64(lb + LQX + ((2 * rg + hpl) * 2 + g) * 128 + c * 8, pk4(aQL));
    bar_lds();
    const u32 kx = lb + LKX + ((rg * 2 + hpl) * 3) * 512 + l * 8;
    u32x2 pM0 = lds_r64(kx), pM1 = lds_r64(kx + 512), pL = lds_r64(kx + 1024);
    const int par = c & 1;
    const bool ina = (4 * g + par) > c, inb = (4 * g + par + 2) > c;
    const float sc = 0.23570226039551584f * LOG2E;
    float la[2][2], lbv[2][2], mx[2][2];
#pragma unroll
    for (int sb = 0; sb < 2; ++sb) {
      u32x2 qlv = lds_r64(lb + LQX + ((2 * rg + sb) * 2 + hpl) * 128 + c * 8);
      u32x2 q0 = pk4(aQ0[sb]), q1 = pk4(aQ1[sb]);
      u32x4 b0 = {q0[0], q0[1], g == 0 ? qlv[0] : 0u, 0u};
      u32x4 b1 = {q1[0], q1[1], 0u, g == 0 ? qlv[1] : 0u};
      u32x2 kp0 = sb ? kM[0][0] : pM0, kp1 = sb ? kM[1][0] : pM1, kpl = sb ? kL[0] : pL;
      f32x4 sp0 = mfma32(u32x4{kp0[0], kp0[1], kpl[0], kpl[1]}, b0, z4);
      f32x4 sp1 = mfma32(u32x4{kp1[0], kp1[1], kpl[0], kpl[1]}, b1, z4);
      f32x4 sc0 = mfma32(u32x4{kM[0][sb][0], kM[0][sb][1], kL[sb][0], kL[sb][1]}, b0, z4);
      f32x4 sc1 = mfma32(u32x4{kM[1][sb][0], kM[1][sb][1], kL[sb][0], kL[sb][1]}, b1, z4);
      const bool dead = (sb == 0) && (rb == 0) && (rg == 0);
      float pa0 = par ? sp0[1] : sp0[0], pb0 = par ? sp0[3] : sp0[2], pa1 = par ? sp1[1] : sp1[0], pb1 = par ? sp1[3] : sp1[2];
      if (dead) { pa0 = pb0 = pa1 = pb1 = -1e30f; }
      la[0][sb] = (ina ? pa0 : (par ? sc0[1] : sc0[0])) * sc;
      lbv[0][sb] = (inb ? pb0 : (par ? sc0[3] : sc0[2])) * sc;
      la[1][sb] = (ina ? pa1 : (par ? sc1[1] : sc1[0])) * sc;
      lbv[1][sb] = (inb ? pb1 : (par ? sc1[3] : sc1[2])) * sc;
      mx[0][sb] = fmaxf(la[0][sb], lbv[0][sb]);
      mx[1][sb] = fmaxf(la[1][sb], lbv[1][sb]);
    }
    float tm = red4<0>(mx[0][0], mx[0][1], mx[1][0], mx[1][1]);
    bcast4(tm, mx[0][0], mx[0][1], mx[1][0], mx[1][1]);
    const u32 vb = lb + LVI + (rg * 32 + 4 * g + q) * VIS + hpl * 80;
#pragma unroll
    for (int sb = 0; sb < 2; ++sb) {
      const u32 v0 = vb + sb * 16 * VIS, v1 = v0 + 16 * VIS;
      u32x4 A0 = frag_tr(v0 + 8 * p, v1 + 8 * p), A1 = frag_tr(v0 + 32 + 8 * p, v1 + 32 + 8 * p);
      u32x4 AL = frag_tr(v0 + 64 + 8 * (p & 1), v1 + 64 + 8 * (p & 1));
      u32x4 P[2];
#pragma unroll
      for (int h = 0; h < 2; ++h) {
        float ea = __builtin_amdgcn_exp2f(la[h][sb] - mx[h][sb]), eb = __builtin_amdgcn_exp2f(lbv[h][sb] - mx[h][sb]);
        u32 wa_ = par ? pk(0.f, ea) : pk(ea, 0.f), wb_ = par ? pk(0.f, eb) : pk(eb, 0.f);
        P[h] = u32x4{ina ? wa_ : 0u, inb ? wb_ : 0u, ina ? 0u : wa_, inb ? 0u : wb_};
      }
      f32x4 O0 = mfma32(A0, P[0], z4), O1 = mfma32(A1, P[1], z4);
      f32x4 L0 = mfma32(AL, P[0], z4), L1 = mfma32(AL, P[1], z4);
      float r0 = __builtin_amdgcn_rcpf(L0[2]), r1 = __builtin_amdgcn_rcpf(L1[2]);
      store_o(sb, O0, O1, g0 ? L1[0] : L0[0], g0 ? L1[1] : L0[1], r0, r1);
    }
  }
}

__global__ __launch_bounds__(512, 2) void k1(const char* __restrict__ XA, const char* __restrict__ WA, const char* __restrict__ PHI,
                                             const char* __restrict__ XB, const char* __restrict__ WB, char* ws) {
  __shared__ __attribute__((aligned(16))) char smem[LDS_BYTES];
  const u32 lb = (u32)(uintptr_t)(LDS_AS char*)smem;
  const int b = blockIdx.x, x = b & 7, j = b >> 3, xx = x & 3;
  const int rb = (xx >> 1) * 8 + (j >> 2), hq = (xx & 1) * 4 + (j & 3);
  if (x < 4) k1_body<0>(XA, WA, PHI, ws, rb, hq, lb);
  else k1_body<1>(XB, WB, PHI, ws, rb, hq, lb);
}

__global__ __launch_bounds__(512, 2) void k2(const char* __restrict__ WOA, const char* __restrict__ WOB, const char* __restrict__ ws, char* __restrict__ out) {
  __shared__ __attribute__((aligned(16))) char smem[576 * 144];
  const u32 lb = (u32)(uintptr_t)(LDS_AS char*)smem;
  const int b = blockIdx.x, x = b & 7, j = b >> 3, xx = x & 3, br = x >> 2;
  const int rb = (xx >> 1) * 8 + (j >> 2), nb = (xx & 1) * 4 + (j & 3);
  const int tid = threadIdx.x, l = tid & 63;
  const int w = __builtin_amdgcn_readfirstlane(tid >> 6);
  const int rg = w & 3, ng = w >> 2;
  const int c = l & 15, g = l >> 4, q = c >> 2, p = c & 3, g0 = g & 1, g1 = g >> 1;
  __amdgpu_buffer_rsrc_t srd = __builtin_amdgcn_make_buffer_rsrc((void*)(br ? WOB : WOA), (short)0, 576 * XROW, 0x00020000);
  u32 vmain, vleft;
  {
    int U = w * 36 + l;
    int rho = U / 9, un = U - rho * 9;
    int gg = (rho >> 3) & 3, qq = (rho >> 1) & 3, hi = rho & 1;
    vmain = (u32)(((hi ? 20 : 0) + 4 * gg + qq) * XROW + nb * 144 + un * 16);
    vleft = (u32)((36 * (2 * gg + hi) + 16 + qq) * XROW + nb * 144 + un * 16);
  }
  const char* ofp = ws + WS_OF + ((size_t)(br * 128 + rb * 8 + rg * 2) * 18) * 1024 + l * 16;
  u32x4 of[18][2];
#pragma unroll
  for (int t = 0; t < 18; ++t) {
    dma_mask(lb + t * 4608 + w * 576, t < 16 ? vmain : vleft, srd, (u32)((t < 16 ? 36 * t : 288 * (t - 16)) * XROW), (1ull << 36) - 1);
    asm volatile("global_load_dwordx4 %0, %1, off" : "=v"(of[t][0]) : "v"(ofp + t * 1024) : "memory");
    asm volatile("global_load_dwordx4 %0, %1, off" : "=v"(of[t][1]) : "v"(ofp + (18 + t) * 1024) : "memory");
  }
  const u32 qa = lb + (16 * g1 + 8 * g0 + 2 * q) * 144 + ng * 64 + 8 * p;
  const u32 q4 = lb + (16 * g1 + 8 * g0 + 2 * q) * 144 + 128 + 8 * (p & 1);
  const f32x4 z4 = {0.f, 0.f, 0.f, 0.f};
  f32x4 a0[2] = {z4, z4}, a1[2] = {z4, z4}, a4 = z4;
#pragma unroll
  for (int t = 0; t < 18; ++t) {
    if (t % 3 == 0) {
      asm volatile("s_waitcnt vmcnt(%6)\n\ts_barrier"
                   : "+v"(of[t][0]), "+v"(of[t][1]), "+v"(of[t + 1][0]), "+v"(of[t + 1][1]), "+v"(of[t + 2][0]), "+v"(of[t + 2][1])
                   : "n"(3 * (15 - t))
                   : "memory");
    }
    const u32 o0 = t * 4608, o1 = o0 + 144;
    u32x4 f0 = frag_tr(qa + o0, qa + o1), f1 = frag_tr(qa + o0 + 32, qa + o1 + 32), f4 = frag_tr(q4 + o0, q4 + o1);
    a0[0] = mfma32(f0, of[t][0], a0[0]);
    a0[1] = mfma32(f0, of[t][1], a0[1]);
    a1[0] = mfma32(f1, of[t][0], a1[0]);
    a1[1] = mfma32(f1, of[t][1], a1[1]);
    u32x4 xs;
#pragma unroll
    for (int e = 0; e < 4; ++e) xs[e] = ng ? of[t][1][e] : of[t][0][e];
    a4 = mfma32(f4, xs, a4);
  }
  char* op = out + (size_t)(rb * 128 + rg * 32 + c) * 2304 + br * 1152 + nb * 144;
#pragma unroll
  for (int sb = 0; sb < 2; ++sb) {
    *(u32x2*)(op + sb * 16 * 2304 + ng * 64 + 8 * g) = pk4(a0[sb]);
    *(u32x2*)(op + sb * 16 * 2304 + ng * 64 + 32 + 8 * g) = pk4(a1[sb]);
  }
  if (g < 2) *(u32x2*)(op + ng * 16 * 2304 + 128 + 8 * g) = pk4(a4);
}
}

extern "C" void kernel_launch(void* const* d_in, const int* in_sizes, int n_in, void* d_out, int out_size, void* d_ws, size_t ws_size,
                              hipStream_t stream) {
  hk::k1<<<256, 512, 0, stream>>>((const char*)d_in[0], (const char*)d_in[1], (const char*)d_in[2], (const char*)d_in[4], (const char*)d_in[5],
                                  (char*)d_ws);
  hk::k2<<<256, 512, 0, stream>>>((const char*)d_in[3], (const char*)d_in[6], (const char*)d_ws, (char*)d_out);
}
```

```cpp
#include <hip/hip_runtime.h>
#include <stdint.h>

namespace hk {
typedef unsigned int u32;
typedef unsigned long long u64;
typedef unsigned short u16;
typedef float f32x4 __attribute__((ext_vector_type(4)));
typedef float f32x2 __attribute__((ext_vector_type(2)));
typedef u32 u32x4 __attribute__((ext_vector_type(4)));
typedef u32 u32x2 __attribute__((ext_vector_type(2)));
typedef __bf16 bf16x8 __attribute__((ext_vector_type(8)));
typedef __bf16 bf16x2 __attribute__((ext_vector_type(2)));
typedef short s16x4 __attribute__((ext_vector_type(4)));
#define LDS_AS __attribute__((address_space(3)))
#define GLB_AS __attribute__((address_space(1)))
#define DEVI __device__ __forceinline__
#define SCHED_FENCE() __builtin_amdgcn_sched_barrier(0)

template <int V> struct IC { static constexpr int value = V; };
template <int I, int N, class F>
DEVI void static_for(F&& f) {
  if constexpr (I < N) { f(IC<I>{}); static_for<I + 1, N>(f); }
}

constexpr int XROW = 1152, WROW = 3456;
constexpr size_t WS_OF = 0;
constexpr size_t WS_SLAB = 5u << 20;
constexpr size_t WS_FLAG = 6u << 20;
constexpr size_t WS_EPOCH = WS_FLAG + 65536;
constexpr size_t WS_OFLAG = WS_FLAG + 2 * 65536;
constexpr size_t WS_OEPOCH = WS_FLAG + 3 * 65536;
constexpr int SLAB_UNITS = 368;
constexpr int XR = 1024, XS = 16384;
constexpr int WR = XR + 4 * XS, WU = 9216;
constexpr int LM = WR + 8 * WU;
constexpr int LPHF = LM;
constexpr int LSUM = LM + 8192;
constexpr int LKVF = LM + 11264;
constexpr int LHALO = LM;
constexpr int KLX = LM + 18432;
constexpr int QLX = KLX + 2304;
constexpr int LVI = XR, VIS = 168;
constexpr int LPK = XR + 21504, PKS = 296;
constexpr int LKX = XR + 24576;
constexpr int LWO8 = XR + 49152;
constexpr int LOUT = XR;
constexpr int LDS_BYTES = 163840;
static_assert(QLX + 2048 <= LDS_BYTES, "lds");

DEVI u32x2 tr_read(u32 a) { return __builtin_bit_cast(u32x2, __builtin_amdgcn_ds_read_tr16_b64_v4i16((LDS_AS s16x4*)a)); }
DEVI u32x4 frag_tr(u32 a0, u32 a1) { u32x2 x = tr_read(a0), y = tr_read(a1); return u32x4{x[0], x[1], y[0], y[1]}; }
DEVI u32x4 lds_r128(u32 a) { return *(LDS_AS u32x4*)a; }
DEVI u32x2 lds_r64(u32 a) { return *(LDS_AS u32x2*)a; }
DEVI u32 lds_r32(u32 a) { return *(LDS_AS u32*)a; }
DEVI u32 lds_r16(u32 a) { return *(LDS_AS u16*)a; }
DEVI void lds_w128(u32 a, u32x4 v) { *(LDS_AS u32x4*)a = v; }
DEVI void lds_w64(u32 a, u32x2 v) { *(LDS_AS u32x2*)a = v; }
DEVI void lds_w32(u32 a, u32 v) { *(LDS_AS u32*)a = v; }
DEVI f32x4 mfma32(u32x4 a, u32x4 b, f32x4 c) {
  return __builtin_amdgcn_mfma_f32_16x16x32_bf16(__builtin_bit_cast(bf16x8, a), __builtin_bit_cast(bf16x8, b), c, 0, 0, 0);
}
DEVI u32 pk(float a, float b) { f32x2 v = {a, b}; return __builtin_bit_cast(u32, __builtin_convertvector(v, bf16x2)); }
DEVI u32x2 pk4(f32x4 v) { return u32x2{pk(v[0], v[1]), pk(v[2], v[3])}; }
DEVI float max3(float a, float b, float c) { return fmaxf(fmaxf(a, b), c); }

DEVI void dma_full(u32 lds, u32 voff, __amdgpu_buffer_rsrc_t srd, u32 soff) {
  asm volatile("s_mov_b32 m0, %0\n\ts_nop 0\n\tbuffer_load_dwordx4 %1, %2, %3 offen lds" ::"s"(lds), "v"(voff), "s"(srd), "s"(soff) : "memory");
}
DEVI void dma_mask(u32 lds, u32 voff, __amdgpu_buffer_rsrc_t srd, u32 soff, u64 mask) {
  asm volatile("s_mov_b32 m0, %0\n\ts_mov_b64 exec, %4\n\tbuffer_load_dwordx4 %1, %2, %3 offen lds\n\ts_mov_b64 exec, -1" ::"s"(lds), "v"(voff), "s"(srd),
               "s"(soff), "s"(mask)
               : "memory");
}
template <int N>
DEVI void wait_bar() { asm volatile("s_waitcnt vmcnt(%0) lgkmcnt(0)\n\ts_barrier" ::"n"(N) : "memory"); }
DEVI void bar_lds() { asm volatile("s_waitcnt lgkmcnt(0)\n\ts_barrier" ::: "memory"); }
DEVI void bar_only() { asm volatile("s_barrier" ::: "memory"); }

DEVI float redmax4(float a, float b, float c, float d, float ninf) {
  auto r1 = __builtin_amdgcn_permlane32_swap(__float_as_uint(a), __float_as_uint(b), false, false);
  auto r2 = __builtin_amdgcn_permlane32_swap(__float_as_uint(c), __float_as_uint(d), false, false);
  u32 r10 = r1[0], r11 = r1[1], r20 = r2[0], r21 = r2[1];
  float ab = max3(__uint_as_float(r10), __uint_as_float(r11), ninf);
  float cd = max3(__uint_as_float(r20), __uint_as_float(r21), ninf);
  auto r3 = __builtin_amdgcn_permlane16_swap(__float_as_uint(ab), __float_as_uint(cd), false, false);
  u32 r30 = r3[0], r31 = r3[1];
  return max3(__uint_as_float(r30), __uint_as_float(r31), ninf);
}
DEVI void bcast4(float t, float& a, float& b, float& c, float& d) {
  u32 tu = __float_as_uint(t);
  auto s1 = __builtin_amdgcn_permlane16_swap(tu, tu, false, false);
  u32 x = s1[0], y = s1[1];
  auto s2 = __builtin_amdgcn_permlane32_swap(x, x, false, false);
  auto s3 = __builtin_amdgcn_permlane32_swap(y, y, false, false);
  u32 s20 = s2[0], s21 = s2[1], s30 = s3[0], s31 = s3[1];
  a = __uint_as_float(s20); b = __uint_as_float(s21); c = __uint_as_float(s30); d = __uint_as_float(s31);
}

constexpr float LOG2E = 1.4426950408889634f;
constexpr u32 ONES = 0x3f803f80u;

struct HH {
  u32x4 f0, f1, f2, f3;
  f32x4 T0, T1, TL, S;
  float m0, m1, n0, n1;
  f32x4 p0, p1, q0, q1;
  float l0, l1;
  u32x4 b0, b1; u32 bl;
};
template <int ST>
DEVI void hh_stage(HH& s, u32x2 ma, u32x2 mb, u32x2 left, u32 phf, u32 sumf, bool lo, float ninf) {
  const f32x4 z = {0.f, 0.f, 0.f, 0.f};
  if constexpr (ST == 0) {
    s.f0 = lds_r128(phf); s.f1 = lds_r128(phf + 1024); s.f2 = lds_r128(phf + 2048); s.f3 = lds_r128(phf + 3072);
  } else if constexpr (ST == 1) {
    u32x4 b0 = {ma[0], ma[1], left[0], left[1]}, b1 = {mb[0], mb[1], left[0], left[1]};
    s.T0 = mfma32(s.f0, b0, z);
    s.T1 = mfma32(s.f1, b1, z);
    s.TL = mfma32(s.f3, b1, mfma32(s.f2, b0, z));
  } else if constexpr (ST == 2) {
    float a = max3(max3(s.T0[0], s.T0[1], s.T0[2]), s.T0[3], s.TL[0]);
    float b = max3(max3(s.T1[0], s.T1[1], s.T1[2]), s.T1[3], s.TL[1]);
    float c = max3(max3(-s.T0[0], -s.T0[1], -s.T0[2]), -s.T0[3], -s.TL[0]);
    float d = max3(max3(-s.T1[0], -s.T1[1], -s.T1[2]), -s.T1[3], -s.TL[1]);
    float t = redmax4(a, b, c, d, ninf) * (-LOG2E);
    bcast4(t, s.m0, s.m1, s.n0, s.n1);
    s.f0 = lds_r128(sumf); s.f1 = lds_r128(sumf + 1024); s.f2 = lds_r128(sumf + 2048);
  } else if constexpr (ST == 3) {
#pragma unroll
    for (int r = 0; r < 4; ++r) {
      s.p0[r] = __builtin_amdgcn_exp2f(fmaf(s.T0[r], LOG2E, s.m0));
      s.q0[r] = __builtin_amdgcn_exp2f(fmaf(s.T0[r], -LOG2E, s.n0));
    }
    s.l0 = __builtin_amdgcn_exp2f(fmaf(s.TL[0], lo ? LOG2E : -LOG2E, lo ? s.m0 : s.n0));
  } else if constexpr (ST == 4) {
#pragma unroll
    for (int r = 0; r < 4; ++r) {
      s.p1[r] = __builtin_amdgcn_exp2f(fmaf(s.T1[r], LOG2E, s.m1));
      s.q1[r] = __builtin_amdgcn_exp2f(fmaf(s.T1[r], -LOG2E, s.n1));
    }
    s.l1 = __builtin_amdgcn_exp2f(fmaf(s.TL[1], lo ? LOG2E : -LOG2E, lo ? s.m1 : s.n1));
  } else if constexpr (ST == 5) {
    u32x2 a = pk4(s.p0), b = pk4(s.q0), c = pk4(s.p1), d = pk4(s.q1);
    u32x4 bl = {pk(s.l0, s.l1), 0u, 0u, 0u};
    s.S = mfma32(s.f2, bl, mfma32(s.f1, u32x4{c[0], c[1], d[0], d[1]}, mfma32(s.f0, u32x4{a[0], a[1], b[0], b[1]}, z)));
  } else if constexpr (ST == 6) {
    float r0 = __builtin_amdgcn_rcpf(s.S[0]), r1 = __builtin_amdgcn_rcpf(s.S[1]);
    float r2 = __builtin_amdgcn_rcpf(s.S[2]), r3 = __builtin_amdgcn_rcpf(s.S[3]);
    u32x2 a = pk4(s.p0 * r0), b = pk4(s.q0 * r1), c = pk4(s.p1 * r2), d = pk4(s.q1 * r3);
    s.b0 = u32x4{a[0], a[1], b[0], b[1]};
    s.b1 = u32x4{c[0], c[1], d[0], d[1]};
    s.bl = pk(s.l0 * (lo ? r0 : r1), s.l1 * (lo ? r2 : r3));
  }
}

constexpr int dma_op(int br, int T, int j) {
  const int nx = br ? 3 : 2;
  if (T < 1 || T > 52) return 0;
  const int n = (T - 1) >> 1, half = (T - 1) & 1;
  if (n < 5) return half == 0 ? (j < nx ? 100 + 4 * (n + 4) + j : 0) : (j < 2 ? 200 + 2 * (n + 4) + j : 0);
  if (n < 9) return j < 2 ? 200 + 2 * (9 + 2 * (n - 5) + half) + j : 0;
  if (br == 0 && T >= 36 && T <= 43) {
    if (T <= 38 || j > 1) return 0;
    if (T <= 41) return 200 + 2 * 25 + 1 + 2 * (T - 39) + j;
    return j == 0 ? 200 + 2 * 28 + 1 + (T - 42) : 200 + 2 * 35 + (T - 42);
  }
  if (br == 1 && j == 1 && (T == 38 || T == 39)) return 200 + 2 * 35 + (T - 38);
  return j == 0 ? 200 + 2 * (n + 8) + half : 0;
}
constexpr int op_chunk(int op) { return op >= 200 ? (op - 200) >> 1 : (op - 100) >> 2; }
constexpr int wait_n(int br, int T, int c) {
  const int nx = br ? 3 : 2, ne = br ? 1 : 3;
  int cnt = c <= 3 ? (3 - c) * (nx + 2) + (c == 0 ? ne : 0) : 0;
  for (int t = 1; t < T; ++t)
    for (int j = 0; j < 3; ++j) {
      const int op = dma_op(br, t, j);
      if (op) { if (op_chunk(op) == c) cnt = 0; else ++cnt; }
    }
  return cnt;
}

#define STAMP(i)
#define STAMP_FLUSH()

DEVI u64 next_tag(u32x2 e) {
  const u64 E = ((u64)(u32)__builtin_amdgcn_readfirstlane(e[1]) << 32) | (u32)__builtin_amdgcn_readfirstlane(e[0]);
  return E * 6364136223846793005ull + 1442695040888963407ull;
}
DEVI void poll_flags(GLB_AS u64* f, u64 tag) {
  for (int spin = 0; spin < (1 << 20); ++spin) {
    u64 v = __hip_atomic_load(f, __ATOMIC_RELAXED, __HIP_MEMORY_SCOPE_AGENT);
    if (__all(v == tag)) break;
    __builtin_amdgcn_s_sleep(1);
  }
  asm volatile("" ::: "memory");
}

template <int BR>
DEVI void body(const char* __restrict__ X, const char* __restrict__ W, const char* __restrict__ PHI, const char* __restrict__ WO, char* ws, char* out,
               int rb, int hq, u32 lb) {
  int tid = threadIdx.x, l = tid & 63;
  const int w = __builtin_amdgcn_readfirstlane(tid >> 6);
  const int rg = w & 3, hpl = w >> 2;
  int c = l & 15, g = l >> 4, q = c >> 2, p = c & 3, g0 = g & 1, g1 = g >> 1;
  int lr = (l * 57) >> 9, lu = l - lr * 9;
  auto refresh = [&]() {
    asm volatile("" : "+v"(tid));
    l = tid & 63; c = l & 15; g = l >> 4; q = c >> 2; p = c & 3; g0 = g & 1; g1 = g >> 1;
    lr = (l * 57) >> 9; lu = l - lr * 9;
  };
  STAMP(0);
  __amdgpu_buffer_rsrc_t srdX = __builtin_amdgcn_make_buffer_rsrc((void*)X, (short)0, 2048 * XROW, 0x00020000);
  __amdgpu_buffer_rsrc_t srdW = __builtin_amdgcn_make_buffer_rsrc((void*)W, (short)0, 576 * WROW, 0x00020000);
  const int rot = (rb + 2 * hq) % 9;
  constexpr int NX = BR ? 3 : 2;
  constexpr u64 M36 = (1ull << 36) - 1;

  u32 vx[2], vq[2], vh = 0;
#pragma unroll
  for (int i = 0; i < 2; ++i) {
    int row = 16 * w + 8 * i + (l >> 3);
    vx[i] = (u32)((rb * 128 + row) * XROW + (((l & 7) ^ ((row >> 1) & 7)) << 4));
  }
#pragma unroll
  for (int i = 0; i < 2; ++i) vq[i] = (u32)((8 * w + 4 * (lr & 1) + 2 * i + ((lr >> 1) & 1)) * WROW + hq * 144 + lu * 16);
  if (BR) {
    int row = 2 * w + ((l >> 3) & 1);
    int grow = rb * 128 - 16 + row; grow = grow < 0 ? 0 : grow;
    vh = (u32)(grow * XROW + (((l & 7) ^ ((row >> 1) & 7)) << 4));
  }
  auto kc_of = [&](int cc) { int k = cc + rot; return k >= 9 ? k - 9 : k; };
  auto dma_x = [&](int cc, int i) {
    const int kc = kc_of(cc);
    if (i < 2) dma_full(lb + XR + (cc & 3) * XS + w * 2048 + i * 1024, vx[i], srdX, (u32)(kc * 128));
    else dma_mask(lb + LHALO + cc * 2048 + w * 256, vh, srdX, (u32)(kc * 128), 0xffffull);
  };
  u32 vo[2] = {0, 0}, vo8[2] = {0, 0};
  __amdgpu_buffer_rsrc_t srdO = __builtin_amdgcn_make_buffer_rsrc((void*)WO, (short)0, 576 * XROW, 0x00020000);
  auto dma_w = [&](int n, int i) {
    if (n < 27) {
      const int kc = kc_of(n % 9), sec = n < 9 ? 1152 : (n < 18 ? 2304 : 0);
      dma_mask(lb + WR + (n & 7) * WU + (2 * w + i) * 576, vq[i], srdW, (u32)(kc * 64 * WROW + sec), M36);
    } else if (n < 35) {
      dma_mask(lb + WR + (n & 7) * WU + (2 * w + i) * 576, vo[i], srdO, (u32)((n - 27) * 72 * XROW), M36);
    } else {
      dma_mask(lb + LWO8 + (2 * w + i) * 576, vo8[i], srdO, 0u, M36);
    }
  };
  u32x2 ep = {0, 0}, ep2;
  static_for<0, 4>([&](auto cc) {
    dma_x(cc.value, 0); dma_x(cc.value, 1);
    if (BR) dma_x(cc.value, 2);
    dma_w(cc.value, 0); dma_w(cc.value, 1);
    if (cc.value == 0) {
      const char* ea = ws + WS_OEPOCH + (BR * 16 + rb) * 256;
      asm volatile("global_load_dwordx2 %0, %1, off sc1" : "=v"(ep2) : "v"(ea) : "memory");
      if (BR == 0) {
        const char* eb = ws + WS_EPOCH + hq * 256;
        asm volatile("global_load_dwordx2 %0, %1, off sc1" : "=v"(ep) : "v"(eb) : "memory");
        __amdgpu_buffer_rsrc_t srdP = __builtin_amdgcn_make_buffer_rsrc((void*)PHI, (short)0, 32 * 648, 0x00020000);
        dma_mask(lb + LKVF + w * 336, (u32)(hq * 2592 + (w * 21 + l) * 16), srdP, 0, (1ull << 21) - 1);
      }
    }
    if (cc.value < 3) bar_only();
  });
  STAMP(1);

  const u32 xa0 = lb + XR + (rg * 32 + hpl * 16 + c) * 128 + ((g ^ ((c >> 1) & 7)) << 4);
  const u32 xa1 = xa0 ^ 64;
  const u32 xb0 = xa0 + 2048 - hpl * 4096, xb1 = xb0 ^ 64;
  const u32 xh0 = lb + LHALO + c * 128 + ((g ^ ((c >> 1) & 7)) << 4);
  const u32 xh1 = xh0 ^ 64;
  const u32 wrow = lb + WR + (16 * g1 + 8 * g0 + 2 * q) * 144;
  const u32 wa = wrow + hpl * 72 + 8 * p;
  const u32 wl = wrow + 32 + (p & 1) * 72;
  const u32 wh = rg < 2 ? wa + rg * 40 : wl;
  float ninf = -__builtin_inff();
  asm volatile("" : "+s"(ninf));

  u32x4 xf[18][2];
  const f32x4 z4 = {0.f, 0.f, 0.f, 0.f};
  f32x4 a0[2], a1[2], aL, aH;
  u32x4 f0, f1, fl, fh, xh;
  auto load_frags = [&](auto TT) {
    constexpr int T = TT.value, n = T >> 1, ks = T & 1;
    constexpr u32 o0 = (n & 7) * WU + ks * 32 * 144, o1 = o0 + 144;
    if constexpr (T < 18) {
      xf[T][0] = lds_r128((ks ? xa1 : xa0) + (n & 3) * XS);
      xf[T][1] = lds_r128((ks ? xb1 : xb0) + (n & 3) * XS);
    }
    f0 = frag_tr(wa + o0, wa + o1);
    f1 = frag_tr(wa + o0 + 40, wa + o1 + 40);
    fl = frag_tr(wl + o0, wl + o1);
    if constexpr (BR == 1 && T < 36) {
      fh = frag_tr(wh + o0, wh + o1);
      xh = lds_r128((ks ? xh1 : xh0) + (n % 9) * 2048);
    }
  };
  auto step_dma = [&](auto TT, auto JJ) {
    constexpr int op = dma_op(BR, TT.value, JJ.value);
    if constexpr (op >= 200) dma_w((op - 200) >> 1, op & 1);
    else if constexpr (op >= 100) dma_x((op - 100) >> 2, op & 3);
  };
  auto pass = [&](auto PP, auto&& hook) {
    constexpr int P = PP.value;
    a0[0] = z4; a0[1] = z4; a1[0] = z4; a1[1] = z4; aL = z4; aH = z4;
    static_for<0, 18>([&](auto tt) {
      constexpr int t = tt.value, T = P * 18 + t;
      if constexpr ((T & 1) && (T >> 1) + 1 <= 26) wait_bar<wait_n(BR, T, (T >> 1) + 1)>();
      u32x4 c0 = f0, c1 = f1, cl = fl, ch = fh, cx = xh;
      if constexpr (T + 1 < 54) load_frags(IC<T + 1>{});
      SCHED_FENCE();
      a0[0] = mfma32(c0, xf[t][0], a0[0]);
      a0[1] = mfma32(c0, xf[t][1], a0[1]);
      step_dma(IC<T>{}, IC<0>{});
      a1[0] = mfma32(c1, xf[t][0], a1[0]);
      a1[1] = mfma32(c1, xf[t][1], a1[1]);
      step_dma(IC<T>{}, IC<1>{});
      aL = mfma32(cl, xf[t][0], aL);
      asm volatile("" : "+v"(aL));
      if constexpr (BR == 1 && P < 2) { aH = mfma32(ch, cx, aH); asm volatile("" : "+v"(aH)); }
      step_dma(IC<T>{}, IC<2>{});
      hook(tt);
      SCHED_FENCE();
    });
  };

  wait_bar<wait_n(BR, 0, 0)>();
  STAMP(2);
  load_frags(IC<0>{});
  u64 tag = 0, tag2 = 0;
  pass(IC<0>{}, [&](auto tt) {
    if constexpr (tt.value == 1) {
      asm volatile("" : "+v"(ep), "+v"(ep2));
      if (BR == 0) tag = next_tag(ep);
      tag2 = next_tag(ep2);
    }
    if constexpr (BR == 0 && tt.value == 4) {
      const int h = rg & 1, lf = rg >> 1;
      const int f = lf ? 16 + (q & 1) : c;
      const bool rowok = lf ? (p == h) : true;
      const u32 pb = lb + LKVF + (hpl * 2 + h) * 648 + f * 2;
      const int d0 = 2 * h + 4 * g;
      u32 e0 = lds_r16(pb + (d0 + 0) * 36), e1 = lds_r16(pb + (d0 + 1) * 36), e2 = lds_r16(pb + (d0 + 2) * 36), e3 = lds_r16(pb + (d0 + 3) * 36);
      const int dl = h ? 0 : 16;
      u32 l0 = lds_r16(pb + dl * 36), l1 = lds_r16(pb + (dl + 1) * 36);
      u32 lw = (g == 0) ? (l0 | (l1 << 16)) : 0u;
      u32x4 fr = {e0 | (e1 << 16), e2 | (e3 << 16), h ? 0u : lw, h ? lw : 0u};
      if (!rowok) fr = u32x4{0, 0, 0, 0};
      lds_w128(lb + LPHF + (hpl * 4 + rg) * 1024 + l * 16, fr);
      if (w < 3) {
        u32 a = (p == 2 * w) ? ONES : 0u, b = (p == 2 * w + 1) ? ONES : 0u;
        u32 x = ((p & 1) == g1) ? ((p >> 1) ? 0x3f800000u : 0x00003f80u) : 0u;
        lds_w128(lb + LSUM + w * 1024 + l * 16, w < 2 ? u32x4{a, a, b, b} : u32x4{x, 0u, 0u, 0u});
      }
    }
  });
  STAMP(3);
  refresh();
  u32x2 kM[2][2], kH = {0, 0};
#pragma unroll
  for (int sb = 0; sb < 2; ++sb) { kM[0][sb] = pk4(a0[sb]); kM[1][sb] = pk4(a1[sb]); }
  auto unswap = [&](u32x2& a, u32x2& b) {
    u32x2 x = a, y = b;
#pragma unroll
    for (int e = 0; e < 2; ++e) { a[e] = hpl ? y[e] : x[e]; b[e] = hpl ? x[e] : y[e]; }
  };
  if (BR) { unswap(kM[0][0], kM[0][1]); unswap(kM[1][0], kM[1][1]); }
  if (g < 2) lds_w64(lb + KLX + ((1 + 2 * rg + hpl) * 2 + g) * 128 + c * 8, pk4(aL));
  if (BR) {
    kH = pk4(aH);
    if (w == 2 && g < 2) lds_w64(lb + KLX + g * 128 + c * 8, kH);
  }
  const u32 phf = lb + LPHF + hpl * 4096 + l * 16, sumf = lb + LSUM + l * 16;
  const bool lo = g < 2;
  HH hs;
  u32x2 kLs;
  pass(IC<1>{}, [&](auto tt) {
    constexpr int t = tt.value;
    if constexpr (BR == 0) {
      if constexpr (t >= 2) {
        constexpr int sb = (t - 2) >> 3, st = (t - 2) & 7;
        if constexpr (st == 0) kLs = lds_r64(lb + KLX + ((1 + 2 * rg + (sb ^ hpl)) * 2 + hpl) * 128 + c * 8);
        if constexpr (st < 7) hh_stage<st>(hs, kM[0][sb], kM[1][sb], kLs, phf, sumf, lo, ninf);
        if constexpr (st == 7) {
          const u32 pa = lb + LPK + (rg * 32 + (sb ^ hpl) * 16 + c) * PKS + hpl * 144;
          lds_w64(pa + 8 * g, u32x2{hs.b0[0], hs.b0[1]});
          lds_w64(pa + 32 + 8 * g, u32x2{hs.b0[2], hs.b0[3]});
          lds_w64(pa + 64 + 8 * g, u32x2{hs.b1[0], hs.b1[1]});
          lds_w64(pa + 96 + 8 * g, u32x2{hs.b1[2], hs.b1[3]});
          lds_w32(pa + 128 + 4 * g, hs.bl);
        }
      }
    } else if constexpr (t == 2) {
      const u32 kx = lb + LKX + l * 8;
      if (rg < 3) {
        const u32 b = kx + (((rg + 1) * 2 + hpl) * 2) * 512;
        lds_w64(b, kM[0][1]); lds_w64(b + 512, kM[1][1]);
      }
      if (rg < 2) lds_w64(kx + (hpl * 2 + rg) * 512, kH);
    }
  });
  STAMP(4);
  refresh();
  {
    constexpr int VROW0 = BR ? 16 : 0;
#pragma unroll
    for (int sb = 0; sb < 2; ++sb) {
      const u32 va = lb + LVI + (VROW0 + rg * 32 + (sb ^ hpl) * 16 + c) * VIS + hpl * 80;
      lds_w64(va + 8 * g, pk4(a0[sb]));
      lds_w64(va + 32 + 8 * g, pk4(a1[sb]));
    }
    u32x2 vl = pk4(aL);
    lds_w64(lb + LVI + (VROW0 + rg * 32 + hpl * 16 + c) * VIS + g0 * 80 + 64 + 8 * g1, u32x2{g1 ? vl[1] : vl[0], ONES});
    if (BR) {
      u32x2 hv = pk4(aH);
      if (rg < 2) lds_w64(lb + LVI + c * VIS + hpl * 80 + rg * 32 + 8 * g, hv);
      if (w == 2) lds_w64(lb + LVI + c * VIS + g0 * 80 + 64 + 8 * g1, u32x2{g1 ? hv[1] : hv[0], ONES});
    }
  }
  {
    const int hi = lr & 1;
#pragma unroll
    for (int i = 0; i < 2; ++i) {
      const int qq = 2 * i + ((lr >> 1) & 1);
      vo[i] = (u32)((36 * hpl + 20 * hi + 4 * rg + qq) * XROW + hq * 144 + lu * 16);
      vo8[i] = (u32)((36 * (8 * hpl + 2 * rg + hi) + 16 + qq) * XROW + hq * 144 + lu * 16);
    }
  }
  __amdgpu_buffer_rsrc_t srdS = __builtin_amdgcn_make_buffer_rsrc((void*)(ws + WS_SLAB), (short)0, 128 * 6144, 0x00020000);
  bool h0set = (0x47 >> (c & 7)) & 1;
  GLB_AS u64* flags = (GLB_AS u64*)(ws + WS_FLAG + hq * 256);
  if (BR == 0) {
    bar_lds();
    const u32 slab = (u32)((hq * 16 + rb) * 6144);
    const int rr = 4 * (4 * g0 + q) + 2 * g1;
    const u32 prow = lb + LPK + rr * PKS + hpl * 144, vrow = lb + LVI + rr * VIS + hpl * 80;
    if (rg < 2) {
      f32x4 dpm = z4, dmm = z4, dpl = z4, dml = z4;
      const u32 pa = prow + rg * 64 + 8 * p, va = vrow + rg * 32 + 8 * p, vl = vrow + 64 + 8 * (p & 1);
#pragma unroll
      for (int t = 0; t < 4; ++t) {
        u32x4 ap = frag_tr(pa + t * 32 * PKS, pa + t * 32 * PKS + PKS);
        u32x4 am = frag_tr(pa + 32 + t * 32 * PKS, pa + 32 + t * 32 * PKS + PKS);
        u32x4 bv = frag_tr(va + t * 32 * VIS, va + t * 32 * VIS + VIS);
        u32x4 bl = frag_tr(vl + t * 32 * VIS, vl + t * 32 * VIS + VIS);
        dpm = mfma32(ap, bv, dpm); dmm = mfma32(am, bv, dmm);
        dpl = mfma32(ap, bl, dpl); dml = mfma32(am, bl, dml);
      }
      u32x2 a = pk4(dpm), b = pk4(dmm);
      __builtin_amdgcn_raw_buffer_store_b128(u32x4{a[0], a[1], b[0], b[1]}, srdS, slab + (hpl * 2 + rg) * 1024 + l * 16, 0, 16);
      a = pk4(dpl); b = pk4(dml);
      if (((c >> 3) == hpl) && (h0set == (rg == 0)))
        __builtin_amdgcn_raw_buffer_store_b128(u32x4{a[0], a[1], b[0], b[1]}, srdS, slab + 4096 + l * 16, 0, 16);
    } else if (rg == 2) {
      f32x4 d0 = z4, d1 = z4, dl = z4;
      const u32 pa = prow + 128 + 8 * (p >> 1), va = vrow + 8 * p, vl = vrow + 64 + 8 * (p & 1);
#pragma unroll
      for (int t = 0; t < 4; ++t) {
        u32x4 af = frag_tr(pa + t * 32 * PKS, pa + t * 32 * PKS + PKS);
        u32x4 b0 = frag_tr(va + t * 32 * VIS, va + t * 32 * VIS + VIS);
        u32x4 b1 = frag_tr(va + 32 + t * 32 * VIS, va + 32 + t * 32 * VIS + VIS);
        u32x4 bl = frag_tr(vl + t * 32 * VIS, vl + t * 32 * VIS + VIS);
        d0 = mfma32(af, b0, d0); d1 = mfma32(af, b1, d1); dl = mfma32(af, bl, dl);
      }
      u32 alm = pk(g0 ? d0[2] : d0[0], g0 ? d1[3] : d1[1]);
      float x0 = g0 ? dl[2] : dl[0], x1 = g0 ? dl[3] : dl[1];
      u32 all = pk(h0set ? x0 : 0.f, h0set ? 0.f : x1);
      __builtin_amdgcn_raw_buffer_store_b32(alm, srdS, slab + 5120 + hpl * 256 + l * 4, 0, 16);
      if ((c >> 3) == hpl) __builtin_amdgcn_raw_buffer_store_b32(all, srdS, slab + 5632 + l * 4, 0, 16);
    }
  }
  STAMP(5);
  u32x4 gv[16];
  constexpr int TPUB = 3, TPOLL = 12;
  pass(IC<2>{}, [&](auto tt) {
    constexpr int t = tt.value;
    if constexpr (BR == 0) {
      if constexpr (t == TPUB - 1) {
        asm volatile("s_waitcnt vmcnt(0)" ::: "memory");
      }
      if constexpr (t == TPUB) {
        if (tid == 0) __hip_atomic_store(flags + rb, tag, __ATOMIC_RELAXED, __HIP_MEMORY_SCOPE_AGENT);
      }
      if constexpr (t == TPOLL) {
        STAMP(6);
        if (w < 6) {
          poll_flags(flags + c, tag);
          if (rb == 0 && tid == 0)
            __hip_atomic_store((GLB_AS u64*)(ws + WS_EPOCH + hq * 256), tag, __ATOMIC_RELAXED, __HIP_MEMORY_SCOPE_AGENT);
          const u32 go = (u32)(hq * 16 * 6144 + (tid < SLAB_UNITS ? tid : 0) * 16);
#pragma unroll
          for (int i = 0; i < 16; ++i) gv[i] = __builtin_amdgcn_raw_buffer_load_b128(srdS, go, i * 6144, 16);
        }
        STAMP(7);
      }
    }
  });
  STAMP(8);
  refresh();
  if (g < 2) lds_w64(lb + QLX + ((2 * rg + hpl) * 2 + g) * 128 + c * 8, pk4(aL));
  bar_lds();
  dma_w(34, 0); dma_w(34, 1);
  __amdgpu_buffer_rsrc_t srdF = __builtin_amdgcn_make_buffer_rsrc((void*)(ws + WS_OF), (short)0, 256 * 18 * 1024, 0x00020000);
  const u32 ofo = (u32)((BR * 128 + rb * 8 + rg * 2) * 18 * 1024);
  const int hp = hq * 2 + hpl;
  auto store_o = [&](int sb, f32x4 o0, f32x4 o1, float l0, float l1, float r0, float r1) {
    u32x2 a = pk4(o0 * r0), b = pk4(o1 * r1);
    __builtin_amdgcn_raw_buffer_store_b128(u32x4{a[0], a[1], b[0], b[1]}, srdF, ofo + (sb * 18 + hp) * 1024 + l * 16, 0, 16);
    float rr = g0 ? r1 : r0;
    u32 lv = pk(l0 * rr, l1 * rr);
    auto sw = __builtin_amdgcn_permlane16_swap(lv, lv, false, false);
    u32 s0 = sw[0], s1 = sw[1];
    if (g == 0)
      __builtin_amdgcn_raw_buffer_store_b64(u32x2{s0, s1}, srdF, ofo + (sb * 18 + 16 + (hq >> 2)) * 1024 + (c + 16 * (hq & 3)) * 16 + 8 * hpl, 0, 16);
  };

  if (BR == 0) {
    HH h0, h1;
    u32x2 qa0 = pk4(a0[0]), qb0 = pk4(a1[0]), qa1 = pk4(a0[1]), qb1 = pk4(a1[1]);
    u32x2 ql0 = lds_r64(lb + QLX + ((2 * rg + hpl) * 2 + hpl) * 128 + c * 8), ql1 = lds_r64(lb + QLX + ((2 * rg + 1 - hpl) * 2 + hpl) * 128 + c * 8);
    static_for<0, 7>([&](auto st) {
      hh_stage<st.value>(h0, qa0, qb0, ql0, phf, sumf, lo, ninf);
      hh_stage<st.value>(h1, qa1, qb1, ql1, phf, sumf, lo, ninf);
    });
    STAMP(9);
    if (w < 6) {
      float s[8] = {0.f, 0.f, 0.f, 0.f, 0.f, 0.f, 0.f, 0.f};
#pragma unroll
      for (int i = 0; i < 16; ++i)
#pragma unroll
        for (int e = 0; e < 4; ++e) {
          u32 v = gv[i][e];
          s[2 * e] += __uint_as_float(v << 16);
          s[2 * e + 1] += __uint_as_float(v & 0xffff0000u);
        }
      if (tid < SLAB_UNITS) lds_w128(lb + LKVF + tid * 16, u32x4{pk(s[0], s[1]), pk(s[2], s[3]), pk(s[4], s[5]), pk(s[6], s[7])});
    }
    bar_lds();
    STAMP(10);
    const u32 kf = lb + LKVF;
    h0set = (0x47 >> (c & 7)) & 1;
    u32x4 AM0 = lds_r128(kf + (hpl * 2) * 1024 + l * 16), AM1 = lds_r128(kf + (hpl * 2 + 1) * 1024 + l * 16);
    const int l8 = g * 16 + (c & 7) + 8 * hpl;
    u32x4 AMLm = lds_r128(kf + 4096 + l8 * 16);
    u32 ALm = lds_r32(kf + 5120 + hpl * 256 + l * 4), ALL = lds_r32(kf + 5632 + l8 * 4);
    u32x4 AML0, AML1;
#pragma unroll
    for (int e = 0; e < 4; ++e) { AML0[e] = h0set ? AMLm[e] : 0u; AML1[e] = h0set ? 0u : AMLm[e]; }
#pragma unroll
    for (int sb = 0; sb < 2; ++sb) {
      const HH& h = sb ? h1 : h0;
      u32x4 bl = {h.bl, 0u, 0u, 0u};
      f32x4 O0 = mfma32(u32x4{ALm & 0xffffu, 0u, 0u, 0u}, bl, mfma32(AM0, h.b0, z4));
      f32x4 O1 = mfma32(u32x4{ALm & 0xffff0000u, 0u, 0u, 0u}, bl, mfma32(AM1, h.b1, z4));
      f32x4 OL = mfma32(u32x4{ALL, 0u, 0u, 0u}, bl, mfma32(AML1, h.b1, mfma32(AML0, h.b0, z4)));
      float r0 = __builtin_amdgcn_rcpf(OL[2] + 1e-6f), r1 = __builtin_amdgcn_rcpf(OL[3] + 1e-6f);
      store_o(sb ^ hpl, O0, O1, OL[0], OL[1], r0, r1);
    }
  } else {
    const u32 kx = lb + LKX + ((rg * 2 + hpl) * 2) * 512 + l * 8;
    u32x2 pM0 = lds_r64(kx), pM1 = lds_r64(kx + 512);
    u32x2 kL[3];
#pragma unroll
    for (int i = 0; i < 3; ++i) kL[i] = lds_r64(lb + KLX + ((2 * rg + i) * 2 + hpl) * 128 + c * 8);
    const int par = c & 1;
    const bool ina = (4 * g + par) > c, inb = (4 * g + par + 2) > c;
    const float sc = 0.23570226039551584f * LOG2E;
    float la[2][2], lbv[2][2], mx[2][2];
    u32x2 qM[2][2] = {{pk4(a0[0]), pk4(a0[1])}, {pk4(a1[0]), pk4(a1[1])}};
    unswap(qM[0][0], qM[0][1]); unswap(qM[1][0], qM[1][1]);
#pragma unroll
    for (int sb = 0; sb < 2; ++sb) {
      u32x2 qlv = lds_r64(lb + QLX + ((2 * rg + sb) * 2 + hpl) * 128 + c * 8);
      u32x2 q0 = qM[0][sb], q1 = qM[1][sb];
      u32x4 b0 = {q0[0], q0[1], g == 0 ? qlv[0] : 0u, 0u};
      u32x4 b1 = {q1[0], q1[1], 0u, g == 0 ? qlv[1] : 0u};
      u32x2 kp0 = sb ? kM[0][0] : pM0, kp1 = sb ? kM[1][0] : pM1, kpl = kL[sb], kcl = kL[sb + 1];
      f32x4 sp0 = mfma32(u32x4{kp0[0], kp0[1], kpl[0], kpl[1]}, b0, z4);
      f32x4 sp1 = mfma32(u32x4{kp1[0], kp1[1], kpl[0], kpl[1]}, b1, z4);
      f32x4 sc0 = mfma32(u32x4{kM[0][sb][0], kM[0][sb][1], kcl[0], kcl[1]}, b0, z4);
      f32x4 sc1 = mfma32(u32x4{kM[1][sb][0], kM[1][sb][1], kcl[0], kcl[1]}, b1, z4);
      const bool dead = (sb == 0) && (rb == 0) && (rg == 0);
      float pa0 = par ? sp0[1] : sp0[0], pb0 = par ? sp0[3] : sp0[2], pa1 = par ? sp1[1] : sp1[0], pb1 = par ? sp1[3] : sp1[2];
      if (dead) { pa0 = pb0 = pa1 = pb1 = -1e30f; }
      la[0][sb] = (ina ? pa0 : (par ? sc0[1] : sc0[0])) * sc;
      lbv[0][sb] = (inb ? pb0 : (par ? sc0[3] : sc0[2])) * sc;
      la[1][sb] = (ina ? pa1 : (par ? sc1[1] : sc1[0])) * sc;
      lbv[1][sb] = (inb ? pb1 : (par ? sc1[3] : sc1[2])) * sc;
      mx[0][sb] = fmaxf(la[0][sb], lbv[0][sb]);
      mx[1][sb] = fmaxf(la[1][sb], lbv[1][sb]);
    }
    float tm = redmax4(mx[0][0], mx[0][1], mx[1][0], mx[1][1], ninf);
    bcast4(tm, mx[0][0], mx[0][1], mx[1][0], mx[1][1]);
    const u32 vb = lb + LVI + (rg * 32 + 4 * g + q) * VIS + hpl * 80;
#pragma unroll
    for (int sb = 0; sb < 2; ++sb) {
      const u32 v0 = vb + sb * 16 * VIS, v1 = v0 + 16 * VIS;
      u32x4 A0 = frag_tr(v0 + 8 * p, v1 + 8 * p), A1 = frag_tr(v0 + 32 + 8 * p, v1 + 32 + 8 * p);
      u32x4 AL = frag_tr(v0 + 64 + 8 * (p & 1), v1 + 64 + 8 * (p & 1));
      u32x4 P[2];
#pragma unroll
      for (int h = 0; h < 2; ++h) {
        float ea = __builtin_amdgcn_exp2f(la[h][sb] - mx[h][sb]), eb = __builtin_amdgcn_exp2f(lbv[h][sb] - mx[h][sb]);
        u32 wa_ = par ? pk(0.f, ea) : pk(ea, 0.f), wb_ = par ? pk(0.f, eb) : pk(eb, 0.f);
        P[h] = u32x4{ina ? wa_ : 0u, inb ? wb_ : 0u, ina ? 0u : wa_, inb ? 0u : wb_};
      }
      f32x4 O0 = mfma32(A0, P[0], z4), O1 = mfma32(A1, P[1], z4);
      f32x4 L0 = mfma32(AL, P[0], z4), L1 = mfma32(AL, P[1], z4);
      float r0 = __builtin_amdgcn_rcpf(L0[2]), r1 = __builtin_amdgcn_rcpf(L1[2]);
      store_o(sb, O0, O1, g0 ? L1[0] : L0[0], g0 ? L1[1] : L0[1], r0, r1);
    }
  }
  STAMP(11);
  refresh();
  wait_bar<0>();
  GLB_AS u64* oflags = (GLB_AS u64*)(ws + WS_OFLAG + (BR * 16 + rb) * 256);
  if (tid == 0) __hip_atomic_store(oflags + hq, tag2, __ATOMIC_RELAXED, __HIP_MEMORY_SCOPE_AGENT);
  STAMP(12);
  const u32 wrow2 = lb + WR + (16 * g1 + 8 * g0 + 2 * q) * 144;
  const u32 oa = wrow2 + 8 * p, oa4 = wrow2 + 128 + 8 * (p & 1);
  poll_flags(oflags + (l & 7), tag2);
  STAMP(13);
  if (hq == 0 && tid == 0) __hip_atomic_store((GLB_AS u64*)(ws + WS_OEPOCH + (BR * 16 + rb) * 256), tag2, __ATOMIC_RELAXED, __HIP_MEMORY_SCOPE_AGENT);
  u32x4 of[18];
  const u32 ofl = (u32)((BR * 128 + rb * 8 + w) * 18 * 1024 + l * 16);
#pragma unroll
  for (int t = 0; t < 18; ++t) of[t] = __builtin_amdgcn_raw_buffer_load_b128(srdF, ofl, t * 1024, 16);
  f32x4 acc[5] = {z4, z4, z4, z4, z4};
#pragma unroll
  for (int t = 0; t < 18; ++t) {
    const u32 o0 = (t < 16 ? ((27 + (t >> 1)) & 7) * WU : LWO8 - WR) + (t & 1) * 32 * 144, o1 = o0 + 144;
#pragma unroll
    for (int i = 0; i < 4; ++i) acc[i] = mfma32(frag_tr(oa + o0 + 32 * i, oa + o1 + 32 * i), of[t], acc[i]);
    acc[4] = mfma32(frag_tr(oa4 + o0, oa4 + o1), of[t], acc[4]);
  }
  STAMP(14);
  const u32 so = lb + LOUT + w * 2304;
#pragma unroll
  for (int i = 0; i < 4; ++i) lds_w64(so + c * 144 + 32 * i + 8 * g, pk4(acc[i]));
  if (g < 2) lds_w64(so + c * 144 + 128 + 8 * g, pk4(acc[4]));
  char* op = out + (size_t)(rb * 128 + w * 16) * 2304 + BR * 1152 + hq * 144;
#pragma unroll
  for (int j = 0; j < 3; ++j) {
    const int u = l + 64 * j, r = (u * 57) >> 9, un = u - r * 9;
    if (j < 2 || l < 16) *(u32x4*)(op + r * 2304 + un * 16) = lds_r128(so + u * 16);
  }
  STAMP(15);
  STAMP_FLUSH();
}

__global__ __launch_bounds__(512, 2) void fused(const char* __restrict__ XA, const char* __restrict__ WA, const char* __restrict__ PHI,
                                                const char* __restrict__ WOA, const char* __restrict__ XB, const char* __restrict__ WB,
                                                const char* __restrict__ WOB, char* ws, char* out) {
  __shared__ __attribute__((aligned(16))) char smem[LDS_BYTES];
  const u32 lb = (u32)(uintptr_t)(LDS_AS char*)smem;
  const int b = blockIdx.x, x = b & 7, j = b >> 3, xx = x & 3;
  const int rb = (xx >> 1) * 8 + (j >> 2), hq = (xx & 1) * 4 + (j & 3);
  if (x < 4) body<0>(XA, WA, PHI, WOA, ws, out, rb, hq, lb);
  else body<1>(XB, WB, PHI, WOB, ws, out, rb, hq, lb);
}
}

extern "C" void kernel_launch(void* const* d_in, const int* in_sizes, int n_in, void* d_out, int out_size, void* d_ws, size_t ws_size,
                              hipStream_t stream) {
  hk::fused<<<256, 512, 0, stream>>>((const char*)d_in[0], (const char*)d_in[1], (const char*)d_in[2], (const char*)d_in[3], (const char*)d_in[4],
                                     (const char*)d_in[5], (const char*)d_in[6], (char*)d_ws, (char*)d_out);
}
```

```cpp
#include <hip/hip_runtime.h>
#include <stdint.h>

namespace hk {
typedef unsigned int u32;
typedef unsigned long long u64;
typedef unsigned short u16;
typedef float f32x4 __attribute__((ext_vector_type(4)));
typedef float f32x2 __attribute__((ext_vector_type(2)));
typedef u32 u32x4 __attribute__((ext_vector_type(4)));
typedef u32 u32x2 __attribute__((ext_vector_type(2)));
typedef __bf16 bf16x8 __attribute__((ext_vector_type(8)));
typedef __bf16 bf16x2 __attribute__((ext_vector_type(2)));
typedef short s16x4 __attribute__((ext_vector_type(4)));
#define LDS_AS __attribute__((address_space(3)))
#define GLB_AS __attribute__((address_space(1)))
#define DEVI __device__ __forceinline__
#define SCHED_FENCE() __builtin_amdgcn_sched_barrier(0)

template <int V> struct IC { static constexpr int value = V; };
template <int I, int N, class F>
DEVI void static_for(F&& f) {
  if constexpr (I < N) { f(IC<I>{}); static_for<I + 1, N>(f); }
}

constexpr int XROW = 1152, WROW = 3456;
constexpr size_t WS_OF = 0;
constexpr int SLAB = 6400;
constexpr size_t WS_SLAB = 5u << 20;
constexpr size_t WS_FLAG = 6u << 20;
constexpr size_t WS_EPOCH = WS_FLAG + 65536;
constexpr size_t WS_OFLAG = WS_FLAG + 2 * 65536;
constexpr size_t WS_OEPOCH = WS_FLAG + 3 * 65536;
constexpr u32 OOB = 0x7fffffffu;
constexpr u32 SENT = 0xffffffffu;
constexpr int XR = 1024, XS = 16384;
constexpr int WR = XR + 4 * XS, WU = 9216;
constexpr int LM = WR + 8 * WU;
constexpr int LPHF = LM;
constexpr int LSUM = LM + 8192;
constexpr int LKVF = LM + 11264;
constexpr int LHALO = LM;
constexpr int KLX = LM + 18432;
constexpr int QLX = KLX + 2304;
constexpr int LVI = XR, VIS = 168;
constexpr int LPK = XR + 21504, PKS = 296;
constexpr int LKX = XR + 24576;
constexpr int WB = WR - 3 * WU;
constexpr int LDS_BYTES = 163840;
static_assert(QLX + 2048 <= LDS_BYTES, "lds");

DEVI u32x2 tr_read(u32 a) { return __builtin_bit_cast(u32x2, __builtin_amdgcn_ds_read_tr16_b64_v4i16((LDS_AS s16x4*)a)); }
DEVI u32x4 frag_tr(u32 a0, u32 a1) { u32x2 x = tr_read(a0), y = tr_read(a1); return u32x4{x[0], x[1], y[0], y[1]}; }
DEVI u32x4 lds_r128(u32 a) { return *(LDS_AS u32x4*)a; }
DEVI u32x2 lds_r64(u32 a) { return *(LDS_AS u32x2*)a; }
DEVI u32 lds_r32(u32 a) { return *(LDS_AS u32*)a; }
DEVI u32 lds_r16(u32 a) { return *(LDS_AS u16*)a; }
DEVI void lds_w128(u32 a, u32x4 v) { *(LDS_AS u32x4*)a = v; }
DEVI void lds_w64(u32 a, u32x2 v) { *(LDS_AS u32x2*)a = v; }
DEVI void lds_w32(u32 a, u32 v) { *(LDS_AS u32*)a = v; }
DEVI f32x4 mfma32(u32x4 a, u32x4 b, f32x4 c) {
  return __builtin_amdgcn_mfma_f32_16x16x32_bf16(__builtin_bit_cast(bf16x8, a), __builtin_bit_cast(bf16x8, b), c, 0, 0, 0);
}
DEVI u32 pk(float a, float b) { f32x2 v = {a, b}; return __builtin_bit_cast(u32, __builtin_convertvector(v, bf16x2)); }
DEVI u32x2 pk4(f32x4 v) { return u32x2{pk(v[0], v[1]), pk(v[2], v[3])}; }
DEVI float max3(float a, float b, float c) { return fmaxf(fmaxf(a, b), c); }
DEVI u32 umax(u32 a, u32 b) { return a > b ? a : b; }
DEVI u32 umax3(u32 a, u32 b, u32 c) { return umax(umax(a, b), c); }
DEVI u32 umax4(u32 m, u32x4 v) { return umax3(umax3(m, v[0], v[1]), v[2], v[3]); }
DEVI u32 ns(u32 v) { return v < 0xfffffffeu ? v : 0xfffffffeu; }

DEVI void dma_full(u32 lds, u32 voff, __amdgpu_buffer_rsrc_t srd, u32 soff) {
  asm volatile("s_mov_b32 m0, %0\n\ts_nop 0\n\tbuffer_load_dwordx4 %1, %2, %3 offen lds" ::"s"(lds), "v"(voff), "s"(srd), "s"(soff) : "memory");
}
DEVI void dma_mask(u32 lds, u32 voff, __amdgpu_buffer_rsrc_t srd, u32 soff, u64 mask) {
  asm volatile("s_mov_b32 m0, %0\n\ts_mov_b64 exec, %4\n\tbuffer_load_dwordx4 %1, %2, %3 offen lds\n\ts_mov_b64 exec, -1" ::"s"(lds), "v"(voff), "s"(srd),
               "s"(soff), "s"(mask)
               : "memory");
}
template <int N>
DEVI void wait_bar() { asm volatile("s_waitcnt vmcnt(%0) lgkmcnt(0)\n\ts_barrier" ::"n"(N) : "memory"); }
template <int N>
DEVI void wait_vm_bar() { asm volatile("s_waitcnt vmcnt(%0)\n\ts_barrier" ::"n"(N) : "memory"); }
DEVI void bar_lds() { asm volatile("s_waitcnt lgkmcnt(0)\n\ts_barrier" ::: "memory"); }
DEVI void bar_only() { asm volatile("s_barrier" ::: "memory"); }

DEVI float redmax4(float a, float b, float c, float d, float ninf) {
  auto r1 = __builtin_amdgcn_permlane32_swap(__float_as_uint(a), __float_as_uint(b), false, false);
  auto r2 = __builtin_amdgcn_permlane32_swap(__float_as_uint(c), __float_as_uint(d), false, false);
  u32 r10 = r1[0], r11 = r1[1], r20 = r2[0], r21 = r2[1];
  float ab = max3(__uint_as_float(r10), __uint_as_float(r11), ninf);
  float cd = max3(__uint_as_float(r20), __uint_as_float(r21), ninf);
  auto r3 = __builtin_amdgcn_permlane16_swap(__float_as_uint(ab), __float_as_uint(cd), false, false);
  u32 r30 = r3[0], r31 = r3[1];
  return max3(__uint_as_float(r30), __uint_as_float(r31), ninf);
}
DEVI void bcast4(float t, float& a, float& b, float& c, float& d) {
  u32 tu = __float_as_uint(t);
  auto s1 = __builtin_amdgcn_permlane16_swap(tu, tu, false, false);
  u32 x = s1[0], y = s1[1];
  auto s2 = __builtin_amdgcn_permlane32_swap(x, x, false, false);
  auto s3 = __builtin_amdgcn_permlane32_swap(y, y, false, false);
  u32 s20 = s2[0], s21 = s2[1], s30 = s3[0], s31 = s3[1];
  a = __uint_as_float(s20); b = __uint_as_float(s21); c = __uint_as_float(s30); d = __uint_as_float(s31);
}

constexpr float LOG2E = 1.4426950408889634f;
constexpr u32 ONES = 0x3f803f80u;

struct HH {
  u32x4 f0, f1, f2, f3;
  f32x4 T0, T1, TL, S;
  float m0, m1, n0, n1;
  f32x4 p0, p1, q0, q1;
  float l0, l1;
  u32x4 b0, b1; u32 bl;
};
template <int ST>
DEVI void hh_stage(HH& s, u32x2 ma, u32x2 mb, u32x2 left, u32 phf, u32 sumf, bool lo, float ninf) {
  const f32x4 z = {0.f, 0.f, 0.f, 0.f};
  if constexpr (ST == 0) {
    s.f0 = lds_r128(phf); s.f1 = lds_r128(phf + 1024); s.f2 = lds_r128(phf + 2048); s.f3 = lds_r128(phf + 3072);
  } else if constexpr (ST == 1) {
    u32x4 b0 = {ma[0], ma[1], left[0], left[1]}, b1 = {mb[0], mb[1], left[0], left[1]};
    s.T0 = mfma32(s.f0, b0, z);
    s.T1 = mfma32(s.f1, b1, z);
    s.TL = mfma32(s.f3, b1, mfma32(s.f2, b0, z));
  } else if constexpr (ST == 2) {
    float a = max3(max3(s.T0[0], s.T0[1], s.T0[2]), s.T0[3], s.TL[0]);
    float b = max3(max3(s.T1[0], s.T1[1], s.T1[2]), s.T1[3], s.TL[1]);
    float c = max3(max3(-s.T0[0], -s.T0[1], -s.T0[2]), -s.T0[3], -s.TL[0]);
    float d = max3(max3(-s.T1[0], -s.T1[1], -s.T1[2]), -s.T1[3], -s.TL[1]);
    float t = redmax4(a, b, c, d, ninf) * (-LOG2E);
    bcast4(t, s.m0, s.m1, s.n0, s.n1);
    s.f0 = lds_r128(sumf); s.f1 = lds_r128(sumf + 1024); s.f2 = lds_r128(sumf + 2048);
  } else if constexpr (ST == 3) {
#pragma unroll
    for (int r = 0; r < 4; ++r) {
      s.p0[r] = __builtin_amdgcn_exp2f(fmaf(s.T0[r], LOG2E, s.m0));
      s.q0[r] = __builtin_amdgcn_exp2f(fmaf(s.T0[r], -LOG2E, s.n0));
    }
    s.l0 = __builtin_amdgcn_exp2f(fmaf(s.TL[0], lo ? LOG2E : -LOG2E, lo ? s.m0 : s.n0));
  } else if constexpr (ST == 4) {
#pragma unroll
    for (int r = 0; r < 4; ++r) {
      s.p1[r] = __builtin_amdgcn_exp2f(fmaf(s.T1[r], LOG2E, s.m1));
      s.q1[r] = __builtin_amdgcn_exp2f(fmaf(s.T1[r], -LOG2E, s.n1));
    }
    s.l1 = __builtin_amdgcn_exp2f(fmaf(s.TL[1], lo ? LOG2E : -LOG2E, lo ? s.m1 : s.n1));
  } else if constexpr (ST == 5) {
    u32x2 a = pk4(s.p0), b = pk4(s.q0), c = pk4(s.p1), d = pk4(s.q1);
    u32x4 bl = {pk(s.l0, s.l1), 0u, 0u, 0u};
    s.S = mfma32(s.f2, bl, mfma32(s.f1, u32x4{c[0], c[1], d[0], d[1]}, mfma32(s.f0, u32x4{a[0], a[1], b[0], b[1]}, z)));
  } else if constexpr (ST == 6) {
    float r0 = __builtin_amdgcn_rcpf(s.S[0]), r1 = __builtin_amdgcn_rcpf(s.S[1]);
    float r2 = __builtin_amdgcn_rcpf(s.S[2]), r3 = __builtin_amdgcn_rcpf(s.S[3]);
    u32x2 a = pk4(s.p0 * r0), b = pk4(s.q0 * r1), c = pk4(s.p1 * r2), d = pk4(s.q1 * r3);
    s.b0 = u32x4{a[0], a[1], b[0], b[1]};
    s.b1 = u32x4{c[0], c[1], d[0], d[1]};
    s.bl = pk(s.l0 * (lo ? r0 : r1), s.l1 * (lo ? r2 : r3));
  }
}

#define L2ST 4
#define PRE 6
#define PRO 2
#define QK 3
#define QV 2
constexpr int T_CLR = 19, T_FLAG = 27, T_FLD = 33, T_FCHK = 37;
constexpr int T_SLAB = 42, T_L1 = 49, T_L1C = 53;
struct Sched {
  int op[54][3];
  int pos_step[55];
  int pos_hook[54];
  int last[36];
  int total;
};
constexpr int ext_ops(int br, int T) {
  return T == T_CLR ? (br ? 3 : 4) : T == T_FLD ? 1 : br ? 0 : T == T_SLAB ? 2 : T == T_L1 ? 4 : T == T_L1C ? 1 : 0;
}
constexpr int op_chunk(int op) { return op >= 200 ? (op - 200) >> 1 : (op - 100) >> 2; }
constexpr int op_earliest(int br, int op) {
  const int n = op_chunk(op);
  if (op < 200) return n < 4 ? 0 : 2 * (n - 4) + 1;
  if (n < 8) return 0;
  if (n < 27) return 2 * (n - 8) + 3;
  if (n < 30) return br ? 21 : T_SLAB + 1;
  return 2 * (n - 11) + 3;
}
constexpr Sched make_sched(int br) {
  Sched s{};
  const int nx = br ? 3 : 2;
  int list[128] = {}, n = 0;
  for (int c = 0; c < 36; ++c) {
    if (c < 9) for (int i = 0; i < nx; ++i) list[n++] = 100 + 4 * c + i;
    list[n++] = 200 + 2 * c; list[n++] = 200 + 2 * c + 1;
  }
  int pos = 0, k = 0;
  for (; k < PRO * (nx + 2); ++k) { s.last[op_chunk(list[k])] = pos++; if (k == nx + 1) pos += br ? 1 : 3; }
  for (int T = 0; T < 54; ++T) {
    s.pos_step[T] = pos;
    const int q = T < 18 ? QK : (T < 50 ? QV : 3);
    for (int j = 0; j < 3; ++j) {
      s.op[T][j] = 0;
      if (j < q && k < n && op_earliest(br, list[k]) <= T) { s.op[T][j] = list[k]; s.last[op_chunk(list[k])] = pos++; ++k; }
    }
    s.pos_hook[T] = pos;
    pos += ext_ops(br, T);
  }
  s.pos_step[54] = pos;
  s.total = k == n ? pos : -1;
  return s;
}
template <int BR> struct SC { static constexpr Sched s = make_sched(BR); };
static_assert(SC<0>::s.total > 0 && SC<1>::s.total > 0, "schedule incomplete");
constexpr int imin(int a, int b) { return a < b ? a : b; }
template <int BR> constexpr int wait_n(int T, int c) {
  int n = SC<BR>::s.pos_step[T] - SC<BR>::s.last[c] - 1;
  if (T == T_FLAG) n = imin(n, SC<BR>::s.pos_step[T] - SC<BR>::s.pos_step[T_CLR + 1]);
  return n;
}
template <int BR> constexpr int wait_h(int T, int T0) { return SC<BR>::s.pos_hook[T] - SC<BR>::s.pos_step[T0 + 1]; }

#define STAMP(i)
#define RSTAMP(i)
#define STAMP_FLUSH()

DEVI u64 next_tag(u32x2 e) {
  const u64 E = ((u64)(u32)__builtin_amdgcn_readfirstlane(e[1]) << 32) | (u32)__builtin_amdgcn_readfirstlane(e[0]);
  return E * 6364136223846793005ull + 1442695040888963407ull;
}
DEVI void poll_flags(GLB_AS u64* f, u64 tag) {
  for (int spin = 0; spin < (1 << 20); ++spin) {
    u64 v = __hip_atomic_load(f, __ATOMIC_RELAXED, __HIP_MEMORY_SCOPE_AGENT);
    if (__all(v == tag)) break;
    __builtin_amdgcn_s_sleep(1);
  }
  asm volatile("" ::: "memory");
}

template <int BR>
DEVI void body(const char* __restrict__ X, const char* __restrict__ W, const char* __restrict__ PHI, const char* __restrict__ WO, char* ws, char* out,
               int rb, int hq, u32 lb) {
  int tid = threadIdx.x, l = tid & 63;
  const int w = __builtin_amdgcn_readfirstlane(tid >> 6);
  const int rg = w & 3, hpl = w >> 2;
  int c = l & 15, g = l >> 4, q = c >> 2, p = c & 3, g0 = g & 1, g1 = g >> 1;
  int lr = (l * 57) >> 9, lu = l - lr * 9;
  auto refresh = [&]() {
    asm volatile("" : "+v"(tid));
    l = tid & 63; c = l & 15; g = l >> 4; q = c >> 2; p = c & 3; g0 = g & 1; g1 = g >> 1;
    lr = (l * 57) >> 9; lu = l - lr * 9;
  };
  STAMP(0);
  RSTAMP(30);
  __amdgpu_buffer_rsrc_t srdX = __builtin_amdgcn_make_buffer_rsrc((void*)X, (short)0, 2048 * XROW, 0x00020000);
  __amdgpu_buffer_rsrc_t srdW = __builtin_amdgcn_make_buffer_rsrc((void*)W, (short)0, 576 * WROW, 0x00020000);
#define ROT (rb + 2 * hq) % 9
  const int rot = ROT;
  constexpr int NX = BR ? 3 : 2;
  constexpr u64 M36 = (1ull << 36) - 1;

  u32 vx[2], vq[2], vh = 0;
#pragma unroll
  for (int i = 0; i < 2; ++i) {
    int row = 16 * w + 8 * i + (l >> 3);
    vx[i] = (u32)((rb * 128 + row) * XROW + (((l & 7) ^ ((row >> 1) & 7)) << 4));
  }
#pragma unroll
  for (int i = 0; i < 2; ++i) vq[i] = (u32)((8 * w + 4 * (lr & 1) + 2 * i + ((lr >> 1) & 1)) * WROW + hq * 144 + lu * 16);
  if (BR) {
    int row = 2 * w + ((l >> 3) & 1);
    int grow = rb * 128 - 16 + row; grow = grow < 0 ? 0 : grow;
    vh = (u32)(grow * XROW + (((l & 7) ^ ((row >> 1) & 7)) << 4));
  }
  auto kc_of = [&](int cc) { int k = cc + rot; return k >= 9 ? k - 9 : k; };
  auto dma_x = [&](int cc, int i) {
    const int kc = kc_of(cc);
    if (i < 2) dma_full(lb + XR + (cc & 3) * XS + w * 2048 + i * 1024, vx[i], srdX, (u32)(kc * 128));
    else dma_mask(lb + LHALO + cc * 2048 + w * 256, vh, srdX, (u32)(kc * 128), 0xffffull);
  };
  u32 vo[2] = {0, 0}, vo8[2] = {0, 0};
  __amdgpu_buffer_rsrc_t srdO = __builtin_amdgcn_make_buffer_rsrc((void*)WO, (short)0, 576 * XROW, 0x00020000);
  auto dma_w = [&](int n, int i) {
    if (n < 27) {
      const int kc = kc_of(n % 9), sec = n < 9 ? 1152 : (n < 18 ? 2304 : 0);
      dma_mask(lb + WR + ((n + 5) & 7) * WU + (2 * w + i) * 576, vq[i], srdW, (u32)(kc * 64 * WROW + sec), M36);
    } else if (n < 35) {
      dma_mask(lb + WB + (n - 27) * WU + (2 * w + i) * 576, vo[i], srdO, (u32)((n - 27) * 72 * XROW), M36);
    } else {
      dma_mask(lb + WB + 8 * WU + (2 * w + i) * 576, vo8[i], srdO, 0u, M36);
    }
  };
  u32x2 ep = {0, 0}, ep2;
  asm volatile("" ::"s"(srdX), "s"(srdW));
  STAMP(16);
  static_for<0, PRO>([&](auto cc) {
    dma_x(cc.value, 0); dma_x(cc.value, 1);
    if (BR) dma_x(cc.value, 2);
    dma_w(cc.value, 0); dma_w(cc.value, 1);
    if (cc.value == 0) {
      const char* ea = ws + WS_OEPOCH + (BR * 16 + rb) * 256;
      asm volatile("global_load_dwordx2 %0, %1, off sc1" : "=v"(ep2) : "v"(ea) : "memory");
      if (BR == 0) {
        const char* eb = ws + WS_EPOCH + hq * 256;
        asm volatile("global_load_dwordx2 %0, %1, off sc1" : "=v"(ep) : "v"(eb) : "memory");
        __amdgpu_buffer_rsrc_t srdP = __builtin_amdgcn_make_buffer_rsrc((void*)PHI, (short)0, 32 * 648, 0x00020000);
        dma_mask(lb + LKVF + w * 336, (u32)(hq * 2592 + (w * 21 + l) * 16), srdP, 0, (1ull << 21) - 1);
      }
    }
    if (cc.value == 0) STAMP(17);
    if (cc.value < PRO - 1) bar_only();
    if (cc.value == 0) STAMP(18);
  });
  STAMP(1);

  const u32 xa0 = lb + XR + (rg * 32 + hpl * 16 + c) * 128 + ((g ^ ((c >> 1) & 7)) << 4);
  const u32 xa1 = xa0 ^ 64;
  const u32 xb0 = xa0 + 2048 - hpl * 4096, xb1 = xb0 ^ 64;
  const u32 xh0 = lb + LHALO + c * 128 + ((g ^ ((c >> 1) & 7)) << 4);
  const u32 xh1 = xh0 ^ 64;
  const u32 wrow = lb + WR + (16 * g1 + 8 * g0 + 2 * q) * 144;
  const u32 wa = wrow + hpl * 72 + 8 * p;
  const u32 wl = wrow + 32 + (p & 1) * 72;
  const u32 wh = rg < 2 ? wa + rg * 40 : wl;
  float ninf = -__builtin_inff();
  asm volatile("" : "+s"(ninf));

  u32x4 xf[18][2];
  const f32x4 z4 = {0.f, 0.f, 0.f, 0.f};
  f32x4 a0[2], a1[2], aL, aH;
  u32x4 f0, f1, fl, fh, xh;
  auto load_frags = [&](auto TT, auto JJ) {
    constexpr int T = TT.value, j = JJ.value, n = T >> 1, ks = T & 1;
    constexpr u32 o0 = ((n + 5) & 7) * WU + ks * 32 * 144, o1 = o0 + 144;
    if constexpr (T < 54) {
      if constexpr (j == 0) {
        if constexpr (T < 18) xf[T][0] = lds_r128((ks ? xa1 : xa0) + (n & 3) * XS);
        f0 = frag_tr(wa + o0, wa + o1);
      } else if constexpr (j == 1) {
        if constexpr (T < 18) xf[T][1] = lds_r128((ks ? xb1 : xb0) + (n & 3) * XS);
        f1 = frag_tr(wa + o0 + 40, wa + o1 + 40);
      } else if constexpr (j == 2) {
        fl = frag_tr(wl + o0, wl + o1);
      } else if constexpr (BR == 1 && T < 36) {
        fh = frag_tr(wh + o0, wh + o1);
        xh = lds_r128((ks ? xh1 : xh0) + (n % 9) * 2048);
      }
    }
  };
  auto step_dma = [&](auto TT, auto JJ) {
    constexpr int op = SC<BR>::s.op[TT.value][JJ.value];
    if constexpr (op >= 200) dma_w((op - 200) >> 1, op & 1);
    else if constexpr (op >= 100) dma_x((op - 100) >> 2, op & 3);
  };
  auto pass = [&](auto PP, auto&& hook) {
    constexpr int P = PP.value;
    a0[0] = z4; a0[1] = z4; a1[0] = z4; a1[1] = z4; aL = z4; aH = z4;
    static_for<0, 18>([&](auto tt) {
      constexpr int t = tt.value, T = P * 18 + t;
      if constexpr ((T & 1) && (T >> 1) + 1 <= 26) {
        if constexpr (T <= 9 || T == 19 || T == 37) wait_bar<wait_n<BR>(T, (T >> 1) + 1)>();
        else wait_vm_bar<wait_n<BR>(T, (T >> 1) + 1)>();
      }
      u32x4 c0 = f0, c1 = f1, cl = fl, ch = fh, cx = xh;
      SCHED_FENCE();
      a0[0] = mfma32(c0, xf[t][0], a0[0]);
      load_frags(IC<T + 1>{}, IC<0>{});
      SCHED_FENCE();
      a0[1] = mfma32(c0, xf[t][1], a0[1]);
      load_frags(IC<T + 1>{}, IC<1>{});
      SCHED_FENCE();
      a1[0] = mfma32(c1, xf[t][0], a1[0]);
      load_frags(IC<T + 1>{}, IC<2>{});
      step_dma(IC<T>{}, IC<0>{});
      SCHED_FENCE();
      a1[1] = mfma32(c1, xf[t][1], a1[1]);
      load_frags(IC<T + 1>{}, IC<3>{});
      step_dma(IC<T>{}, IC<1>{});
      SCHED_FENCE();
      aL = mfma32(cl, xf[t][0], aL);
      asm volatile("" : "+v"(aL));
      if constexpr (BR == 1 && P < 2) { aH = mfma32(ch, cx, aH); asm volatile("" : "+v"(aH)); }
      step_dma(IC<T>{}, IC<2>{});
      hook(tt);
      SCHED_FENCE();
    });
  };

  wait_bar<wait_n<BR>(0, 0)>();
  STAMP(2);
  static_for<0, 4>([&](auto j) { load_frags(IC<0>{}, j); });
  u64 tag = 0, tag2 = 0;
  pass(IC<0>{}, [&](auto tt) {
    if constexpr (tt.value == 1) {
      asm volatile("" : "+v"(ep), "+v"(ep2));
      if (BR == 0) tag = next_tag(ep);
      tag2 = next_tag(ep2);
    }
    if constexpr (BR == 0 && tt.value == 4) {
      const int h = rg & 1, lf = rg >> 1;
      const int f = lf ? 16 + (q & 1) : c;
      const bool rowok = lf ? (p == h) : true;
      const u32 pb = lb + LKVF + (hpl * 2 + h) * 648 + f * 2;
      const int d0 = 2 * h + 4 * g;
      u32 e0 = lds_r16(pb + (d0 + 0) * 36), e1 = lds_r16(pb + (d0 + 1) * 36), e2 = lds_r16(pb + (d0 + 2) * 36), e3 = lds_r16(pb + (d0 + 3) * 36);
      const int dl = h ? 0 : 16;
      u32 l0 = lds_r16(pb + dl * 36), l1 = lds_r16(pb + (dl + 1) * 36);
      u32 lw = (g == 0) ? (l0 | (l1 << 16)) : 0u;
      u32x4 fr = {e0 | (e1 << 16), e2 | (e3 << 16), h ? 0u : lw, h ? lw : 0u};
      if (!rowok) fr = u32x4{0, 0, 0, 0};
      lds_w128(lb + LPHF + (hpl * 4 + rg) * 1024 + l * 16, fr);
      if (w < 3) {
        u32 a = (p == 2 * w) ? ONES : 0u, b = (p == 2 * w + 1) ? ONES : 0u;
        u32 x = ((p & 1) == g1) ? ((p >> 1) ? 0x3f800000u : 0x00003f80u) : 0u;
        lds_w128(lb + LSUM + w * 1024 + l * 16, w < 2 ? u32x4{a, a, b, b} : u32x4{x, 0u, 0u, 0u});
      }
    }
  });
  STAMP(3);
  refresh();
  u32x2 kM[2][2], kH = {0, 0};
#pragma unroll
  for (int sb = 0; sb < 2; ++sb) { kM[0][sb] = pk4(a0[sb]); kM[1][sb] = pk4(a1[sb]); }
  auto unswap = [&](u32x2& a, u32x2& b) {
    u32x2 x = a, y = b;
#pragma unroll
    for (int e = 0; e < 2; ++e) { a[e] = hpl ? y[e] : x[e]; b[e] = hpl ? x[e] : y[e]; }
  };
  if (BR) { unswap(kM[0][0], kM[0][1]); unswap(kM[1][0], kM[1][1]); }
  if (g < 2) lds_w64(lb + KLX + ((1 + 2 * rg + hpl) * 2 + g) * 128 + c * 8, pk4(aL));
  if (BR) {
    kH = pk4(aH);
    if (w == 2 && g < 2) lds_w64(lb + KLX + g * 128 + c * 8, kH);
  }
  const u32 phf = lb + LPHF + hpl * 4096 + l * 16, sumf = lb + LSUM + l * 16;
  const bool lo = g < 2;
  HH hs;
  u32x2 kLs;
  __amdgpu_buffer_rsrc_t srdF = __builtin_amdgcn_make_buffer_rsrc((void*)(ws + WS_OF), (short)0, 256 * 18 * 1024, 0x00020000);
  __amdgpu_buffer_rsrc_t srdS = __builtin_amdgcn_make_buffer_rsrc((void*)(ws + WS_SLAB), (short)0, 136 * SLAB, 0x00020000);
  GLB_AS u64* kflags = (GLB_AS u64*)(ws + WS_FLAG + hq * 256);
  GLB_AS u64* oflags = (GLB_AS u64*)(ws + WS_OFLAG + (BR * 16 + rb) * 256);
  u32x2 fv;
  pass(IC<1>{}, [&](auto tt) {
    constexpr int t = tt.value;
    if constexpr (t == T_CLR - 18) {
      const u32x4 sv = {SENT, SENT, SENT, SENT};
      const u32 ob = (u32)((BR * 128 + rb * 8 + w) * 18 * 1024);
      __builtin_amdgcn_raw_buffer_store_b128(sv, srdF, ob + hq * 2048 + l * 16, 0, 16);
      __builtin_amdgcn_raw_buffer_store_b128(sv, srdF, ob + hq * 2048 + 1024 + l * 16, 0, 16);
      __builtin_amdgcn_raw_buffer_store_b128(sv, srdF, g == 0 ? ob + (16 + (hq >> 2)) * 1024 + (hq & 3) * 256 + c * 16 : OOB, 0, 16);
      if (BR == 0) {
        const u32 so = tid < 400 ? (u32)((hq * 16 + rb) * SLAB + tid * 16) : tid < 425 ? (u32)((128 + hq) * SLAB + rb * 400 + (tid - 400) * 16) : OOB;
        __builtin_amdgcn_raw_buffer_store_b128(sv, srdS, so, 0, 16);
      }
    }
    if constexpr (t == T_FLAG - 18) {
      if (tid == 0) {
        __hip_atomic_store(oflags + hq, tag2, __ATOMIC_RELAXED, __HIP_MEMORY_SCOPE_AGENT);
        if (BR == 0) __hip_atomic_store(kflags + rb, tag, __ATOMIC_RELAXED, __HIP_MEMORY_SCOPE_AGENT);
      }
    }
    if constexpr (t == T_FLD - 18) {
      GLB_AS u64* fa = (BR == 0 && g == 0) ? kflags + c : oflags + (l & 7);
      asm volatile("global_load_dwordx2 %0, %1, off sc1" : "=v"(fv) : "v"(fa) : "memory");
    }
    if constexpr (BR == 0) {
      if constexpr (t >= 2) {
        constexpr int sb = (t - 2) >> 3, st = (t - 2) & 7;
        if constexpr (st == 0) kLs = lds_r64(lb + KLX + ((1 + 2 * rg + (sb ^ hpl)) * 2 + hpl) * 128 + c * 8);
        if constexpr (st < 7) hh_stage<st>(hs, kM[0][sb], kM[1][sb], kLs, phf, sumf, lo, ninf);
        if constexpr (st == 7) {
          const u32 pa = lb + LPK + (rg * 32 + (sb ^ hpl) * 16 + c) * PKS + hpl * 144;
          lds_w64(pa + 8 * g, u32x2{hs.b0[0], hs.b0[1]});
          lds_w64(pa + 32 + 8 * g, u32x2{hs.b0[2], hs.b0[3]});
          lds_w64(pa + 64 + 8 * g, u32x2{hs.b1[0], hs.b1[1]});
          lds_w64(pa + 96 + 8 * g, u32x2{hs.b1[2], hs.b1[3]});
          lds_w32(pa + 128 + 4 * g, hs.bl);
        }
      }
    } else if constexpr (t == 2) {
      const u32 kx = lb + LKX + l * 8;
      if (rg < 3) {
        const u32 b = kx + (((rg + 1) * 2 + hpl) * 2) * 512;
        lds_w64(b, kM[0][1]); lds_w64(b + 512, kM[1][1]);
      }
      if (rg < 2) lds_w64(kx + (hpl * 2 + rg) * 512, kH);
    }
  });
  STAMP(4);
  refresh();
  {
    constexpr int VROW0 = BR ? 16 : 0;
#pragma unroll
    for (int sb = 0; sb < 2; ++sb) {
      const u32 va = lb + LVI + (VROW0 + rg * 32 + (sb ^ hpl) * 16 + c) * VIS + hpl * 80;
      lds_w64(va + 8 * g, pk4(a0[sb]));
      lds_w64(va + 32 + 8 * g, pk4(a1[sb]));
    }
    u32x2 vl = pk4(aL);
    lds_w64(lb + LVI + (VROW0 + rg * 32 + hpl * 16 + c) * VIS + g0 * 80 + 64 + 8 * g1, u32x2{g1 ? vl[1] : vl[0], ONES});
    if (BR) {
      u32x2 hv = pk4(aH);
      if (rg < 2) lds_w64(lb + LVI + c * VIS + hpl * 80 + rg * 32 + 8 * g, hv);
      if (w == 2) lds_w64(lb + LVI + c * VIS + g0 * 80 + 64 + 8 * g1, u32x2{g1 ? hv[1] : hv[0], ONES});
    }
  }
  {
    const int hi = lr & 1;
#pragma unroll
    for (int i = 0; i < 2; ++i) {
      const int qq = 2 * i + ((lr >> 1) & 1);
      vo[i] = (u32)((36 * hpl + 20 * hi + 4 * rg + qq) * XROW + hq * 144 + lu * 16);
      vo8[i] = (u32)((36 * (8 * hpl + 2 * rg + hi) + 16 + qq) * XROW + hq * 144 + lu * 16);
    }
  }
  STAMP(5);
  f32x4 dp = z4, dm = z4, df = z4;
  u32x4 ap, am, af, bv;
  u32 g0_, g1_, g2_, g3_;
  const int rr = 4 * (4 * g0 + q) + 2 * g1;
  const u32 pa = lb + LPK + rr * PKS + hpl * 144 + (rg & 1) * 64 + 8 * p;
  const u32 pf = lb + LPK + rr * PKS + hpl * 144 + 128 + 8 * (p >> 1);
  const u32 va = lb + LVI + rr * VIS + hpl * 80 + (rg < 2 ? rg * 32 + 8 * p : 64 + 8 * (p & 1));
  pass(IC<2>{}, [&](auto tt) {
    constexpr int t = tt.value, T = t + 36;
    if constexpr (T == T_FCHK) {
      asm volatile("s_waitcnt vmcnt(%1)" : "+v"(fv) : "n"(wait_h<BR>(T, T_FLD)) : "memory");
      const u64 want = (BR == 0 && g == 0) ? tag : tag2;
      if (!__all((((u64)fv[1] << 32) | fv[0]) == want)) poll_flags((BR == 0 && g == 0) ? kflags + c : oflags + (l & 7), want);
      if (tid == 0) {
        if (BR == 0 && rb == 0) __hip_atomic_store((GLB_AS u64*)(ws + WS_EPOCH + hq * 256), tag, __ATOMIC_RELAXED, __HIP_MEMORY_SCOPE_AGENT);
        if (hq == 0) __hip_atomic_store((GLB_AS u64*)(ws + WS_OEPOCH + (BR * 16 + rb) * 256), tag2, __ATOMIC_RELAXED, __HIP_MEMORY_SCOPE_AGENT);
      }
    }
    if constexpr (BR == 0) {
      constexpr int kt = T - (T_SLAB - 5);
      if constexpr (kt >= 1 && kt <= 4) {
        dp = mfma32(ap, bv, dp); dm = mfma32(am, bv, dm); df = mfma32(af, bv, df);
        asm volatile("" : "+v"(dp), "+v"(dm), "+v"(df));
      }
      if constexpr (kt >= 0 && kt < 4) {
        ap = frag_tr(pa + kt * 32 * PKS, pa + kt * 32 * PKS + PKS);
        am = frag_tr(pa + 32 + kt * 32 * PKS, pa + 32 + kt * 32 * PKS + PKS);
        af = frag_tr(pf + kt * 32 * PKS, pf + kt * 32 * PKS + PKS);
        bv = frag_tr(va + kt * 32 * VIS, va + kt * 32 * VIS + VIS);
      }
      if constexpr (T == T_SLAB) {
        const u32 slab = (u32)((hq * 16 + rb) * SLAB);
        const bool h0set = (0x47 >> (c & 7)) & 1, h = rg & 1, own = (c >> 3) == hpl;
        u32x2 a = pk4(dp), b = pk4(dm);
        const u32 o1 = rg < 2 ? slab + (hpl * 2 + rg) * 1024 + l * 16 : (own && h0set != h) ? slab + 4096 + l * 16 : OOB;
        __builtin_amdgcn_raw_buffer_store_b128(u32x4{ns(a[0]), ns(a[1]), ns(b[0]), ns(b[1])}, srdS, o1, 0, 16);
        const float x0 = g0 ? df[2] : df[0], x1 = g0 ? df[3] : df[1];
        const bool first = rg < 2 ? !h : h0set;
        const u32 o2 = rg < 2 ? slab + 5120 + (hpl * 2 + rg) * 256 + l * 4 : own ? slab + 6144 + l * 4 : OOB;
        __builtin_amdgcn_raw_buffer_store_b32(ns(pk(first ? x0 : 0.f, first ? 0.f : x1)), srdS, o2, 0, 16);
        STAMP(6);
      }
      if constexpr (T == T_L1 || T == T_L1C) {
        const int dw = w * 16 + c;
        const u32 dof = (u32)((rb * 100 + (dw < 100 ? dw : 99)) * 4);
        const u32 go = (u32)((hq * 16 + 4 * g) * SLAB) + dof;
        if constexpr (T == T_L1) {
          asm volatile("buffer_load_dword %0, %4, %5, 0 offen sc1\n\tbuffer_load_dword %1, %4, %5, %6 offen sc1\n\t"
                       "buffer_load_dword %2, %4, %5, %7 offen sc1\n\tbuffer_load_dword %3, %4, %5, %8 offen sc1"
                       : "=&v"(g0_), "=&v"(g1_), "=&v"(g2_), "=&v"(g3_)
                       : "v"(go), "s"(srdS), "s"(SLAB), "s"(2 * SLAB), "s"(3 * SLAB)
                       : "memory");
        } else {
          asm volatile("s_waitcnt vmcnt(%4)" : "+v"(g0_), "+v"(g1_), "+v"(g2_), "+v"(g3_) : "n"(wait_h<BR>(T, T_L1)) : "memory");
          _Pragma("nounroll") for (int spin = 0; spin < (1 << 20); ++spin) {
      asm volatile("" ::: "memory");
            if (!__any(umax(umax3(g0_, g1_, g2_), g3_) == SENT)) break;
            g0_ = __builtin_amdgcn_raw_buffer_load_b32(srdS, go, 0, 16);
            g1_ = __builtin_amdgcn_raw_buffer_load_b32(srdS, go, SLAB, 16);
            g2_ = __builtin_amdgcn_raw_buffer_load_b32(srdS, go, 2 * SLAB, 16);
            g3_ = __builtin_amdgcn_raw_buffer_load_b32(srdS, go, 3 * SLAB, 16);
          }
          const u32 sel = c == 0 ? 0x00003f80u : c == 1 ? 0x3f800000u : 0u;
          f32x4 sum = mfma32(u32x4{sel, sel, sel, sel}, u32x4{g0_, g1_, g2_, g3_}, z4);
          __builtin_amdgcn_raw_buffer_store_b32(ns(pk(sum[0], sum[1])), srdS, g == 0 ? (u32)((128 + hq) * SLAB) + dof : OOB, 0, 16);
          STAMP(7);
        }
      }
    }
  });
  STAMP(8);
  refresh();
  if (g < 2) lds_w64(lb + QLX + ((2 * rg + hpl) * 2 + g) * 128 + c * 8, pk4(aL));
  bar_lds();
  const u32 ofo = (u32)((BR * 128 + rb * 8 + rg * 2) * 18 * 1024);
  const int hp = hq * 2 + hpl;
  auto store_o = [&](int sb, f32x4 o0, f32x4 o1, float l0, float l1, float r0, float r1) {
    u32x2 a = pk4(o0 * r0), b = pk4(o1 * r1);
    __builtin_amdgcn_raw_buffer_store_b128(u32x4{ns(a[0]), ns(a[1]), ns(b[0]), ns(b[1])}, srdF, ofo + (sb * 18 + hp) * 1024 + l * 16, 0, 16);
    float rr = g0 ? r1 : r0;
    u32 lv = ns(pk(l0 * rr, l1 * rr));
    auto sw = __builtin_amdgcn_permlane16_swap(lv, lv, false, false);
    u32 s0 = sw[0], s1 = sw[1];
    __builtin_amdgcn_raw_buffer_store_b64(u32x2{s0, s1}, srdF, g == 0 ? ofo + (sb * 18 + 16 + (hq >> 2)) * 1024 + (c + 16 * (hq & 3)) * 16 + 8 * hpl : OOB, 0, 16);
  };

  if (BR == 0) {
    HH h0, h1;
    u32x2 qa0 = pk4(a0[0]), qb0 = pk4(a1[0]), qa1 = pk4(a0[1]), qb1 = pk4(a1[1]);
    u32x2 ql0 = lds_r64(lb + QLX + ((2 * rg + hpl) * 2 + hpl) * 128 + c * 8), ql1 = lds_r64(lb + QLX + ((2 * rg + 1 - hpl) * 2 + hpl) * 128 + c * 8);
    const u32 kf = (u32)((128 + hq) * SLAB);
    const int l8 = g * 16 + (c & 7) + 8 * hpl;
    u32x4 AM0, AM1, AMLm;
    u32 AL0, AL1, ALL;
    auto load_kv = [&]() {
      AM0 = __builtin_amdgcn_raw_buffer_load_b128(srdS, kf + hpl * 2048 + l * 16, 0, 16);
      AM1 = __builtin_amdgcn_raw_buffer_load_b128(srdS, kf + hpl * 2048 + 1024 + l * 16, 0, 16);
      AMLm = __builtin_amdgcn_raw_buffer_load_b128(srdS, kf + 4096 + l8 * 16, 0, 16);
      AL0 = __builtin_amdgcn_raw_buffer_load_b32(srdS, kf + 5120 + hpl * 512 + l * 4, 0, 16);
      AL1 = __builtin_amdgcn_raw_buffer_load_b32(srdS, kf + 5120 + hpl * 512 + 256 + l * 4, 0, 16);
      ALL = __builtin_amdgcn_raw_buffer_load_b32(srdS, kf + 6144 + l8 * 4, 0, 16);
    };
    static_for<0, 7>([&](auto st) {
      if (st.value == L2ST) { SCHED_FENCE(); load_kv(); SCHED_FENCE(); }
      hh_stage<st.value>(h0, qa0, qb0, ql0, phf, sumf, lo, ninf);
      hh_stage<st.value>(h1, qa1, qb1, ql1, phf, sumf, lo, ninf);
    });
    STAMP(9);
    _Pragma("nounroll") for (int spin = 0; spin < (1 << 20); ++spin) {
      asm volatile("" ::: "memory");
      if (!__any(umax4(umax4(umax4(umax3(AL0, AL1, ALL), AM0), AM1), AMLm) == SENT)) break;
      load_kv();
    }
    STAMP(10);
    const bool h0set = (0x47 >> (c & 7)) & 1;
    u32x4 AML0, AML1;
#pragma unroll
    for (int e = 0; e < 4; ++e) { AML0[e] = h0set ? AMLm[e] : 0u; AML1[e] = h0set ? 0u : AMLm[e]; }
#pragma unroll
    for (int sb = 0; sb < 2; ++sb) {
      const HH& h = sb ? h1 : h0;
      u32x4 bl = {h.bl, 0u, 0u, 0u};
      f32x4 O0 = mfma32(u32x4{AL0, 0u, 0u, 0u}, bl, mfma32(AM0, h.b0, z4));
      f32x4 O1 = mfma32(u32x4{AL1, 0u, 0u, 0u}, bl, mfma32(AM1, h.b1, z4));
      f32x4 OL = mfma32(u32x4{ALL, 0u, 0u, 0u}, bl, mfma32(AML1, h.b1, mfma32(AML0, h.b0, z4)));
      float r0 = __builtin_amdgcn_rcpf(OL[2] + 1e-6f), r1 = __builtin_amdgcn_rcpf(OL[3] + 1e-6f);
      store_o(sb ^ hpl, O0, O1, OL[0], OL[1], r0, r1);
    }
  } else {
    const u32 kx = lb + LKX + ((rg * 2 + hpl) * 2) * 512 + l * 8;
    u32x2 pM0 = lds_r64(kx), pM1 = lds_r64(kx + 512);
    u32x2 kL[3];
#pragma unroll
    for (int i = 0; i < 3; ++i) kL[i] = lds_r64(lb + KLX + ((2 * rg + i) * 2 + hpl) * 128 + c * 8);
    const int par = c & 1;
    const bool ina = (4 * g + par) > c, inb = (4 * g + par + 2) > c;
    const float sc = 0.23570226039551584f * LOG2E;
    float la[2][2], lbv[2][2], mx[2][2];
    u32x2 qM[2][2] = {{pk4(a0[0]), pk4(a0[1])}, {pk4(a1[0]), pk4(a1[1])}};
    unswap(qM[0][0], qM[0][1]); unswap(qM[1][0], qM[1][1]);
#pragma unroll
    for (int sb = 0; sb < 2; ++sb) {
      u32x2 qlv = lds_r64(lb + QLX + ((2 * rg + sb) * 2 + hpl) * 128 + c * 8);
      u32x2 q0 = qM[0][sb], q1 = qM[1][sb];
      u32x4 b0 = {q0[0], q0[1], g == 0 ? qlv[0] : 0u, 0u};
      u32x4 b1 = {q1[0], q1[1], 0u, g == 0 ? qlv[1] : 0u};
      u32x2 kp0 = sb ? kM[0][0] : pM0, kp1 = sb ? kM[1][0] : pM1, kpl = kL[sb], kcl = kL[sb + 1];
      f32x4 sp0 = mfma32(u32x4{kp0[0], kp0[1], kpl[0], kpl[1]}, b0, z4);
      f32x4 sp1 = mfma32(u32x4{kp1[0], kp1[1], kpl[0], kpl[1]}, b1, z4);
      f32x4 sc0 = mfma32(u32x4{kM[0][sb][0], kM[0][sb][1], kcl[0], kcl[1]}, b0, z4);
      f32x4 sc1 = mfma32(u32x4{kM[1][sb][0], kM[1][sb][1], kcl[0], kcl[1]}, b1, z4);
      const bool dead = (sb == 0) && (rb == 0) && (rg == 0);
      float pa0 = par ? sp0[1] : sp0[0], pb0 = par ? sp0[3] : sp0[2], pa1 = par ? sp1[1] : sp1[0], pb1 = par ? sp1[3] : sp1[2];
      if (dead) { pa0 = pb0 = pa1 = pb1 = -1e30f; }
      la[0][sb] = (ina ? pa0 : (par ? sc0[1] : sc0[0])) * sc;
      lbv[0][sb] = (inb ? pb0 : (par ? sc0[3] : sc0[2])) * sc;
      la[1][sb] = (ina ? pa1 : (par ? sc1[1] : sc1[0])) * sc;
      lbv[1][sb] = (inb ? pb1 : (par ? sc1[3] : sc1[2])) * sc;
      mx[0][sb] = fmaxf(la[0][sb], lbv[0][sb]);
      mx[1][sb] = fmaxf(la[1][sb], lbv[1][sb]);
    }
    float tm = redmax4(mx[0][0], mx[0][1], mx[1][0], mx[1][1], ninf);
    bcast4(tm, mx[0][0], mx[0][1], mx[1][0], mx[1][1]);
    const u32 vb = lb + LVI + (rg * 32 + 4 * g + q) * VIS + hpl * 80;
#pragma unroll
    for (int sb = 0; sb < 2; ++sb) {
      const u32 v0 = vb + sb * 16 * VIS, v1 = v0 + 16 * VIS;
      u32x4 A0 = frag_tr(v0 + 8 * p, v1 + 8 * p), A1 = frag_tr(v0 + 32 + 8 * p, v1 + 32 + 8 * p);
      u32x4 AL = frag_tr(v0 + 64 + 8 * (p & 1), v1 + 64 + 8 * (p & 1));
      u32x4 P[2];
#pragma unroll
      for (int h = 0; h < 2; ++h) {
        float ea = __builtin_amdgcn_exp2f(la[h][sb] - mx[h][sb]), eb = __builtin_amdgcn_exp2f(lbv[h][sb] - mx[h][sb]);
        u32 wa_ = par ? pk(0.f, ea) : pk(ea, 0.f), wb_ = par ? pk(0.f, eb) : pk(eb, 0.f);
        P[h] = u32x4{ina ? wa_ : 0u, inb ? wb_ : 0u, ina ? 0u : wa_, inb ? 0u : wb_};
      }
      f32x4 O0 = mfma32(A0, P[0], z4), O1 = mfma32(A1, P[1], z4);
      f32x4 L0 = mfma32(AL, P[0], z4), L1 = mfma32(AL, P[1], z4);
      float r0 = __builtin_amdgcn_rcpf(L0[2]), r1 = __builtin_amdgcn_rcpf(L1[2]);
      store_o(sb, O0, O1, g0 ? L1[0] : L0[0], g0 ? L1[1] : L0[1], r0, r1);
    }
  }
  STAMP(11);
  refresh();
  wait_bar<4>();
  STAMP(12);
  const u32 wrow2 = lb + WB + (16 * g1 + 8 * g0 + 2 * q) * 144;
  const u32 oa = wrow2 + 8 * p, oa4 = wrow2 + 128 + 8 * (p & 1);
  u32x4 wf[18][5], of[18];
  auto load_wf = [&](auto TT) {
    constexpr int t = TT.value;
    if constexpr (t < 18) {
      const u32 cb = t < 2 ? 8 * WU : (u32)(((((t - 2) >> 1) + hq) & 7) * WU);
      const u32 a = oa + cb + (t & 1) * 32 * 144, a4 = oa4 + cb + (t & 1) * 32 * 144;
#pragma unroll
      for (int i = 0; i < 4; ++i) wf[t][i] = frag_tr(a + 32 * i, a + 144 + 32 * i);
      wf[t][4] = frag_tr(a4, a4 + 144);
    }
  };
  const u32 ofl = (u32)((BR * 128 + rb * 8 + w) * 18 * 1024 + l * 16);
  of[0] = __builtin_amdgcn_raw_buffer_load_b128(srdF, ofl, 16 * 1024, 16);
  of[1] = __builtin_amdgcn_raw_buffer_load_b128(srdF, ofl, 17 * 1024, 16);
  SCHED_FENCE();
  static_for<0, PRE>([&](auto t) { load_wf(t); });
  SCHED_FENCE();
  _Pragma("nounroll") for (int spin = 0; spin < (1 << 20); ++spin) {
      asm volatile("" ::: "memory");
    if (!__any(umax4(umax4(0u, of[0]), of[1]) == SENT)) break;
    of[0] = __builtin_amdgcn_raw_buffer_load_b128(srdF, ofl, 16 * 1024, 16);
    of[1] = __builtin_amdgcn_raw_buffer_load_b128(srdF, ofl, 17 * 1024, 16);
  }
  STAMP(13);
#pragma unroll
  for (int t = 2; t < 18; ++t) of[t] = __builtin_amdgcn_raw_buffer_load_b128(srdF, ofl, ((t - 2 + 2 * hq) & 15) * 1024, 16);
  SCHED_FENCE();
  f32x4 acc[5] = {z4, z4, z4, z4, z4};
  static_for<0, 18>([&](auto tt) {
    constexpr int t = tt.value;
    if constexpr (t >= 2) {
      _Pragma("nounroll") for (int spin = 0; spin < (1 << 20); ++spin) {
      asm volatile("" ::: "memory");
        if (!__any(umax4(0u, of[t]) == SENT)) break;
        of[t] = __builtin_amdgcn_raw_buffer_load_b128(srdF, ofl, ((t - 2 + 2 * hq) & 15) * 1024, 16);
      }
    }
#pragma unroll
    for (int i = 0; i < 5; ++i) acc[i] = mfma32(wf[t][i], of[t], acc[i]);
    load_wf(IC<t + PRE>{});
    SCHED_FENCE();
  });
  STAMP(14);
  char* op = out + (size_t)(rb * 128 + w * 16 + c) * 2304 + BR * 1152 + hq * 144;
#pragma unroll
  for (int i = 0; i < 2; ++i) {
    u32x2 a = pk4(acc[2 * i]), b = pk4(acc[2 * i + 1]);
    auto s0 = __builtin_amdgcn_permlane16_swap(a[0], b[0], false, false);
    auto s1 = __builtin_amdgcn_permlane16_swap(a[1], b[1], false, false);
    u32 s00 = s0[0], s01 = s0[1], s10 = s1[0], s11 = s1[1];
    *(u32x4*)(op + 64 * i + 32 * g0 + 16 * g1) = u32x4{s00, s10, s01, s11};
  }
  if (g < 2) *(u32x2*)(op + 128 + 8 * g) = pk4(acc[4]);
  STAMP(15);
  RSTAMP(31);
  STAMP_FLUSH();
}

__global__ __launch_bounds__(512, 2) void fused(const char* __restrict__ XA, const char* __restrict__ WA, const char* __restrict__ PHI,
                                                const char* __restrict__ WOA, const char* __restrict__ XB, const char* __restrict__ WB,
                                                const char* __restrict__ WOB, char* ws, char* out) {
  __shared__ __attribute__((aligned(16))) char smem[LDS_BYTES];
  const u32 lb = (u32)(uintptr_t)(LDS_AS char*)smem;
  const int b = blockIdx.x, x = b & 7, j = b >> 3, xx = x & 3;
  const int rb = xx * 4 + (j >> 3), hq = j & 7;
  if (x < 4) body<0>(XA, WA, PHI, WOA, ws, out, rb, hq, lb);
  else body<1>(XB, WB, PHI, WOB, ws, out, rb, hq, lb);
}
}

extern "C" void kernel_launch(void* const* d_in, const int* in_sizes, int n_in, void* d_out, int out_size, void* d_ws, size_t ws_size,
                              hipStream_t stream) {
  hk::fused<<<256, 512, 0, stream>>>((const char*)d_in[0], (const char*)d_in[1], (const char*)d_in[2], (const char*)d_in[3], (const char*)d_in[4],
                                     (const char*)d_in[5], (const char*)d_in[6], (char*)d_ws, (char*)d_out);
}
```

```cpp
#include <hip/hip_runtime.h>
#include <stdint.h>

namespace hk {
typedef unsigned int u32;
typedef unsigned long long u64;
typedef unsigned short u16;
typedef float f32x4 __attribute__((ext_vector_type(4)));
typedef float f32x2 __attribute__((ext_vector_type(2)));
typedef u32 u32x4 __attribute__((ext_vector_type(4)));
typedef u32 u32x2 __attribute__((ext_vector_type(2)));
typedef __bf16 bf16x8 __attribute__((ext_vector_type(8)));
typedef __bf16 bf16x2 __attribute__((ext_vector_type(2)));
typedef short s16x4 __attribute__((ext_vector_type(4)));
#define LDS_AS __attribute__((address_space(3)))
#define GLB_AS __attribute__((address_space(1)))
#define DEVI __device__ __forceinline__
#define SCHED_FENCE() __builtin_amdgcn_sched_barrier(0)

template <int V> struct IC { static constexpr int value = V; };
template <int I, int N, class F>
DEVI void static_for(F&& f) {
  if constexpr (I < N) { f(IC<I>{}); static_for<I + 1, N>(f); }
}

constexpr int XROW = 1152, WROW = 3456;
constexpr size_t WS_OF = 0;
constexpr int SLAB = 6400;
constexpr size_t WS_SLAB = 5u << 20;
constexpr size_t WS_FLAG = 6u << 20;
constexpr size_t WS_EPOCH = WS_FLAG + 65536;
constexpr size_t WS_OFLAG = WS_FLAG + 2 * 65536;
constexpr size_t WS_OEPOCH = WS_FLAG + 3 * 65536;
constexpr u32 OOB = 0x7fffffffu;
constexpr u32 SENT = 0xffffffffu;
constexpr int XR = 1024, XS = 16384;
constexpr int WR = XR + 4 * XS, WU = 9216;
constexpr int LM = WR + 8 * WU;
constexpr int LPHF = LM;
constexpr int LSUM = LM + 8192;
constexpr int LKVF = LM + 11264;
constexpr int LHALO = LM;
constexpr int KLX = LM + 18432;
constexpr int QLX = KLX + 2304;
constexpr int LVI = XR, VIS = 168;
constexpr int LPK = XR + 21504, PKS = 296;
constexpr int LKX = XR + 24576;
constexpr int WB = WR - 3 * WU;
constexpr int LDS_BYTES = 163840;
static_assert(QLX + 2048 <= LDS_BYTES, "lds");

DEVI u32x2 tr_read(u32 a) { return __builtin_bit_cast(u32x2, __builtin_amdgcn_ds_read_tr16_b64_v4i16((LDS_AS s16x4*)a)); }
DEVI u32x4 frag_tr(u32 a0, u32 a1) { u32x2 x = tr_read(a0), y = tr_read(a1); return u32x4{x[0], x[1], y[0], y[1]}; }
DEVI u32x4 lds_r128(u32 a) { return *(LDS_AS u32x4*)a; }
DEVI u32x2 lds_r64(u32 a) { return *(LDS_AS u32x2*)a; }
DEVI u32 lds_r32(u32 a) { return *(LDS_AS u32*)a; }
DEVI u32 lds_r16(u32 a) { return *(LDS_AS u16*)a; }
DEVI void lds_w128(u32 a, u32x4 v) { *(LDS_AS u32x4*)a = v; }
DEVI void lds_w64(u32 a, u32x2 v) { *(LDS_AS u32x2*)a = v; }
DEVI void lds_w32(u32 a, u32 v) { *(LDS_AS u32*)a = v; }
DEVI f32x4 mfma32(u32x4 a, u32x4 b, f32x4 c) {
  return __builtin_amdgcn_mfma_f32_16x16x32_bf16(__builtin_bit_cast(bf16x8, a), __builtin_bit_cast(bf16x8, b), c, 0, 0, 0);
}
DEVI u32 pk(float a, float b) { f32x2 v = {a, b}; return __builtin_bit_cast(u32, __builtin_convertvector(v, bf16x2)); }
DEVI u32x2 pk4(f32x4 v) { return u32x2{pk(v[0], v[1]), pk(v[2], v[3])}; }
DEVI float max3(float a, float b, float c) { return fmaxf(fmaxf(a, b), c); }
DEVI u32 umax(u32 a, u32 b) { return a > b ? a : b; }
DEVI u32 umax3(u32 a, u32 b, u32 c) { return umax(umax(a, b), c); }
DEVI u32 umax4(u32 m, u32x4 v) { return umax3(umax3(m, v[0], v[1]), v[2], v[3]); }
DEVI u32 ns(u32 v) { return v < 0xfffffffeu ? v : 0xfffffffeu; }

DEVI void dma_full(u32 lds, u32 voff, __amdgpu_buffer_rsrc_t srd, u32 soff) {
  asm volatile("s_mov_b32 m0, %0\n\ts_nop 0\n\tbuffer_load_dwordx4 %1, %2, %3 offen lds" ::"s"(lds), "v"(voff), "s"(srd), "s"(soff) : "memory");
}
DEVI void dma_mask(u32 lds, u32 voff, __amdgpu_buffer_rsrc_t srd, u32 soff, u64 mask) {
  asm volatile("s_mov_b32 m0, %0\n\ts_mov_b64 exec, %4\n\tbuffer_load_dwordx4 %1, %2, %3 offen lds\n\ts_mov_b64 exec, -1" ::"s"(lds), "v"(voff), "s"(srd),
               "s"(soff), "s"(mask)
               : "memory");
}
template <int N>
DEVI void wait_bar() { asm volatile("s_waitcnt vmcnt(%0) lgkmcnt(0)\n\ts_barrier" ::"n"(N) : "memory"); }
template <int N>
DEVI void wait_vm_bar() { asm volatile("s_waitcnt vmcnt(%0)\n\ts_barrier" ::"n"(N) : "memory"); }
DEVI void bar_lds() { asm volatile("s_waitcnt lgkmcnt(0)\n\ts_barrier" ::: "memory"); }
DEVI void bar_only() { asm volatile("s_barrier" ::: "memory"); }

DEVI float redmax4(float a, float b, float c, float d, float ninf) {
  auto r1 = __builtin_amdgcn_permlane32_swap(__float_as_uint(a), __float_as_uint(b), false, false);
  auto r2 = __builtin_amdgcn_permlane32_swap(__float_as_uint(c), __float_as_uint(d), false, false);
  u32 r10 = r1[0], r11 = r1[1], r20 = r2[0], r21 = r2[1];
  float ab = max3(__uint_as_float(r10), __uint_as_float(r11), ninf);
  float cd = max3(__uint_as_float(r20), __uint_as_float(r21), ninf);
  auto r3 = __builtin_amdgcn_permlane16_swap(__float_as_uint(ab), __float_as_uint(cd), false, false);
  u32 r30 = r3[0], r31 = r3[1];
  return max3(__uint_as_float(r30), __uint_as_float(r31), ninf);
}
DEVI void bcast4(float t, float& a, float& b, float& c, float& d) {
  u32 tu = __float_as_uint(t);
  auto s1 = __builtin_amdgcn_permlane16_swap(tu, tu, false, false);
  u32 x = s1[0], y = s1[1];
  auto s2 = __builtin_amdgcn_permlane32_swap(x, x, false, false);
  auto s3 = __builtin_amdgcn_permlane32_swap(y, y, false, false);
  u32 s20 = s2[0], s21 = s2[1], s30 = s3[0], s31 = s3[1];
  a = __uint_as_float(s20); b = __uint_as_float(s21); c = __uint_as_float(s30); d = __uint_as_float(s31);
}

constexpr float LOG2E = 1.4426950408889634f;
constexpr u32 ONES = 0x3f803f80u;

struct HH {
  u32x4 f0, f1, f2, f3;
  f32x4 T0, T1, TL, S;
  float m0, m1, n0, n1;
  f32x4 p0, p1, q0, q1;
  float l0, l1;
  u32x4 b0, b1; u32 bl;
};
template <int ST>
DEVI void hh_stage(HH& s, u32x2 ma, u32x2 mb, u32x2 left, u32 phf, u32 sumf, bool lo, float ninf) {
  const f32x4 z = {0.f, 0.f, 0.f, 0.f};
  if constexpr (ST == 0) {
    s.f0 = lds_r128(phf); s.f1 = lds_r128(phf + 1024); s.f2 = lds_r128(phf + 2048); s.f3 = lds_r128(phf + 3072);
  } else if constexpr (ST == 1) {
    u32x4 b0 = {ma[0], ma[1], left[0], left[1]}, b1 = {mb[0], mb[1], left[0], left[1]};
    s.T0 = mfma32(s.f0, b0, z);
    s.T1 = mfma32(s.f1, b1, z);
    s.TL = mfma32(s.f3, b1, mfma32(s.f2, b0, z));
  } else if constexpr (ST == 2) {
    float a = max3(max3(s.T0[0], s.T0[1], s.T0[2]), s.T0[3], s.TL[0]);
    float b = max3(max3(s.T1[0], s.T1[1], s.T1[2]), s.T1[3], s.TL[1]);
    float c = max3(max3(-s.T0[0], -s.T0[1], -s.T0[2]), -s.T0[3], -s.TL[0]);
    float d = max3(max3(-s.T1[0], -s.T1[1], -s.T1[2]), -s.T1[3], -s.TL[1]);
    float t = redmax4(a, b, c, d, ninf) * (-LOG2E);
    bcast4(t, s.m0, s.m1, s.n0, s.n1);
    s.f0 = lds_r128(sumf); s.f1 = lds_r128(sumf + 1024); s.f2 = lds_r128(sumf + 2048);
  } else if constexpr (ST == 3) {
#pragma unroll
    for (int r = 0; r < 4; ++r) {
      s.p0[r] = __builtin_amdgcn_exp2f(fmaf(s.T0[r], LOG2E, s.m0));
      s.q0[r] = __builtin_amdgcn_exp2f(fmaf(s.T0[r], -LOG2E, s.n0));
    }
    s.l0 = __builtin_amdgcn_exp2f(fmaf(s.TL[0], lo ? LOG2E : -LOG2E, lo ? s.m0 : s.n0));
  } else if constexpr (ST == 4) {
#pragma unroll
    for (int r = 0; r < 4; ++r) {
      s.p1[r] = __builtin_amdgcn_exp2f(fmaf(s.T1[r], LOG2E, s.m1));
      s.q1[r] = __builtin_amdgcn_exp2f(fmaf(s.T1[r], -LOG2E, s.n1));
    }
    s.l1 = __builtin_amdgcn_exp2f(fmaf(s.TL[1], lo ? LOG2E : -LOG2E, lo ? s.m1 : s.n1));
  } else if constexpr (ST == 5) {
    u32x2 a = pk4(s.p0), b = pk4(s.q0), c = pk4(s.p1), d = pk4(s.q1);
    u32x4 bl = {pk(s.l0, s.l1), 0u, 0u, 0u};
    s.S = mfma32(s.f2, bl, mfma32(s.f1, u32x4{c[0], c[1], d[0], d[1]}, mfma32(s.f0, u32x4{a[0], a[1], b[0], b[1]}, z)));
  } else if constexpr (ST == 6) {
    float r0 = __builtin_amdgcn_rcpf(s.S[0]), r1 = __builtin_amdgcn_rcpf(s.S[1]);
    float r2 = __builtin_amdgcn_rcpf(s.S[2]), r3 = __builtin_amdgcn_rcpf(s.S[3]);
    u32x2 a = pk4(s.p0 * r0), b = pk4(s.q0 * r1), c = pk4(s.p1 * r2), d = pk4(s.q1 * r3);
    s.b0 = u32x4{a[0], a[1], b[0], b[1]};
    s.b1 = u32x4{c[0], c[1], d[0], d[1]};
    s.bl = pk(s.l0 * (lo ? r0 : r1), s.l1 * (lo ? r2 : r3));
  }
}

#define L2ST 4
#define PRE 6
#define PRO 2
#define QK 3
#define QV 2
constexpr int T_CLR = 19, T_FLAG = 27, T_FLD = 33, T_FCHK = 37;
constexpr int T_SLAB = 42, T_L1 = 49, T_L1C = 53;
struct Sched {
  int op[54][3];
  int pos_step[55];
  int pos_hook[54];
  int last[36];
  int total;
};
constexpr int ext_ops(int br, int T) {
  return T == T_CLR ? (br ? 3 : 4) : T == T_FLD ? 1 : br ? 0 : T == T_SLAB ? 2 : T == T_L1 ? 4 : T == T_L1C ? 1 : 0;
}
constexpr int op_chunk(int op) { return op >= 200 ? (op - 200) >> 1 : (op - 100) >> 2; }
constexpr int op_earliest(int br, int op) {
  const int n = op_chunk(op);
  if (op < 200) return n < 4 ? 0 : 2 * (n - 4) + 1;
  if (n < 8) return 0;
  if (n < 27) return 2 * (n - 8) + 3;
  if (n < 30) return br ? 21 : T_SLAB + 1;
  return 2 * (n - 11) + 3;
}
constexpr Sched make_sched(int br) {
  Sched s{};
  const int nx = br ? 3 : 2;
  int list[128] = {}, n = 0;
  for (int c = 0; c < 36; ++c) {
    if (c < 9) for (int i = 0; i < nx; ++i) list[n++] = 100 + 4 * c + i;
    list[n++] = 200 + 2 * c; list[n++] = 200 + 2 * c + 1;
  }
  int pos = 0, k = 0;
  for (; k < PRO * (nx + 2); ++k) { s.last[op_chunk(list[k])] = pos++; if (k == nx + 1) pos += br ? 1 : 3; }
  for (int T = 0; T < 54; ++T) {
    s.pos_step[T] = pos;
    const int q = T < 18 ? QK : (T < 50 ? QV : 3);
    for (int j = 0; j < 3; ++j) {
      s.op[T][j] = 0;
      if (j < q && k < n && op_earliest(br, list[k]) <= T) { s.op[T][j] = list[k]; s.last[op_chunk(list[k])] = pos++; ++k; }
    }
    s.pos_hook[T] = pos;
    pos += ext_ops(br, T);
  }
  s.pos_step[54] = pos;
  s.total = k == n ? pos : -1;
  return s;
}
template <int BR> struct SC { static constexpr Sched s = make_sched(BR); };
static_assert(SC<0>::s.total > 0 && SC<1>::s.total > 0, "schedule incomplete");
constexpr int imin(int a, int b) { return a < b ? a : b; }
template <int BR> constexpr int wait_n(int T, int c) {
  int n = SC<BR>::s.pos_step[T] - SC<BR>::s.last[c] - 1;
  if (T == T_FLAG) n = imin(n, SC<BR>::s.pos_step[T] - SC<BR>::s.pos_step[T_CLR + 1]);
  return n;
}
template <int BR> constexpr int wait_h(int T, int T0) { return SC<BR>::s.pos_hook[T] - SC<BR>::s.pos_step[T0 + 1]; }

#define STAMP(i) do { if ((i) == 99 && BR == 0) rtk = __builtin_amdgcn_s_memrealtime(); } while (0)
#define RSTAMP(i) do { if ((i) == 30) { rt0 = __builtin_amdgcn_s_memrealtime(); rtk = rt0; } } while (0)
#define STAMP_FLUSH() do { while (__builtin_amdgcn_s_memrealtime() - rt0 < 4000 + (rtk - rt0)) __builtin_amdgcn_s_sleep(1); } while (0)

DEVI u64 next_tag(u32x2 e) {
  const u64 E = ((u64)(u32)__builtin_amdgcn_readfirstlane(e[1]) << 32) | (u32)__builtin_amdgcn_readfirstlane(e[0]);
  return E * 6364136223846793005ull + 1442695040888963407ull;
}
DEVI void poll_flags(GLB_AS u64* f, u64 tag) {
  for (int spin = 0; spin < (1 << 20); ++spin) {
    u64 v = __hip_atomic_load(f, __ATOMIC_RELAXED, __HIP_MEMORY_SCOPE_AGENT);
    if (__all(v == tag)) break;
    __builtin_amdgcn_s_sleep(1);
  }
  asm volatile("" ::: "memory");
}

template <int BR>
DEVI void body(const char* __restrict__ X, const char* __restrict__ W, const char* __restrict__ PHI, const char* __restrict__ WO, char* ws, char* out,
               int rb, int hq, u32 lb) {
  u64 rt0 = 0, rtk = 0;
  int tid = threadIdx.x, l = tid & 63;
  const int w = __builtin_amdgcn_readfirstlane(tid >> 6);
  const int rg = w & 3, hpl = w >> 2;
  int c = l & 15, g = l >> 4, q = c >> 2, p = c & 3, g0 = g & 1, g1 = g >> 1;
  int lr = (l * 57) >> 9, lu = l - lr * 9;
  auto refresh = [&]() {
    asm volatile("" : "+v"(tid));
    l = tid & 63; c = l & 15; g = l >> 4; q = c >> 2; p = c & 3; g0 = g & 1; g1 = g >> 1;
    lr = (l * 57) >> 9; lu = l - lr * 9;
  };
  STAMP(0);
  RSTAMP(30);
  __amdgpu_buffer_rsrc_t srdX = __builtin_amdgcn_make_buffer_rsrc((void*)X, (short)0, 2048 * XROW, 0x00020000);
  __amdgpu_buffer_rsrc_t srdW = __builtin_amdgcn_make_buffer_rsrc((void*)W, (short)0, 576 * WROW, 0x00020000);
#define ROT (rb + 2 * hq) % 9
  const int rot = ROT;
  constexpr int NX = BR ? 3 : 2;
  constexpr u64 M36 = (1ull << 36) - 1;

  u32 vx[2], vq[2], vh = 0;
#pragma unroll
  for (int i = 0; i < 2; ++i) {
    int row = 16 * w + 8 * i + (l >> 3);
    vx[i] = (u32)((rb * 128 + row) * XROW + (((l & 7) ^ ((row >> 1) & 7)) << 4));
  }
#pragma unroll
  for (int i = 0; i < 2; ++i) vq[i] = (u32)((8 * w + 4 * (lr & 1) + 2 * i + ((lr >> 1) & 1)) * WROW + hq * 144 + lu * 16);
  if (BR) {
    int row = 2 * w + ((l >> 3) & 1);
    int grow = rb * 128 - 16 + row; grow = grow < 0 ? 0 : grow;
    vh = (u32)(grow * XROW + (((l & 7) ^ ((row >> 1) & 7)) << 4));
  }
  auto kc_of = [&](int cc) { int k = cc + rot; return k >= 9 ? k - 9 : k; };
  auto dma_x = [&](int cc, int i) {
    const int kc = kc_of(cc);
    if (i < 2) dma_full(lb + XR + (cc & 3) * XS + w * 2048 + i * 1024, vx[i], srdX, (u32)(kc * 128));
    else dma_mask(lb + LHALO + cc * 2048 + w * 256, vh, srdX, (u32)(kc * 128), 0xffffull);
  };
  u32 vo[2] = {0, 0}, vo8[2] = {0, 0};
  __amdgpu_buffer_rsrc_t srdO = __builtin_amdgcn_make_buffer_rsrc((void*)WO, (short)0, 576 * XROW, 0x00020000);
  auto dma_w = [&](int n, int i) {
    if (n < 27) {
      const int kc = kc_of(n % 9), sec = n < 9 ? 1152 : (n < 18 ? 2304 : 0);
      dma_mask(lb + WR + ((n + 5) & 7) * WU + (2 * w + i) * 576, vq[i], srdW, (u32)(kc * 64 * WROW + sec), M36);
    } else if (n < 35) {
      dma_mask(lb + WB + (n - 27) * WU + (2 * w + i) * 576, vo[i], srdO, (u32)((n - 27) * 72 * XROW), M36);
    } else {
      dma_mask(lb + WB + 8 * WU + (2 * w + i) * 576, vo8[i], srdO, 0u, M36);
    }
  };
  u32x2 ep = {0, 0}, ep2;
  asm volatile("" ::"s"(srdX), "s"(srdW));
  STAMP(16);
  static_for<0, PRO>([&](auto cc) {
    dma_x(cc.value, 0); dma_x(cc.value, 1);
    if (BR) dma_x(cc.value, 2);
    dma_w(cc.value, 0); dma_w(cc.value, 1);
    if (cc.value == 0) {
      const char* ea = ws + WS_OEPOCH + (BR * 16 + rb) * 256;
      asm volatile("global_load_dwordx2 %0, %1, off sc1" : "=v"(ep2) : "v"(ea) : "memory");
      if (BR == 0) {
        const char* eb = ws + WS_EPOCH + hq * 256;
        asm volatile("global_load_dwordx2 %0, %1, off sc1" : "=v"(ep) : "v"(eb) : "memory");
        __amdgpu_buffer_rsrc_t srdP = __builtin_amdgcn_make_buffer_rsrc((void*)PHI, (short)0, 32 * 648, 0x00020000);
        dma_mask(lb + LKVF + w * 336, (u32)(hq * 2592 + (w * 21 + l) * 16), srdP, 0, (1ull << 21) - 1);
      }
    }
    if (cc.value == 0) STAMP(17);
    if (cc.value < PRO - 1) bar_only();
    if (cc.value == 0) STAMP(18);
  });
  STAMP(1);

  const u32 xa0 = lb + XR + (rg * 32 + hpl * 16 + c) * 128 + ((g ^ ((c >> 1) & 7)) << 4);
  const u32 xa1 = xa0 ^ 64;
  const u32 xb0 = xa0 + 2048 - hpl * 4096, xb1 = xb0 ^ 64;
  const u32 xh0 = lb + LHALO + c * 128 + ((g ^ ((c >> 1) & 7)) << 4);
  const u32 xh1 = xh0 ^ 64;
  const u32 wrow = lb + WR + (16 * g1 + 8 * g0 + 2 * q) * 144;
  const u32 wa = wrow + hpl * 72 + 8 * p;
  const u32 wl = wrow + 32 + (p & 1) * 72;
  const u32 wh = rg < 2 ? wa + rg * 40 : wl;
  float ninf = -__builtin_inff();
  asm volatile("" : "+s"(ninf));

  u32x4 xf[18][2];
  const f32x4 z4 = {0.f, 0.f, 0.f, 0.f};
  f32x4 a0[2], a1[2], aL, aH;
  u32x4 f0, f1, fl, fh, xh;
  auto load_frags = [&](auto TT, auto JJ) {
    constexpr int T = TT.value, j = JJ.value, n = T >> 1, ks = T & 1;
    constexpr u32 o0 = ((n + 5) & 7) * WU + ks * 32 * 144, o1 = o0 + 144;
    if constexpr (T < 54) {
      if constexpr (j == 0) {
        if constexpr (T < 18) xf[T][0] = lds_r128((ks ? xa1 : xa0) + (n & 3) * XS);
        f0 = frag_tr(wa + o0, wa + o1);
      } else if constexpr (j == 1) {
        if constexpr (T < 18) xf[T][1] = lds_r128((ks ? xb1 : xb0) + (n & 3) * XS);
        f1 = frag_tr(wa + o0 + 40, wa + o1 + 40);
      } else if constexpr (j == 2) {
        fl = frag_tr(wl + o0, wl + o1);
      } else if constexpr (BR == 1 && T < 36) {
        fh = frag_tr(wh + o0, wh + o1);
        xh = lds_r128((ks ? xh1 : xh0) + (n % 9) * 2048);
      }
    }
  };
  auto step_dma = [&](auto TT, auto JJ) {
    constexpr int op = SC<BR>::s.op[TT.value][JJ.value];
    if constexpr (op >= 200) dma_w((op - 200) >> 1, op & 1);
    else if constexpr (op >= 100) dma_x((op - 100) >> 2, op & 3);
  };
  auto pass = [&](auto PP, auto&& hook) {
    constexpr int P = PP.value;
    a0[0] = z4; a0[1] = z4; a1[0] = z4; a1[1] = z4; aL = z4; aH = z4;
    static_for<0, 18>([&](auto tt) {
      constexpr int t = tt.value, T = P * 18 + t;
      if constexpr ((T & 1) && (T >> 1) + 1 <= 26) {
        if constexpr (T <= 9 || T == 19 || T == 37) wait_bar<wait_n<BR>(T, (T >> 1) + 1)>();
        else wait_vm_bar<wait_n<BR>(T, (T >> 1) + 1)>();
      }
      u32x4 c0 = f0, c1 = f1, cl = fl, ch = fh, cx = xh;
      SCHED_FENCE();
      a0[0] = mfma32(c0, xf[t][0], a0[0]);
      load_frags(IC<T + 1>{}, IC<0>{});
      SCHED_FENCE();
      a0[1] = mfma32(c0, xf[t][1], a0[1]);
      load_frags(IC<T + 1>{}, IC<1>{});
      SCHED_FENCE();
      a1[0] = mfma32(c1, xf[t][0], a1[0]);
      load_frags(IC<T + 1>{}, IC<2>{});
      step_dma(IC<T>{}, IC<0>{});
      SCHED_FENCE();
      a1[1] = mfma32(c1, xf[t][1], a1[1]);
      load_frags(IC<T + 1>{}, IC<3>{});
      step_dma(IC<T>{}, IC<1>{});
      SCHED_FENCE();
      aL = mfma32(cl, xf[t][0], aL);
      asm volatile("" : "+v"(aL));
      if constexpr (BR == 1 && P < 2) { aH = mfma32(ch, cx, aH); asm volatile("" : "+v"(aH)); }
      step_dma(IC<T>{}, IC<2>{});
      hook(tt);
      SCHED_FENCE();
    });
  };

  wait_bar<wait_n<BR>(0, 0)>();
  STAMP(2);
  static_for<0, 4>([&](auto j) { load_frags(IC<0>{}, j); });
  u64 tag = 0, tag2 = 0;
  pass(IC<0>{}, [&](auto tt) {
    if constexpr (tt.value == 1) {
      asm volatile("" : "+v"(ep), "+v"(ep2));
      if (BR == 0) tag = next_tag(ep);
      tag2 = next_tag(ep2);
    }
    if constexpr (BR == 0 && tt.value == 4) {
      const int h = rg & 1, lf = rg >> 1;
      const int f = lf ? 16 + (q & 1) : c;
      const bool rowok = lf ? (p == h) : true;
      const u32 pb = lb + LKVF + (hpl * 2 + h) * 648 + f * 2;
      const int d0 = 2 * h + 4 * g;
      u32 e0 = lds_r16(pb + (d0 + 0) * 36), e1 = lds_r16(pb + (d0 + 1) * 36), e2 = lds_r16(pb + (d0 + 2) * 36), e3 = lds_r16(pb + (d0 + 3) * 36);
      const int dl = h ? 0 : 16;
      u32 l0 = lds_r16(pb + dl * 36), l1 = lds_r16(pb + (dl + 1) * 36);
      u32 lw = (g == 0) ? (l0 | (l1 << 16)) : 0u;
      u32x4 fr = {e0 | (e1 << 16), e2 | (e3 << 16), h ? 0u : lw, h ? lw : 0u};
      if (!rowok) fr = u32x4{0, 0, 0, 0};
      lds_w128(lb + LPHF + (hpl * 4 + rg) * 1024 + l * 16, fr);
      if (w < 3) {
        u32 a = (p == 2 * w) ? ONES : 0u, b = (p == 2 * w + 1) ? ONES : 0u;
        u32 x = ((p & 1) == g1) ? ((p >> 1) ? 0x3f800000u : 0x00003f80u) : 0u;
        lds_w128(lb + LSUM + w * 1024 + l * 16, w < 2 ? u32x4{a, a, b, b} : u32x4{x, 0u, 0u, 0u});
      }
    }
  });
  STAMP(3);
  refresh();
  u32x2 kM[2][2], kH = {0, 0};
#pragma unroll
  for (int sb = 0; sb < 2; ++sb) { kM[0][sb] = pk4(a0[sb]); kM[1][sb] = pk4(a1[sb]); }
  auto unswap = [&](u32x2& a, u32x2& b) {
    u32x2 x = a, y = b;
#pragma unroll
    for (int e = 0; e < 2; ++e) { a[e] = hpl ? y[e] : x[e]; b[e] = hpl ? x[e] : y[e]; }
  };
  if (BR) { unswap(kM[0][0], kM[0][1]); unswap(kM[1][0], kM[1][1]); }
  if (g < 2) lds_w64(lb + KLX + ((1 + 2 * rg + hpl) * 2 + g) * 128 + c * 8, pk4(aL));
  if (BR) {
    kH = pk4(aH);
    if (w == 2 && g < 2) lds_w64(lb + KLX + g * 128 + c * 8, kH);
  }
  const u32 phf = lb + LPHF + hpl * 4096 + l * 16, sumf = lb + LSUM + l * 16;
  const bool lo = g < 2;
  HH hs;
  u32x2 kLs;
  __amdgpu_buffer_rsrc_t srdF = __builtin_amdgcn_make_buffer_rsrc((void*)(ws + WS_OF), (short)0, 256 * 18 * 1024, 0x00020000);
  __amdgpu_buffer_rsrc_t srdS = __builtin_amdgcn_make_buffer_rsrc((void*)(ws + WS_SLAB), (short)0, 136 * SLAB, 0x00020000);
  GLB_AS u64* kflags = (GLB_AS u64*)(ws + WS_FLAG + hq * 256);
  GLB_AS u64* oflags = (GLB_AS u64*)(ws + WS_OFLAG + (BR * 16 + rb) * 256);
  u32x2 fv;
  pass(IC<1>{}, [&](auto tt) {
    constexpr int t = tt.value;
    if constexpr (t == T_CLR - 18) {
      const u32x4 sv = {SENT, SENT, SENT, SENT};
      const u32 ob = (u32)((BR * 128 + rb * 8 + w) * 18 * 1024);
      __builtin_amdgcn_raw_buffer_store_b128(sv, srdF, ob + hq * 2048 + l * 16, 0, 16);
      __builtin_amdgcn_raw_buffer_store_b128(sv, srdF, ob + hq * 2048 + 1024 + l * 16, 0, 16);
      __builtin_amdgcn_raw_buffer_store_b128(sv, srdF, g == 0 ? ob + (16 + (hq >> 2)) * 1024 + (hq & 3) * 256 + c * 16 : OOB, 0, 16);
      if (BR == 0) {
        const u32 so = tid < 400 ? (u32)((hq * 16 + rb) * SLAB + tid * 16) : tid < 425 ? (u32)((128 + hq) * SLAB + rb * 400 + (tid - 400) * 16) : OOB;
        __builtin_amdgcn_raw_buffer_store_b128(sv, srdS, so, 0, 16);
      }
    }
    if constexpr (t == T_FLAG - 18) {
      if (tid == 0) {
        __hip_atomic_store(oflags + hq, tag2, __ATOMIC_RELAXED, __HIP_MEMORY_SCOPE_AGENT);
        if (BR == 0) __hip_atomic_store(kflags + rb, tag, __ATOMIC_RELAXED, __HIP_MEMORY_SCOPE_AGENT);
      }
    }
    if constexpr (t == T_FLD - 18) {
      GLB_AS u64* fa = (BR == 0 && g == 0) ? kflags + c : oflags + (l & 7);
      asm volatile("global_load_dwordx2 %0, %1, off sc1" : "=v"(fv) : "v"(fa) : "memory");
    }
    if constexpr (BR == 0) {
      if constexpr (t >= 2) {
        constexpr int sb = (t - 2) >> 3, st = (t - 2) & 7;
        if constexpr (st == 0) kLs = lds_r64(lb + KLX + ((1 + 2 * rg + (sb ^ hpl)) * 2 + hpl) * 128 + c * 8);
        if constexpr (st < 7) hh_stage<st>(hs, kM[0][sb], kM[1][sb], kLs, phf, sumf, lo, ninf);
        if constexpr (st == 7) {
          const u32 pa = lb + LPK + (rg * 32 + (sb ^ hpl) * 16 + c) * PKS + hpl * 144;
          lds_w64(pa + 8 * g, u32x2{hs.b0[0], hs.b0[1]});
          lds_w64(pa + 32 + 8 * g, u32x2{hs.b0[2], hs.b0[3]});
          lds_w64(pa + 64 + 8 * g, u32x2{hs.b1[0], hs.b1[1]});
          lds_w64(pa + 96 + 8 * g, u32x2{hs.b1[2], hs.b1[3]});
          lds_w32(pa + 128 + 4 * g, hs.bl);
        }
      }
    } else if constexpr (t == 2) {
      const u32 kx = lb + LKX + l * 8;
      if (rg < 3) {
        const u32 b = kx + (((rg + 1) * 2 + hpl) * 2) * 512;
        lds_w64(b, kM[0][1]); lds_w64(b + 512, kM[1][1]);
      }
      if (rg < 2) lds_w64(kx + (hpl * 2 + rg) * 512, kH);
    }
  });
  STAMP(4);
  refresh();
  {
    constexpr int VROW0 = BR ? 16 : 0;
#pragma unroll
    for (int sb = 0; sb < 2; ++sb) {
      const u32 va = lb + LVI + (VROW0 + rg * 32 + (sb ^ hpl) * 16 + c) * VIS + hpl * 80;
      lds_w64(va + 8 * g, pk4(a0[sb]));
      lds_w64(va + 32 + 8 * g, pk4(a1[sb]));
    }
    u32x2 vl = pk4(aL);
    lds_w64(lb + LVI + (VROW0 + rg * 32 + hpl * 16 + c) * VIS + g0 * 80 + 64 + 8 * g1, u32x2{g1 ? vl[1] : vl[0], ONES});
    if (BR) {
      u32x2 hv = pk4(aH);
      if (rg < 2) lds_w64(lb + LVI + c * VIS + hpl * 80 + rg * 32 + 8 * g, hv);
      if (w == 2) lds_w64(lb + LVI + c * VIS + g0 * 80 + 64 + 8 * g1, u32x2{g1 ? hv[1] : hv[0], ONES});
    }
  }
  {
    const int hi = lr & 1;
#pragma unroll
    for (int i = 0; i < 2; ++i) {
      const int qq = 2 * i + ((lr >> 1) & 1);
      vo[i] = (u32)((36 * hpl + 20 * hi + 4 * rg + qq) * XROW + hq * 144 + lu * 16);
      vo8[i] = (u32)((36 * (8 * hpl + 2 * rg + hi) + 16 + qq) * XROW + hq * 144 + lu * 16);
    }
  }
  STAMP(5);
  f32x4 dp = z4, dm = z4, df = z4;
  u32x4 ap, am, af, bv;
  u32 g0_, g1_, g2_, g3_;
  const int rr = 4 * (4 * g0 + q) + 2 * g1;
  const u32 pa = lb + LPK + rr * PKS + hpl * 144 + (rg & 1) * 64 + 8 * p;
  const u32 pf = lb + LPK + rr * PKS + hpl * 144 + 128 + 8 * (p >> 1);
  const u32 va = lb + LVI + rr * VIS + hpl * 80 + (rg < 2 ? rg * 32 + 8 * p : 64 + 8 * (p & 1));
  pass(IC<2>{}, [&](auto tt) {
    constexpr int t = tt.value, T = t + 36;
    if constexpr (T == T_FCHK) {
      asm volatile("s_waitcnt vmcnt(%1)" : "+v"(fv) : "n"(wait_h<BR>(T, T_FLD)) : "memory");
      const u64 want = (BR == 0 && g == 0) ? tag : tag2;
      if (!__all((((u64)fv[1] << 32) | fv[0]) == want)) poll_flags((BR == 0 && g == 0) ? kflags + c : oflags + (l & 7), want);
      if (tid == 0) {
        if (BR == 0 && rb == 0) __hip_atomic_store((GLB_AS u64*)(ws + WS_EPOCH + hq * 256), tag, __ATOMIC_RELAXED, __HIP_MEMORY_SCOPE_AGENT);
        if (hq == 0) __hip_atomic_store((GLB_AS u64*)(ws + WS_OEPOCH + (BR * 16 + rb) * 256), tag2, __ATOMIC_RELAXED, __HIP_MEMORY_SCOPE_AGENT);
      }
    }
    if constexpr (BR == 0) {
      constexpr int kt = T - (T_SLAB - 5);
      if constexpr (kt >= 1 && kt <= 4) {
        dp = mfma32(ap, bv, dp); dm = mfma32(am, bv, dm); df = mfma32(af, bv, df);
        asm volatile("" : "+v"(dp), "+v"(dm), "+v"(df));
      }
      if constexpr (kt >= 0 && kt < 4) {
        ap = frag_tr(pa + kt * 32 * PKS, pa + kt * 32 * PKS + PKS);
        am = frag_tr(pa + 32 + kt * 32 * PKS, pa + 32 + kt * 32 * PKS + PKS);
        af = frag_tr(pf + kt * 32 * PKS, pf + kt * 32 * PKS + PKS);
        bv = frag_tr(va + kt * 32 * VIS, va + kt * 32 * VIS + VIS);
      }
      if constexpr (T == T_SLAB) {
        const u32 slab = (u32)((hq * 16 + rb) * SLAB);
        const bool h0set = (0x47 >> (c & 7)) & 1, h = rg & 1, own = (c >> 3) == hpl;
        u32x2 a = pk4(dp), b = pk4(dm);
        const u32 o1 = rg < 2 ? slab + (hpl * 2 + rg) * 1024 + l * 16 : (own && h0set != h) ? slab + 4096 + l * 16 : OOB;
        __builtin_amdgcn_raw_buffer_store_b128(u32x4{ns(a[0]), ns(a[1]), ns(b[0]), ns(b[1])}, srdS, o1, 0, 16);
        const float x0 = g0 ? df[2] : df[0], x1 = g0 ? df[3] : df[1];
        const bool first = rg < 2 ? !h : h0set;
        const u32 o2 = rg < 2 ? slab + 5120 + (hpl * 2 + rg) * 256 + l * 4 : own ? slab + 6144 + l * 4 : OOB;
        __builtin_amdgcn_raw_buffer_store_b32(ns(pk(first ? x0 : 0.f, first ? 0.f : x1)), srdS, o2, 0, 16);
        STAMP(6);
      }
      if constexpr (T == T_L1 || T == T_L1C) {
        const int dw = w * 16 + c;
        const u32 dof = (u32)((rb * 100 + (dw < 100 ? dw : 99)) * 4);
        const u32 go = (u32)((hq * 16 + 4 * g) * SLAB) + dof;
        if constexpr (T == T_L1) {
          asm volatile("buffer_load_dword %0, %4, %5, 0 offen sc1\n\tbuffer_load_dword %1, %4, %5, %6 offen sc1\n\t"
                       "buffer_load_dword %2, %4, %5, %7 offen sc1\n\tbuffer_load_dword %3, %4, %5, %8 offen sc1"
                       : "=&v"(g0_), "=&v"(g1_), "=&v"(g2_), "=&v"(g3_)
                       : "v"(go), "s"(srdS), "s"(SLAB), "s"(2 * SLAB), "s"(3 * SLAB)
                       : "memory");
        } else {
          asm volatile("s_waitcnt vmcnt(%4)" : "+v"(g0_), "+v"(g1_), "+v"(g2_), "+v"(g3_) : "n"(wait_h<BR>(T, T_L1)) : "memory");
          _Pragma("nounroll") for (int spin = 0; spin < (1 << 20); ++spin) {
      asm volatile("" ::: "memory");
            if (!__any(umax(umax3(g0_, g1_, g2_), g3_) == SENT)) break;
            g0_ = __builtin_amdgcn_raw_buffer_load_b32(srdS, go, 0, 16);
            g1_ = __builtin_amdgcn_raw_buffer_load_b32(srdS, go, SLAB, 16);
            g2_ = __builtin_amdgcn_raw_buffer_load_b32(srdS, go, 2 * SLAB, 16);
            g3_ = __builtin_amdgcn_raw_buffer_load_b32(srdS, go, 3 * SLAB, 16);
          }
          const u32 sel = c == 0 ? 0x00003f80u : c == 1 ? 0x3f800000u : 0u;
          f32x4 sum = mfma32(u32x4{sel, sel, sel, sel}, u32x4{g0_, g1_, g2_, g3_}, z4);
          __builtin_amdgcn_raw_buffer_store_b32(ns(pk(sum[0], sum[1])), srdS, g == 0 ? (u32)((128 + hq) * SLAB) + dof : OOB, 0, 16);
          STAMP(7);
        }
      }
    }
  });
  STAMP(8);
  refresh();
  if (g < 2) lds_w64(lb + QLX + ((2 * rg + hpl) * 2 + g) * 128 + c * 8, pk4(aL));
  bar_lds();
  const u32 ofo = (u32)((BR * 128 + rb * 8 + rg * 2) * 18 * 1024);
  const int hp = hq * 2 + hpl;
  auto store_o = [&](int sb, f32x4 o0, f32x4 o1, float l0, float l1, float r0, float r1) {
    u32x2 a = pk4(o0 * r0), b = pk4(o1 * r1);
    __builtin_amdgcn_raw_buffer_store_b128(u32x4{ns(a[0]), ns(a[1]), ns(b[0]), ns(b[1])}, srdF, ofo + (sb * 18 + hp) * 1024 + l * 16, 0, 16);
    float rr = g0 ? r1 : r0;
    u32 lv = ns(pk(l0 * rr, l1 * rr));
    auto sw = __builtin_amdgcn_permlane16_swap(lv, lv, false, false);
    u32 s0 = sw[0], s1 = sw[1];
    __builtin_amdgcn_raw_buffer_store_b64(u32x2{s0, s1}, srdF, g == 0 ? ofo + (sb * 18 + 16 + (hq >> 2)) * 1024 + (c + 16 * (hq & 3)) * 16 + 8 * hpl : OOB, 0, 16);
  };

  if (BR == 0) {
    HH h0, h1;
    u32x2 qa0 = pk4(a0[0]), qb0 = pk4(a1[0]), qa1 = pk4(a0[1]), qb1 = pk4(a1[1]);
    u32x2 ql0 = lds_r64(lb + QLX + ((2 * rg + hpl) * 2 + hpl) * 128 + c * 8), ql1 = lds_r64(lb + QLX + ((2 * rg + 1 - hpl) * 2 + hpl) * 128 + c * 8);
    const u32 kf = (u32)((128 + hq) * SLAB);
    const int l8 = g * 16 + (c & 7) + 8 * hpl;
    u32x4 AM0, AM1, AMLm;
    u32 AL0, AL1, ALL;
    auto load_kv = [&]() {
      AM0 = __builtin_amdgcn_raw_buffer_load_b128(srdS, kf + hpl * 2048 + l * 16, 0, 16);
      AM1 = __builtin_amdgcn_raw_buffer_load_b128(srdS, kf + hpl * 2048 + 1024 + l * 16, 0, 16);
      AMLm = __builtin_amdgcn_raw_buffer_load_b128(srdS, kf + 4096 + l8 * 16, 0, 16);
      AL0 = __builtin_amdgcn_raw_buffer_load_b32(srdS, kf + 5120 + hpl * 512 + l * 4, 0, 16);
      AL1 = __builtin_amdgcn_raw_buffer_load_b32(srdS, kf + 5120 + hpl * 512 + 256 + l * 4, 0, 16);
      ALL = __builtin_amdgcn_raw_buffer_load_b32(srdS, kf + 6144 + l8 * 4, 0, 16);
    };
    static_for<0, 7>([&](auto st) {
      if (st.value == L2ST) { SCHED_FENCE(); load_kv(); SCHED_FENCE(); }
      hh_stage<st.value>(h0, qa0, qb0, ql0, phf, sumf, lo, ninf);
      hh_stage<st.value>(h1, qa1, qb1, ql1, phf, sumf, lo, ninf);
    });
    STAMP(9);
    _Pragma("nounroll") for (int spin = 0; spin < (1 << 20); ++spin) {
      asm volatile("" ::: "memory");
      if (!__any(umax4(umax4(umax4(umax3(AL0, AL1, ALL), AM0), AM1), AMLm) == SENT)) break;
      load_kv();
    }
    STAMP(10);
    const bool h0set = (0x47 >> (c & 7)) & 1;
    u32x4 AML0, AML1;
#pragma unroll
    for (int e = 0; e < 4; ++e) { AML0[e] = h0set ? AMLm[e] : 0u; AML1[e] = h0set ? 0u : AMLm[e]; }
#pragma unroll
    for (int sb = 0; sb < 2; ++sb) {
      const HH& h = sb ? h1 : h0;
      u32x4 bl = {h.bl, 0u, 0u, 0u};
      f32x4 O0 = mfma32(u32x4{AL0, 0u, 0u, 0u}, bl, mfma32(AM0, h.b0, z4));
      f32x4 O1 = mfma32(u32x4{AL1, 0u, 0u, 0u}, bl, mfma32(AM1, h.b1, z4));
      f32x4 OL = mfma32(u32x4{ALL, 0u, 0u, 0u}, bl, mfma32(AML1, h.b1, mfma32(AML0, h.b0, z4)));
      float r0 = __builtin_amdgcn_rcpf(OL[2] + 1e-6f), r1 = __builtin_amdgcn_rcpf(OL[3] + 1e-6f);
      store_o(sb ^ hpl, O0, O1, OL[0], OL[1], r0, r1);
    }
  } else {
    const u32 kx = lb + LKX + ((rg * 2 + hpl) * 2) * 512 + l * 8;
    u32x2 pM0 = lds_r64(kx), pM1 = lds_r64(kx + 512);
    u32x2 kL[3];
#pragma unroll
    for (int i = 0; i < 3; ++i) kL[i] = lds_r64(lb + KLX + ((2 * rg + i) * 2 + hpl) * 128 + c * 8);
    const int par = c & 1;
    const bool ina = (4 * g + par) > c, inb = (4 * g + par + 2) > c;
    const float sc = 0.23570226039551584f * LOG2E;
    float la[2][2], lbv[2][2], mx[2][2];
    u32x2 qM[2][2] = {{pk4(a0[0]), pk4(a0[1])}, {pk4(a1[0]), pk4(a1[1])}};
    unswap(qM[0][0], qM[0][1]); unswap(qM[1][0], qM[1][1]);
#pragma unroll
    for (int sb = 0; sb < 2; ++sb) {
      u32x2 qlv = lds_r64(lb + QLX + ((2 * rg + sb) * 2 + hpl) * 128 + c * 8);
      u32x2 q0 = qM[0][sb], q1 = qM[1][sb];
      u32x4 b0 = {q0[0], q0[1], g == 0 ? qlv[0] : 0u, 0u};
      u32x4 b1 = {q1[0], q1[1], 0u, g == 0 ? qlv[1] : 0u};
      u32x2 kp0 = sb ? kM[0][0] : pM0, kp1 = sb ? kM[1][0] : pM1, kpl = kL[sb], kcl = kL[sb + 1];
      f32x4 sp0 = mfma32(u32x4{kp0[0], kp0[1], kpl[0], kpl[1]}, b0, z4);
      f32x4 sp1 = mfma32(u32x4{kp1[0], kp1[1], kpl[0], kpl[1]}, b1, z4);
      f32x4 sc0 = mfma32(u32x4{kM[0][sb][0], kM[0][sb][1], kcl[0], kcl[1]}, b0, z4);
      f32x4 sc1 = mfma32(u32x4{kM[1][sb][0], kM[1][sb][1], kcl[0], kcl[1]}, b1, z4);
      const bool dead = (sb == 0) && (rb == 0) && (rg == 0);
      float pa0 = par ? sp0[1] : sp0[0], pb0 = par ? sp0[3] : sp0[2], pa1 = par ? sp1[1] : sp1[0], pb1 = par ? sp1[3] : sp1[2];
      if (dead) { pa0 = pb0 = pa1 = pb1 = -1e30f; }
      la[0][sb] = (ina ? pa0 : (par ? sc0[1] : sc0[0])) * sc;
      lbv[0][sb] = (inb ? pb0 : (par ? sc0[3] : sc0[2])) * sc;
      la[1][sb] = (ina ? pa1 : (par ? sc1[1] : sc1[0])) * sc;
      lbv[1][sb] = (inb ? pb1 : (par ? sc1[3] : sc1[2])) * sc;
      mx[0][sb] = fmaxf(la[0][sb], lbv[0][sb]);
      mx[1][sb] = fmaxf(la[1][sb], lbv[1][sb]);
    }
    float tm = redmax4(mx[0][0], mx[0][1], mx[1][0], mx[1][1], ninf);
    bcast4(tm, mx[0][0], mx[0][1], mx[1][0], mx[1][1]);
    const u32 vb = lb + LVI + (rg * 32 + 4 * g + q) * VIS + hpl * 80;
#pragma unroll
    for (int sb = 0; sb < 2; ++sb) {
      const u32 v0 = vb + sb * 16 * VIS, v1 = v0 + 16 * VIS;
      u32x4 A0 = frag_tr(v0 + 8 * p, v1 + 8 * p), A1 = frag_tr(v0 + 32 + 8 * p, v1 + 32 + 8 * p);
      u32x4 AL = frag_tr(v0 + 64 + 8 * (p & 1), v1 + 64 + 8 * (p & 1));
      u32x4 P[2];
#pragma unroll
      for (int h = 0; h < 2; ++h) {
        float ea = __builtin_amdgcn_exp2f(la[h][sb] - mx[h][sb]), eb = __builtin_amdgcn_exp2f(lbv[h][sb] - mx[h][sb]);
        u32 wa_ = par ? pk(0.f, ea) : pk(ea, 0.f), wb_ = par ? pk(0.f, eb) : pk(eb, 0.f);
        P[h] = u32x4{ina ? wa_ : 0u, inb ? wb_ : 0u, ina ? 0u : wa_, inb ? 0u : wb_};
      }
      f32x4 O0 = mfma32(A0, P[0], z4), O1 = mfma32(A1, P[1], z4);
      f32x4 L0 = mfma32(AL, P[0], z4), L1 = mfma32(AL, P[1], z4);
      float r0 = __builtin_amdgcn_rcpf(L0[2]), r1 = __builtin_amdgcn_rcpf(L1[2]);
      store_o(sb, O0, O1, g0 ? L1[0] : L0[0], g0 ? L1[1] : L0[1], r0, r1);
    }
  }
  STAMP(11);
  refresh();
  wait_bar<4>();
  STAMP(12);
  const u32 wrow2 = lb + WB + (16 * g1 + 8 * g0 + 2 * q) * 144;
  const u32 oa = wrow2 + 8 * p, oa4 = wrow2 + 128 + 8 * (p & 1);
  u32x4 wf[18][5], of[18];
  auto load_wf = [&](auto TT) {
    constexpr int t = TT.value;
    if constexpr (t < 18) {
      const u32 cb = t < 2 ? 8 * WU : (u32)(((((t - 2) >> 1) + hq) & 7) * WU);
      const u32 a = oa + cb + (t & 1) * 32 * 144, a4 = oa4 + cb + (t & 1) * 32 * 144;
#pragma unroll
      for (int i = 0; i < 4; ++i) wf[t][i] = frag_tr(a + 32 * i, a + 144 + 32 * i);
      wf[t][4] = frag_tr(a4, a4 + 144);
    }
  };
  const u32 ofl = (u32)((BR * 128 + rb * 8 + w) * 18 * 1024 + l * 16);
  of[0] = __builtin_amdgcn_raw_buffer_load_b128(srdF, ofl, 16 * 1024, 16);
  of[1] = __builtin_amdgcn_raw_buffer_load_b128(srdF, ofl, 17 * 1024, 16);
  SCHED_FENCE();
  static_for<0, PRE>([&](auto t) { load_wf(t); });
  SCHED_FENCE();
  _Pragma("nounroll") for (int spin = 0; spin < (1 << 20); ++spin) {
      asm volatile("" ::: "memory");
    if (!__any(umax4(umax4(0u, of[0]), of[1]) == SENT)) break;
    of[0] = __builtin_amdgcn_raw_buffer_load_b128(srdF, ofl, 16 * 1024, 16);
    of[1] = __builtin_amdgcn_raw_buffer_load_b128(srdF, ofl, 17 * 1024, 16);
  }
  STAMP(13);
#pragma unroll
  for (int t = 2; t < 18; ++t) of[t] = __builtin_amdgcn_raw_buffer_load_b128(srdF, ofl, ((t - 2 + 2 * hq) & 15) * 1024, 16);
  SCHED_FENCE();
  f32x4 acc[5] = {z4, z4, z4, z4, z4};
  static_for<0, 18>([&](auto tt) {
    constexpr int t = tt.value;
    if constexpr (t >= 2) {
      _Pragma("nounroll") for (int spin = 0; spin < (1 << 20); ++spin) {
      asm volatile("" ::: "memory");
        if (!__any(umax4(0u, of[t]) == SENT)) break;
        of[t] = __builtin_amdgcn_raw_buffer_load_b128(srdF, ofl, ((t - 2 + 2 * hq) & 15) * 1024, 16);
      }
    }
#pragma unroll
    for (int i = 0; i < 5; ++i) acc[i] = mfma32(wf[t][i], of[t], acc[i]);
    load_wf(IC<t + PRE>{});
    SCHED_FENCE();
  });
  STAMP(14);
  char* op = out + (size_t)(rb * 128 + w * 16 + c) * 2304 + BR * 1152 + hq * 144;
#pragma unroll
  for (int i = 0; i < 2; ++i) {
    u32x2 a = pk4(acc[2 * i]), b = pk4(acc[2 * i + 1]);
    auto s0 = __builtin_amdgcn_permlane16_swap(a[0], b[0], false, false);
    auto s1 = __builtin_amdgcn_permlane16_swap(a[1], b[1], false, false);
    u32 s00 = s0[0], s01 = s0[1], s10 = s1[0], s11 = s1[1];
    *(u32x4*)(op + 64 * i + 32 * g0 + 16 * g1) = u32x4{s00, s10, s01, s11};
  }
  if (g < 2) *(u32x2*)(op + 128 + 8 * g) = pk4(acc[4]);
  STAMP(15);
  RSTAMP(31);
  STAMP_FLUSH();
}

__global__ __launch_bounds__(512, 2) void fused(const char* __restrict__ XA, const char* __restrict__ WA, const char* __restrict__ PHI,
                                                const char* __restrict__ WOA, const char* __restrict__ XB, const char* __restrict__ WB,
                                                const char* __restrict__ WOB, char* ws, char* out) {
  __shared__ __attribute__((aligned(16))) char smem[LDS_BYTES];
  const u32 lb = (u32)(uintptr_t)(LDS_AS char*)smem;
  const int b = blockIdx.x, x = b & 7, j = b >> 3, xx = x & 3;
  const int rb = xx * 4 + (j >> 3), hq = j & 7;
  if (x < 4) body<0>(XA, WA, PHI, WOA, ws, out, rb, hq, lb);
  else body<1>(XB, WB, PHI, WOB, ws, out, rb, hq, lb);
}
}

extern "C" void kernel_launch(void* const* d_in, const int* in_sizes, int n_in, void* d_out, int out_size, void* d_ws, size_t ws_size,
                              hipStream_t stream) {
  hk::fused<<<256, 512, 0, stream>>>((const char*)d_in[0], (const char*)d_in[1], (const char*)d_in[2], (const char*)d_in[3], (const char*)d_in[4],
                                     (const char*)d_in[5], (const char*)d_in[6], (char*)d_ws, (char*)d_out);
}
```

```cpp
#define TOUCH 0
#define T_PV 100
#define T_PQ 101
#define T_PO 22
#include <hip/hip_runtime.h>
#include <stdint.h>

namespace hk {
typedef unsigned int u32;
typedef unsigned long long u64;
typedef unsigned short u16;
typedef float f32x4 __attribute__((ext_vector_type(4)));
typedef float f32x2 __attribute__((ext_vector_type(2)));
typedef u32 u32x4 __attribute__((ext_vector_type(4)));
typedef u32 u32x2 __attribute__((ext_vector_type(2)));
typedef __bf16 bf16x8 __attribute__((ext_vector_type(8)));
typedef __bf16 bf16x2 __attribute__((ext_vector_type(2)));
typedef short s16x4 __attribute__((ext_vector_type(4)));
#define LDS_AS __attribute__((address_space(3)))
#define GLB_AS __attribute__((address_space(1)))
#define DEVI __device__ __forceinline__
#define SCHED_FENCE() __builtin_amdgcn_sched_barrier(0)

template <int V> struct IC { static constexpr int value = V; };
template <int I, int N, class F>
DEVI void static_for(F&& f) {
  if constexpr (I < N) { f(IC<I>{}); static_for<I + 1, N>(f); }
}

constexpr int XROW = 1152, WROW = 3456;
constexpr size_t WS_OF = 0;
constexpr int SLAB = 6400;
constexpr size_t WS_SLAB = 5u << 20;
constexpr size_t WS_FLAG = 6u << 20;
constexpr size_t WS_EPOCH = WS_FLAG + 65536;
constexpr size_t WS_OFLAG = WS_FLAG + 2 * 65536;
constexpr size_t WS_OEPOCH = WS_FLAG + 3 * 65536;
constexpr u32 OOB = 0x7fffffffu;
constexpr u32 SENT = 0xffffffffu;
constexpr int XR = 1024, XS = 16384;
constexpr int WR = XR + 4 * XS, WU = 9216;
constexpr int LM = WR + 8 * WU;
constexpr int LPHF = LM;
constexpr int LSUM = LM + 8192;
constexpr int LKVF = LM + 11264;
constexpr int LHALO = LM;
constexpr int KLX = LM + 18432;
constexpr int QLX = KLX + 2304;
constexpr int LVI = XR, VIS = 168;
constexpr int LPK = XR + 21504, PKS = 296;
constexpr int LKX = XR + 24576;
constexpr int WB = WR - 3 * WU;
constexpr int LDS_BYTES = 163840;
static_assert(QLX + 2048 <= LDS_BYTES, "lds");

DEVI u32x2 tr_read(u32 a) { return __builtin_bit_cast(u32x2, __builtin_amdgcn_ds_read_tr16_b64_v4i16((LDS_AS s16x4*)a)); }
DEVI u32x4 frag_tr(u32 a0, u32 a1) { u32x2 x = tr_read(a0), y = tr_read(a1); return u32x4{x[0], x[1], y[0], y[1]}; }
DEVI u32x4 lds_r128(u32 a) { return *(LDS_AS u32x4*)a; }
DEVI u32x2 lds_r64(u32 a) { return *(LDS_AS u32x2*)a; }
DEVI u32 lds_r32(u32 a) { return *(LDS_AS u32*)a; }
DEVI u32 lds_r16(u32 a) { return *(LDS_AS u16*)a; }
DEVI void lds_w128(u32 a, u32x4 v) { *(LDS_AS u32x4*)a = v; }
DEVI void lds_w64(u32 a, u32x2 v) { *(LDS_AS u32x2*)a = v; }
DEVI void lds_w32(u32 a, u32 v) { *(LDS_AS u32*)a = v; }
DEVI f32x4 mfma32(u32x4 a, u32x4 b, f32x4 c) {
  return __builtin_amdgcn_mfma_f32_16x16x32_bf16(__builtin_bit_cast(bf16x8, a), __builtin_bit_cast(bf16x8, b), c, 0, 0, 0);
}
DEVI u32 pk(float a, float b) { f32x2 v = {a, b}; return __builtin_bit_cast(u32, __builtin_convertvector(v, bf16x2)); }
DEVI u32x2 pk4(f32x4 v) { return u32x2{pk(v[0], v[1]), pk(v[2], v[3])}; }
DEVI float max3(float a, float b, float c) { return fmaxf(fmaxf(a, b), c); }
DEVI u32 umax(u32 a, u32 b) { return a > b ? a : b; }
DEVI u32 umax3(u32 a, u32 b, u32 c) { return umax(umax(a, b), c); }
DEVI u32 umax4(u32 m, u32x4 v) { return umax3(umax3(m, v[0], v[1]), v[2], v[3]); }
DEVI u32 ns(u32 v) { return v < 0xfffffffeu ? v : 0xfffffffeu; }

DEVI void dma_full(u32 lds, u32 voff, __amdgpu_buffer_rsrc_t srd, u32 soff) {
  asm volatile("s_mov_b32 m0, %0\n\ts_nop 0\n\tbuffer_load_dwordx4 %1, %2, %3 offen lds" ::"s"(lds), "v"(voff), "s"(srd), "s"(soff) : "memory");
}
DEVI void dma_mask(u32 lds, u32 voff, __amdgpu_buffer_rsrc_t srd, u32 soff, u64 mask) {
  asm volatile("s_mov_b32 m0, %0\n\ts_mov_b64 exec, %4\n\tbuffer_load_dwordx4 %1, %2, %3 offen lds\n\ts_mov_b64 exec, -1" ::"s"(lds), "v"(voff), "s"(srd),
               "s"(soff), "s"(mask)
               : "memory");
}
template <int N>
DEVI void wait_bar() { asm volatile("s_waitcnt vmcnt(%0) lgkmcnt(0)\n\ts_barrier" ::"n"(N) : "memory"); }
template <int N>
DEVI void wait_vm_bar() { asm volatile("s_waitcnt vmcnt(%0)\n\ts_barrier" ::"n"(N) : "memory"); }
DEVI void bar_lds() { asm volatile("s_waitcnt lgkmcnt(0)\n\ts_barrier" ::: "memory"); }
DEVI void bar_only() { asm volatile("s_barrier" ::: "memory"); }

DEVI float redmax4(float a, float b, float c, float d, float ninf) {
  auto r1 = __builtin_amdgcn_permlane32_swap(__float_as_uint(a), __float_as_uint(b), false, false);
  auto r2 = __builtin_amdgcn_permlane32_swap(__float_as_uint(c), __float_as_uint(d), false, false);
  u32 r10 = r1[0], r11 = r1[1], r20 = r2[0], r21 = r2[1];
  float ab = max3(__uint_as_float(r10), __uint_as_float(r11), ninf);
  float cd = max3(__uint_as_float(r20), __uint_as_float(r21), ninf);
  auto r3 = __builtin_amdgcn_permlane16_swap(__float_as_uint(ab), __float_as_uint(cd), false, false);
  u32 r30 = r3[0], r31 = r3[1];
  return max3(__uint_as_float(r30), __uint_as_float(r31), ninf);
}
DEVI void bcast4(float t, float& a, float& b, float& c, float& d) {
  u32 tu = __float_as_uint(t);
  auto s1 = __builtin_amdgcn_permlane16_swap(tu, tu, false, false);
  u32 x = s1[0], y = s1[1];
  auto s2 = __builtin_amdgcn_permlane32_swap(x, x, false, false);
  auto s3 = __builtin_amdgcn_permlane32_swap(y, y, false, false);
  u32 s20 = s2[0], s21 = s2[1], s30 = s3[0], s31 = s3[1];
  a = __uint_as_float(s20); b = __uint_as_float(s21); c = __uint_as_float(s30); d = __uint_as_float(s31);
}

constexpr float LOG2E = 1.4426950408889634f;
constexpr u32 ONES = 0x3f803f80u;

struct HH {
  u32x4 f0, f1, f2, f3;
  f32x4 T0, T1, TL, S;
  float m0, m1, n0, n1;
  f32x4 p0, p1, q0, q1;
  float l0, l1;
  u32x4 b0, b1; u32 bl;
};
template <int ST>
DEVI void hh_stage(HH& s, u32x2 ma, u32x2 mb, u32x2 left, u32 phf, u32 sumf, bool lo, float ninf) {
  const f32x4 z = {0.f, 0.f, 0.f, 0.f};
  if constexpr (ST == 0) {
    s.f0 = lds_r128(phf); s.f1 = lds_r128(phf + 1024); s.f2 = lds_r128(phf + 2048); s.f3 = lds_r128(phf + 3072);
  } else if constexpr (ST == 1) {
    u32x4 b0 = {ma[0], ma[1], left[0], left[1]}, b1 = {mb[0], mb[1], left[0], left[1]};
    s.T0 = mfma32(s.f0, b0, z);
    s.T1 = mfma32(s.f1, b1, z);
    s.TL = mfma32(s.f3, b1, mfma32(s.f2, b0, z));
  } else if constexpr (ST == 2) {
    float a = max3(max3(s.T0[0], s.T0[1], s.T0[2]), s.T0[3], s.TL[0]);
    float b = max3(max3(s.T1[0], s.T1[1], s.T1[2]), s.T1[3], s.TL[1]);
    float c = max3(max3(-s.T0[0], -s.T0[1], -s.T0[2]), -s.T0[3], -s.TL[0]);
    float d = max3(max3(-s.T1[0], -s.T1[1], -s.T1[2]), -s.T1[3], -s.TL[1]);
    float t = redmax4(a, b, c, d, ninf) * (-LOG2E);
    bcast4(t, s.m0, s.m1, s.n0, s.n1);
    s.f0 = lds_r128(sumf); s.f1 = lds_r128(sumf + 1024); s.f2 = lds_r128(sumf + 2048);
  } else if constexpr (ST == 3) {
#pragma unroll
    for (int r = 0; r < 4; ++r) {
      s.p0[r] = __builtin_amdgcn_exp2f(fmaf(s.T0[r], LOG2E, s.m0));
      s.q0[r] = __builtin_amdgcn_exp2f(fmaf(s.T0[r], -LOG2E, s.n0));
    }
    s.l0 = __builtin_amdgcn_exp2f(fmaf(s.TL[0], lo ? LOG2E : -LOG2E, lo ? s.m0 : s.n0));
  } else if constexpr (ST == 4) {
#pragma unroll
    for (int r = 0; r < 4; ++r) {
      s.p1[r] = __builtin_amdgcn_exp2f(fmaf(s.T1[r], LOG2E, s.m1));
      s.q1[r] = __builtin_amdgcn_exp2f(fmaf(s.T1[r], -LOG2E, s.n1));
    }
    s.l1 = __builtin_amdgcn_exp2f(fmaf(s.TL[1], lo ? LOG2E : -LOG2E, lo ? s.m1 : s.n1));
  } else if constexpr (ST == 5) {
    u32x2 a = pk4(s.p0), b = pk4(s.q0), c = pk4(s.p1), d = pk4(s.q1);
    u32x4 bl = {pk(s.l0, s.l1), 0u, 0u, 0u};
    s.S = mfma32(s.f2, bl, mfma32(s.f1, u32x4{c[0], c[1], d[0], d[1]}, mfma32(s.f0, u32x4{a[0], a[1], b[0], b[1]}, z)));
  } else if constexpr (ST == 6) {
    float r0 = __builtin_amdgcn_rcpf(s.S[0]), r1 = __builtin_amdgcn_rcpf(s.S[1]);
    float r2 = __builtin_amdgcn_rcpf(s.S[2]), r3 = __builtin_amdgcn_rcpf(s.S[3]);
    u32x2 a = pk4(s.p0 * r0), b = pk4(s.q0 * r1), c = pk4(s.p1 * r2), d = pk4(s.q1 * r3);
    s.b0 = u32x4{a[0], a[1], b[0], b[1]};
    s.b1 = u32x4{c[0], c[1], d[0], d[1]};
    s.bl = pk(s.l0 * (lo ? r0 : r1), s.l1 * (lo ? r2 : r3));
  }
}

#define L2ST 4
#define PRE 6
#define PRO 2
#define QK 3
#define QV 2
constexpr int T_CLR = 19, T_FLAG = 27, T_FLD = 33, T_FCHK = 37;
constexpr int T_SLAB = 42, T_L1 = 49, T_L1C = 53;
struct Sched {
  int op[54][3];
  int pos_step[55];
  int pos_hook[54];
  int last[36];
  int total;
};
constexpr int LSCR = QLX + 2048;
constexpr int ext_ops(int br, int T) {
  return (T == T_PV || T == T_PQ || T == T_PO) + (T == T_CLR ? (br ? 3 : 4) : T == T_FLD ? 1 : br ? 0 : T == T_SLAB ? 2 : T == T_L1 ? 4 : T == T_L1C ? 1 : 0);
}
constexpr int op_chunk(int op) { return op >= 200 ? (op - 200) >> 1 : (op - 100) >> 2; }
constexpr int op_earliest(int br, int op) {
  const int n = op_chunk(op);
  if (op < 200) return n < 4 ? 0 : 2 * (n - 4) + 1;
  if (n < 8) return 0;
  if (n < 27) return 2 * (n - 8) + 3;
  if (n < 30) return br ? 21 : T_SLAB + 1;
  return 2 * (n - 11) + 3;
}
constexpr Sched make_sched(int br) {
  Sched s{};
  const int nx = br ? 3 : 2;
  int list[128] = {}, n = 0;
  for (int c = 0; c < 36; ++c) {
    if (c < 9) for (int i = 0; i < nx; ++i) list[n++] = 100 + 4 * c + i;
    list[n++] = 200 + 2 * c; list[n++] = 200 + 2 * c + 1;
  }
  int pos = 0, k = 0;
  for (; k < PRO * (nx + 2); ++k) { s.last[op_chunk(list[k])] = pos++; if (k == nx + 1) pos += (br ? 1 : 3) + TOUCH; }
  for (int T = 0; T < 54; ++T) {
    s.pos_step[T] = pos;
    const int q = T < 18 ? QK : (T < 50 ? QV : 3);
    for (int j = 0; j < 3; ++j) {
      s.op[T][j] = 0;
      if (j < q && k < n && op_earliest(br, list[k]) <= T) { s.op[T][j] = list[k]; s.last[op_chunk(list[k])] = pos++; ++k; }
    }
    s.pos_hook[T] = pos;
    pos += ext_ops(br, T);
  }
  s.pos_step[54] = pos;
  s.total = k == n ? pos : -1;
  return s;
}
template <int BR> struct SC { static constexpr Sched s = make_sched(BR); };
static_assert(SC<0>::s.total > 0 && SC<1>::s.total > 0, "schedule incomplete");
constexpr int imin(int a, int b) { return a < b ? a : b; }
template <int BR> constexpr int wait_n(int T, int c) {
  int n = SC<BR>::s.pos_step[T] - SC<BR>::s.last[c] - 1;
  if (T == T_FLAG) n = imin(n, SC<BR>::s.pos_step[T] - SC<BR>::s.pos_step[T_CLR + 1]);
  return n;
}
template <int BR> constexpr int wait_h(int T, int T0) { return SC<BR>::s.pos_hook[T] - SC<BR>::s.pos_step[T0 + 1]; }

#define STAMP(i)
#define RSTAMP(i)
#define STAMP_FLUSH()

DEVI u64 next_tag(u32x2 e) {
  const u64 E = ((u64)(u32)__builtin_amdgcn_readfirstlane(e[1]) << 32) | (u32)__builtin_amdgcn_readfirstlane(e[0]);
  return E * 6364136223846793005ull + 1442695040888963407ull;
}
DEVI void poll_flags(GLB_AS u64* f, u64 tag) {
  for (int spin = 0; spin < (1 << 20); ++spin) {
    u64 v = __hip_atomic_load(f, __ATOMIC_RELAXED, __HIP_MEMORY_SCOPE_AGENT);
    if (__all(v == tag)) break;
    __builtin_amdgcn_s_sleep(1);
  }
  asm volatile("" ::: "memory");
}

template <int BR>
DEVI void body(const char* __restrict__ X, const char* __restrict__ W, const char* __restrict__ PHI, const char* __restrict__ WO, char* ws, char* out,
               int rb, int hq, int jx, u32 lb) {
  int tid = threadIdx.x, l = tid & 63;
  const int w = __builtin_amdgcn_readfirstlane(tid >> 6);
  const int rg = w & 3, hpl = w >> 2;
  int c = l & 15, g = l >> 4, q = c >> 2, p = c & 3, g0 = g & 1, g1 = g >> 1;
  int lr = (l * 57) >> 9, lu = l - lr * 9;
  auto refresh = [&]() {
    asm volatile("" : "+v"(tid));
    l = tid & 63; c = l & 15; g = l >> 4; q = c >> 2; p = c & 3; g0 = g & 1; g1 = g >> 1;
    lr = (l * 57) >> 9; lu = l - lr * 9;
  };
  STAMP(0);
  RSTAMP(30);
  __amdgpu_buffer_rsrc_t srdX = __builtin_amdgcn_make_buffer_rsrc((void*)X, (short)0, 2048 * XROW, 0x00020000);
  __amdgpu_buffer_rsrc_t srdW = __builtin_amdgcn_make_buffer_rsrc((void*)W, (short)0, 576 * WROW, 0x00020000);
#define ROT (rb + 2 * hq) % 9
  const int rot = ROT;
  constexpr int NX = BR ? 3 : 2;
  constexpr u64 M36 = (1ull << 36) - 1;

  u32 vx[2], vq[2], vh = 0;
#pragma unroll
  for (int i = 0; i < 2; ++i) {
    int row = 16 * w + 8 * i + (l >> 3);
    vx[i] = (u32)((rb * 128 + row) * XROW + (((l & 7) ^ ((row >> 1) & 7)) << 4));
  }
#pragma unroll
  for (int i = 0; i < 2; ++i) vq[i] = (u32)((8 * w + 4 * (lr & 1) + 2 * i + ((lr >> 1) & 1)) * WROW + hq * 144 + lu * 16);
  if (BR) {
    int row = 2 * w + ((l >> 3) & 1);
    int grow = rb * 128 - 16 + row; grow = grow < 0 ? 0 : grow;
    vh = (u32)(grow * XROW + (((l & 7) ^ ((row >> 1) & 7)) << 4));
  }
  auto kc_of = [&](int cc) { int k = cc + rot; return k >= 9 ? k - 9 : k; };
  auto dma_x = [&](int cc, int i) {
    const int kc = kc_of(cc);
    if (i < 2) dma_full(lb + XR + (cc & 3) * XS + w * 2048 + i * 1024, vx[i], srdX, (u32)(kc * 128));
    else dma_mask(lb + LHALO + cc * 2048 + w * 256, vh, srdX, (u32)(kc * 128), 0xffffull);
  };
  u32 vo[2] = {0, 0}, vo8[2] = {0, 0};
  __amdgpu_buffer_rsrc_t srdO = __builtin_amdgcn_make_buffer_rsrc((void*)WO, (short)0, 576 * XROW, 0x00020000);
  auto dma_w = [&](int n, int i) {
    if (n < 27) {
      const int kc = kc_of(n % 9), sec = n < 9 ? 1152 : (n < 18 ? 2304 : 0);
      dma_mask(lb + WR + ((n + 5) & 7) * WU + (2 * w + i) * 576, vq[i], srdW, (u32)(kc * 64 * WROW + sec), M36);
    } else if (n < 35) {
      dma_mask(lb + WB + (n - 27) * WU + (2 * w + i) * 576, vo[i], srdO, (u32)((n - 27) * 72 * XROW), M36);
    } else {
      dma_mask(lb + WB + 8 * WU + (2 * w + i) * 576, vo8[i], srdO, 0u, M36);
    }
  };
  u32x2 ep = {0, 0}, ep2;
  asm volatile("" ::"s"(srdX), "s"(srdW));
  STAMP(16);
  static_for<0, PRO>([&](auto cc) {
    dma_x(cc.value, 0); dma_x(cc.value, 1);
    if (BR) dma_x(cc.value, 2);
    dma_w(cc.value, 0); dma_w(cc.value, 1);
    if (cc.value == 0) {
      const char* ea = ws + WS_OEPOCH + (BR * 16 + rb) * 256;
      asm volatile("global_load_dwordx2 %0, %1, off sc1" : "=v"(ep2) : "v"(ea) : "memory");
      if (BR == 0) {
        const char* eb = ws + WS_EPOCH + hq * 256;
        asm volatile("global_load_dwordx2 %0, %1, off sc1" : "=v"(ep) : "v"(eb) : "memory");
        __amdgpu_buffer_rsrc_t srdP = __builtin_amdgcn_make_buffer_rsrc((void*)PHI, (short)0, 32 * 648, 0x00020000);
        dma_mask(lb + LKVF + w * 336, (u32)(hq * 2592 + (w * 21 + l) * 16), srdP, 0, (1ull << 21) - 1);
      }
    }
    if (cc.value == 0 && TOUCH) {
      const char* ta = WO + (l & 1) * (576 * XROW - 4);
      if (l & 2) ta = ws + ((l & 1) ? WS_SLAB + (hq * 16 + rb) * SLAB : WS_OF + (BR * 128 + rb * 8 + w) * 18 * 1024);
      if (l & 4) ta = (l & 2) ? W + (l & 1) * (576 * WROW - 4) : out + (size_t)(rb * 128 + (l & 1) * 127) * 2304 + BR * 1152;
      asm volatile("s_mov_b32 m0, %0\n\ts_mov_b64 exec, 0xff\n\tglobal_load_lds_dword %1, off\n\ts_mov_b64 exec, -1" ::"s"(lb + LSCR), "v"(ta) : "memory");
    }
    if (cc.value == 0) STAMP(17);
    if (cc.value < PRO - 1) bar_only();
    if (cc.value == 0) STAMP(18);
  });
  STAMP(1);

  const u32 xa0 = lb + XR + (rg * 32 + hpl * 16 + c) * 128 + ((g ^ ((c >> 1) & 7)) << 4);
  const u32 xa1 = xa0 ^ 64;
  const u32 xb0 = xa0 + 2048 - hpl * 4096, xb1 = xb0 ^ 64;
  const u32 xh0 = lb + LHALO + c * 128 + ((g ^ ((c >> 1) & 7)) << 4);
  const u32 xh1 = xh0 ^ 64;
  const u32 wrow = lb + WR + (16 * g1 + 8 * g0 + 2 * q) * 144;
  const u32 wa = wrow + hpl * 72 + 8 * p;
  const u32 wl = wrow + 32 + (p & 1) * 72;
  const u32 wh = rg < 2 ? wa + rg * 40 : wl;
  float ninf = -__builtin_inff();
  asm volatile("" : "+s"(ninf));

  u32x4 xf[18][2];
  const f32x4 z4 = {0.f, 0.f, 0.f, 0.f};
  f32x4 a0[2], a1[2], aL, aH;
  u32x4 f0, f1, fl, fh, xh;
  auto load_frags = [&](auto TT, auto JJ) {
    constexpr int T = TT.value, j = JJ.value, n = T >> 1, ks = T & 1;
    constexpr u32 o0 = ((n + 5) & 7) * WU + ks * 32 * 144, o1 = o0 + 144;
    if constexpr (T < 54) {
      if constexpr (j == 0) {
        if constexpr (T < 18) xf[T][0] = lds_r128((ks ? xa1 : xa0) + (n & 3) * XS);
        f0 = frag_tr(wa + o0, wa + o1);
      } else if constexpr (j == 1) {
        if constexpr (T < 18) xf[T][1] = lds_r128((ks ? xb1 : xb0) + (n & 3) * XS);
        f1 = frag_tr(wa + o0 + 40, wa + o1 + 40);
      } else if constexpr (j == 2) {
        fl = frag_tr(wl + o0, wl + o1);
      } else if constexpr (BR == 1 && T < 36) {
        fh = frag_tr(wh + o0, wh + o1);
        xh = lds_r128((ks ? xh1 : xh0) + (n % 9) * 2048);
      }
    }
  };
  auto step_dma = [&](auto TT, auto JJ) {
    constexpr int op = SC<BR>::s.op[TT.value][JJ.value];
    if constexpr (op >= 200) dma_w((op - 200) >> 1, op & 1);
    else if constexpr (op >= 100) dma_x((op - 100) >> 2, op & 3);
  };
  auto pass = [&](auto PP, auto&& hook) {
    constexpr int P = PP.value;
    a0[0] = z4; a0[1] = z4; a1[0] = z4; a1[1] = z4; aL = z4; aH = z4;
    static_for<0, 18>([&](auto tt) {
      constexpr int t = tt.value, T = P * 18 + t;
      if constexpr ((T & 1) && (T >> 1) + 1 <= 26) {
        if constexpr (T <= 9 || T == 19 || T == 37) wait_bar<wait_n<BR>(T, (T >> 1) + 1)>();
        else wait_vm_bar<wait_n<BR>(T, (T >> 1) + 1)>();
      }
      u32x4 c0 = f0, c1 = f1, cl = fl, ch = fh, cx = xh;
      SCHED_FENCE();
      a0[0] = mfma32(c0, xf[t][0], a0[0]);
      load_frags(IC<T + 1>{}, IC<0>{});
      SCHED_FENCE();
      a0[1] = mfma32(c0, xf[t][1], a0[1]);
      load_frags(IC<T + 1>{}, IC<1>{});
      SCHED_FENCE();
      a1[0] = mfma32(c1, xf[t][0], a1[0]);
      load_frags(IC<T + 1>{}, IC<2>{});
      step_dma(IC<T>{}, IC<0>{});
      SCHED_FENCE();
      a1[1] = mfma32(c1, xf[t][1], a1[1]);
      load_frags(IC<T + 1>{}, IC<3>{});
      step_dma(IC<T>{}, IC<1>{});
      SCHED_FENCE();
      aL = mfma32(cl, xf[t][0], aL);
      asm volatile("" : "+v"(aL));
      if constexpr (BR == 1 && P < 2) { aH = mfma32(ch, cx, aH); asm volatile("" : "+v"(aH)); }
      step_dma(IC<T>{}, IC<2>{});
      if constexpr (T == T_PV || T == T_PQ || T == T_PO) {
        const int idx = w * 21 + l, r9 = (idx * 57) >> 9;
        const bool ok = l < 21 && idx < 162;
        if constexpr (T == T_PO) {
          const u32 po = ok ? (u32)((162 * jx + idx) * 128) : OOB;
          asm volatile("s_mov_b32 m0, %0\n\ts_nop 0\n\tbuffer_load_dword %1, %2, 0 offen lds" ::"s"(lb + LSCR), "v"(po), "s"(srdO) : "memory");
        } else {
          const u32 po = ok ? (u32)((18 * jx + r9) * WROW + (idx - 9 * r9) * 128) : OOB;
          asm volatile("s_mov_b32 m0, %0\n\ts_nop 0\n\tbuffer_load_dword %1, %2, %3 offen lds" ::"s"(lb + LSCR), "v"(po), "s"(srdW), "s"(T == T_PV ? 2304 : 0) : "memory");
        }
      }
      hook(tt);
      SCHED_FENCE();
    });
  };

  wait_bar<wait_n<BR>(0, 0)>();
  STAMP(2);
  static_for<0, 4>([&](auto j) { load_frags(IC<0>{}, j); });
  u64 tag = 0, tag2 = 0;
  pass(IC<0>{}, [&](auto tt) {
    if constexpr (tt.value == 1) {
      asm volatile("" : "+v"(ep), "+v"(ep2));
      if (BR == 0) tag = next_tag(ep);
      tag2 = next_tag(ep2);
    }
    if constexpr (BR == 0 && tt.value == 4) {
      const int h = rg & 1, lf = rg >> 1;
      const int f = lf ? 16 + (q & 1) : c;
      const bool rowok = lf ? (p == h) : true;
      const u32 pb = lb + LKVF + (hpl * 2 + h) * 648 + f * 2;
      const int d0 = 2 * h + 4 * g;
      u32 e0 = lds_r16(pb + (d0 + 0) * 36), e1 = lds_r16(pb + (d0 + 1) * 36), e2 = lds_r16(pb + (d0 + 2) * 36), e3 = lds_r16(pb + (d0 + 3) * 36);
      const int dl = h ? 0 : 16;
      u32 l0 = lds_r16(pb + dl * 36), l1 = lds_r16(pb + (dl + 1) * 36);
      u32 lw = (g == 0) ? (l0 | (l1 << 16)) : 0u;
      u32x4 fr = {e0 | (e1 << 16), e2 | (e3 << 16), h ? 0u : lw, h ? lw : 0u};
      if (!rowok) fr = u32x4{0, 0, 0, 0};
      lds_w128(lb + LPHF + (hpl * 4 + rg) * 1024 + l * 16, fr);
      if (w < 3) {
        u32 a = (p == 2 * w) ? ONES : 0u, b = (p == 2 * w + 1) ? ONES : 0u;
        u32 x = ((p & 1) == g1) ? ((p >> 1) ? 0x3f800000u : 0x00003f80u) : 0u;
        lds_w128(lb + LSUM + w * 1024 + l * 16, w < 2 ? u32x4{a, a, b, b} : u32x4{x, 0u, 0u, 0u});
      }
    }
  });
  STAMP(3);
  refresh();
  u32x2 kM[2][2], kH = {0, 0};
#pragma unroll
  for (int sb = 0; sb < 2; ++sb) { kM[0][sb] = pk4(a0[sb]); kM[1][sb] = pk4(a1[sb]); }
  auto unswap = [&](u32x2& a, u32x2& b) {
    u32x2 x = a, y = b;
#pragma unroll
    for (int e = 0; e < 2; ++e) { a[e] = hpl ? y[e] : x[e]; b[e] = hpl ? x[e] : y[e]; }
  };
  if (BR) { unswap(kM[0][0], kM[0][1]); unswap(kM[1][0], kM[1][1]); }
  if (g < 2) lds_w64(lb + KLX + ((1 + 2 * rg + hpl) * 2 + g) * 128 + c * 8, pk4(aL));
  if (BR) {
    kH = pk4(aH);
    if (w == 2 && g < 2) lds_w64(lb + KLX + g * 128 + c * 8, kH);
  }
  const u32 phf = lb + LPHF + hpl * 4096 + l * 16, sumf = lb + LSUM + l * 16;
  const bool lo = g < 2;
  HH hs;
  u32x2 kLs;
  __amdgpu_buffer_rsrc_t srdF = __builtin_amdgcn_make_buffer_rsrc((void*)(ws + WS_OF), (short)0, 256 * 18 * 1024, 0x00020000);
  __amdgpu_buffer_rsrc_t srdS = __builtin_amdgcn_make_buffer_rsrc((void*)(ws + WS_SLAB), (short)0, 136 * SLAB, 0x00020000);
  GLB_AS u64* kflags = (GLB_AS u64*)(ws + WS_FLAG + hq * 256);
  GLB_AS u64* oflags = (GLB_AS u64*)(ws + WS_OFLAG + (BR * 16 + rb) * 256);
  u32x2 fv;
  pass(IC<1>{}, [&](auto tt) {
    constexpr int t = tt.value;
    if constexpr (t == T_CLR - 18) {
      const u32x4 sv = {SENT, SENT, SENT, SENT};
      const u32 ob = (u32)((BR * 128 + rb * 8 + w) * 18 * 1024);
      __builtin_amdgcn_raw_buffer_store_b128(sv, srdF, ob + hq * 2048 + l * 16, 0, 16);
      __builtin_amdgcn_raw_buffer_store_b128(sv, srdF, ob + hq * 2048 + 1024 + l * 16, 0, 16);
      __builtin_amdgcn_raw_buffer_store_b128(sv, srdF, g == 0 ? ob + (16 + (hq >> 2)) * 1024 + (hq & 3) * 256 + c * 16 : OOB, 0, 16);
      if (BR == 0) {
        const u32 so = tid < 400 ? (u32)((hq * 16 + rb) * SLAB + tid * 16) : tid < 425 ? (u32)((128 + hq) * SLAB + rb * 400 + (tid - 400) * 16) : OOB;
        __builtin_amdgcn_raw_buffer_store_b128(sv, srdS, so, 0, 16);
      }
    }
    if constexpr (t == T_FLAG - 18) {
      if (tid == 0) {
        __hip_atomic_store(oflags + hq, tag2, __ATOMIC_RELAXED, __HIP_MEMORY_SCOPE_AGENT);
        if (BR == 0) __hip_atomic_store(kflags + rb, tag, __ATOMIC_RELAXED, __HIP_MEMORY_SCOPE_AGENT);
      }
    }
    if constexpr (t == T_FLD - 18) {
      GLB_AS u64* fa = (BR == 0 && g == 0) ? kflags + c : oflags + (l & 7);
      asm volatile("global_load_dwordx2 %0, %1, off sc1" : "=v"(fv) : "v"(fa) : "memory");
    }
    if constexpr (BR == 0) {
      if constexpr (t >= 2) {
        constexpr int sb = (t - 2) >> 3, st = (t - 2) & 7;
        if constexpr (st == 0) kLs = lds_r64(lb + KLX + ((1 + 2 * rg + (sb ^ hpl)) * 2 + hpl) * 128 + c * 8);
        if constexpr (st < 7) hh_stage<st>(hs, kM[0][sb], kM[1][sb], kLs, phf, sumf, lo, ninf);
        if constexpr (st == 7) {
          const u32 pa = lb + LPK + (rg * 32 + (sb ^ hpl) * 16 + c) * PKS + hpl * 144;
          lds_w64(pa + 8 * g, u32x2{hs.b0[0], hs.b0[1]});
          lds_w64(pa + 32 + 8 * g, u32x2{hs.b0[2], hs.b0[3]});
          lds_w64(pa + 64 + 8 * g, u32x2{hs.b1[0], hs.b1[1]});
          lds_w64(pa + 96 + 8 * g, u32x2{hs.b1[2], hs.b1[3]});
          lds_w32(pa + 128 + 4 * g, hs.bl);
        }
      }
    } else if constexpr (t == 2) {
      const u32 kx = lb + LKX + l * 8;
      if (rg < 3) {
        const u32 b = kx + (((rg + 1) * 2 + hpl) * 2) * 512;
        lds_w64(b, kM[0][1]); lds_w64(b + 512, kM[1][1]);
      }
      if (rg < 2) lds_w64(kx + (hpl * 2 + rg) * 512, kH);
    }
  });
  STAMP(4);
  refresh();
  {
    constexpr int VROW0 = BR ? 16 : 0;
#pragma unroll
    for (int sb = 0; sb < 2; ++sb) {
      const u32 va = lb + LVI + (VROW0 + rg * 32 + (sb ^ hpl) * 16 + c) * VIS + hpl * 80;
      lds_w64(va + 8 * g, pk4(a0[sb]));
      lds_w64(va + 32 + 8 * g, pk4(a1[sb]));
    }
    u32x2 vl = pk4(aL);
    lds_w64(lb + LVI + (VROW0 + rg * 32 + hpl * 16 + c) * VIS + g0 * 80 + 64 + 8 * g1, u32x2{g1 ? vl[1] : vl[0], ONES});
    if (BR) {
      u32x2 hv = pk4(aH);
      if (rg < 2) lds_w64(lb + LVI + c * VIS + hpl * 80 + rg * 32 + 8 * g, hv);
      if (w == 2) lds_w64(lb + LVI + c * VIS + g0 * 80 + 64 + 8 * g1, u32x2{g1 ? hv[1] : hv[0], ONES});
    }
  }
  {
    const int hi = lr & 1;
#pragma unroll
    for (int i = 0; i < 2; ++i) {
      const int qq = 2 * i + ((lr >> 1) & 1);
      vo[i] = (u32)((36 * hpl + 20 * hi + 4 * rg + qq) * XROW + hq * 144 + lu * 16);
      vo8[i] = (u32)((36 * (8 * hpl + 2 * rg + hi) + 16 + qq) * XROW + hq * 144 + lu * 16);
    }
  }
  STAMP(5);
  f32x4 dp = z4, dm = z4, df = z4;
  u32x4 ap, am, af, bv;
  u32 g0_, g1_, g2_, g3_;
  const int rr = 4 * (4 * g0 + q) + 2 * g1;
  const u32 pa = lb + LPK + rr * PKS + hpl * 144 + (rg & 1) * 64 + 8 * p;
  const u32 pf = lb + LPK + rr * PKS + hpl * 144 + 128 + 8 * (p >> 1);
  const u32 va = lb + LVI + rr * VIS + hpl * 80 + (rg < 2 ? rg * 32 + 8 * p : 64 + 8 * (p & 1));
  pass(IC<2>{}, [&](auto tt) {
    constexpr int t = tt.value, T = t + 36;
    if constexpr (T == T_FCHK) {
      asm volatile("s_waitcnt vmcnt(%1)" : "+v"(fv) : "n"(wait_h<BR>(T, T_FLD)) : "memory");
      const u64 want = (BR == 0 && g == 0) ? tag : tag2;
      if (!__all((((u64)fv[1] << 32) | fv[0]) == want)) poll_flags((BR == 0 && g == 0) ? kflags + c : oflags + (l & 7), want);
      if (tid == 0) {
        if (BR == 0 && rb == 0) __hip_atomic_store((GLB_AS u64*)(ws + WS_EPOCH + hq * 256), tag, __ATOMIC_RELAXED, __HIP_MEMORY_SCOPE_AGENT);
        if (hq == 0) __hip_atomic_store((GLB_AS u64*)(ws + WS_OEPOCH + (BR * 16 + rb) * 256), tag2, __ATOMIC_RELAXED, __HIP_MEMORY_SCOPE_AGENT);
      }
    }
    if constexpr (BR == 0) {
      constexpr int kt = T - (T_SLAB - 5);
      if constexpr (kt >= 1 && kt <= 4) {
        dp = mfma32(ap, bv, dp); dm = mfma32(am, bv, dm); df = mfma32(af, bv, df);
        asm volatile("" : "+v"(dp), "+v"(dm), "+v"(df));
      }
      if constexpr (kt >= 0 && kt < 4) {
        ap = frag_tr(pa + kt * 32 * PKS, pa + kt * 32 * PKS + PKS);
        am = frag_tr(pa + 32 + kt * 32 * PKS, pa + 32 + kt * 32 * PKS + PKS);
        af = frag_tr(pf + kt * 32 * PKS, pf + kt * 32 * PKS + PKS);
        bv = frag_tr(va + kt * 32 * VIS, va + kt * 32 * VIS + VIS);
      }
      if constexpr (T == T_SLAB) {
        const u32 slab = (u32)((hq * 16 + rb) * SLAB);
        const bool h0set = (0x47 >> (c & 7)) & 1, h = rg & 1, own = (c >> 3) == hpl;
        u32x2 a = pk4(dp), b = pk4(dm);
        const u32 o1 = rg < 2 ? slab + (hpl * 2 + rg) * 1024 + l * 16 : (own && h0set != h) ? slab + 4096 + l * 16 : OOB;
        __builtin_amdgcn_raw_buffer_store_b128(u32x4{ns(a[0]), ns(a[1]), ns(b[0]), ns(b[1])}, srdS, o1, 0, 16);
        const float x0 = g0 ? df[2] : df[0], x1 = g0 ? df[3] : df[1];
        const bool first = rg < 2 ? !h : h0set;
        const u32 o2 = rg < 2 ? slab + 5120 + (hpl * 2 + rg) * 256 + l * 4 : own ? slab + 6144 + l * 4 : OOB;
        __builtin_amdgcn_raw_buffer_store_b32(ns(pk(first ? x0 : 0.f, first ? 0.f : x1)), srdS, o2, 0, 16);
        STAMP(6);
      }
      if constexpr (T == T_L1 || T == T_L1C) {
        const int dw = w * 16 + c;
        const u32 dof = (u32)((rb * 100 + (dw < 100 ? dw : 99)) * 4);
        const u32 go = (u32)((hq * 16 + 4 * g) * SLAB) + dof;
        if constexpr (T == T_L1) {
          asm volatile("buffer_load_dword %0, %4, %5, 0 offen sc1\n\tbuffer_load_dword %1, %4, %5, %6 offen sc1\n\t"
                       "buffer_load_dword %2, %4, %5, %7 offen sc1\n\tbuffer_load_dword %3, %4, %5, %8 offen sc1"
                       : "=&v"(g0_), "=&v"(g1_), "=&v"(g2_), "=&v"(g3_)
                       : "v"(go), "s"(srdS), "s"(SLAB), "s"(2 * SLAB), "s"(3 * SLAB)
                       : "memory");
        } else {
          asm volatile("s_waitcnt vmcnt(%4)" : "+v"(g0_), "+v"(g1_), "+v"(g2_), "+v"(g3_) : "n"(wait_h<BR>(T, T_L1)) : "memory");
          _Pragma("nounroll") for (int spin = 0; spin < (1 << 20); ++spin) {
      asm volatile("" ::: "memory");
            if (!__any(umax(umax3(g0_, g1_, g2_), g3_) == SENT)) break;
            g0_ = __builtin_amdgcn_raw_buffer_load_b32(srdS, go, 0, 16);
            g1_ = __builtin_amdgcn_raw_buffer_load_b32(srdS, go, SLAB, 16);
            g2_ = __builtin_amdgcn_raw_buffer_load_b32(srdS, go, 2 * SLAB, 16);
            g3_ = __builtin_amdgcn_raw_buffer_load_b32(srdS, go, 3 * SLAB, 16);
          }
          const u32 sel = c == 0 ? 0x00003f80u : c == 1 ? 0x3f800000u : 0u;
          f32x4 sum = mfma32(u32x4{sel, sel, sel, sel}, u32x4{g0_, g1_, g2_, g3_}, z4);
          __builtin_amdgcn_raw_buffer_store_b32(ns(pk(sum[0], sum[1])), srdS, g == 0 ? (u32)((128 + hq) * SLAB) + dof : OOB, 0, 16);
          STAMP(7);
        }
      }
    }
  });
  STAMP(8);
  refresh();
  if (g < 2) lds_w64(lb + QLX + ((2 * rg + hpl) * 2 + g) * 128 + c * 8, pk4(aL));
  bar_lds();
  const u32 ofo = (u32)((BR * 128 + rb * 8 + rg * 2) * 18 * 1024);
  const int hp = hq * 2 + hpl;
  auto store_o = [&](int sb, f32x4 o0, f32x4 o1, float l0, float l1, float r0, float r1) {
    u32x2 a = pk4(o0 * r0), b = pk4(o1 * r1);
    __builtin_amdgcn_raw_buffer_store_b128(u32x4{ns(a[0]), ns(a[1]), ns(b[0]), ns(b[1])}, srdF, ofo + (sb * 18 + hp) * 1024 + l * 16, 0, 16);
    float rr = g0 ? r1 : r0;
    u32 lv = ns(pk(l0 * rr, l1 * rr));
    auto sw = __builtin_amdgcn_permlane16_swap(lv, lv, false, false);
    u32 s0 = sw[0], s1 = sw[1];
    __builtin_amdgcn_raw_buffer_store_b64(u32x2{s0, s1}, srdF, g == 0 ? ofo + (sb * 18 + 16 + (hq >> 2)) * 1024 + (c + 16 * (hq & 3)) * 16 + 8 * hpl : OOB, 0, 16);
  };

  if (BR == 0) {
    HH h0, h1;
    u32x2 qa0 = pk4(a0[0]), qb0 = pk4(a1[0]), qa1 = pk4(a0[1]), qb1 = pk4(a1[1]);
    u32x2 ql0 = lds_r64(lb + QLX + ((2 * rg + hpl) * 2 + hpl) * 128 + c * 8), ql1 = lds_r64(lb + QLX + ((2 * rg + 1 - hpl) * 2 + hpl) * 128 + c * 8);
    const u32 kf = (u32)((128 + hq) * SLAB);
    const int l8 = g * 16 + (c & 7) + 8 * hpl;
    u32x4 AM0, AM1, AMLm;
    u32 AL0, AL1, ALL;
    auto load_kv = [&]() {
      AM0 = __builtin_amdgcn_raw_buffer_load_b128(srdS, kf + hpl * 2048 + l * 16, 0, 16);
      AM1 = __builtin_amdgcn_raw_buffer_load_b128(srdS, kf + hpl * 2048 + 1024 + l * 16, 0, 16);
      AMLm = __builtin_amdgcn_raw_buffer_load_b128(srdS, kf + 4096 + l8 * 16, 0, 16);
      AL0 = __builtin_amdgcn_raw_buffer_load_b32(srdS, kf + 5120 + hpl * 512 + l * 4, 0, 16);
      AL1 = __builtin_amdgcn_raw_buffer_load_b32(srdS, kf + 5120 + hpl * 512 + 256 + l * 4, 0, 16);
      ALL = __builtin_amdgcn_raw_buffer_load_b32(srdS, kf + 6144 + l8 * 4, 0, 16);
    };
    static_for<0, 7>([&](auto st) {
      if (st.value == L2ST) { SCHED_FENCE(); load_kv(); SCHED_FENCE(); }
      hh_stage<st.value>(h0, qa0, qb0, ql0, phf, sumf, lo, ninf);
      hh_stage<st.value>(h1, qa1, qb1, ql1, phf, sumf, lo, ninf);
    });
    STAMP(9);
    _Pragma("nounroll") for (int spin = 0; spin < (1 << 20); ++spin) {
      asm volatile("" ::: "memory");
      if (!__any(umax4(umax4(umax4(umax3(AL0, AL1, ALL), AM0), AM1), AMLm) == SENT)) break;
      load_kv();
    }
    STAMP(10);
    const bool h0set = (0x47 >> (c & 7)) & 1;
    u32x4 AML0, AML1;
#pragma unroll
    for (int e = 0; e < 4; ++e) { AML0[e] = h0set ? AMLm[e] : 0u; AML1[e] = h0set ? 0u : AMLm[e]; }
#pragma unroll
    for (int sb = 0; sb < 2; ++sb) {
      const HH& h = sb ? h1 : h0;
      u32x4 bl = {h.bl, 0u, 0u, 0u};
      f32x4 O0 = mfma32(u32x4{AL0, 0u, 0u, 0u}, bl, mfma32(AM0, h.b0, z4));
      f32x4 O1 = mfma32(u32x4{AL1, 0u, 0u, 0u}, bl, mfma32(AM1, h.b1, z4));
      f32x4 OL = mfma32(u32x4{ALL, 0u, 0u, 0u}, bl, mfma32(AML1, h.b1, mfma32(AML0, h.b0, z4)));
      float r0 = __builtin_amdgcn_rcpf(OL[2] + 1e-6f), r1 = __builtin_amdgcn_rcpf(OL[3] + 1e-6f);
      store_o(sb ^ hpl, O0, O1, OL[0], OL[1], r0, r1);
    }
  } else {
    const u32 kx = lb + LKX + ((rg * 2 + hpl) * 2) * 512 + l * 8;
    u32x2 pM0 = lds_r64(kx), pM1 = lds_r64(kx + 512);
    u32x2 kL[3];
#pragma unroll
    for (int i = 0; i < 3; ++i) kL[i] = lds_r64(lb + KLX + ((2 * rg + i) * 2 + hpl) * 128 + c * 8);
    const int par = c & 1;
    const bool ina = (4 * g + par) > c, inb = (4 * g + par + 2) > c;
    const float sc = 0.23570226039551584f * LOG2E;
    float la[2][2], lbv[2][2], mx[2][2];
    u32x2 qM[2][2] = {{pk4(a0[0]), pk4(a0[1])}, {pk4(a1[0]), pk4(a1[1])}};
    unswap(qM[0][0], qM[0][1]); unswap(qM[1][0], qM[1][1]);
#pragma unroll
    for (int sb = 0; sb < 2; ++sb) {
      u32x2 qlv = lds_r64(lb + QLX + ((2 * rg + sb) * 2 + hpl) * 128 + c * 8);
      u32x2 q0 = qM[0][sb], q1 = qM[1][sb];
      u32x4 b0 = {q0[0], q0[1], g == 0 ? qlv[0] : 0u, 0u};
      u32x4 b1 = {q1[0], q1[1], 0u, g == 0 ? qlv[1] : 0u};
      u32x2 kp0 = sb ? kM[0][0] : pM0, kp1 = sb ? kM[1][0] : pM1, kpl = kL[sb], kcl = kL[sb + 1];
      f32x4 sp0 = mfma32(u32x4{kp0[0], kp0[1], kpl[0], kpl[1]}, b0, z4);
      f32x4 sp1 = mfma32(u32x4{kp1[0], kp1[1], kpl[0], kpl[1]}, b1, z4);
      f32x4 sc0 = mfma32(u32x4{kM[0][sb][0], kM[0][sb][1], kcl[0], kcl[1]}, b0, z4);
      f32x4 sc1 = mfma32(u32x4{kM[1][sb][0], kM[1][sb][1], kcl[0], kcl[1]}, b1, z4);
      const bool dead = (sb == 0) && (rb == 0) && (rg == 0);
      float pa0 = par ? sp0[1] : sp0[0], pb0 = par ? sp0[3] : sp0[2], pa1 = par ? sp1[1] : sp1[0], pb1 = par ? sp1[3] : sp1[2];
      if (dead) { pa0 = pb0 = pa1 = pb1 = -1e30f; }
      la[0][sb] = (ina ? pa0 : (par ? sc0[1] : sc0[0])) * sc;
      lbv[0][sb] = (inb ? pb0 : (par ? sc0[3] : sc0[2])) * sc;
      la[1][sb] = (ina ? pa1 : (par ? sc1[1] : sc1[0])) * sc;
      lbv[1][sb] = (inb ? pb1 : (par ? sc1[3] : sc1[2])) * sc;
      mx[0][sb] = fmaxf(la[0][sb], lbv[0][sb]);
      mx[1][sb] = fmaxf(la[1][sb], lbv[1][sb]);
    }
    float tm = redmax4(mx[0][0], mx[0][1], mx[1][0], mx[1][1], ninf);
    bcast4(tm, mx[0][0], mx[0][1], mx[1][0], mx[1][1]);
    const u32 vb = lb + LVI + (rg * 32 + 4 * g + q) * VIS + hpl * 80;
#pragma unroll
    for (int sb = 0; sb < 2; ++sb) {
      const u32 v0 = vb + sb * 16 * VIS, v1 = v0 + 16 * VIS;
      u32x4 A0 = frag_tr(v0 + 8 * p, v1 + 8 * p), A1 = frag_tr(v0 + 32 + 8 * p, v1 + 32 + 8 * p);
      u32x4 AL = frag_tr(v0 + 64 + 8 * (p & 1), v1 + 64 + 8 * (p & 1));
      u32x4 P[2];
#pragma unroll
      for (int h = 0; h < 2; ++h) {
        float ea = __builtin_amdgcn_exp2f(la[h][sb] - mx[h][sb]), eb = __builtin_amdgcn_exp2f(lbv[h][sb] - mx[h][sb]);
        u32 wa_ = par ? pk(0.f, ea) : pk(ea, 0.f), wb_ = par ? pk(0.f, eb) : pk(eb, 0.f);
        P[h] = u32x4{ina ? wa_ : 0u, inb ? wb_ : 0u, ina ? 0u : wa_, inb ? 0u : wb_};
      }
      f32x4 O0 = mfma32(A0, P[0], z4), O1 = mfma32(A1, P[1], z4);
      f32x4 L0 = mfma32(AL, P[0], z4), L1 = mfma32(AL, P[1], z4);
      float r0 = __builtin_amdgcn_rcpf(L0[2]), r1 = __builtin_amdgcn_rcpf(L1[2]);
      store_o(sb, O0, O1, g0 ? L1[0] : L0[0], g0 ? L1[1] : L0[1], r0, r1);
    }
  }
  STAMP(11);
  refresh();
  wait_bar<4>();
  STAMP(12);
  const u32 wrow2 = lb + WB + (16 * g1 + 8 * g0 + 2 * q) * 144;
  const u32 oa = wrow2 + 8 * p, oa4 = wrow2 + 128 + 8 * (p & 1);
  u32x4 wf[18][5], of[18];
  auto load_wf = [&](auto TT) {
    constexpr int t = TT.value;
    if constexpr (t < 18) {
      const u32 cb = t < 2 ? 8 * WU : (u32)(((((t - 2) >> 1) + hq) & 7) * WU);
      const u32 a = oa + cb + (t & 1) * 32 * 144, a4 = oa4 + cb + (t & 1) * 32 * 144;
#pragma unroll
      for (int i = 0; i < 4; ++i) wf[t][i] = frag_tr(a + 32 * i, a + 144 + 32 * i);
      wf[t][4] = frag_tr(a4, a4 + 144);
    }
  };
  const u32 ofl = (u32)((BR * 128 + rb * 8 + w) * 18 * 1024 + l * 16);
  of[0] = __builtin_amdgcn_raw_buffer_load_b128(srdF, ofl, 16 * 1024, 16);
  of[1] = __builtin_amdgcn_raw_buffer_load_b128(srdF, ofl, 17 * 1024, 16);
  SCHED_FENCE();
  static_for<0, PRE>([&](auto t) { load_wf(t); });
  SCHED_FENCE();
  _Pragma("nounroll") for (int spin = 0; spin < (1 << 20); ++spin) {
      asm volatile("" ::: "memory");
    if (!__any(umax4(umax4(0u, of[0]), of[1]) == SENT)) break;
    of[0] = __builtin_amdgcn_raw_buffer_load_b128(srdF, ofl, 16 * 1024, 16);
    of[1] = __builtin_amdgcn_raw_buffer_load_b128(srdF, ofl, 17 * 1024, 16);
  }
  STAMP(13);
#pragma unroll
  for (int t = 2; t < 18; ++t) of[t] = __builtin_amdgcn_raw_buffer_load_b128(srdF, ofl, ((t - 2 + 2 * hq) & 15) * 1024, 16);
  SCHED_FENCE();
  f32x4 acc[5] = {z4, z4, z4, z4, z4};
  static_for<0, 18>([&](auto tt) {
    constexpr int t = tt.value;
    if constexpr (t >= 2) {
      _Pragma("nounroll") for (int spin = 0; spin < (1 << 20); ++spin) {
      asm volatile("" ::: "memory");
        if (!__any(umax4(0u, of[t]) == SENT)) break;
        of[t] = __builtin_amdgcn_raw_buffer_load_b128(srdF, ofl, ((t - 2 + 2 * hq) & 15) * 1024, 16);
      }
    }
#pragma unroll
    for (int i = 0; i < 5; ++i) acc[i] = mfma32(wf[t][i], of[t], acc[i]);
    load_wf(IC<t + PRE>{});
    SCHED_FENCE();
  });
  STAMP(14);
  char* op = out + (size_t)(rb * 128 + w * 16 + c) * 2304 + BR * 1152 + hq * 144;
#pragma unroll
  for (int i = 0; i < 2; ++i) {
    u32x2 a = pk4(acc[2 * i]), b = pk4(acc[2 * i + 1]);
    auto s0 = __builtin_amdgcn_permlane16_swap(a[0], b[0], false, false);
    auto s1 = __builtin_amdgcn_permlane16_swap(a[1], b[1], false, false);
    u32 s00 = s0[0], s01 = s0[1], s10 = s1[0], s11 = s1[1];
    *(u32x4*)(op + 64 * i + 32 * g0 + 16 * g1) = u32x4{s00, s10, s01, s11};
  }
  if (g < 2) *(u32x2*)(op + 128 + 8 * g) = pk4(acc[4]);
  STAMP(15);
  RSTAMP(31);
  STAMP_FLUSH();
}

__global__ __launch_bounds__(512, 2) void fused(const char* __restrict__ XA, const char* __restrict__ WA, const char* __restrict__ PHI,
                                                const char* __restrict__ WOA, const char* __restrict__ XB, const char* __restrict__ WB,
                                                const char* __restrict__ WOB, char* ws, char* out) {
  __shared__ __attribute__((aligned(16))) char smem[LDS_BYTES];
  const u32 lb = (u32)(uintptr_t)(LDS_AS char*)smem;
  const int b = blockIdx.x, x = b & 7, j = b >> 3, xx = x & 3;
  const int rb = xx * 4 + (j >> 3), hq = j & 7;
  if (x < 4) body<0>(XA, WA, PHI, WOA, ws, out, rb, hq, j, lb);
  else body<1>(XB, WB, PHI, WOB, ws, out, rb, hq, j, lb);
}
}

extern "C" void kernel_launch(void* const* d_in, const int* in_sizes, int n_in, void* d_out, int out_size, void* d_ws, size_t ws_size,
                              hipStream_t stream) {
  hk::fused<<<256, 512, 0, stream>>>((const char*)d_in[0], (const char*)d_in[1], (const char*)d_in[2], (const char*)d_in[3], (const char*)d_in[4],
                                     (const char*)d_in[5], (const char*)d_in[6], (char*)d_ws, (char*)d_out);
}
```

```cpp
#include <hip/hip_runtime.h>
#include <stdint.h>

namespace hk {
typedef unsigned int u32;
typedef unsigned long long u64;
typedef unsigned short u16;
typedef float f32x4 __attribute__((ext_vector_type(4)));
typedef float f32x2 __attribute__((ext_vector_type(2)));
typedef u32 u32x4 __attribute__((ext_vector_type(4)));
typedef u32 u32x2 __attribute__((ext_vector_type(2)));
typedef __bf16 bf16x8 __attribute__((ext_vector_type(8)));
typedef __bf16 bf16x2 __attribute__((ext_vector_type(2)));
typedef short s16x4 __attribute__((ext_vector_type(4)));
#define LDS_AS __attribute__((address_space(3)))
#define GLB_AS __attribute__((address_space(1)))
#define DEVI __device__ __forceinline__
#define SCHED_FENCE() __builtin_amdgcn_sched_barrier(0)

template <int V> struct IC { static constexpr int value = V; };
template <int I, int N, class F>
DEVI void static_for(F&& f) {
  if constexpr (I < N) { f(IC<I>{}); static_for<I + 1, N>(f); }
}

constexpr int XROW = 1152, WROW = 3456;
constexpr size_t WS_OF = 0;
constexpr int SLAB = 6400;
constexpr size_t WS_SLAB = 5u << 20;
constexpr size_t WS_FLAG = 6u << 20;
constexpr size_t WS_EPOCH = WS_FLAG + 65536;
constexpr size_t WS_OFLAG = WS_FLAG + 2 * 65536;
constexpr size_t WS_OEPOCH = WS_FLAG + 3 * 65536;
constexpr u32 OOB = 0x7fffffffu;
constexpr u32 SENT = 0xffffffffu;
constexpr int WU = 9216;
constexpr int LPK = 1024, PKS = 296;
constexpr int S0 = 3968, SS = 34944;
constexpr int SK = 16384, SV = SK + WU + 16;
constexpr int WQ = S0 + SS;
constexpr int LT = WQ + 9 * WU;
constexpr int LM = S0 + 4 * SS;
constexpr int LPHF = LM;
constexpr int LSUM = LPHF + 8192;
constexpr int LHALO = LM;
constexpr int KLX = LM + 11264;
constexpr int QLX = KLX + 2304;
constexpr int LSCR = QLX + 2048;
constexpr int VIS = 168;
constexpr int lvi(int br) { return br ? 1024 : LT; }
constexpr int LKX = 25600;
constexpr int wo_at(int br, int m) {
  if (br == 0) return m < 4 ? 1024 + m * WU : m < 6 ? LT + (m - 4) * WU : WQ + (m - 6) * WU;
  return m < 2 ? LT + m * WU : m == 2 ? LM : WQ + (m - 3) * WU;
}
constexpr int LDS_BYTES = 163840;
static_assert(LSCR + 512 <= LDS_BYTES && LPK + 2688 <= S0 && LPK + 128 * PKS <= WQ && LT + 128 * VIS <= LM && 1024 + 144 * VIS <= LKX && LKX + 8192 <= WQ &&
              SV + WU <= SS && 3 * WU <= SS && 7 * WU <= 2 * SS && S0 % 128 == 0 && SS % 128 == 0, "lds");

DEVI u32x2 tr_read(u32 a) { return __builtin_bit_cast(u32x2, __builtin_amdgcn_ds_read_tr16_b64_v4i16((LDS_AS s16x4*)a)); }
DEVI u32x4 frag_tr(u32 a0, u32 a1) { u32x2 x = tr_read(a0), y = tr_read(a1); return u32x4{x[0], x[1], y[0], y[1]}; }
DEVI u32x4 lds_r128(u32 a) { return *(LDS_AS u32x4*)a; }
DEVI u32x2 lds_r64(u32 a) { return *(LDS_AS u32x2*)a; }
DEVI u32 lds_r32(u32 a) { return *(LDS_AS u32*)a; }
DEVI u32 lds_r16(u32 a) { return *(LDS_AS u16*)a; }
DEVI void lds_w128(u32 a, u32x4 v) { *(LDS_AS u32x4*)a = v; }
DEVI void lds_w64(u32 a, u32x2 v) { *(LDS_AS u32x2*)a = v; }
DEVI void lds_w32(u32 a, u32 v) { *(LDS_AS u32*)a = v; }
DEVI f32x4 mfma32(u32x4 a, u32x4 b, f32x4 c) {
  return __builtin_amdgcn_mfma_f32_16x16x32_bf16(__builtin_bit_cast(bf16x8, a), __builtin_bit_cast(bf16x8, b), c, 0, 0, 0);
}
DEVI u32 pk(float a, float b) { f32x2 v = {a, b}; return __builtin_bit_cast(u32, __builtin_convertvector(v, bf16x2)); }
DEVI u32x2 pk4(f32x4 v) { return u32x2{pk(v[0], v[1]), pk(v[2], v[3])}; }
DEVI float max3(float a, float b, float c) { return fmaxf(fmaxf(a, b), c); }
DEVI u32 umax(u32 a, u32 b) { return a > b ? a : b; }
DEVI u32 umax3(u32 a, u32 b, u32 c) { return umax(umax(a, b), c); }
DEVI u32 umax4(u32 m, u32x4 v) { return umax3(umax3(m, v[0], v[1]), v[2], v[3]); }
DEVI u32 ns(u32 v) { return v < 0xfffffffeu ? v : 0xfffffffeu; }

DEVI void dma_full(u32 lds, u32 voff, __amdgpu_buffer_rsrc_t srd, u32 soff) {
  asm volatile("s_mov_b32 m0, %0\n\ts_nop 0\n\tbuffer_load_dwordx4 %1, %2, %3 offen lds" ::"s"(lds), "v"(voff), "s"(srd), "s"(soff) : "memory");
}
DEVI void dma_mask(u32 lds, u32 voff, __amdgpu_buffer_rsrc_t srd, u32 soff, u64 mask) {
  asm volatile("s_mov_b32 m0, %0\n\ts_mov_b64 exec, %4\n\tbuffer_load_dwordx4 %1, %2, %3 offen lds\n\ts_mov_b64 exec, -1" ::"s"(lds), "v"(voff), "s"(srd),
               "s"(soff), "s"(mask)
               : "memory");
}
template <int N>
DEVI void wait_bar() { asm volatile("s_waitcnt vmcnt(%0) lgkmcnt(0)\n\ts_barrier" ::"n"(N) : "memory"); }
template <int N>
DEVI void wait_vm_bar() { asm volatile("s_waitcnt vmcnt(%0)\n\ts_barrier" ::"n"(N) : "memory"); }
DEVI void bar_lds() { asm volatile("s_waitcnt lgkmcnt(0)\n\ts_barrier" ::: "memory"); }
DEVI void bar_only() { asm volatile("s_barrier" ::: "memory"); }

DEVI float redmax4(float a, float b, float c, float d, float ninf) {
  auto r1 = __builtin_amdgcn_permlane32_swap(__float_as_uint(a), __float_as_uint(b), false, false);
  auto r2 = __builtin_amdgcn_permlane32_swap(__float_as_uint(c), __float_as_uint(d), false, false);
  u32 r10 = r1[0], r11 = r1[1], r20 = r2[0], r21 = r2[1];
  float ab = max3(__uint_as_float(r10), __uint_as_float(r11), ninf);
  float cd = max3(__uint_as_float(r20), __uint_as_float(r21), ninf);
  auto r3 = __builtin_amdgcn_permlane16_swap(__float_as_uint(ab), __float_as_uint(cd), false, false);
  u32 r30 = r3[0], r31 = r3[1];
  return max3(__uint_as_float(r30), __uint_as_float(r31), ninf);
}
DEVI void bcast4(float t, float& a, float& b, float& c, float& d) {
  u32 tu = __float_as_uint(t);
  auto s1 = __builtin_amdgcn_permlane16_swap(tu, tu, false, false);
  u32 x = s1[0], y = s1[1];
  auto s2 = __builtin_amdgcn_permlane32_swap(x, x, false, false);
  auto s3 = __builtin_amdgcn_permlane32_swap(y, y, false, false);
  u32 s20 = s2[0], s21 = s2[1], s30 = s3[0], s31 = s3[1];
  a = __uint_as_float(s20); b = __uint_as_float(s21); c = __uint_as_float(s30); d = __uint_as_float(s31);
}

constexpr float LOG2E = 1.4426950408889634f;
constexpr u32 ONES = 0x3f803f80u;

struct HH {
  u32x4 f0, f1, f2, f3;
  u32x4 b0, b1;
  u32 bl;
  f32x4 T0, T1, TL, S;
  float a, b, c, d;
  f32x4 p0, q0, p1, q1;
  float l0, l1;
};
constexpr int HH_N = 106;
DEVI void swap32(float& a, float& b) {
  auto r = __builtin_amdgcn_permlane32_swap(__float_as_uint(a), __float_as_uint(b), false, false);
  u32 x = r[0], y = r[1]; a = __uint_as_float(x); b = __uint_as_float(y);
}
DEVI void swap16(float& a, float& b) {
  auto r = __builtin_amdgcn_permlane16_swap(__float_as_uint(a), __float_as_uint(b), false, false);
  u32 x = r[0], y = r[1]; a = __uint_as_float(x); b = __uint_as_float(y);
}
template <int I, bool QS>
DEVI void hh_step(HH& s, u32 lft, u32 phf, u32 sumf, bool lo, float ninf) {
  const f32x4 z = {0.f, 0.f, 0.f, 0.f};
  if constexpr (I == 0) {
    u32x2 x = lds_r64(lft); s.b0[2] = x[0]; s.b0[3] = x[1];
    asm volatile("" ::: "memory");
    u32x2 y = lds_r64(lft); s.b1[2] = y[0]; s.b1[3] = y[1];
    s.f0 = lds_r128(phf);
  } else if constexpr (I == 1) { s.f1 = lds_r128(phf + 1024); s.f2 = lds_r128(phf + 2048);
  } else if constexpr (I == 2) { s.f3 = lds_r128(phf + 3072);
  } else if constexpr (I == 3) { s.T0 = mfma32(s.f0, s.b0, z);
  } else if constexpr (I == 4) { s.TL = mfma32(s.f2, s.b0, z);
  } else if constexpr (I == 5) { s.T1 = mfma32(s.f1, s.b1, z);
  } else if constexpr (I == 6) { s.TL = mfma32(s.f3, s.b1, s.TL);
  } else if constexpr (I == 7) { s.f0 = lds_r128(sumf); s.f1 = lds_r128(sumf + 1024); s.f2 = lds_r128(sumf + 2048);
  } else if constexpr (I == 8) { s.a = max3(s.T0[0], s.T0[1], s.T0[2]);
  } else if constexpr (I == 9) { s.c = max3(-s.T0[0], -s.T0[1], -s.T0[2]);
  } else if constexpr (I == 10) { s.b = max3(s.T1[0], s.T1[1], s.T1[2]);
  } else if constexpr (I == 11) { s.d = max3(-s.T1[0], -s.T1[1], -s.T1[2]);
  } else if constexpr (I == 12) {
  } else if constexpr (I == 13) { s.a = max3(s.a, s.T0[3], s.TL[0]);
  } else if constexpr (I == 14) { s.b = max3(s.b, s.T1[3], s.TL[1]);
  } else if constexpr (I == 15) { s.c = max3(s.c, -s.T0[3], -s.TL[0]);
  } else if constexpr (I == 16) { s.d = max3(s.d, -s.T1[3], -s.TL[1]);
  } else if constexpr (I == 17) { swap32(s.a, s.b);
  } else if constexpr (I == 18) { swap32(s.c, s.d);
  } else if constexpr (I == 19) { s.a = max3(s.a, s.b, ninf);
  } else if constexpr (I == 20) { s.c = max3(s.c, s.d, ninf);
  } else if constexpr (I == 21) { swap16(s.a, s.c);
  } else if constexpr (I == 22) { s.a = max3(s.a, s.c, ninf);
  } else if constexpr (I == 23) { s.a *= -LOG2E;
  } else if constexpr (I == 24) { s.c = s.a; swap16(s.a, s.c);
  } else if constexpr (I == 25) { s.b = s.a; swap32(s.a, s.b);
  } else if constexpr (I == 26) { s.d = s.c; swap32(s.c, s.d);
  } else if constexpr (I == 27) { s.l0 = lo ? s.a : s.c;
  } else if constexpr (I == 28) { s.l1 = lo ? s.b : s.d;
  } else if constexpr (I == 29) { s.l0 = fmaf(s.TL[0], lo ? LOG2E : -LOG2E, s.l0);
  } else if constexpr (I == 30) { s.l1 = fmaf(s.TL[1], lo ? LOG2E : -LOG2E, s.l1);
  } else if constexpr (I == 31) { s.l0 = __builtin_amdgcn_exp2f(s.l0);
  } else if constexpr (I == 32) { s.l1 = __builtin_amdgcn_exp2f(s.l1);
  } else if constexpr (I == 33) { s.bl = pk(s.l0, s.l1);
  } else if constexpr (I == 34) { s.S = mfma32(s.f2, u32x4{s.bl, 0u, 0u, 0u}, z);
  } else if constexpr (I < 77) {
    constexpr int h = (I - 35) / 21, k = (I - 35) % 21;
    f32x4& T = h ? s.T1 : s.T0; f32x4& p = h ? s.p1 : s.p0; f32x4& q = h ? s.q1 : s.q0; u32x4& o = h ? s.b1 : s.b0;
    const float m = h ? s.b : s.a, n = h ? s.d : s.c;
    constexpr bool arg = k < 2 || ((k & 1) && k < 15);
    constexpr int e = arg ? (k < 2 ? k : (k + 1) / 2) : (k < 15 ? (k - 2) / 2 : 7);
    if constexpr (k < 16) {
      if constexpr (arg) { if constexpr (e < 4) p[e] = fmaf(T[e], LOG2E, m); else q[e - 4] = fmaf(T[e - 4], -LOG2E, n); }
      else { if constexpr (e < 4) p[e] = __builtin_amdgcn_exp2f(p[e]); else q[e - 4] = __builtin_amdgcn_exp2f(q[e - 4]); }
    } else if constexpr (k < 18) { o[k - 16] = pk(p[2 * (k - 16)], p[2 * (k - 16) + 1]);
    } else if constexpr (k < 20) { o[k - 16] = pk(q[2 * (k - 18)], q[2 * (k - 18) + 1]);
    } else { s.S = mfma32(h ? s.f1 : s.f0, o, s.S); }
  } else if constexpr (I == 77) {
  } else if constexpr (I == 78) { s.c = __builtin_amdgcn_rcpf(s.S[1]);
  } else if constexpr (I == 79) { s.d = __builtin_amdgcn_rcpf(s.S[3]);
  } else if constexpr (I == 80) { if constexpr (QS) s.c *= s.S[0]; else s.a = __builtin_amdgcn_rcpf(s.S[0]);
  } else if constexpr (I == 81) { if constexpr (QS) s.d *= s.S[2]; else s.b = __builtin_amdgcn_rcpf(s.S[2]);
  } else if constexpr (I < 106) {
    constexpr int v = (I - 82) / 6, k = (I - 82) % 6;
    if constexpr (!(QS && v >= 2)) {
      f32x4& x = v == 0 ? s.q0 : v == 1 ? s.q1 : v == 2 ? s.p0 : s.p1;
      u32x4& o = (v & 1) ? s.b1 : s.b0;
      const float r = v == 0 ? s.c : v == 1 ? s.d : v == 2 ? s.a : s.b;
      if constexpr (k < 4) x[k] *= r;
      else o[(v < 2 ? 2 : 0) + k - 4] = pk(x[2 * (k - 4)], x[2 * (k - 4) + 1]);
    }
  }
}
template <bool QS>
DEVI void hh_left(HH& s, bool lo) {
  if constexpr (QS) s.bl = pk(s.l0 * (lo ? 1.f : s.c), s.l1 * (lo ? 1.f : s.d));
  else s.bl = pk(s.l0 * (lo ? s.a : s.c), s.l1 * (lo ? s.b : s.d));
}

#define L2ST 56
#define PRE 6
#define PRO 2
#define LEAD 8
#define QA 2
#define QT 1
#define E_PO 36
#define E_POB 30
#define Q_L1 11
#define Q_L1C 17
constexpr int EM = 36, EQ = 44, NEV = 62;
constexpr int Q_SLAB = 2;
constexpr int e_clr(int br) { return br ? EQ : EM + 1; }
constexpr int q_flag(int br) { return br ? 9 : 0; }
constexpr int q_fld(int br) { return br ? 12 : 5; }
constexpr int q_fchk(int br) { return br ? 16 : 9; }
struct Sched {
  int op[NEV][3];
  int nop[NEV];
  int pos_ev[NEV + 1];
  int pos_hook[NEV];
  int last[36];
  int total;
};
constexpr int ext_ops(int br, int E) {
  return (E == (br ? E_POB : E_PO)) + (E >= e_clr(br) && E < e_clr(br) + (br ? 3 : 4)) + (E == EQ + q_fld(br)) +
         (br ? 0 : E == EQ + Q_SLAB ? 2 : E == EQ + Q_L1 ? 4 : E == EQ + Q_L1C ? 1 : 0);
}
constexpr int op_chunk(int op) { return op < 200 ? (op - 100) >> 2 : op < 236 ? ((op - 200) >> 1) % 9 : (op - 200) >> 1; }
constexpr int op_earliest(int br, int op) {
  const int n = op_chunk(op);
  if (n < 9) return 4 * n - 1 - LEAD;
  if (n < 27) return n < 21 ? 23 : n < 25 ? 27 : 31;
  const int m = n - 27;
  if (br == 0) return m < 6 ? EQ + 3 : EQ + 6;
  return m < 2 ? 31 : m == 2 ? EQ : EQ + 2 * (m - 3) + 3;
}
constexpr int op_deadline(int br, int op) {
  const int n = op_chunk(op);
  if (n < 9) return 4 * n - 1;
  if (n < 27) return br == 0 || n == 18 ? EQ : EQ + 2 * (n - 18) - 1;
  return NEV;
}
static_assert(LEAD <= 12, "ring");
constexpr Sched make_sched(int br) {
  Sched s{};
  const int nx = br ? 3 : 2;
  int list[128] = {}, when[128] = {}, n = 0;
  for (int c = 0; c < 9; ++c) {
    for (int i = 0; i < nx; ++i) list[n++] = 100 + 4 * c + i;
    list[n++] = 200 + 2 * c; list[n++] = 201 + 2 * c; list[n++] = 218 + 2 * c; list[n++] = 219 + 2 * c;
  }
  for (int c = 18; c < 36; ++c) { list[n++] = 200 + 2 * c; list[n++] = 201 + 2 * c; }
  for (int k = 0; k < n; ++k) when[k] = 1000;
  int pos = 0, done = 0;
  for (; done < PRO * (nx + 4); ++done) { s.last[op_chunk(list[done])] = pos++; when[done] = -1; if (done == nx + 3) pos += br ? 1 : 3; }
  for (int E = 0; E < NEV; ++E) {
    s.pos_ev[E] = pos;
    int j = 0;
    for (int k = 0; k < n; ++k) {
      if (when[k] != 1000 || op_earliest(br, list[k]) > E) continue;
      const int c = op_chunk(list[k]);
      int q = E < EM ? (c < 9 ? QA : QT) : E < EQ && br ? 0 : ext_ops(br, E) ? 1 : 2;
      if (j >= q) break;
      s.op[E][j++] = list[k]; when[k] = E; ++done;
      s.last[c] = pos++;
      if (E >= op_deadline(br, list[k])) s.total = -1000;
    }
    s.nop[E] = j;
    s.pos_hook[E] = pos;
    pos += ext_ops(br, E);
  }
  s.pos_ev[NEV] = pos;
  s.total += done == n ? pos : -1000;
  return s;
}
template <int BR> struct SC { static constexpr Sched s = make_sched(BR); };
static_assert(SC<0>::s.total > 0 && SC<1>::s.total > 0, "schedule incomplete");
constexpr int imin(int a, int b) { return a < b ? a : b; }
template <int BR> constexpr int wait_n(int E, int c) {
  int n = SC<BR>::s.pos_ev[E] - SC<BR>::s.last[c] - 1;
  if (E == EQ + q_flag(BR)) n = imin(n, SC<BR>::s.pos_ev[E] - SC<BR>::s.pos_ev[e_clr(BR) + (BR ? 3 : 4)]);
  return n;
}
template <int BR> constexpr int wait_h(int E, int E0) { return SC<BR>::s.pos_hook[E] - SC<BR>::s.pos_ev[E0 + 1]; }

#define STAMP(i)
#define RSTAMP(i)
#define STAMP_FLUSH()
#define STAMP_DECL

DEVI u64 next_tag(u32x2 e) {
  const u64 E = ((u64)(u32)__builtin_amdgcn_readfirstlane(e[1]) << 32) | (u32)__builtin_amdgcn_readfirstlane(e[0]);
  return E * 6364136223846793005ull + 1442695040888963407ull;
}
DEVI void poll_flags(GLB_AS u64* f, u64 tag) {
  for (int spin = 0; spin < (1 << 20); ++spin) {
    u64 v = __hip_atomic_load(f, __ATOMIC_RELAXED, __HIP_MEMORY_SCOPE_AGENT);
    if (__all(v == tag)) break;
    __builtin_amdgcn_s_sleep(1);
  }
  asm volatile("" ::: "memory");
}

template <int BR>
DEVI void body(const char* __restrict__ X, const char* __restrict__ W, const char* __restrict__ PHI, const char* __restrict__ WO, char* ws, char* out,
               int rb, int hq, int jx, u32 lb) {
  int tid = threadIdx.x;
  asm volatile("" : "+v"(tid));
  int l = tid & 63;
  const int w = __builtin_amdgcn_readfirstlane(tid >> 6);
  const int rg = w & 3, hpl = w >> 2;
  int c = l & 15, g = l >> 4, q = c >> 2, p = c & 3, g0 = g & 1, g1 = g >> 1;
  int lr = (l * 57) >> 9, lu = l - lr * 9;
  auto refresh = [&]() {
    asm volatile("" : "+v"(tid));
    l = tid & 63; c = l & 15; g = l >> 4; q = c >> 2; p = c & 3; g0 = g & 1; g1 = g >> 1;
    lr = (l * 57) >> 9; lu = l - lr * 9;
  };
  STAMP_DECL
  STAMP(0);
  RSTAMP(30);
  __amdgpu_buffer_rsrc_t srdX = __builtin_amdgcn_make_buffer_rsrc((void*)X, (short)0, 2048 * XROW, 0x00020000);
  __amdgpu_buffer_rsrc_t srdW = __builtin_amdgcn_make_buffer_rsrc((void*)W, (short)0, 576 * WROW, 0x00020000);
#define ROT (rb + 2 * hq) % 9
  const int rot = ROT;
  constexpr u64 M36 = (1ull << 36) - 1;

  u32 vx[2], vq[2], vh = 0;
#pragma unroll
  for (int i = 0; i < 2; ++i) {
    int row = 16 * w + 8 * i + (l >> 3);
    vx[i] = (u32)((rb * 128 + row) * XROW + (((l & 7) ^ ((row >> 1) & 7)) << 4));
  }
#pragma unroll
  for (int i = 0; i < 2; ++i) vq[i] = (u32)((8 * w + 4 * (lr & 1) + 2 * i + ((lr >> 1) & 1)) * WROW + hq * 144 + lu * 16);
  if (BR) {
    int row = 2 * w + ((l >> 3) & 1);
    int grow = rb * 128 - 16 + row; grow = grow < 0 ? 0 : grow;
    vh = (u32)(grow * XROW + (((l & 7) ^ ((row >> 1) & 7)) << 4));
  }
  auto kc_of = [&](int cc) { int k = cc + rot; return k >= 9 ? k - 9 : k; };
  __amdgpu_buffer_rsrc_t srdO = __builtin_amdgcn_make_buffer_rsrc((void*)WO, (short)0, 576 * XROW, 0x00020000);
  auto dma_op = [&](auto OP) {
    constexpr int op = OP.value;
    if constexpr (op < 200) {
      constexpr int cc = (op - 100) >> 2, i = op & 3;
      const int kc = kc_of(cc);
      if constexpr (i < 2) dma_full(lb + S0 + (cc & 3) * SS + w * 2048 + i * 1024, vx[i], srdX, (u32)(kc * 128));
      else dma_mask(lb + LHALO + (cc & 3) * 2048 + w * 256, vh, srdX, (u32)(kc * 128), 0xffffull);
    } else {
      constexpr int n = (op - 200) >> 1, i = op & 1;
      constexpr int at = n < 9 ? S0 + (n & 3) * SS + SK : n < 18 ? S0 + ((n - 9) & 3) * SS + SV : n < 27 ? WQ + (n - 18) * WU : wo_at(BR, n - 27);
      const u32 dst = lb + at + (2 * w + i) * 576;
      if constexpr (n < 27) {
        const int kc = kc_of(n % 9), sec = n < 9 ? 1152 : (n < 18 ? 2304 : 0);
        dma_mask(dst, vq[i], srdW, (u32)(kc * 64 * WROW + sec), M36);
      } else {
        const int hi = lr & 1, qq = 2 * i + ((lr >> 1) & 1);
        if constexpr (n > 27)
          dma_mask(dst, (u32)((36 * hpl + 20 * hi + 4 * rg + qq) * XROW + hq * 144 + lu * 16), srdO, (u32)(((n - 28 + hq) & 7) * 72 * XROW), M36);
        else dma_mask(dst, (u32)((36 * (8 * hpl + 2 * rg + hi) + 16 + qq) * XROW + hq * 144 + lu * 16), srdO, 0u, M36);
      }
    }
  };
  auto ev_dma = [&](auto EE, auto II) {
    if constexpr (II.value < SC<BR>::s.nop[EE.value]) dma_op(IC<SC<BR>::s.op[EE.value][II.value]>{});
  };
  auto warm_wo = [&]() {
    const int idx = w * 21 + l;
    const u32 po = l < 21 && idx < 162 ? (u32)((162 * jx + idx) * 128) : OOB;
    asm volatile("s_mov_b32 m0, %0\n\ts_nop 0\n\tbuffer_load_dword %1, %2, 0 offen lds" ::"s"(lb + LSCR), "v"(po), "s"(srdO) : "memory");
  };
  u32x2 ep = {0, 0}, ep2;
  asm volatile("" ::"s"(srdX), "s"(srdW));
  STAMP(16);
  static_for<0, PRO>([&](auto cc) {
    constexpr int c_ = cc.value;
    dma_op(IC<100 + 4 * c_>{}); dma_op(IC<101 + 4 * c_>{});
    if constexpr (BR) dma_op(IC<102 + 4 * c_>{});
    dma_op(IC<200 + 2 * c_>{}); dma_op(IC<201 + 2 * c_>{}); dma_op(IC<218 + 2 * c_>{}); dma_op(IC<219 + 2 * c_>{});
    if constexpr (c_ == 0) {
      const char* ea = ws + WS_OEPOCH + (BR * 16 + rb) * 256;
      asm volatile("global_load_dwordx2 %0, %1, off sc1" : "=v"(ep2) : "v"(ea) : "memory");
      if (BR == 0) {
        const char* eb = ws + WS_EPOCH + hq * 256;
        asm volatile("global_load_dwordx2 %0, %1, off sc1" : "=v"(ep) : "v"(eb) : "memory");
        __amdgpu_buffer_rsrc_t srdP = __builtin_amdgcn_make_buffer_rsrc((void*)PHI, (short)0, 32 * 648, 0x00020000);
        dma_mask(lb + LPK + w * 336, (u32)(hq * 2592 + (w * 21 + l) * 16), srdP, 0, (1ull << 21) - 1);
      }
      STAMP(17);
    }
    if constexpr (c_ < PRO - 1) bar_only();
    if constexpr (c_ == 0) STAMP(18);
  });
  STAMP(1);

  const u32 xa0 = lb + S0 + (rg * 32 + hpl * 16 + c) * 128 + ((g ^ ((c >> 1) & 7)) << 4);
  const u32 xa1 = xa0 ^ 64;
  const u32 xb0 = xa0 + 2048 - hpl * 4096, xb1 = xb0 ^ 64;
  const u32 xh0 = lb + LHALO + c * 128 + ((g ^ ((c >> 1) & 7)) << 4);
  const u32 xh1 = xh0 ^ 64;
  const u32 wrow = lb + (16 * g1 + 8 * g0 + 2 * q) * 144;
  const u32 wa = wrow + hpl * 72 + 8 * p;
  const u32 wl = wrow + 32 + (p & 1) * 72 + (p >> 1) * (SV - SK);
  const u32 wh = wa + (rg & 1) * 40 + (rg >> 1) * (SV - SK);
  float ninf = -__builtin_inff();
  asm volatile("" : "+s"(ninf));

  u32x4 xf[18][2];
  const f32x4 z4 = {0.f, 0.f, 0.f, 0.f};
  f32x4 k0[2] = {z4, z4}, k1[2] = {z4, z4}, a0[2] = {z4, z4}, a1[2] = {z4, z4}, aL = z4, aH = z4, aHL = z4;
  u32x4 f0, f1, fl, fh, xh;
  auto load_kv_frags = [&](auto HH_, auto JJ) {
    constexpr int H = HH_.value, j = JJ.value, T = H >> 1, n = T >> 1, ks = T & 1;
    constexpr u32 ok = S0 + (n & 3) * SS + SK + ks * 32 * 144, ov = ok + SV - SK;
    if constexpr (H < 36) {
      if constexpr ((H & 1) == 0) {
        if constexpr (j == 0) { xf[T][0] = lds_r128((ks ? xa1 : xa0) + (n & 3) * SS); f0 = frag_tr(wa + ok, wa + ok + 144); }
        if constexpr (j == 1) { xf[T][1] = lds_r128((ks ? xb1 : xb0) + (n & 3) * SS); f1 = frag_tr(wa + ok + 40, wa + ok + 184); }
      } else {
        if constexpr (j == 0) f0 = frag_tr(wa + ov, wa + ov + 144);
        if constexpr (j == 1) f1 = frag_tr(wa + ov + 40, wa + ov + 184);
        if constexpr (j == 2) fl = frag_tr(wl + ok, wl + ok + 144);
        if constexpr (j == 3 && BR == 1) { fh = frag_tr(wh + ok, wh + ok + 144); xh = lds_r128((ks ? xh1 : xh0) + (n & 3) * 2048); }
      }
    }
  };
  wait_bar<wait_n<BR>(0, 0)>();
  STAMP(2);
  load_kv_frags(IC<0>{}, IC<0>{}); load_kv_frags(IC<0>{}, IC<1>{});
  u64 tag = 0, tag2 = 0;
  static_for<0, 36>([&](auto hh_) {
    constexpr int H = hh_.value, T = H >> 1;
    if constexpr ((H & 3) == 3 && H < 35) {
      wait_bar<wait_n<BR>(H, (H + 1) >> 2)>();
    }
    u32x4 c0 = f0, c1 = f1, cl = fl, ch = fh, cx = xh;
    f32x4(&d0)[2] = (H & 1) ? a0 : k0;
    f32x4(&d1)[2] = (H & 1) ? a1 : k1;
    SCHED_FENCE();
    d0[0] = mfma32(c0, xf[T][0], d0[0]);
    load_kv_frags(IC<H + 1>{}, IC<0>{});
    SCHED_FENCE();
    d0[1] = mfma32(c0, xf[T][1], d0[1]);
    load_kv_frags(IC<H + 1>{}, IC<1>{});
    ev_dma(hh_, IC<0>{});
    SCHED_FENCE();
    d1[0] = mfma32(c1, xf[T][0], d1[0]);
    load_kv_frags(IC<H + 1>{}, IC<2>{});
    SCHED_FENCE();
    d1[1] = mfma32(c1, xf[T][1], d1[1]);
    load_kv_frags(IC<H + 1>{}, IC<3>{});
    ev_dma(hh_, IC<1>{});
    ev_dma(hh_, IC<2>{});
    SCHED_FENCE();
    if constexpr (H & 1) {
      aL = mfma32(cl, xf[T][0], aL);
      asm volatile("" : "+v"(aL));
      if constexpr (BR == 1) {
        aH = mfma32(ch, cx, aH); aHL = mfma32(cl, cx, aHL);
        asm volatile("" : "+v"(aH), "+v"(aHL));
      }
    }
    if constexpr (H == (BR ? E_POB : E_PO)) warm_wo();
    if constexpr (H == 3) {
      asm volatile("" : "+v"(ep), "+v"(ep2));
      if (BR == 0) tag = next_tag(ep);
      tag2 = next_tag(ep2);
    }
    if constexpr (BR == 0 && H == 5) {
      const int h = rg & 1, lf = rg >> 1;
      const int f = lf ? 16 + (q & 1) : c;
      const bool rowok = lf ? (p == h) : true;
      const u32 pb = lb + LPK + (hpl * 2 + h) * 648 + f * 2;
      const int d0 = 2 * h + 4 * g;
      u32 e0 = lds_r16(pb + (d0 + 0) * 36), e1 = lds_r16(pb + (d0 + 1) * 36), e2 = lds_r16(pb + (d0 + 2) * 36), e3 = lds_r16(pb + (d0 + 3) * 36);
      const int dl = h ? 0 : 16;
      u32 l0 = lds_r16(pb + dl * 36), l1 = lds_r16(pb + (dl + 1) * 36);
      u32 lw = (g == 0) ? (l0 | (l1 << 16)) : 0u;
      u32x4 fr = {e0 | (e1 << 16), e2 | (e3 << 16), h ? 0u : lw, h ? lw : 0u};
      if (!rowok) fr = u32x4{0, 0, 0, 0};
      lds_w128(lb + LPHF + (hpl * 4 + rg) * 1024 + l * 16, fr);
      if (w < 3) {
        u32 a = (p == 2 * w) ? ONES : 0u, b = (p == 2 * w + 1) ? ONES : 0u;
        u32 x = ((p & 1) == g1) ? ((p >> 1) ? 0x3f800000u : 0x00003f80u) : 0u;
        lds_w128(lb + LSUM + w * 1024 + l * 16, w < 2 ? u32x4{a, a, b, b} : u32x4{x, 0u, 0u, 0u});
      }
    }
    SCHED_FENCE();
  });
  STAMP(3);
  refresh();
  const u32 wqa = lb + WQ + (16 * g1 + 8 * g0 + 2 * q) * 144 + hpl * 72 + 8 * p;
  const u32 wql = lb + WQ + (16 * g1 + 8 * g0 + 2 * q) * 144 + 32 + (p & 1) * 72;
  auto load_q_frags = [&](auto TT, auto JJ) {
    constexpr int t = TT.value, j = JJ.value;
    constexpr u32 o = (t >> 1) * WU + (t & 1) * 32 * 144;
    if constexpr (t < 18) {
      if constexpr (j == 0) f0 = frag_tr(wqa + o, wqa + o + 144);
      if constexpr (j == 1) f1 = frag_tr(wqa + o + 40, wqa + o + 184);
      if constexpr (j == 2) fl = frag_tr(wql + o, wql + o + 144);
    }
  };
  if constexpr (BR == 1) {
    wait_bar<wait_n<BR>(EQ, 18)>();
    static_for<0, 3>([&](auto j) { load_q_frags(IC<0>{}, j); });
  }
  u32x2 kM[2][2];
#pragma unroll
  for (int sb = 0; sb < 2; ++sb) { kM[0][sb] = pk4(k0[sb]); kM[1][sb] = pk4(k1[sb]); }
  auto unswap = [&](u32x2& a, u32x2& b) {
    u32x2 x = a, y = b;
#pragma unroll
    for (int e = 0; e < 2; ++e) { a[e] = hpl ? y[e] : x[e]; b[e] = hpl ? x[e] : y[e]; }
  };
  {
    constexpr int VROW0 = BR ? 16 : 0;
    auto left = [&](f32x4 v, int blk) {
      u32x2 x = pk4(v);
      const u32 va = lb + lvi(BR) + (VROW0 - 16 + blk * 16 + c) * VIS + g0 * 80 + 64;
      lds_w64(g1 ? va : lb + KLX + (blk * 2 + g0) * 128 + c * 8, u32x2{x[0], g1 ? ONES : x[1]});
      lds_w64(g1 ? va + 8 : lb + LSCR + 256 + c * 8, u32x2{x[1], ONES});
    };
    left(aL, 1 + 2 * rg + hpl);
#pragma unroll
    for (int sb = 0; sb < 2; ++sb) {
      const u32 va = lb + lvi(BR) + (VROW0 + rg * 32 + (sb ^ hpl) * 16 + c) * VIS + hpl * 80;
      lds_w64(va + 8 * g, pk4(a0[sb]));
      lds_w64(va + 32 + 8 * g, pk4(a1[sb]));
    }
    if constexpr (BR == 1) {
      unswap(kM[0][0], kM[0][1]); unswap(kM[1][0], kM[1][1]);
      if (w == 2) left(aHL, 0);
      const u32 kx = lb + LKX + l * 8;
      lds_w64(rg < 2 ? kx + (hpl * 2 + rg) * 512 : lb + lvi(BR) + c * VIS + hpl * 80 + (rg & 1) * 32 + 8 * g, pk4(aH));
      if (rg < 3) {
        const u32 b = kx + (((rg + 1) * 2 + hpl) * 2) * 512;
        lds_w64(b, kM[0][1]); lds_w64(b + 512, kM[1][1]);
      }
    }
  }
  const u32 phf = lb + LPHF + hpl * 4096 + l * 16, sumf = lb + LSUM + l * 16;
  const bool lo = g < 2;
  __amdgpu_buffer_rsrc_t srdF = __builtin_amdgcn_make_buffer_rsrc((void*)(ws + WS_OF), (short)0, 256 * 18 * 1024, 0x00020000);
  __amdgpu_buffer_rsrc_t srdS = __builtin_amdgcn_make_buffer_rsrc((void*)(ws + WS_SLAB), (short)0, 136 * SLAB, 0x00020000);
  GLB_AS u64* kflags = (GLB_AS u64*)(ws + WS_FLAG + hq * 256);
  GLB_AS u64* oflags = (GLB_AS u64*)(ws + WS_OFLAG + (BR * 16 + rb) * 256);
  auto mark = [&](auto EE) {
    constexpr int e = EE.value - e_clr(BR);
    const u32x4 sv = {SENT, SENT, SENT, SENT};
    const u32 ob = (u32)((BR * 128 + rb * 8 + w) * 18 * 1024);
    if constexpr (e == 0 || e == 1) __builtin_amdgcn_raw_buffer_store_b128(sv, srdF, l * 16, ob + hq * 2048 + e * 1024, 16);
    if constexpr (e == 2) __builtin_amdgcn_raw_buffer_store_b128(sv, srdF, g == 0 ? c * 16 : OOB, ob + (16 + (hq >> 2)) * 1024 + (hq & 3) * 256, 16);
    if constexpr (BR == 0 && e == 3) {
      const u32 so = tid < 400 ? (u32)((hq * 16 + rb) * SLAB + tid * 16) : tid < 425 ? (u32)((128 + hq) * SLAB + rb * 400 + (tid - 400) * 16) : OOB;
      __builtin_amdgcn_raw_buffer_store_b128(sv, srdS, so, 0, 16);
    }
  };
  if constexpr (BR == 0) {
    bar_lds();
    STAMP(4);
    HH h0, h1;
    h0.b0[0] = kM[0][0][0]; h0.b0[1] = kM[0][0][1]; h0.b1[0] = kM[1][0][0]; h0.b1[1] = kM[1][0][1];
    h1.b0[0] = kM[0][1][0]; h1.b0[1] = kM[0][1][1]; h1.b1[0] = kM[1][1][0]; h1.b1[1] = kM[1][1][1];
    const u32 kl0 = lb + KLX + ((1 + 2 * rg + hpl) * 2 + hpl) * 128 + c * 8, kl1 = lb + KLX + ((2 + 2 * rg - hpl) * 2 + hpl) * 128 + c * 8;
    static_for<0, HH_N>([&](auto st) {
      if constexpr (st.value >= 8 && (st.value - 8) % 12 == 0 && (st.value - 8) / 12 < EQ - EM) {
        constexpr int E = EM + (st.value - 8) / 12;
        SCHED_FENCE();
        ev_dma(IC<E>{}, IC<0>{}); ev_dma(IC<E>{}, IC<1>{});
        if constexpr (E == E_PO) warm_wo();
        mark(IC<E>{});
        SCHED_FENCE();
      }
      hh_step<st.value, false>(h0, kl0, phf, sumf, lo, ninf);
      hh_step<st.value, false>(h1, kl1, phf, sumf, lo, ninf);
    });
#pragma unroll
    for (int sb = 0; sb < 2; ++sb) {
      HH& hs = sb ? h1 : h0;
      hh_left<false>(hs, lo);
      const u32 pa = lb + LPK + (rg * 32 + (sb ^ hpl) * 16 + c) * PKS + hpl * 144;
      lds_w64(pa + 8 * g, u32x2{hs.b0[0], hs.b0[1]});
      lds_w64(pa + 32 + 8 * g, u32x2{hs.b0[2], hs.b0[3]});
      lds_w64(pa + 64 + 8 * g, u32x2{hs.b1[0], hs.b1[1]});
      lds_w64(pa + 96 + 8 * g, u32x2{hs.b1[2], hs.b1[3]});
      lds_w32(pa + 128 + 4 * g, hs.bl);
    }
    STAMP(5);
    wait_bar<wait_n<BR>(EQ, 26)>();
    static_for<0, 3>([&](auto j) { load_q_frags(IC<0>{}, j); });
  }
  refresh();
  f32x4 dp = z4, dm = z4, df = z4;
  u32x4 ap[4], am[4], af[4], bv[4];
  u32 g0_, g1_, g2_, g3_;
  u32x2 fv;
  const int rr = 4 * (4 * g0 + q) + 2 * g1;
  const u32 pa = lb + LPK + rr * PKS + hpl * 144 + (rg & 1) * 64 + 8 * p;
  const u32 pf = lb + LPK + rr * PKS + hpl * 144 + 128 + 8 * (p >> 1);
  const u32 va = lb + lvi(BR) + rr * VIS + hpl * 80 + (rg < 2 ? rg * 32 + 8 * p : 64 + 8 * (p & 1));
  auto hook = [&](auto tt, auto jj) {
    constexpr int t = tt.value, E = EQ + t, j = jj.value;
    if constexpr (BR == 0 && j < 4) {
      if constexpr (t == Q_SLAB - 2) {
        ap[j] = frag_tr(pa + j * 32 * PKS, pa + j * 32 * PKS + PKS);
        am[j] = frag_tr(pa + 32 + j * 32 * PKS, pa + 32 + j * 32 * PKS + PKS);
        af[j] = frag_tr(pf + j * 32 * PKS, pf + j * 32 * PKS + PKS);
        bv[j] = frag_tr(va + j * 32 * VIS, va + j * 32 * VIS + VIS);
      }
      if constexpr (t == Q_SLAB - 1) {
        dp = mfma32(ap[j], bv[j], dp); dm = mfma32(am[j], bv[j], dm); df = mfma32(af[j], bv[j], df);
        asm volatile("" : "+v"(dp), "+v"(dm), "+v"(df));
      }
    }
    if constexpr (j != 4) return;
    if constexpr (BR == 1) mark(IC<E>{});
    if constexpr (t == q_flag(BR)) {
      if (tid == 0) {
        __hip_atomic_store(oflags + hq, tag2, __ATOMIC_RELAXED, __HIP_MEMORY_SCOPE_AGENT);
        if (BR == 0) __hip_atomic_store(kflags + rb, tag, __ATOMIC_RELAXED, __HIP_MEMORY_SCOPE_AGENT);
      }
    }
    if constexpr (t == q_fld(BR)) {
      GLB_AS u64* fa = (BR == 0 && g == 0) ? kflags + c : oflags + (l & 7);
      asm volatile("global_load_dwordx2 %0, %1, off sc1" : "=v"(fv) : "v"(fa) : "memory");
    }
    if constexpr (t == q_fchk(BR)) {
      asm volatile("s_waitcnt vmcnt(%1)" : "+v"(fv) : "n"(wait_h<BR>(E, EQ + q_fld(BR))) : "memory");
      const u64 want = (BR == 0 && g == 0) ? tag : tag2;
      if (!__all((((u64)fv[1] << 32) | fv[0]) == want)) poll_flags((BR == 0 && g == 0) ? kflags + c : oflags + (l & 7), want);
      if (tid == 0) {
        if (BR == 0 && rb == 0) __hip_atomic_store((GLB_AS u64*)(ws + WS_EPOCH + hq * 256), tag, __ATOMIC_RELAXED, __HIP_MEMORY_SCOPE_AGENT);
        if (hq == 0) __hip_atomic_store((GLB_AS u64*)(ws + WS_OEPOCH + (BR * 16 + rb) * 256), tag2, __ATOMIC_RELAXED, __HIP_MEMORY_SCOPE_AGENT);
      }
    }
    if constexpr (BR == 0) {
      if constexpr (t == Q_SLAB) {
        const u32 slab = (u32)((hq * 16 + rb) * SLAB);
        const bool h0set = (0x47 >> (c & 7)) & 1, h = rg & 1, own = (c >> 3) == hpl;
        u32x2 a = pk4(dp), b = pk4(dm);
        const u32 o1 = rg < 2 ? slab + (hpl * 2 + rg) * 1024 + l * 16 : (own && h0set != h) ? slab + 4096 + l * 16 : OOB;
        __builtin_amdgcn_raw_buffer_store_b128(u32x4{ns(a[0]), ns(a[1]), ns(b[0]), ns(b[1])}, srdS, o1, 0, 16);
        const float x0 = g0 ? df[2] : df[0], x1 = g0 ? df[3] : df[1];
        const bool first = rg < 2 ? !h : h0set;
        const u32 o2 = rg < 2 ? slab + 5120 + (hpl * 2 + rg) * 256 + l * 4 : own ? slab + 6144 + l * 4 : OOB;
        __builtin_amdgcn_raw_buffer_store_b32(ns(pk(first ? x0 : 0.f, first ? 0.f : x1)), srdS, o2, 0, 16);
        STAMP(6);
      }
      if constexpr (t == Q_L1 || t == Q_L1C) {
        const int dw = w * 16 + c;
        const u32 dof = (u32)((rb * 100 + (dw < 100 ? dw : 99)) * 4);
        const u32 go = (u32)((hq * 16 + 4 * g) * SLAB) + dof;
        if constexpr (t == Q_L1) {
          asm volatile("buffer_load_dword %0, %4, %5, 0 offen sc1\n\tbuffer_load_dword %1, %4, %5, %6 offen sc1\n\t"
                       "buffer_load_dword %2, %4, %5, %7 offen sc1\n\tbuffer_load_dword %3, %4, %5, %8 offen sc1"
                       : "=&v"(g0_), "=&v"(g1_), "=&v"(g2_), "=&v"(g3_)
                       : "v"(go), "s"(srdS), "s"(SLAB), "s"(2 * SLAB), "s"(3 * SLAB)
                       : "memory");
        } else {
          asm volatile("s_waitcnt vmcnt(%4)" : "+v"(g0_), "+v"(g1_), "+v"(g2_), "+v"(g3_) : "n"(wait_h<BR>(E, EQ + Q_L1)) : "memory");
          _Pragma("nounroll") for (int spin = 0; spin < (1 << 20); ++spin) {
            asm volatile("" ::: "memory");
            if (!__any(umax(umax3(g0_, g1_, g2_), g3_) == SENT)) break;
            g0_ = __builtin_amdgcn_raw_buffer_load_b32(srdS, go, 0, 16);
            g1_ = __builtin_amdgcn_raw_buffer_load_b32(srdS, go, SLAB, 16);
            g2_ = __builtin_amdgcn_raw_buffer_load_b32(srdS, go, 2 * SLAB, 16);
            g3_ = __builtin_amdgcn_raw_buffer_load_b32(srdS, go, 3 * SLAB, 16);
          }
          const u32 sel = c == 0 ? 0x00003f80u : c == 1 ? 0x3f800000u : 0u;
          f32x4 sum = mfma32(u32x4{sel, sel, sel, sel}, u32x4{g0_, g1_, g2_, g3_}, z4);
          __builtin_amdgcn_raw_buffer_store_b32(ns(pk(sum[0], sum[1])), srdS, g == 0 ? (u32)((128 + hq) * SLAB) + dof : OOB, 0, 16);
          STAMP(7);
        }
      }
    }
  };
  a0[0] = z4; a0[1] = z4; a1[0] = z4; a1[1] = z4; aL = z4;
  static_for<0, 18>([&](auto tt) {
    constexpr int t = tt.value, E = EQ + t;
    if constexpr (BR == 0) {
      if constexpr (t == 3 || t == 6) bar_only();
    } else if constexpr ((t & 1) && t < 17) {
      if constexpr (t == 1) wait_bar<wait_n<BR>(E, 18 + (t >> 1) + 1)>();
      else wait_vm_bar<wait_n<BR>(E, 18 + (t >> 1) + 1)>();
    }
    u32x4 c0 = f0, c1 = f1, cl = fl;
    SCHED_FENCE();
    a0[0] = mfma32(c0, xf[t][0], a0[0]);
    load_q_frags(IC<t + 1>{}, IC<0>{});
    hook(tt, IC<0>{});
    SCHED_FENCE();
    a0[1] = mfma32(c0, xf[t][1], a0[1]);
    load_q_frags(IC<t + 1>{}, IC<1>{});
    hook(tt, IC<1>{});
    ev_dma(IC<E>{}, IC<0>{});
    SCHED_FENCE();
    a1[0] = mfma32(c1, xf[t][0], a1[0]);
    load_q_frags(IC<t + 1>{}, IC<2>{});
    hook(tt, IC<2>{});
    SCHED_FENCE();
    a1[1] = mfma32(c1, xf[t][1], a1[1]);
    hook(tt, IC<3>{});
    ev_dma(IC<E>{}, IC<1>{});
    SCHED_FENCE();
    aL = mfma32(cl, xf[t][0], aL);
    asm volatile("" : "+v"(aL));
    hook(tt, IC<4>{});
    SCHED_FENCE();
  });
  STAMP(8);
  refresh();
  if (g < 2) lds_w64(lb + QLX + ((2 * rg + hpl) * 2 + g) * 128 + c * 8, pk4(aL));
  bar_lds();
  const u32 ofo = (u32)((BR * 128 + rb * 8 + rg * 2) * 18 * 1024);
  const int hp = hq * 2 + hpl;
  auto store_o = [&](int sb, f32x4 o0, f32x4 o1, float l0, float l1, float r0, float r1) {
    u32x2 a = pk4(o0 * r0), b = pk4(o1 * r1);
    __builtin_amdgcn_raw_buffer_store_b128(u32x4{ns(a[0]), ns(a[1]), ns(b[0]), ns(b[1])}, srdF, ofo + (sb * 18 + hp) * 1024 + l * 16, 0, 16);
    float rr = g0 ? r1 : r0;
    u32 lv = ns(pk(l0 * rr, l1 * rr));
    auto sw = __builtin_amdgcn_permlane16_swap(lv, lv, false, false);
    u32 s0 = sw[0], s1 = sw[1];
    __builtin_amdgcn_raw_buffer_store_b64(u32x2{s0, s1}, srdF, g == 0 ? ofo + (sb * 18 + 16 + (hq >> 2)) * 1024 + (c + 16 * (hq & 3)) * 16 + 8 * hpl : OOB, 0, 16);
  };

  if (BR == 0) {
    HH h0, h1;
    {
      u32x2 qa0 = pk4(a0[0]), qb0 = pk4(a1[0]), qa1 = pk4(a0[1]), qb1 = pk4(a1[1]);
      h0.b0[0] = qa0[0]; h0.b0[1] = qa0[1]; h0.b1[0] = qb0[0]; h0.b1[1] = qb0[1];
      h1.b0[0] = qa1[0]; h1.b0[1] = qa1[1]; h1.b1[0] = qb1[0]; h1.b1[1] = qb1[1];
    }
    const u32 ql0 = lb + QLX + ((2 * rg + hpl) * 2 + hpl) * 128 + c * 8, ql1 = lb + QLX + ((2 * rg + 1 - hpl) * 2 + hpl) * 128 + c * 8;
    const u32 kf = (u32)((128 + hq) * SLAB);
    const int l8 = g * 16 + (c & 7) + 8 * hpl;
    u32x4 AM0, AM1, AMLm;
    u32 AL0, AL1, ALL;
    auto load_kv = [&]() {
      AM0 = __builtin_amdgcn_raw_buffer_load_b128(srdS, kf + hpl * 2048 + l * 16, 0, 16);
      AM1 = __builtin_amdgcn_raw_buffer_load_b128(srdS, kf + hpl * 2048 + 1024 + l * 16, 0, 16);
      AMLm = __builtin_amdgcn_raw_buffer_load_b128(srdS, kf + 4096 + l8 * 16, 0, 16);
      AL0 = __builtin_amdgcn_raw_buffer_load_b32(srdS, kf + 5120 + hpl * 512 + l * 4, 0, 16);
      AL1 = __builtin_amdgcn_raw_buffer_load_b32(srdS, kf + 5120 + hpl * 512 + 256 + l * 4, 0, 16);
      ALL = __builtin_amdgcn_raw_buffer_load_b32(srdS, kf + 6144 + l8 * 4, 0, 16);
    };
    static_for<0, HH_N>([&](auto st) {
      if (st.value == L2ST) { SCHED_FENCE(); load_kv(); SCHED_FENCE(); }
      hh_step<st.value, true>(h0, ql0, phf, sumf, lo, ninf);
      hh_step<st.value, true>(h1, ql1, phf, sumf, lo, ninf);
    });
    hh_left<true>(h0, lo);
    hh_left<true>(h1, lo);
    STAMP(9);
    _Pragma("nounroll") for (int spin = 0; spin < (1 << 20); ++spin) {
      asm volatile("" ::: "memory");
      if (!__any(umax4(umax4(umax4(umax3(AL0, AL1, ALL), AM0), AM1), AMLm) == SENT)) break;
      load_kv();
    }
    STAMP(10);
    const bool h0set = (0x47 >> (c & 7)) & 1;
    u32x4 AML0, AML1;
#pragma unroll
    for (int e = 0; e < 4; ++e) { AML0[e] = h0set ? AMLm[e] : 0u; AML1[e] = h0set ? 0u : AMLm[e]; }
#pragma unroll
    for (int sb = 0; sb < 2; ++sb) {
      const HH& h = sb ? h1 : h0;
      u32x4 bl = {h.bl, 0u, 0u, 0u};
      f32x4 O0 = mfma32(u32x4{AL0, 0u, 0u, 0u}, bl, mfma32(AM0, h.b0, z4));
      f32x4 O1 = mfma32(u32x4{AL1, 0u, 0u, 0u}, bl, mfma32(AM1, h.b1, z4));
      f32x4 OL = mfma32(u32x4{ALL, 0u, 0u, 0u}, bl, mfma32(AML1, h.b1, mfma32(AML0, h.b0, z4)));
      float r0 = __builtin_amdgcn_rcpf(fmaf(h.S[0], 1e-6f, OL[2])), r1 = __builtin_amdgcn_rcpf(fmaf(h.S[2], 1e-6f, OL[3]));
      store_o(sb ^ hpl, O0, O1, OL[0], OL[1], r0, r1);
    }
  } else {
    const u32 kx = lb + LKX + ((rg * 2 + hpl) * 2) * 512 + l * 8;
    u32x2 pM0 = lds_r64(kx), pM1 = lds_r64(kx + 512);
    u32x2 kL[3];
#pragma unroll
    for (int i = 0; i < 3; ++i) kL[i] = lds_r64(lb + KLX + ((2 * rg + i) * 2 + hpl) * 128 + c * 8);
    const int par = c & 1;
    const bool ina = (4 * g + par) > c, inb = (4 * g + par + 2) > c;
    const float sc = 0.23570226039551584f * LOG2E;
    float la[2][2], lbv[2][2], mx[2][2];
    u32x2 qM[2][2] = {{pk4(a0[0]), pk4(a0[1])}, {pk4(a1[0]), pk4(a1[1])}};
    unswap(qM[0][0], qM[0][1]); unswap(qM[1][0], qM[1][1]);
#pragma unroll
    for (int sb = 0; sb < 2; ++sb) {
      u32x2 qlv = lds_r64(lb + QLX + ((2 * rg + sb) * 2 + hpl) * 128 + c * 8);
      u32x2 q0 = qM[0][sb], q1 = qM[1][sb];
      u32x4 b0 = {q0[0], q0[1], g == 0 ? qlv[0] : 0u, 0u};
      u32x4 b1 = {q1[0], q1[1], 0u, g == 0 ? qlv[1] : 0u};
      u32x2 kp0 = sb ? kM[0][0] : pM0, kp1 = sb ? kM[1][0] : pM1, kpl = kL[sb], kcl = kL[sb + 1];
      f32x4 sp0 = mfma32(u32x4{kp0[0], kp0[1], kpl[0], kpl[1]}, b0, z4);
      f32x4 sp1 = mfma32(u32x4{kp1[0], kp1[1], kpl[0], kpl[1]}, b1, z4);
      f32x4 sc0 = mfma32(u32x4{kM[0][sb][0], kM[0][sb][1], kcl[0], kcl[1]}, b0, z4);
      f32x4 sc1 = mfma32(u32x4{kM[1][sb][0], kM[1][sb][1], kcl[0], kcl[1]}, b1, z4);
      const bool dead = (sb == 0) && (rb == 0) && (rg == 0);
      float pa0 = par ? sp0[1] : sp0[0], pb0 = par ? sp0[3] : sp0[2], pa1 = par ? sp1[1] : sp1[0], pb1 = par ? sp1[3] : sp1[2];
      if (dead) { pa0 = pb0 = pa1 = pb1 = -1e30f; }
      la[0][sb] = (ina ? pa0 : (par ? sc0[1] : sc0[0])) * sc;
      lbv[0][sb] = (inb ? pb0 : (par ? sc0[3] : sc0[2])) * sc;
      la[1][sb] = (ina ? pa1 : (par ? sc1[1] : sc1[0])) * sc;
      lbv[1][sb] = (inb ? pb1 : (par ? sc1[3] : sc1[2])) * sc;
      mx[0][sb] = fmaxf(la[0][sb], lbv[0][sb]);
      mx[1][sb] = fmaxf(la[1][sb], lbv[1][sb]);
    }
    float tm = redmax4(mx[0][0], mx[0][1], mx[1][0], mx[1][1], ninf);
    bcast4(tm, mx[0][0], mx[0][1], mx[1][0], mx[1][1]);
    const u32 vb = lb + lvi(BR) + (rg * 32 + 4 * g + q) * VIS + hpl * 80;
#pragma unroll
    for (int sb = 0; sb < 2; ++sb) {
      const u32 v0 = vb + sb * 16 * VIS, v1 = v0 + 16 * VIS;
      u32x4 A0 = frag_tr(v0 + 8 * p, v1 + 8 * p), A1 = frag_tr(v0 + 32 + 8 * p, v1 + 32 + 8 * p);
      u32x4 AL = frag_tr(v0 + 64 + 8 * (p & 1), v1 + 64 + 8 * (p & 1));
      u32x4 P[2];
#pragma unroll
      for (int h = 0; h < 2; ++h) {
        float ea = __builtin_amdgcn_exp2f(la[h][sb] - mx[h][sb]), eb = __builtin_amdgcn_exp2f(lbv[h][sb] - mx[h][sb]);
        u32 wa_ = par ? pk(0.f, ea) : pk(ea, 0.f), wb_ = par ? pk(0.f, eb) : pk(eb, 0.f);
        P[h] = u32x4{ina ? wa_ : 0u, inb ? wb_ : 0u, ina ? 0u : wa_, inb ? 0u : wb_};
      }
      f32x4 O0 = mfma32(A0, P[0], z4), O1 = mfma32(A1, P[1], z4);
      f32x4 L0 = mfma32(AL, P[0], z4), L1 = mfma32(AL, P[1], z4);
      float r0 = __builtin_amdgcn_rcpf(L0[2]), r1 = __builtin_amdgcn_rcpf(L1[2]);
      store_o(sb, O0, O1, g0 ? L1[0] : L0[0], g0 ? L1[1] : L0[1], r0, r1);
    }
  }
  STAMP(11);
  refresh();
  wait_bar<4>();
  STAMP(12);
  const u32 wrow2 = lb + (16 * g1 + 8 * g0 + 2 * q) * 144;
  const u32 oa = wrow2 + 8 * p, oa4 = wrow2 + 128 + 8 * (p & 1);
  u32x4 wf[18][5], of[18];
  auto load_wf = [&](auto TT) {
    constexpr int t = TT.value;
    if constexpr (t < 18) {
      constexpr u32 cb = wo_at(BR, t >> 1);
      const u32 a = oa + cb + (t & 1) * 32 * 144, a4 = oa4 + cb + (t & 1) * 32 * 144;
#pragma unroll
      for (int i = 0; i < 4; ++i) wf[t][i] = frag_tr(a + 32 * i, a + 144 + 32 * i);
      wf[t][4] = frag_tr(a4, a4 + 144);
    }
  };
  const u32 ofl = (u32)((BR * 128 + rb * 8 + w) * 18 * 1024 + l * 16);
  of[0] = __builtin_amdgcn_raw_buffer_load_b128(srdF, ofl, 16 * 1024, 16);
  of[1] = __builtin_amdgcn_raw_buffer_load_b128(srdF, ofl, 17 * 1024, 16);
  SCHED_FENCE();
  static_for<0, PRE>([&](auto t) { load_wf(t); });
  SCHED_FENCE();
  _Pragma("nounroll") for (int spin = 0; spin < (1 << 20); ++spin) {
      asm volatile("" ::: "memory");
    if (!__any(umax4(umax4(0u, of[0]), of[1]) == SENT)) break;
    of[0] = __builtin_amdgcn_raw_buffer_load_b128(srdF, ofl, 16 * 1024, 16);
    of[1] = __builtin_amdgcn_raw_buffer_load_b128(srdF, ofl, 17 * 1024, 16);
  }
  STAMP(13);
#pragma unroll
  for (int t = 2; t < 18; ++t) of[t] = __builtin_amdgcn_raw_buffer_load_b128(srdF, ofl, ((t - 2 + 2 * hq) & 15) * 1024, 16);
  SCHED_FENCE();
  f32x4 acc[5] = {z4, z4, z4, z4, z4};
  static_for<0, 18>([&](auto tt) {
    constexpr int t = tt.value;
    if constexpr (t >= 2) {
      _Pragma("nounroll") for (int spin = 0; spin < (1 << 20); ++spin) {
      asm volatile("" ::: "memory");
        if (!__any(umax4(0u, of[t]) == SENT)) break;
        of[t] = __builtin_amdgcn_raw_buffer_load_b128(srdF, ofl, ((t - 2 + 2 * hq) & 15) * 1024, 16);
      }
    }
#pragma unroll
    for (int i = 0; i < 5; ++i) acc[i] = mfma32(wf[t][i], of[t], acc[i]);
    load_wf(IC<t + PRE>{});
    SCHED_FENCE();
  });
  STAMP(14);
  char* op = out + (size_t)(rb * 128 + w * 16 + c) * 2304 + BR * 1152 + hq * 144;
#pragma unroll
  for (int i = 0; i < 2; ++i) {
    u32x2 a = pk4(acc[2 * i]), b = pk4(acc[2 * i + 1]);
    auto s0 = __builtin_amdgcn_permlane16_swap(a[0], b[0], false, false);
    auto s1 = __builtin_amdgcn_permlane16_swap(a[1], b[1], false, false);
    u32 s00 = s0[0], s01 = s0[1], s10 = s1[0], s11 = s1[1];
    *(u32x4*)(op + 64 * i + 32 * g0 + 16 * g1) = u32x4{s00, s10, s01, s11};
  }
  if (g < 2) *(u32x2*)(op + 128 + 8 * g) = pk4(acc[4]);
  STAMP(15);
  RSTAMP(31);
  STAMP_FLUSH();
  __builtin_amdgcn_endpgm();
}

__global__ __launch_bounds__(512, 2) void fused(const char* __restrict__ XA, const char* __restrict__ WA, const char* __restrict__ PHI,
                                                const char* __restrict__ WOA, const char* __restrict__ XB, const char* __restrict__ WB,
                                                const char* __restrict__ WOB, char* ws, char* out) {
  __shared__ __attribute__((aligned(16))) char smem[LDS_BYTES];
  const u32 lb = (u32)(uintptr_t)(LDS_AS char*)smem;
  const int b = blockIdx.x, x = b & 7, j = b >> 3, xx = x & 3;
  const int rb = xx * 4 + (j >> 3), hq = j & 7;
  if (x < 4) body<0>(XA, WA, PHI, WOA, ws, out, rb, hq, j, lb);
  else body<1>(XB, WB, PHI, WOB, ws, out, rb, hq, j, lb);
}
}

extern "C" void kernel_launch(void* const* d_in, const int* in_sizes, int n_in, void* d_out, int out_size, void* d_ws, size_t ws_size,
                              hipStream_t stream) {
  hk::fused<<<256, 512, 0, stream>>>((const char*)d_in[0], (const char*)d_in[1], (const char*)d_in[2], (const char*)d_in[3], (const char*)d_in[4],
                                     (const char*)d_in[5], (const char*)d_in[6], (char*)d_ws, (char*)d_out);
}
```

```cpp
#define Q_L1 9
#define Q_L1C 14
#define L2ST 70
#include <hip/hip_runtime.h>
#include <stdint.h>

namespace hk {
typedef unsigned int u32;
typedef unsigned long long u64;
typedef unsigned short u16;
typedef float f32x4 __attribute__((ext_vector_type(4)));
typedef float f32x2 __attribute__((ext_vector_type(2)));
typedef u32 u32x4 __attribute__((ext_vector_type(4)));
typedef u32 u32x2 __attribute__((ext_vector_type(2)));
typedef __bf16 bf16x8 __attribute__((ext_vector_type(8)));
typedef __bf16 bf16x2 __attribute__((ext_vector_type(2)));
typedef short s16x4 __attribute__((ext_vector_type(4)));
#define LDS_AS __attribute__((address_space(3)))
#define GLB_AS __attribute__((address_space(1)))
#define DEVI __device__ __forceinline__
#define SCHED_FENCE() __builtin_amdgcn_sched_barrier(0)

template <int V> struct IC { static constexpr int value = V; };
template <int I, int N, class F>
DEVI void static_for(F&& f) {
  if constexpr (I < N) { f(IC<I>{}); static_for<I + 1, N>(f); }
}

constexpr int XROW = 1152, WROW = 3456;
constexpr size_t WS_OF = 0;
constexpr int SLAB = 6400;
constexpr size_t WS_SLAB = 5u << 20;
constexpr size_t WS_FLAG = 6u << 20;
constexpr size_t WS_EPOCH = WS_FLAG + 65536;
constexpr size_t WS_OFLAG = WS_FLAG + 4096;
constexpr u32 OOB = 0x7fffffffu;
constexpr u32 SENT = 0xffffffffu;
constexpr int WU = 9216;
constexpr int LPK = 1024, PKS = 296;
constexpr int S0 = 3968, SS = 34944;
constexpr int SK = 16384, SV = SK + WU + 16;
constexpr int WQ = S0 + SS;
constexpr int LT = WQ + 9 * WU;
constexpr int LM = S0 + 4 * SS;
constexpr int LPHF = LM;
constexpr int LSUM = LPHF + 8192;
constexpr int LHALO = LM;
constexpr int KLX = LM + 11264;
constexpr int QLX = KLX + 2304;
constexpr int LSCR = QLX + 2048;
constexpr int VIS = 168;
constexpr int lvi(int br) { return br ? 1024 : LT; }
constexpr int LKX = 25600;
constexpr int wo_at(int br, int m) {
  if (br == 0) return m < 4 ? 1024 + m * WU : m < 6 ? LT + (m - 4) * WU : WQ + (m - 6) * WU;
  return m < 2 ? LT + m * WU : m == 2 ? LM : WQ + (m - 3) * WU;
}
constexpr int LDS_BYTES = 163840;
static_assert(LSCR + 512 <= LDS_BYTES && LPK + 2688 <= S0 && LPK + 128 * PKS <= WQ && LT + 128 * VIS <= LM && 1024 + 144 * VIS <= LKX && LKX + 8192 <= WQ &&
              SV + WU <= SS && 3 * WU <= SS && 7 * WU <= 2 * SS && S0 % 128 == 0 && SS % 128 == 0, "lds");

DEVI u32x2 tr_read(u32 a) { return __builtin_bit_cast(u32x2, __builtin_amdgcn_ds_read_tr16_b64_v4i16((LDS_AS s16x4*)a)); }
DEVI u32x4 frag_tr(u32 a0, u32 a1) { u32x2 x = tr_read(a0), y = tr_read(a1); return u32x4{x[0], x[1], y[0], y[1]}; }
DEVI u32x4 lds_r128(u32 a) { return *(LDS_AS u32x4*)a; }
DEVI u32x2 lds_r64(u32 a) { return *(LDS_AS u32x2*)a; }
DEVI u32 lds_r32(u32 a) { return *(LDS_AS u32*)a; }
DEVI u32 lds_r16(u32 a) { return *(LDS_AS u16*)a; }
DEVI void lds_w128(u32 a, u32x4 v) { *(LDS_AS u32x4*)a = v; }
DEVI void lds_w64(u32 a, u32x2 v) { *(LDS_AS u32x2*)a = v; }
DEVI void lds_w32(u32 a, u32 v) { *(LDS_AS u32*)a = v; }
DEVI f32x4 mfma32(u32x4 a, u32x4 b, f32x4 c) {
  return __builtin_amdgcn_mfma_f32_16x16x32_bf16(__builtin_bit_cast(bf16x8, a), __builtin_bit_cast(bf16x8, b), c, 0, 0, 0);
}
DEVI u32 pk(float a, float b) { f32x2 v = {a, b}; return __builtin_bit_cast(u32, __builtin_convertvector(v, bf16x2)); }
DEVI u32x2 pk4(f32x4 v) { return u32x2{pk(v[0], v[1]), pk(v[2], v[3])}; }
DEVI float max3(float a, float b, float c) { return fmaxf(fmaxf(a, b), c); }
DEVI u32 umax(u32 a, u32 b) { return a > b ? a : b; }
DEVI u32 umax3(u32 a, u32 b, u32 c) { return umax(umax(a, b), c); }
DEVI u32 umax4(u32 m, u32x4 v) { return umax3(umax3(m, v[0], v[1]), v[2], v[3]); }
DEVI u32 ns(u32 v) { return v < 0xfffffffeu ? v : 0xfffffffeu; }

DEVI void dma_full(u32 lds, u32 voff, __amdgpu_buffer_rsrc_t srd, u32 soff) {
  asm volatile("s_mov_b32 m0, %0\n\ts_nop 0\n\tbuffer_load_dwordx4 %1, %2, %3 offen lds" ::"s"(lds), "v"(voff), "s"(srd), "s"(soff) : "memory");
}
DEVI void dma_mask(u32 lds, u32 voff, __amdgpu_buffer_rsrc_t srd, u32 soff, u64 mask) {
  asm volatile("s_mov_b32 m0, %0\n\ts_mov_b64 exec, %4\n\tbuffer_load_dwordx4 %1, %2, %3 offen lds\n\ts_mov_b64 exec, -1" ::"s"(lds), "v"(voff), "s"(srd),
               "s"(soff), "s"(mask)
               : "memory");
}
template <int N>
DEVI void wait_bar() { asm volatile("s_waitcnt vmcnt(%0) lgkmcnt(0)\n\ts_barrier" ::"n"(N) : "memory"); }
template <int N>
DEVI void wait_vm_bar() { asm volatile("s_waitcnt vmcnt(%0)\n\ts_barrier" ::"n"(N) : "memory"); }
DEVI void bar_lds() { asm volatile("s_waitcnt lgkmcnt(0)\n\ts_barrier" ::: "memory"); }
DEVI void bar_only() { asm volatile("s_barrier" ::: "memory"); }

DEVI float redmax4(float a, float b, float c, float d, float ninf) {
  auto r1 = __builtin_amdgcn_permlane32_swap(__float_as_uint(a), __float_as_uint(b), false, false);
  auto r2 = __builtin_amdgcn_permlane32_swap(__float_as_uint(c), __float_as_uint(d), false, false);
  u32 r10 = r1[0], r11 = r1[1], r20 = r2[0], r21 = r2[1];
  float ab = max3(__uint_as_float(r10), __uint_as_float(r11), ninf);
  float cd = max3(__uint_as_float(r20), __uint_as_float(r21), ninf);
  auto r3 = __builtin_amdgcn_permlane16_swap(__float_as_uint(ab), __float_as_uint(cd), false, false);
  u32 r30 = r3[0], r31 = r3[1];
  return max3(__uint_as_float(r30), __uint_as_float(r31), ninf);
}
DEVI void bcast4(float t, float& a, float& b, float& c, float& d) {
  u32 tu = __float_as_uint(t);
  auto s1 = __builtin_amdgcn_permlane16_swap(tu, tu, false, false);
  u32 x = s1[0], y = s1[1];
  auto s2 = __builtin_amdgcn_permlane32_swap(x, x, false, false);
  auto s3 = __builtin_amdgcn_permlane32_swap(y, y, false, false);
  u32 s20 = s2[0], s21 = s2[1], s30 = s3[0], s31 = s3[1];
  a = __uint_as_float(s20); b = __uint_as_float(s21); c = __uint_as_float(s30); d = __uint_as_float(s31);
}

constexpr float LOG2E = 1.4426950408889634f;
constexpr u32 ONES = 0x3f803f80u;

struct HH {
  u32x4 f0, f1, f2, f3;
  u32x4 b0, b1;
  u32 bl;
  f32x4 T0, T1, TL, S;
  float a, b, c, d;
  f32x4 p0, q0, p1, q1;
  float l0, l1;
};
constexpr int HH_N = 106;
DEVI void swap32(float& a, float& b) {
  auto r = __builtin_amdgcn_permlane32_swap(__float_as_uint(a), __float_as_uint(b), false, false);
  u32 x = r[0], y = r[1]; a = __uint_as_float(x); b = __uint_as_float(y);
}
DEVI void swap16(float& a, float& b) {
  auto r = __builtin_amdgcn_permlane16_swap(__float_as_uint(a), __float_as_uint(b), false, false);
  u32 x = r[0], y = r[1]; a = __uint_as_float(x); b = __uint_as_float(y);
}
template <int I, bool QS>
DEVI void hh_step(HH& s, u32 lft, u32 phf, u32 sumf, bool lo, float ninf) {
  const f32x4 z = {0.f, 0.f, 0.f, 0.f};
  if constexpr (I == 0) {
    u32x2 x = lds_r64(lft); s.b0[2] = x[0]; s.b0[3] = x[1];
    asm volatile("" ::: "memory");
    u32x2 y = lds_r64(lft); s.b1[2] = y[0]; s.b1[3] = y[1];
    s.f0 = lds_r128(phf);
  } else if constexpr (I == 1) { s.f1 = lds_r128(phf + 1024); s.f2 = lds_r128(phf + 2048);
  } else if constexpr (I == 2) { s.f3 = lds_r128(phf + 3072);
  } else if constexpr (I == 3) { s.T0 = mfma32(s.f0, s.b0, z);
  } else if constexpr (I == 4) { s.TL = mfma32(s.f2, s.b0, z);
  } else if constexpr (I == 5) { s.T1 = mfma32(s.f1, s.b1, z);
  } else if constexpr (I == 6) { s.TL = mfma32(s.f3, s.b1, s.TL);
  } else if constexpr (I == 7) { s.f0 = lds_r128(sumf); s.f1 = lds_r128(sumf + 1024); s.f2 = lds_r128(sumf + 2048);
  } else if constexpr (I == 8) { s.a = max3(s.T0[0], s.T0[1], s.T0[2]);
  } else if constexpr (I == 9) { s.c = max3(-s.T0[0], -s.T0[1], -s.T0[2]);
  } else if constexpr (I == 10) { s.b = max3(s.T1[0], s.T1[1], s.T1[2]);
  } else if constexpr (I == 11) { s.d = max3(-s.T1[0], -s.T1[1], -s.T1[2]);
  } else if constexpr (I == 12) {
  } else if constexpr (I == 13) { s.a = max3(s.a, s.T0[3], s.TL[0]);
  } else if constexpr (I == 14) { s.b = max3(s.b, s.T1[3], s.TL[1]);
  } else if constexpr (I == 15) { s.c = max3(s.c, -s.T0[3], -s.TL[0]);
  } else if constexpr (I == 16) { s.d = max3(s.d, -s.T1[3], -s.TL[1]);
  } else if constexpr (I == 17) { swap32(s.a, s.b);
  } else if constexpr (I == 18) { swap32(s.c, s.d);
  } else if constexpr (I == 19) { s.a = max3(s.a, s.b, ninf);
  } else if constexpr (I == 20) { s.c = max3(s.c, s.d, ninf);
  } else if constexpr (I == 21) { swap16(s.a, s.c);
  } else if constexpr (I == 22) { s.a = max3(s.a, s.c, ninf);
  } else if constexpr (I == 23) { s.a *= -LOG2E;
  } else if constexpr (I == 24) { s.c = s.a; swap16(s.a, s.c);
  } else if constexpr (I == 25) { s.b = s.a; swap32(s.a, s.b);
  } else if constexpr (I == 26) { s.d = s.c; swap32(s.c, s.d);
  } else if constexpr (I == 27) { s.l0 = lo ? s.a : s.c;
  } else if constexpr (I == 28) { s.l1 = lo ? s.b : s.d;
  } else if constexpr (I == 29) { s.l0 = fmaf(s.TL[0], lo ? LOG2E : -LOG2E, s.l0);
  } else if constexpr (I == 30) { s.l1 = fmaf(s.TL[1], lo ? LOG2E : -LOG2E, s.l1);
  } else if constexpr (I == 31) { s.l0 = __builtin_amdgcn_exp2f(s.l0);
  } else if constexpr (I == 32) { s.l1 = __builtin_amdgcn_exp2f(s.l1);
  } else if constexpr (I == 33) { s.bl = pk(s.l0, s.l1);
  } else if constexpr (I == 34) { s.S = mfma32(s.f2, u32x4{s.bl, 0u, 0u, 0u}, z);
  } else if constexpr (I < 77) {
    constexpr int h = (I - 35) / 21, k = (I - 35) % 21;
    f32x4& T = h ? s.T1 : s.T0; f32x4& p = h ? s.p1 : s.p0; f32x4& q = h ? s.q1 : s.q0; u32x4& o = h ? s.b1 : s.b0;
    const float m = h ? s.b : s.a, n = h ? s.d : s.c;
    constexpr bool arg = k < 2 || ((k & 1) && k < 15);
    constexpr int e = arg ? (k < 2 ? k : (k + 1) / 2) : (k < 15 ? (k - 2) / 2 : 7);
    if constexpr (k < 16) {
      if constexpr (arg) { if constexpr (e < 4) p[e] = fmaf(T[e], LOG2E, m); else q[e - 4] = fmaf(T[e - 4], -LOG2E, n); }
      else { if constexpr (e < 4) p[e] = __builtin_amdgcn_exp2f(p[e]); else q[e - 4] = __builtin_amdgcn_exp2f(q[e - 4]); }
    } else if constexpr (k < 18) { o[k - 16] = pk(p[2 * (k - 16)], p[2 * (k - 16) + 1]);
    } else if constexpr (k < 20) { o[k - 16] = pk(q[2 * (k - 18)], q[2 * (k - 18) + 1]);
    } else { s.S = mfma32(h ? s.f1 : s.f0, o, s.S); }
  } else if constexpr (I == 77) {
  } else if constexpr (I == 78) { s.c = __builtin_amdgcn_rcpf(s.S[1]);
  } else if constexpr (I == 79) { s.d = __builtin_amdgcn_rcpf(s.S[3]);
  } else if constexpr (I == 80) { if constexpr (QS) s.c *= s.S[0]; else s.a = __builtin_amdgcn_rcpf(s.S[0]);
  } else if constexpr (I == 81) { if constexpr (QS) s.d *= s.S[2]; else s.b = __builtin_amdgcn_rcpf(s.S[2]);
  } else if constexpr (I < 106) {
    constexpr int v = (I - 82) / 6, k = (I - 82) % 6;
    if constexpr (!(QS && v >= 2)) {
      f32x4& x = v == 0 ? s.q0 : v == 1 ? s.q1 : v == 2 ? s.p0 : s.p1;
      u32x4& o = (v & 1) ? s.b1 : s.b0;
      const float r = v == 0 ? s.c : v == 1 ? s.d : v == 2 ? s.a : s.b;
      if constexpr (k < 4) x[k] *= r;
      else o[(v < 2 ? 2 : 0) + k - 4] = pk(x[2 * (k - 4)], x[2 * (k - 4) + 1]);
    }
  }
}
template <bool QS>
DEVI void hh_left(HH& s, bool lo) {
  if constexpr (QS) s.bl = pk(s.l0 * (lo ? 1.f : s.c), s.l1 * (lo ? 1.f : s.d));
  else s.bl = pk(s.l0 * (lo ? s.a : s.c), s.l1 * (lo ? s.b : s.d));
}

#define PRE 7
#define PRO 2
#define LEAD 8
#define QA 2
#define QT 1
#define E_PO 36
#define E_POB 30
#define QW 1
#define HQ0 4
#define HQS 8
#define C0 2
#define CS 3
#define C1 44
#define E_PUB 19
#define E_FLD 40
constexpr int EM = 36, EQ = 44, EH = 62, NEV = 70;
constexpr int Q_SLAB = 2;
constexpr int e_fld(int br) { return br ? EQ + 12 : E_FLD; }
constexpr int e_fchk(int br) { return br ? EQ + 16 : EQ; }
constexpr int clr_at(int E) { return E >= C1 && E < C1 + 3 ? 4 + E - C1 : E >= C0 && (E - C0) % CS == 0 && (E - C0) / CS < 4 ? (E - C0) / CS : -1; }
constexpr bool is_clr(int E) { return clr_at(E) >= 0; }
static_assert((E_PUB & 3) == 3 && E_PUB > C0 + 3 * CS && E_PUB < 35 && E_FLD >= EM && E_FLD < EQ && C1 > C0 + 3 * CS && (C1 < 34 || C1 >= EQ) && C1 < EQ + 6, "marks");
struct Sched {
  int op[NEV][3];
  int nop[NEV];
  int pos_ev[NEV + 1];
  int pos_hook[NEV];
  int last[36];
  int total;
};
constexpr int ext_ops(int br, int E) {
  return (E == (br ? E_POB : E_PO)) + (br && is_clr(E)) + (E == e_fld(br)) +
         (br ? 0 : E == EQ + Q_SLAB ? 2 : E == EQ + Q_L1 ? 4 : E == EQ + Q_L1C ? 1 : 0);
}
constexpr int op_chunk(int op) { return op < 200 ? (op - 100) >> 2 : op < 236 ? ((op - 200) >> 1) % 9 : (op - 200) >> 1; }
constexpr int op_earliest(int br, int op) {
  const int n = op_chunk(op);
  if (n < 9) return 4 * n - 1 - LEAD;
  if (n < 27) return n < 21 ? 23 : n < 25 ? 27 : 31;
  const int m = n - 27;
  if (br == 0) return m < 6 ? EQ + 3 : EQ + 6;
  return m < 2 ? 31 : m == 2 ? EQ : EQ + 2 * (m - 3) + 3;
}
constexpr int op_deadline(int br, int op) {
  const int n = op_chunk(op);
  if (n < 9) return 4 * n - 1;
  if (n < 27) return br == 0 || n == 18 ? EQ : EQ + 2 * (n - 18) - 1;
  return NEV;
}
static_assert(LEAD <= 12, "ring");
constexpr Sched make_sched(int br) {
  Sched s{};
  const int nx = br ? 3 : 2;
  int list[128] = {}, when[128] = {}, n = 0;
  for (int c = 0; c < 9; ++c) {
    for (int i = 0; i < nx; ++i) list[n++] = 100 + 4 * c + i;
    list[n++] = 200 + 2 * c; list[n++] = 201 + 2 * c; list[n++] = 218 + 2 * c; list[n++] = 219 + 2 * c;
  }
  for (int c = 18; c < 36; ++c) { list[n++] = 200 + 2 * c; list[n++] = 201 + 2 * c; }
  for (int k = 0; k < n; ++k) when[k] = 1000;
  int pos = 0, done = 0;
  for (; done < PRO * (nx + 4); ++done) { s.last[op_chunk(list[done])] = pos++; when[done] = -1; if (done == nx + 3) pos += br ? 1 : 2; }
  for (int E = 0; E < NEV; ++E) {
    s.pos_ev[E] = pos;
    int j = 0;
    for (int k = 0; k < n; ++k) {
      if (when[k] != 1000 || op_earliest(br, list[k]) > E) continue;
      const int c = op_chunk(list[k]);
      int q = E < EM ? (c < 9 ? QA : QT) : (E < EQ || E >= EH) && br ? 0 : ext_ops(br, E) ? 1 : 2;
      if (br == 0 && E >= EQ && QW == 1) q = ext_ops(br, E) ? 0 : 1;
      if (j >= q) break;
      s.op[E][j++] = list[k]; when[k] = E; ++done;
      s.last[c] = pos++;
      if (E >= op_deadline(br, list[k])) s.total = -1000;
    }
    s.nop[E] = j;
    s.pos_hook[E] = pos;
    pos += ext_ops(br, E);
  }
  s.pos_ev[NEV] = pos;
  s.total += done == n ? pos : -1000;
  return s;
}
template <int BR> struct SC { static constexpr Sched s = make_sched(BR); };
static_assert(SC<0>::s.total > 0 && SC<1>::s.total > 0, "schedule incomplete");
constexpr int imin(int a, int b) { return a < b ? a : b; }
template <int BR> constexpr int wait_n(int E, int c) {
  int n = SC<BR>::s.pos_ev[E] - SC<BR>::s.last[c] - 1;
  if (BR == 1 && E == E_PUB) n = imin(n, SC<BR>::s.pos_ev[E] - SC<BR>::s.pos_ev[C0 + 3 * CS + 1]);
  if (BR == 1 && E == EQ + 9) n = imin(n, SC<BR>::s.pos_ev[E] - SC<BR>::s.pos_ev[C1 + 3]);
  if (BR == 0 && E == EQ) n = imin(n, SC<BR>::s.pos_ev[E] - SC<BR>::s.pos_ev[E_FLD + 1]);
  return n;
}
template <int BR> constexpr int wait_h(int E, int E0) { return SC<BR>::s.pos_hook[E] - SC<BR>::s.pos_ev[E0 + 1]; }

#define STAMP(i)
#define RSTAMP(i)
#define STAMP_FLUSH()
#define STAMP_DECL

DEVI u64 next_tag(u32x2 e) {
  const u64 E = ((u64)(u32)__builtin_amdgcn_readfirstlane(e[1]) << 32) | (u32)__builtin_amdgcn_readfirstlane(e[0]);
  return E * 6364136223846793005ull + 1442695040888963407ull;
}
DEVI void poll_flags(GLB_AS u64* f, u64 tag) {
  for (int spin = 0; spin < (1 << 20); ++spin) {
    u64 v = __hip_atomic_load(f, __ATOMIC_RELAXED, __HIP_MEMORY_SCOPE_AGENT);
    if (__all(v == tag)) break;
    __builtin_amdgcn_s_sleep(1);
  }
  asm volatile("" ::: "memory");
}

template <int BR>
DEVI void body(const char* __restrict__ X, const char* __restrict__ W, const char* __restrict__ PHI, const char* __restrict__ WO, char* ws, char* out,
               int rb, int hq, int jx, u32 lb) {
  int tid = threadIdx.x;
  asm volatile("" : "+v"(tid));
  int l = tid & 63;
  const int w = __builtin_amdgcn_readfirstlane(tid >> 6);
  const int rg = w & 3, hpl = w >> 2;
  int c = l & 15, g = l >> 4, q = c >> 2, p = c & 3, g0 = g & 1, g1 = g >> 1;
  int lr = (l * 57) >> 9, lu = l - lr * 9;
  auto refresh = [&]() {
    asm volatile("" : "+v"(tid));
    l = tid & 63; c = l & 15; g = l >> 4; q = c >> 2; p = c & 3; g0 = g & 1; g1 = g >> 1;
    lr = (l * 57) >> 9; lu = l - lr * 9;
  };
  STAMP_DECL
  STAMP(0);
  RSTAMP(30);
  __amdgpu_buffer_rsrc_t srdX = __builtin_amdgcn_make_buffer_rsrc((void*)X, (short)0, 2048 * XROW, 0x00020000);
  __amdgpu_buffer_rsrc_t srdW = __builtin_amdgcn_make_buffer_rsrc((void*)W, (short)0, 576 * WROW, 0x00020000);
#define ROT (rb + 2 * hq) % 9
  const int rot = ROT;
  constexpr u64 M36 = (1ull << 36) - 1;

  u32 vx[2], vq[2], vh = 0;
#pragma unroll
  for (int i = 0; i < 2; ++i) {
    int row = 16 * w + 8 * i + (l >> 3);
    vx[i] = (u32)((rb * 128 + row) * XROW + (((l & 7) ^ ((row >> 1) & 7)) << 4));
  }
#pragma unroll
  for (int i = 0; i < 2; ++i) vq[i] = (u32)((8 * w + 4 * (lr & 1) + 2 * i + ((lr >> 1) & 1)) * WROW + hq * 144 + lu * 16);
  if (BR) {
    int row = 2 * w + ((l >> 3) & 1);
    int grow = rb * 128 - 16 + row; grow = grow < 0 ? 0 : grow;
    vh = (u32)(grow * XROW + (((l & 7) ^ ((row >> 1) & 7)) << 4));
  }
  auto kc_of = [&](int cc) { int k = cc + rot; return k >= 9 ? k - 9 : k; };
  __amdgpu_buffer_rsrc_t srdO = __builtin_amdgcn_make_buffer_rsrc((void*)WO, (short)0, 576 * XROW, 0x00020000);
  auto dma_op = [&](auto OP) {
    constexpr int op = OP.value;
    if constexpr (op < 200) {
      constexpr int cc = (op - 100) >> 2, i = op & 3;
      const int kc = kc_of(cc);
      if constexpr (i < 2) dma_full(lb + S0 + (cc & 3) * SS + w * 2048 + i * 1024, vx[i], srdX, (u32)(kc * 128));
      else dma_mask(lb + LHALO + (cc & 3) * 2048 + w * 256, vh, srdX, (u32)(kc * 128), 0xffffull);
    } else {
      constexpr int n = (op - 200) >> 1, i = op & 1;
      constexpr int at = n < 9 ? S0 + (n & 3) * SS + SK : n < 18 ? S0 + ((n - 9) & 3) * SS + SV : n < 27 ? WQ + (n - 18) * WU : wo_at(BR, n - 27);
      const u32 dst = lb + at + (2 * w + i) * 576;
      if constexpr (n < 27) {
        const int kc = kc_of(n % 9), sec = n < 9 ? 1152 : (n < 18 ? 2304 : 0);
        dma_mask(dst, vq[i], srdW, (u32)(kc * 64 * WROW + sec), M36);
      } else {
        const int hi = lr & 1, qq = 2 * i + ((lr >> 1) & 1);
        if constexpr (n > 27)
          dma_mask(dst, (u32)((36 * hpl + 20 * hi + 4 * rg + qq) * XROW + hq * 144 + lu * 16), srdO, (u32)(((n - 28 + hq) & 7) * 72 * XROW), M36);
        else dma_mask(dst, (u32)((36 * (8 * hpl + 2 * rg + hi) + 16 + qq) * XROW + hq * 144 + lu * 16), srdO, 0u, M36);
      }
    }
  };
  auto ev_dma = [&](auto EE, auto II) {
    if constexpr (II.value < SC<BR>::s.nop[EE.value]) dma_op(IC<SC<BR>::s.op[EE.value][II.value]>{});
  };
  auto warm_wo = [&]() {
    const int idx = w * 21 + l;
    const u32 po = l < 21 && idx < 162 ? (u32)((162 * jx + idx) * 128) : OOB;
    asm volatile("s_mov_b32 m0, %0\n\ts_nop 0\n\tbuffer_load_dword %1, %2, 0 offen lds" ::"s"(lb + LSCR), "v"(po), "s"(srdO) : "memory");
  };
  __amdgpu_buffer_rsrc_t srdF = __builtin_amdgcn_make_buffer_rsrc((void*)(ws + WS_OF), (short)0, 256 * 18 * 1024, 0x00020000);
  __amdgpu_buffer_rsrc_t srdS = __builtin_amdgcn_make_buffer_rsrc((void*)(ws + WS_SLAB), (short)0, 136 * SLAB, 0x00020000);
  auto mark = [&](auto EE) {
    constexpr int e = EE.value, f = (e + 2) % 3;
    const u32x4 sv = {SENT, SENT, SENT, SENT};
    const u32 ob = (u32)(((e > 3 ? 128 : 0) + rb * 8 + w) * 18 * 1024);
    if constexpr (e > 0 && f < 2) __builtin_amdgcn_raw_buffer_store_b128(sv, srdF, l * 16, ob + hq * 2048 + f * 1024, 16);
    if constexpr (e > 0 && f == 2) __builtin_amdgcn_raw_buffer_store_b128(sv, srdF, g == 0 ? c * 16 : OOB, ob + (16 + (hq >> 2)) * 1024 + (hq & 3) * 256, 16);
    if constexpr (e == 0) {
      const u32 so = tid < 400 ? (u32)((hq * 16 + rb) * SLAB + tid * 16) : tid < 425 ? (u32)((128 + hq) * SLAB + rb * 400 + (tid - 400) * 16) : OOB;
      __builtin_amdgcn_raw_buffer_store_b128(sv, srdS, so, 0, 16);
    }
  };
  GLB_AS u64* kflags = (GLB_AS u64*)(ws + WS_FLAG + hq * 256);
  GLB_AS u64* oflags = (GLB_AS u64*)(ws + WS_OFLAG + (BR * 16 + rb) * 256);
  u32x2 ep, fv;
  asm volatile("" ::"s"(srdX), "s"(srdW));
  STAMP(16);
  static_for<0, PRO>([&](auto cc) {
    constexpr int c_ = cc.value;
    dma_op(IC<100 + 4 * c_>{}); dma_op(IC<101 + 4 * c_>{});
    if constexpr (BR) dma_op(IC<102 + 4 * c_>{});
    dma_op(IC<200 + 2 * c_>{}); dma_op(IC<201 + 2 * c_>{}); dma_op(IC<218 + 2 * c_>{}); dma_op(IC<219 + 2 * c_>{});
    if constexpr (c_ == 0) {
      const char* ea = ws + WS_EPOCH;
      asm volatile("global_load_dwordx2 %0, %1, off sc1" : "=v"(ep) : "v"(ea) : "memory");
      if (BR == 0) {
        __amdgpu_buffer_rsrc_t srdP = __builtin_amdgcn_make_buffer_rsrc((void*)PHI, (short)0, 32 * 648, 0x00020000);
        dma_mask(lb + LPK + w * 336, (u32)(hq * 2592 + (w * 21 + l) * 16), srdP, 0, (1ull << 21) - 1);
      }
      STAMP(17);
    }
    if constexpr (c_ < PRO - 1) bar_only();
    if constexpr (c_ == 0) STAMP(18);
  });
  STAMP(1);

  const u32 xa0 = lb + S0 + (rg * 32 + hpl * 16 + c) * 128 + ((g ^ ((c >> 1) & 7)) << 4);
  const u32 xa1 = xa0 ^ 64;
  const u32 xb0 = xa0 + 2048 - hpl * 4096, xb1 = xb0 ^ 64;
  const u32 xh0 = lb + LHALO + c * 128 + ((g ^ ((c >> 1) & 7)) << 4);
  const u32 xh1 = xh0 ^ 64;
  const u32 wrow = lb + (16 * g1 + 8 * g0 + 2 * q) * 144;
  const u32 wa = wrow + hpl * 72 + 8 * p;
  const u32 wl = wrow + 32 + (p & 1) * 72 + (p >> 1) * (SV - SK);
  const u32 wh = wa + (rg & 1) * 40 + (rg >> 1) * (SV - SK);
  float ninf = -__builtin_inff();
  asm volatile("" : "+s"(ninf));

  u32x4 xf[18][2];
  const f32x4 z4 = {0.f, 0.f, 0.f, 0.f};
  f32x4 k0[2] = {z4, z4}, k1[2] = {z4, z4}, a0[2] = {z4, z4}, a1[2] = {z4, z4}, aL = z4, aH = z4, aHL = z4;
  u32x4 f0, f1, fl, fh, xh;
  auto load_kv_frags = [&](auto HH_, auto JJ) {
    constexpr int H = HH_.value, j = JJ.value, T = H >> 1, n = T >> 1, ks = T & 1;
    constexpr u32 ok = S0 + (n & 3) * SS + SK + ks * 32 * 144, ov = ok + SV - SK;
    if constexpr (H < 36) {
      if constexpr ((H & 1) == 0) {
        if constexpr (j == 0) { xf[T][0] = lds_r128((ks ? xa1 : xa0) + (n & 3) * SS); f0 = frag_tr(wa + ok, wa + ok + 144); }
        if constexpr (j == 1) { xf[T][1] = lds_r128((ks ? xb1 : xb0) + (n & 3) * SS); f1 = frag_tr(wa + ok + 40, wa + ok + 184); }
      } else {
        if constexpr (j == 0) f0 = frag_tr(wa + ov, wa + ov + 144);
        if constexpr (j == 1) f1 = frag_tr(wa + ov + 40, wa + ov + 184);
        if constexpr (j == 2) fl = frag_tr(wl + ok, wl + ok + 144);
        if constexpr (j == 3 && BR == 1) { fh = frag_tr(wh + ok, wh + ok + 144); xh = lds_r128((ks ? xh1 : xh0) + (n & 3) * 2048); }
      }
    }
  };
  wait_bar<wait_n<BR>(0, 0)>();
  STAMP(2);
  load_kv_frags(IC<0>{}, IC<0>{}); load_kv_frags(IC<0>{}, IC<1>{});
  u64 tag = 0;
  static_for<0, 36>([&](auto hh_) {
    constexpr int H = hh_.value, T = H >> 1;
    if constexpr ((H & 3) == 3 && H < 35) {
      wait_bar<wait_n<BR>(H, (H + 1) >> 2)>();
    }
    u32x4 c0 = f0, c1 = f1, cl = fl, ch = fh, cx = xh;
    f32x4(&d0)[2] = (H & 1) ? a0 : k0;
    f32x4(&d1)[2] = (H & 1) ? a1 : k1;
    SCHED_FENCE();
    d0[0] = mfma32(c0, xf[T][0], d0[0]);
    load_kv_frags(IC<H + 1>{}, IC<0>{});
    SCHED_FENCE();
    d0[1] = mfma32(c0, xf[T][1], d0[1]);
    load_kv_frags(IC<H + 1>{}, IC<1>{});
    ev_dma(hh_, IC<0>{});
    SCHED_FENCE();
    d1[0] = mfma32(c1, xf[T][0], d1[0]);
    load_kv_frags(IC<H + 1>{}, IC<2>{});
    SCHED_FENCE();
    d1[1] = mfma32(c1, xf[T][1], d1[1]);
    load_kv_frags(IC<H + 1>{}, IC<3>{});
    ev_dma(hh_, IC<1>{});
    ev_dma(hh_, IC<2>{});
    SCHED_FENCE();
    if constexpr (H & 1) {
      aL = mfma32(cl, xf[T][0], aL);
      asm volatile("" : "+v"(aL));
      if constexpr (BR == 1) {
        aH = mfma32(ch, cx, aH); aHL = mfma32(cl, cx, aHL);
        asm volatile("" : "+v"(aH), "+v"(aHL));
      }
    }
    if constexpr (H == (BR ? E_POB : E_PO)) warm_wo();
    if constexpr (H == 3) {
      asm volatile("" : "+v"(ep));
      tag = next_tag(ep);
    }
    if constexpr (BR == 1 && is_clr(H)) mark(IC<clr_at(H)>{});
    if constexpr (BR == 1 && H == E_PUB) {
      if (tid < 2) __hip_atomic_store(tid == 0 ? kflags + rb : oflags + hq - 512, tag, __ATOMIC_RELAXED, __HIP_MEMORY_SCOPE_AGENT);
    }
    if constexpr (BR == 0 && H == 5) {
      const int h = rg & 1, lf = rg >> 1;
      const int f = lf ? 16 + (q & 1) : c;
      const bool rowok = lf ? (p == h) : true;
      const u32 pb = lb + LPK + (hpl * 2 + h) * 648 + f * 2;
      const int d0 = 2 * h + 4 * g;
      u32 e0 = lds_r16(pb + (d0 + 0) * 36), e1 = lds_r16(pb + (d0 + 1) * 36), e2 = lds_r16(pb + (d0 + 2) * 36), e3 = lds_r16(pb + (d0 + 3) * 36);
      const int dl = h ? 0 : 16;
      u32 l0 = lds_r16(pb + dl * 36), l1 = lds_r16(pb + (dl + 1) * 36);
      u32 lw = (g == 0) ? (l0 | (l1 << 16)) : 0u;
      u32x4 fr = {e0 | (e1 << 16), e2 | (e3 << 16), h ? 0u : lw, h ? lw : 0u};
      if (!rowok) fr = u32x4{0, 0, 0, 0};
      lds_w128(lb + LPHF + (hpl * 4 + rg) * 1024 + l * 16, fr);
      if (w < 3) {
        u32 a = (p == 2 * w) ? ONES : 0u, b = (p == 2 * w + 1) ? ONES : 0u;
        u32 x = ((p & 1) == g1) ? ((p >> 1) ? 0x3f800000u : 0x00003f80u) : 0u;
        lds_w128(lb + LSUM + w * 1024 + l * 16, w < 2 ? u32x4{a, a, b, b} : u32x4{x, 0u, 0u, 0u});
      }
    }
    SCHED_FENCE();
  });
  STAMP(3);
  refresh();
  const u32 wqa = lb + WQ + (16 * g1 + 8 * g0 + 2 * q) * 144 + hpl * 72 + 8 * p;
  const u32 wql = lb + WQ + (16 * g1 + 8 * g0 + 2 * q) * 144 + 32 + (p & 1) * 72;
  auto load_q_frags = [&](auto TT, auto JJ) {
    constexpr int t = TT.value, j = JJ.value;
    constexpr u32 o = (t >> 1) * WU + (t & 1) * 32 * 144;
    if constexpr (t < 18) {
      if constexpr (j == 0) f0 = frag_tr(wqa + o, wqa + o + 144);
      if constexpr (j == 1) f1 = frag_tr(wqa + o + 40, wqa + o + 184);
      if constexpr (j == 2) fl = frag_tr(wql + o, wql + o + 144);
    }
  };
  if constexpr (BR == 1) {
    wait_bar<wait_n<BR>(EQ, 18)>();
    static_for<0, 3>([&](auto j) { load_q_frags(IC<0>{}, j); });
  }
  u32x2 kM[2][2];
#pragma unroll
  for (int sb = 0; sb < 2; ++sb) { kM[0][sb] = pk4(k0[sb]); kM[1][sb] = pk4(k1[sb]); }
  auto unswap = [&](u32x2& a, u32x2& b) {
    u32x2 x = a, y = b;
#pragma unroll
    for (int e = 0; e < 2; ++e) { a[e] = hpl ? y[e] : x[e]; b[e] = hpl ? x[e] : y[e]; }
  };
  {
    constexpr int VROW0 = BR ? 16 : 0;
    auto left = [&](f32x4 v, int blk) {
      u32x2 x = pk4(v);
      const u32 va = lb + lvi(BR) + (VROW0 - 16 + blk * 16 + c) * VIS + g0 * 80 + 64;
      lds_w64(g1 ? va : lb + KLX + (blk * 2 + g0) * 128 + c * 8, u32x2{x[0], g1 ? ONES : x[1]});
      lds_w64(g1 ? va + 8 : lb + LSCR + 256 + c * 8, u32x2{x[1], ONES});
    };
    left(aL, 1 + 2 * rg + hpl);
#pragma unroll
    for (int sb = 0; sb < 2; ++sb) {
      const u32 va = lb + lvi(BR) + (VROW0 + rg * 32 + (sb ^ hpl) * 16 + c) * VIS + hpl * 80;
      lds_w64(va + 8 * g, pk4(a0[sb]));
      lds_w64(va + 32 + 8 * g, pk4(a1[sb]));
    }
    if constexpr (BR == 1) {
      unswap(kM[0][0], kM[0][1]); unswap(kM[1][0], kM[1][1]);
      if (w == 2) left(aHL, 0);
      const u32 kx = lb + LKX + l * 8;
      lds_w64(rg < 2 ? kx + (hpl * 2 + rg) * 512 : lb + lvi(BR) + c * VIS + hpl * 80 + (rg & 1) * 32 + 8 * g, pk4(aH));
      if (rg < 3) {
        const u32 b = kx + (((rg + 1) * 2 + hpl) * 2) * 512;
        lds_w64(b, kM[0][1]); lds_w64(b + 512, kM[1][1]);
      }
    }
  }
  const u32 phf = lb + LPHF + hpl * 4096 + l * 16, sumf = lb + LSUM + l * 16;
  const bool lo = g < 2;
  auto load_flags = [&]() {
    GLB_AS u64* fa = (BR == 0 && g == 0) ? kflags + c : oflags + (l & 7);
    asm volatile("global_load_dwordx2 %0, %1, off sc1" : "=v"(fv) : "v"(fa) : "memory");
  };
  auto check_flags = [&]() {
    if (!__all((((u64)fv[1] << 32) | fv[0]) == tag)) poll_flags((BR == 0 && g == 0) ? kflags + c : oflags + (l & 7), tag);
  };
  if constexpr (BR == 0) {
    bar_lds();
    STAMP(4);
    HH h0, h1;
    h0.b0[0] = kM[0][0][0]; h0.b0[1] = kM[0][0][1]; h0.b1[0] = kM[1][0][0]; h0.b1[1] = kM[1][0][1];
    h1.b0[0] = kM[0][1][0]; h1.b0[1] = kM[0][1][1]; h1.b1[0] = kM[1][1][0]; h1.b1[1] = kM[1][1][1];
    const u32 kl0 = lb + KLX + ((1 + 2 * rg + hpl) * 2 + hpl) * 128 + c * 8, kl1 = lb + KLX + ((2 + 2 * rg - hpl) * 2 + hpl) * 128 + c * 8;
    static_for<0, HH_N>([&](auto st) {
      if constexpr (st.value >= 8 && (st.value - 8) % 12 == 0 && (st.value - 8) / 12 < EQ - EM) {
        constexpr int E = EM + (st.value - 8) / 12;
        SCHED_FENCE();
        ev_dma(IC<E>{}, IC<0>{}); ev_dma(IC<E>{}, IC<1>{});
        if constexpr (E == E_PO) warm_wo();
        if constexpr (E == E_FLD) load_flags();
        SCHED_FENCE();
      }
      hh_step<st.value, false>(h0, kl0, phf, sumf, lo, ninf);
      hh_step<st.value, false>(h1, kl1, phf, sumf, lo, ninf);
    });
#pragma unroll
    for (int sb = 0; sb < 2; ++sb) {
      HH& hs = sb ? h1 : h0;
      hh_left<false>(hs, lo);
      const u32 pa = lb + LPK + (rg * 32 + (sb ^ hpl) * 16 + c) * PKS + hpl * 144;
      lds_w64(pa + 8 * g, u32x2{hs.b0[0], hs.b0[1]});
      lds_w64(pa + 32 + 8 * g, u32x2{hs.b0[2], hs.b0[3]});
      lds_w64(pa + 64 + 8 * g, u32x2{hs.b1[0], hs.b1[1]});
      lds_w64(pa + 96 + 8 * g, u32x2{hs.b1[2], hs.b1[3]});
      lds_w32(pa + 128 + 4 * g, hs.bl);
    }
    STAMP(5);
    wait_bar<wait_n<BR>(EQ, 26)>();
    asm volatile("" : "+v"(fv));
    static_for<0, 3>([&](auto j) { load_q_frags(IC<0>{}, j); });
  }
  refresh();
  f32x4 dp = z4, dm = z4, df = z4;
  u32x4 ap[4], am[4], af[4], bv[4];
  u32 g0_, g1_, g2_, g3_;
  const int rr = 4 * (4 * g0 + q) + 2 * g1;
  const u32 pa = lb + LPK + rr * PKS + hpl * 144 + (rg & 1) * 64 + 8 * p;
  const u32 pf = lb + LPK + rr * PKS + hpl * 144 + 128 + 8 * (p >> 1);
  const u32 va = lb + lvi(BR) + rr * VIS + hpl * 80 + (rg < 2 ? rg * 32 + 8 * p : 64 + 8 * (p & 1));
  auto hook = [&](auto tt, auto jj) {
    constexpr int t = tt.value, E = EQ + t, j = jj.value;
    if constexpr (BR == 0 && j < 4) {
      if constexpr (t == Q_SLAB - 2) {
        ap[j] = frag_tr(pa + j * 32 * PKS, pa + j * 32 * PKS + PKS);
        am[j] = frag_tr(pa + 32 + j * 32 * PKS, pa + 32 + j * 32 * PKS + PKS);
        af[j] = frag_tr(pf + j * 32 * PKS, pf + j * 32 * PKS + PKS);
        bv[j] = frag_tr(va + j * 32 * VIS, va + j * 32 * VIS + VIS);
      }
      if constexpr (t == Q_SLAB - 1) {
        dp = mfma32(ap[j], bv[j], dp); dm = mfma32(am[j], bv[j], dm); df = mfma32(af[j], bv[j], df);
        asm volatile("" : "+v"(dp), "+v"(dm), "+v"(df));
      }
    }
    if constexpr (j != 4) return;
    if constexpr (BR == 1 && is_clr(E)) mark(IC<clr_at(E)>{});
    if constexpr (BR == 1 && t == 9) {
      if (tid == 0) __hip_atomic_store(oflags + hq, tag, __ATOMIC_RELAXED, __HIP_MEMORY_SCOPE_AGENT);
    }
    if constexpr (BR == 1 && E == e_fld(BR)) load_flags();
    if constexpr (E == e_fchk(BR)) {
      if constexpr (BR == 1) asm volatile("s_waitcnt vmcnt(%1)" : "+v"(fv) : "n"(wait_h<BR>(E, e_fld(BR))) : "memory");
      check_flags();
    }
    if constexpr (BR == 0) {
      if constexpr (t == Q_SLAB) {
        const u32 slab = (u32)((hq * 16 + rb) * SLAB);
        const bool h0set = (0x47 >> (c & 7)) & 1, h = rg & 1, own = (c >> 3) == hpl;
        u32x2 a = pk4(dp), b = pk4(dm);
        const u32 o1 = rg < 2 ? slab + (hpl * 2 + rg) * 1024 + l * 16 : (own && h0set != h) ? slab + 4096 + l * 16 : OOB;
        __builtin_amdgcn_raw_buffer_store_b128(u32x4{ns(a[0]), ns(a[1]), ns(b[0]), ns(b[1])}, srdS, o1, 0, 16);
        const float x0 = g0 ? df[2] : df[0], x1 = g0 ? df[3] : df[1];
        const bool first = rg < 2 ? !h : h0set;
        const u32 o2 = rg < 2 ? slab + 5120 + (hpl * 2 + rg) * 256 + l * 4 : own ? slab + 6144 + l * 4 : OOB;
        __builtin_amdgcn_raw_buffer_store_b32(ns(pk(first ? x0 : 0.f, first ? 0.f : x1)), srdS, o2, 0, 16);
        STAMP(6);
      }
      if constexpr (t == Q_L1 || t == Q_L1C) {
        const int dw = w * 16 + c;
        const u32 dof = (u32)((rb * 100 + (dw < 100 ? dw : 99)) * 4);
        const u32 go = (u32)((hq * 16 + 4 * g) * SLAB) + dof;
        if constexpr (t == Q_L1) {
          asm volatile("buffer_load_dword %0, %4, %5, 0 offen sc1\n\tbuffer_load_dword %1, %4, %5, %6 offen sc1\n\t"
                       "buffer_load_dword %2, %4, %5, %7 offen sc1\n\tbuffer_load_dword %3, %4, %5, %8 offen sc1"
                       : "=&v"(g0_), "=&v"(g1_), "=&v"(g2_), "=&v"(g3_)
                       : "v"(go), "s"(srdS), "s"(SLAB), "s"(2 * SLAB), "s"(3 * SLAB)
                       : "memory");
        } else {
          asm volatile("s_waitcnt vmcnt(%4)" : "+v"(g0_), "+v"(g1_), "+v"(g2_), "+v"(g3_) : "n"(wait_h<BR>(E, EQ + Q_L1)) : "memory");
          _Pragma("nounroll") for (int spin = 0; spin < (1 << 20); ++spin) {
            asm volatile("" ::: "memory");
            if (!__any(umax(umax3(g0_, g1_, g2_), g3_) == SENT)) break;
            g0_ = __builtin_amdgcn_raw_buffer_load_b32(srdS, go, 0, 16);
            g1_ = __builtin_amdgcn_raw_buffer_load_b32(srdS, go, SLAB, 16);
            g2_ = __builtin_amdgcn_raw_buffer_load_b32(srdS, go, 2 * SLAB, 16);
            g3_ = __builtin_amdgcn_raw_buffer_load_b32(srdS, go, 3 * SLAB, 16);
          }
          const u32 sel = c == 0 ? 0x00003f80u : c == 1 ? 0x3f800000u : 0u;
          f32x4 sum = mfma32(u32x4{sel, sel, sel, sel}, u32x4{g0_, g1_, g2_, g3_}, z4);
          __builtin_amdgcn_raw_buffer_store_b32(ns(pk(sum[0], sum[1])), srdS, g == 0 ? (u32)((128 + hq) * SLAB) + dof : OOB, 0, 16);
          STAMP(7);
        }
      }
    }
  };
  a0[0] = z4; a0[1] = z4; a1[0] = z4; a1[1] = z4; aL = z4;
  static_for<0, 18>([&](auto tt) {
    constexpr int t = tt.value, E = EQ + t;
    if constexpr (BR == 0) {
      if constexpr (t == 3 || t == 6) bar_only();
    } else if constexpr ((t & 1) && t < 17) {
      if constexpr (t == 1) wait_bar<wait_n<BR>(E, 18 + (t >> 1) + 1)>();
      else wait_vm_bar<wait_n<BR>(E, 18 + (t >> 1) + 1)>();
    }
    u32x4 c0 = f0, c1 = f1, cl = fl;
    SCHED_FENCE();
    a0[0] = mfma32(c0, xf[t][0], a0[0]);
    load_q_frags(IC<t + 1>{}, IC<0>{});
    hook(tt, IC<0>{});
    SCHED_FENCE();
    a0[1] = mfma32(c0, xf[t][1], a0[1]);
    load_q_frags(IC<t + 1>{}, IC<1>{});
    hook(tt, IC<1>{});
    ev_dma(IC<E>{}, IC<0>{});
    SCHED_FENCE();
    a1[0] = mfma32(c1, xf[t][0], a1[0]);
    load_q_frags(IC<t + 1>{}, IC<2>{});
    hook(tt, IC<2>{});
    SCHED_FENCE();
    a1[1] = mfma32(c1, xf[t][1], a1[1]);
    hook(tt, IC<3>{});
    ev_dma(IC<E>{}, IC<1>{});
    SCHED_FENCE();
    aL = mfma32(cl, xf[t][0], aL);
    asm volatile("" : "+v"(aL));
    hook(tt, IC<4>{});
    SCHED_FENCE();
  });
  STAMP(8);
  refresh();
  if (g < 2) lds_w64(lb + QLX + ((2 * rg + hpl) * 2 + g) * 128 + c * 8, pk4(aL));
  bar_lds();
  const u32 ofo = (u32)((BR * 128 + rb * 8 + rg * 2) * 18 * 1024);
  const int hp = hq * 2 + hpl;
  auto store_o = [&](int sb, f32x4 o0, f32x4 o1, float l0, float l1, float r0, float r1) {
    u32x2 a = pk4(o0 * r0), b = pk4(o1 * r1);
    __builtin_amdgcn_raw_buffer_store_b128(u32x4{ns(a[0]), ns(a[1]), ns(b[0]), ns(b[1])}, srdF, ofo + (sb * 18 + hp) * 1024 + l * 16, 0, 16);
    float rr = g0 ? r1 : r0;
    u32 lv = ns(pk(l0 * rr, l1 * rr));
    auto sw = __builtin_amdgcn_permlane16_swap(lv, lv, false, false);
    u32 s0 = sw[0], s1 = sw[1];
    __builtin_amdgcn_raw_buffer_store_b64(u32x2{s0, s1}, srdF, g == 0 ? ofo + (sb * 18 + 16 + (hq >> 2)) * 1024 + (c + 16 * (hq & 3)) * 16 + 8 * hpl : OOB, 0, 16);
  };

  if (BR == 0) {
    HH h0, h1;
    {
      u32x2 qa0 = pk4(a0[0]), qb0 = pk4(a1[0]), qa1 = pk4(a0[1]), qb1 = pk4(a1[1]);
      h0.b0[0] = qa0[0]; h0.b0[1] = qa0[1]; h0.b1[0] = qb0[0]; h0.b1[1] = qb0[1];
      h1.b0[0] = qa1[0]; h1.b0[1] = qa1[1]; h1.b1[0] = qb1[0]; h1.b1[1] = qb1[1];
    }
    const u32 ql0 = lb + QLX + ((2 * rg + hpl) * 2 + hpl) * 128 + c * 8, ql1 = lb + QLX + ((2 * rg + 1 - hpl) * 2 + hpl) * 128 + c * 8;
    const u32 kf = (u32)((128 + hq) * SLAB);
    const int l8 = g * 16 + (c & 7) + 8 * hpl;
    u32x4 AM0, AM1, AMLm;
    u32 AL0, AL1, ALL;
    auto load_kv = [&]() {
      AM0 = __builtin_amdgcn_raw_buffer_load_b128(srdS, kf + hpl * 2048 + l * 16, 0, 16);
      AM1 = __builtin_amdgcn_raw_buffer_load_b128(srdS, kf + hpl * 2048 + 1024 + l * 16, 0, 16);
      AMLm = __builtin_amdgcn_raw_buffer_load_b128(srdS, kf + 4096 + l8 * 16, 0, 16);
      AL0 = __builtin_amdgcn_raw_buffer_load_b32(srdS, kf + 5120 + hpl * 512 + l * 4, 0, 16);
      AL1 = __builtin_amdgcn_raw_buffer_load_b32(srdS, kf + 5120 + hpl * 512 + 256 + l * 4, 0, 16);
      ALL = __builtin_amdgcn_raw_buffer_load_b32(srdS, kf + 6144 + l8 * 4, 0, 16);
    };
    static_for<0, HH_N>([&](auto st) {
      if (st.value == L2ST) { SCHED_FENCE(); load_kv(); SCHED_FENCE(); }
      if constexpr (st.value >= HQ0 && (st.value - HQ0) % HQS == 0 && (st.value - HQ0) / HQS < NEV - EH) {
        constexpr int E = EH + (st.value - HQ0) / HQS;
        if constexpr (SC<BR>::s.nop[E] > 0) { SCHED_FENCE(); ev_dma(IC<E>{}, IC<0>{}); SCHED_FENCE(); }
      }
      hh_step<st.value, true>(h0, ql0, phf, sumf, lo, ninf);
      hh_step<st.value, true>(h1, ql1, phf, sumf, lo, ninf);
    });
    hh_left<true>(h0, lo);
    hh_left<true>(h1, lo);
    STAMP(9);
    _Pragma("nounroll") for (int spin = 0; spin < (1 << 20); ++spin) {
      asm volatile("" ::: "memory");
      if (!__any(umax4(umax4(umax4(umax3(AL0, AL1, ALL), AM0), AM1), AMLm) == SENT)) break;
      load_kv();
    }
    STAMP(10);
    const bool h0set = (0x47 >> (c & 7)) & 1;
    u32x4 AML0, AML1;
#pragma unroll
    for (int e = 0; e < 4; ++e) { AML0[e] = h0set ? AMLm[e] : 0u; AML1[e] = h0set ? 0u : AMLm[e]; }
#pragma unroll
    for (int sb = 0; sb < 2; ++sb) {
      const HH& h = sb ? h1 : h0;
      u32x4 bl = {h.bl, 0u, 0u, 0u};
      f32x4 O0 = mfma32(u32x4{AL0, 0u, 0u, 0u}, bl, mfma32(AM0, h.b0, z4));
      f32x4 O1 = mfma32(u32x4{AL1, 0u, 0u, 0u}, bl, mfma32(AM1, h.b1, z4));
      f32x4 OL = mfma32(u32x4{ALL, 0u, 0u, 0u}, bl, mfma32(AML1, h.b1, mfma32(AML0, h.b0, z4)));
      float r0 = __builtin_amdgcn_rcpf(fmaf(h.S[0], 1e-6f, OL[2])), r1 = __builtin_amdgcn_rcpf(fmaf(h.S[2], 1e-6f, OL[3]));
      store_o(sb ^ hpl, O0, O1, OL[0], OL[1], r0, r1);
    }
  } else {
    const u32 kx = lb + LKX + ((rg * 2 + hpl) * 2) * 512 + l * 8;
    u32x2 pM0 = lds_r64(kx), pM1 = lds_r64(kx + 512);
    u32x2 kL[3];
#pragma unroll
    for (int i = 0; i < 3; ++i) kL[i] = lds_r64(lb + KLX + ((2 * rg + i) * 2 + hpl) * 128 + c * 8);
    const int par = c & 1;
    const bool ina = (4 * g + par) > c, inb = (4 * g + par + 2) > c;
    const float sc = 0.23570226039551584f * LOG2E;
    float la[2][2], lbv[2][2], mx[2][2];
    u32x2 qM[2][2] = {{pk4(a0[0]), pk4(a0[1])}, {pk4(a1[0]), pk4(a1[1])}};
    unswap(qM[0][0], qM[0][1]); unswap(qM[1][0], qM[1][1]);
#pragma unroll
    for (int sb = 0; sb < 2; ++sb) {
      u32x2 qlv = lds_r64(lb + QLX + ((2 * rg + sb) * 2 + hpl) * 128 + c * 8);
      u32x2 q0 = qM[0][sb], q1 = qM[1][sb];
      u32x4 b0 = {q0[0], q0[1], g == 0 ? qlv[0] : 0u, 0u};
      u32x4 b1 = {q1[0], q1[1], 0u, g == 0 ? qlv[1] : 0u};
      u32x2 kp0 = sb ? kM[0][0] : pM0, kp1 = sb ? kM[1][0] : pM1, kpl = kL[sb], kcl = kL[sb + 1];
      f32x4 sp0 = mfma32(u32x4{kp0[0], kp0[1], kpl[0], kpl[1]}, b0, z4);
      f32x4 sp1 = mfma32(u32x4{kp1[0], kp1[1], kpl[0], kpl[1]}, b1, z4);
      f32x4 sc0 = mfma32(u32x4{kM[0][sb][0], kM[0][sb][1], kcl[0], kcl[1]}, b0, z4);
      f32x4 sc1 = mfma32(u32x4{kM[1][sb][0], kM[1][sb][1], kcl[0], kcl[1]}, b1, z4);
      const bool dead = (sb == 0) && (rb == 0) && (rg == 0);
      float pa0 = par ? sp0[1] : sp0[0], pb0 = par ? sp0[3] : sp0[2], pa1 = par ? sp1[1] : sp1[0], pb1 = par ? sp1[3] : sp1[2];
      if (dead) { pa0 = pb0 = pa1 = pb1 = -1e30f; }
      la[0][sb] = (ina ? pa0 : (par ? sc0[1] : sc0[0])) * sc;
      lbv[0][sb] = (inb ? pb0 : (par ? sc0[3] : sc0[2])) * sc;
      la[1][sb] = (ina ? pa1 : (par ? sc1[1] : sc1[0])) * sc;
      lbv[1][sb] = (inb ? pb1 : (par ? sc1[3] : sc1[2])) * sc;
      mx[0][sb] = fmaxf(la[0][sb], lbv[0][sb]);
      mx[1][sb] = fmaxf(la[1][sb], lbv[1][sb]);
    }
    float tm = redmax4(mx[0][0], mx[0][1], mx[1][0], mx[1][1], ninf);
    bcast4(tm, mx[0][0], mx[0][1], mx[1][0], mx[1][1]);
    const u32 vb = lb + lvi(BR) + (rg * 32 + 4 * g + q) * VIS + hpl * 80;
#pragma unroll
    for (int sb = 0; sb < 2; ++sb) {
      const u32 v0 = vb + sb * 16 * VIS, v1 = v0 + 16 * VIS;
      u32x4 A0 = frag_tr(v0 + 8 * p, v1 + 8 * p), A1 = frag_tr(v0 + 32 + 8 * p, v1 + 32 + 8 * p);
      u32x4 AL = frag_tr(v0 + 64 + 8 * (p & 1), v1 + 64 + 8 * (p & 1));
      u32x4 P[2];
#pragma unroll
      for (int h = 0; h < 2; ++h) {
        float ea = __builtin_amdgcn_exp2f(la[h][sb] - mx[h][sb]), eb = __builtin_amdgcn_exp2f(lbv[h][sb] - mx[h][sb]);
        u32 wa_ = par ? pk(0.f, ea) : pk(ea, 0.f), wb_ = par ? pk(0.f, eb) : pk(eb, 0.f);
        P[h] = u32x4{ina ? wa_ : 0u, inb ? wb_ : 0u, ina ? 0u : wa_, inb ? 0u : wb_};
      }
      f32x4 O0 = mfma32(A0, P[0], z4), O1 = mfma32(A1, P[1], z4);
      f32x4 L0 = mfma32(AL, P[0], z4), L1 = mfma32(AL, P[1], z4);
      float r0 = __builtin_amdgcn_rcpf(L0[2]), r1 = __builtin_amdgcn_rcpf(L1[2]);
      store_o(sb, O0, O1, g0 ? L1[0] : L0[0], g0 ? L1[1] : L0[1], r0, r1);
    }
  }
  STAMP(11);
  refresh();
  wait_bar<4>();
  STAMP(12);
  const u32 wrow2 = lb + (16 * g1 + 8 * g0 + 2 * q) * 144;
  u32 oa[3], oa4[3];
#pragma unroll
  for (int i = 0; i < 3; ++i) {
    oa[i] = wrow2 + 8 * p + i * 57344; oa4[i] = wrow2 + 128 + 8 * (p & 1) + i * 57344;
    asm volatile("" : "+v"(oa[i]), "+v"(oa4[i]));
  }
  u32x4 wf[18][5], of[18];
  auto load_wf = [&](auto TT, auto II) {
    constexpr int t = TT.value, i = II.value;
    if constexpr (t < 18) {
      constexpr u32 cb = wo_at(BR, t >> 1) + (t & 1) * 32 * 144, s = cb / 57344, o = cb % 57344;
      if constexpr (i < 4) wf[t][i] = frag_tr(oa[s] + o + 32 * i, oa[s] + o + 144 + 32 * i);
      else wf[t][4] = frag_tr(oa4[s] + o, oa4[s] + o + 144);
    }
  };
  const u32 ofl = (u32)((BR * 128 + rb * 8 + w) * 18 * 1024 + l * 16);
  f32x4 acc[5];
  _Pragma("nounroll") for (int pass = 0; pass < (1 << 16); ++pass) {
    of[0] = __builtin_amdgcn_raw_buffer_load_b128(srdF, ofl, 16 * 1024, 16);
    of[1] = __builtin_amdgcn_raw_buffer_load_b128(srdF, ofl, 17 * 1024, 16);
    SCHED_FENCE();
    static_for<0, PRE * 5>([&](auto t) { load_wf(IC<t.value / 5>{}, IC<t.value % 5>{}); });
    SCHED_FENCE();
    _Pragma("nounroll") for (int spin = 0; spin < (1 << 20); ++spin) {
      asm volatile("" ::: "memory");
      if (!__any(umax4(umax4(0u, of[0]), of[1]) == SENT)) break;
      of[0] = __builtin_amdgcn_raw_buffer_load_b128(srdF, ofl, 16 * 1024, 16);
      of[1] = __builtin_amdgcn_raw_buffer_load_b128(srdF, ofl, 17 * 1024, 16);
    }
    STAMP(13);
#pragma unroll
    for (int t = 2; t < 18; ++t) of[t] = __builtin_amdgcn_raw_buffer_load_b128(srdF, ofl, ((t - 2 + 2 * hq) & 15) * 1024, 16);
    SCHED_FENCE();
    u32 seen = 0;
    static_for<0, 18>([&](auto tt) {
      constexpr int t = tt.value;
      static_for<0, 5>([&](auto ii) {
        constexpr int i = ii.value;
        acc[i] = mfma32(wf[t][i], of[t], t ? acc[i] : z4);
        if constexpr (i == 1 && t >= 2) seen = umax4(seen, of[t]);
        load_wf(IC<t + PRE>{}, ii);
        SCHED_FENCE();
      });
    });
    if (!__any(seen == SENT)) break;
  }
  STAMP(14);
  if (BR == 0 && rb == 0 && hq == 0 && tid == 0) __hip_atomic_store((GLB_AS u64*)(ws + WS_EPOCH), tag, __ATOMIC_RELAXED, __HIP_MEMORY_SCOPE_AGENT);
  char* op = out + (size_t)(rb * 128 + w * 16 + c) * 2304 + BR * 1152 + hq * 144;
#pragma unroll
  for (int i = 0; i < 2; ++i) {
    u32x2 a = pk4(acc[2 * i]), b = pk4(acc[2 * i + 1]);
    auto s0 = __builtin_amdgcn_permlane16_swap(a[0], b[0], false, false);
    auto s1 = __builtin_amdgcn_permlane16_swap(a[1], b[1], false, false);
    u32 s00 = s0[0], s01 = s0[1], s10 = s1[0], s11 = s1[1];
    *(u32x4*)(op + 64 * i + 32 * g0 + 16 * g1) = u32x4{s00, s10, s01, s11};
  }
  if (g < 2) *(u32x2*)(op + 128 + 8 * g) = pk4(acc[4]);
  STAMP(15);
  RSTAMP(31);
  STAMP_FLUSH();
  __builtin_amdgcn_endpgm();
}

__global__ __launch_bounds__(512, 2) void fused(const char* __restrict__ XA, const char* __restrict__ WA, const char* __restrict__ PHI,
                                                const char* __restrict__ WOA, const char* __restrict__ XB, const char* __restrict__ WB,
                                                const char* __restrict__ WOB, char* ws, char* out) {
  __shared__ __attribute__((aligned(16))) char smem[LDS_BYTES];
  const u32 lb = (u32)(uintptr_t)(LDS_AS char*)smem;
  const int b = blockIdx.x, x = b & 7, j = b >> 3, xx = x & 3;
  const int rb = xx * 4 + (j >> 3), hq = j & 7;
  if (x < 4) body<0>(XA, WA, PHI, WOA, ws, out, rb, hq, j, lb);
  else body<1>(XB, WB, PHI, WOB, ws, out, rb, hq, j, lb);
}
}

extern "C" void kernel_launch(void* const* d_in, const int* in_sizes, int n_in, void* d_out, int out_size, void* d_ws, size_t ws_size,
                              hipStream_t stream) {
  hk::fused<<<256, 512, 0, stream>>>((const char*)d_in[0], (const char*)d_in[1], (const char*)d_in[2], (const char*)d_in[3], (const char*)d_in[4],
                                     (const char*)d_in[5], (const char*)d_in[6], (char*)d_ws, (char*)d_out);
}
```

```cpp
#include <hip/hip_runtime.h>
#include <stdint.h>

namespace hk {
typedef unsigned int u32;
typedef unsigned long long u64;
typedef unsigned short u16;
typedef float f32x4 __attribute__((ext_vector_type(4)));
typedef float f32x2 __attribute__((ext_vector_type(2)));
typedef u32 u32x4 __attribute__((ext_vector_type(4)));
typedef u32 u32x2 __attribute__((ext_vector_type(2)));
typedef __bf16 bf16x8 __attribute__((ext_vector_type(8)));
typedef __bf16 bf16x2 __attribute__((ext_vector_type(2)));
typedef short s16x4 __attribute__((ext_vector_type(4)));
#define LDS_AS __attribute__((address_space(3)))
#define GLB_AS __attribute__((address_space(1)))
#define DEVI __device__ __forceinline__
#define SCHED_FENCE() __builtin_amdgcn_sched_barrier(0)

template <int V> struct IC { static constexpr int value = V; };
template <int I, int N, class F>
DEVI void static_for(F&& f) {
  if constexpr (I < N) { f(IC<I>{}); static_for<I + 1, N>(f); }
}

constexpr int XROW = 1152, WROW = 3456;
constexpr size_t WS_OF = 0;
constexpr int SLAB = 6400;
constexpr size_t WS_SLAB = 5u << 20;
constexpr size_t WS_FLAG = 6u << 20;
constexpr size_t WS_EPOCH = WS_FLAG + 65536;
constexpr size_t WS_OFLAG = WS_FLAG + 4096;
constexpr u32 OOB = 0x7fffffffu;
constexpr u32 SENT = 0xffffffffu;
constexpr int WU = 9216;
constexpr int LPK = 1024, PKS = 296;
constexpr int S0 = 3968, SS = 34944;
constexpr int SK = 16384, SV = SK + WU + 16;
constexpr int WQ = S0 + SS;
constexpr int LT = WQ + 9 * WU;
constexpr int LM = S0 + 4 * SS;
constexpr int LPHF = LM;
constexpr int LSUM = LPHF + 8192;
constexpr int LHALO = LM;
constexpr int KLX = LM + 11264;
constexpr int QLX = KLX + 2304;
constexpr int LSCR = QLX + 2048;
constexpr int VIS = 168;
constexpr int lvi(int br) { return br ? 1024 : LT; }
constexpr int LKX = 25600;
constexpr int wo_at(int br, int m) {
  if (br == 0) return m < 4 ? 1024 + m * WU : m < 6 ? LT + (m - 4) * WU : WQ + (m - 6) * WU;
  return m < 2 ? LT + m * WU : m == 2 ? LM : WQ + (m - 3) * WU;
}
constexpr int LOWN = WQ + 6 * WU;
constexpr int LDS_BYTES = 163840;
static_assert(LSCR + 512 <= LDS_BYTES && LPK + 2688 <= S0 && LPK + 128 * PKS <= WQ && LT + 128 * VIS <= LM && 1024 + 144 * VIS <= LKX && LKX + 8192 <= WQ &&
              SV + WU <= SS && 3 * WU <= SS && 7 * WU <= 2 * SS && S0 % 128 == 0 && SS % 128 == 0, "lds");

DEVI u32x2 tr_read(u32 a) { return __builtin_bit_cast(u32x2, __builtin_amdgcn_ds_read_tr16_b64_v4i16((LDS_AS s16x4*)a)); }
DEVI u32x4 frag_tr(u32 a0, u32 a1) { u32x2 x = tr_read(a0), y = tr_read(a1); return u32x4{x[0], x[1], y[0], y[1]}; }
DEVI u32x4 lds_r128(u32 a) { return *(LDS_AS u32x4*)a; }
DEVI u32x2 lds_r64(u32 a) { return *(LDS_AS u32x2*)a; }
DEVI u32 lds_r32(u32 a) { return *(LDS_AS u32*)a; }
DEVI u32 lds_r16(u32 a) { return *(LDS_AS u16*)a; }
DEVI void lds_w128(u32 a, u32x4 v) { *(LDS_AS u32x4*)a = v; }
DEVI void lds_w64(u32 a, u32x2 v) { *(LDS_AS u32x2*)a = v; }
DEVI void lds_w32(u32 a, u32 v) { *(LDS_AS u32*)a = v; }
DEVI f32x4 mfma32(u32x4 a, u32x4 b, f32x4 c) {
  return __builtin_amdgcn_mfma_f32_16x16x32_bf16(__builtin_bit_cast(bf16x8, a), __builtin_bit_cast(bf16x8, b), c, 0, 0, 0);
}
DEVI u32 pk(float a, float b) { f32x2 v = {a, b}; return __builtin_bit_cast(u32, __builtin_convertvector(v, bf16x2)); }
DEVI u32x2 pk4(f32x4 v) { return u32x2{pk(v[0], v[1]), pk(v[2], v[3])}; }
DEVI float max3(float a, float b, float c) { return fmaxf(fmaxf(a, b), c); }
DEVI u32 umax(u32 a, u32 b) { return a > b ? a : b; }
DEVI u32 umax3(u32 a, u32 b, u32 c) { return umax(umax(a, b), c); }
DEVI u32 umax4(u32 m, u32x4 v) { return umax3(umax3(m, v[0], v[1]), v[2], v[3]); }
DEVI u32 ns(u32 v) { return v < 0xfffffffeu ? v : 0xfffffffeu; }

DEVI void dma_full(u32 lds, u32 voff, __amdgpu_buffer_rsrc_t srd, u32 soff) {
  asm volatile("s_mov_b32 m0, %0\n\ts_nop 0\n\tbuffer_load_dwordx4 %1, %2, %3 offen lds" ::"s"(lds), "v"(voff), "s"(srd), "s"(soff) : "memory");
}
DEVI void dma_mask(u32 lds, u32 voff, __amdgpu_buffer_rsrc_t srd, u32 soff, u64 mask) {
  asm volatile("s_mov_b32 m0, %0\n\ts_mov_b64 exec, %4\n\tbuffer_load_dwordx4 %1, %2, %3 offen lds\n\ts_mov_b64 exec, -1" ::"s"(lds), "v"(voff), "s"(srd),
               "s"(soff), "s"(mask)
               : "memory");
}
template <int N>
DEVI void wait_bar() { asm volatile("s_waitcnt vmcnt(%0) lgkmcnt(0)\n\ts_barrier" ::"n"(N) : "memory"); }
template <int N>
DEVI void wait_vm_bar() { asm volatile("s_waitcnt vmcnt(%0)\n\ts_barrier" ::"n"(N) : "memory"); }
DEVI void bar_lds() { asm volatile("s_waitcnt lgkmcnt(0)\n\ts_barrier" ::: "memory"); }
DEVI void bar_only() { asm volatile("s_barrier" ::: "memory"); }

DEVI float redmax4(float a, float b, float c, float d, float ninf) {
  auto r1 = __builtin_amdgcn_permlane32_swap(__float_as_uint(a), __float_as_uint(b), false, false);
  auto r2 = __builtin_amdgcn_permlane32_swap(__float_as_uint(c), __float_as_uint(d), false, false);
  u32 r10 = r1[0], r11 = r1[1], r20 = r2[0], r21 = r2[1];
  float ab = max3(__uint_as_float(r10), __uint_as_float(r11), ninf);
  float cd = max3(__uint_as_float(r20), __uint_as_float(r21), ninf);
  auto r3 = __builtin_amdgcn_permlane16_swap(__float_as_uint(ab), __float_as_uint(cd), false, false);
  u32 r30 = r3[0], r31 = r3[1];
  return max3(__uint_as_float(r30), __uint_as_float(r31), ninf);
}
DEVI void bcast4(float t, float& a, float& b, float& c, float& d) {
  u32 tu = __float_as_uint(t);
  auto s1 = __builtin_amdgcn_permlane16_swap(tu, tu, false, false);
  u32 x = s1[0], y = s1[1];
  auto s2 = __builtin_amdgcn_permlane32_swap(x, x, false, false);
  auto s3 = __builtin_amdgcn_permlane32_swap(y, y, false, false);
  u32 s20 = s2[0], s21 = s2[1], s30 = s3[0], s31 = s3[1];
  a = __uint_as_float(s20); b = __uint_as_float(s21); c = __uint_as_float(s30); d = __uint_as_float(s31);
}

constexpr float LOG2E = 1.4426950408889634f;
constexpr u32 ONES = 0x3f803f80u;

struct HH {
  u32x4 f0, f1, f2, f3;
  u32x4 b0, b1;
  u32 bl;
  f32x4 T0, T1, TL, S;
  float a, b, c, d;
  f32x4 p0, q0, p1, q1;
  float l0, l1;
};
constexpr int HH_N = 106;
DEVI void swap32(float& a, float& b) {
  auto r = __builtin_amdgcn_permlane32_swap(__float_as_uint(a), __float_as_uint(b), false, false);
  u32 x = r[0], y = r[1]; a = __uint_as_float(x); b = __uint_as_float(y);
}
DEVI void swap16(float& a, float& b) {
  auto r = __builtin_amdgcn_permlane16_swap(__float_as_uint(a), __float_as_uint(b), false, false);
  u32 x = r[0], y = r[1]; a = __uint_as_float(x); b = __uint_as_float(y);
}
template <int I, bool QS>
DEVI void hh_step(HH& s, u32 lft, u32 phf, u32 sumf, bool lo, float ninf) {
  const f32x4 z = {0.f, 0.f, 0.f, 0.f};
  if constexpr (I == 0) {
    u32x2 x = lds_r64(lft); s.b0[2] = x[0]; s.b0[3] = x[1];
    asm volatile("" ::: "memory");
    u32x2 y = lds_r64(lft); s.b1[2] = y[0]; s.b1[3] = y[1];
    s.f0 = lds_r128(phf);
  } else if constexpr (I == 1) { s.f1 = lds_r128(phf + 1024); s.f2 = lds_r128(phf + 2048);
  } else if constexpr (I == 2) { s.f3 = lds_r128(phf + 3072);
  } else if constexpr (I == 3) { s.T0 = mfma32(s.f0, s.b0, z);
  } else if constexpr (I == 4) { s.TL = mfma32(s.f2, s.b0, z);
  } else if constexpr (I == 5) { s.T1 = mfma32(s.f1, s.b1, z);
  } else if constexpr (I == 6) { s.TL = mfma32(s.f3, s.b1, s.TL);
  } else if constexpr (I == 7) { s.f0 = lds_r128(sumf); s.f1 = lds_r128(sumf + 1024); s.f2 = lds_r128(sumf + 2048);
  } else if constexpr (I == 8) { s.a = max3(s.T0[0], s.T0[1], s.T0[2]);
  } else if constexpr (I == 9) { s.c = max3(-s.T0[0], -s.T0[1], -s.T0[2]);
  } else if constexpr (I == 10) { s.b = max3(s.T1[0], s.T1[1], s.T1[2]);
  } else if constexpr (I == 11) { s.d = max3(-s.T1[0], -s.T1[1], -s.T1[2]);
  } else if constexpr (I == 12) {
  } else if constexpr (I == 13) { s.a = max3(s.a, s.T0[3], s.TL[0]);
  } else if constexpr (I == 14) { s.b = max3(s.b, s.T1[3], s.TL[1]);
  } else if constexpr (I == 15) { s.c = max3(s.c, -s.T0[3], -s.TL[0]);
  } else if constexpr (I == 16) { s.d = max3(s.d, -s.T1[3], -s.TL[1]);
  } else if constexpr (I == 17) { swap32(s.a, s.b);
  } else if constexpr (I == 18) { swap32(s.c, s.d);
  } else if constexpr (I == 19) { s.a = max3(s.a, s.b, ninf);
  } else if constexpr (I == 20) { s.c = max3(s.c, s.d, ninf);
  } else if constexpr (I == 21) { swap16(s.a, s.c);
  } else if constexpr (I == 22) { s.a = max3(s.a, s.c, ninf);
  } else if constexpr (I == 23) { s.a *= -LOG2E;
  } else if constexpr (I == 24) { s.c = s.a; swap16(s.a, s.c);
  } else if constexpr (I == 25) { s.b = s.a; swap32(s.a, s.b);
  } else if constexpr (I == 26) { s.d = s.c; swap32(s.c, s.d);
  } else if constexpr (I == 27) { s.l0 = lo ? s.a : s.c;
  } else if constexpr (I == 28) { s.l1 = lo ? s.b : s.d;
  } else if constexpr (I == 29) { s.l0 = fmaf(s.TL[0], lo ? LOG2E : -LOG2E, s.l0);
  } else if constexpr (I == 30) { s.l1 = fmaf(s.TL[1], lo ? LOG2E : -LOG2E, s.l1);
  } else if constexpr (I == 31) { s.l0 = __builtin_amdgcn_exp2f(s.l0);
  } else if constexpr (I == 32) { s.l1 = __builtin_amdgcn_exp2f(s.l1);
  } else if constexpr (I == 33) { s.bl = pk(s.l0, s.l1);
  } else if constexpr (I == 34) { s.S = mfma32(s.f2, u32x4{s.bl, 0u, 0u, 0u}, z);
  } else if constexpr (I < 77) {
    constexpr int h = (I - 35) / 21, k = (I - 35) % 21;
    f32x4& T = h ? s.T1 : s.T0; f32x4& p = h ? s.p1 : s.p0; f32x4& q = h ? s.q1 : s.q0; u32x4& o = h ? s.b1 : s.b0;
    const float m = h ? s.b : s.a, n = h ? s.d : s.c;
    constexpr bool arg = k < 2 || ((k & 1) && k < 15);
    constexpr int e = arg ? (k < 2 ? k : (k + 1) / 2) : (k < 15 ? (k - 2) / 2 : 7);
    if constexpr (k < 16) {
      if constexpr (arg) { if constexpr (e < 4) p[e] = fmaf(T[e], LOG2E, m); else q[e - 4] = fmaf(T[e - 4], -LOG2E, n); }
      else { if constexpr (e < 4) p[e] = __builtin_amdgcn_exp2f(p[e]); else q[e - 4] = __builtin_amdgcn_exp2f(q[e - 4]); }
    } else if constexpr (k < 18) { o[k - 16] = pk(p[2 * (k - 16)], p[2 * (k - 16) + 1]);
    } else if constexpr (k < 20) { o[k - 16] = pk(q[2 * (k - 18)], q[2 * (k - 18) + 1]);
    } else { s.S = mfma32(h ? s.f1 : s.f0, o, s.S); }
  } else if constexpr (I == 77) {
  } else if constexpr (I == 78) { s.c = __builtin_amdgcn_rcpf(s.S[1]);
  } else if constexpr (I == 79) { s.d = __builtin_amdgcn_rcpf(s.S[3]);
  } else if constexpr (I == 80) { if constexpr (QS) s.c *= s.S[0]; else s.a = __builtin_amdgcn_rcpf(s.S[0]);
  } else if constexpr (I == 81) { if constexpr (QS) s.d *= s.S[2]; else s.b = __builtin_amdgcn_rcpf(s.S[2]);
  } else if constexpr (I < 106) {
    constexpr int v = (I - 82) / 6, k = (I - 82) % 6;
    if constexpr (!(QS && v >= 2)) {
      f32x4& x = v == 0 ? s.q0 : v == 1 ? s.q1 : v == 2 ? s.p0 : s.p1;
      u32x4& o = (v & 1) ? s.b1 : s.b0;
      const float r = v == 0 ? s.c : v == 1 ? s.d : v == 2 ? s.a : s.b;
      if constexpr (k < 4) x[k] *= r;
      else o[(v < 2 ? 2 : 0) + k - 4] = pk(x[2 * (k - 4)], x[2 * (k - 4) + 1]);
    }
  }
}
template <bool QS>
DEVI void hh_left(HH& s, bool lo) {
  if constexpr (QS) s.bl = pk(s.l0 * (lo ? 1.f : s.c), s.l1 * (lo ? 1.f : s.d));
  else s.bl = pk(s.l0 * (lo ? s.a : s.c), s.l1 * (lo ? s.b : s.d));
}

#define L2ST 24
#define PRE 7
#define PRO 2
#define LEAD 8
#define QA 2
#define QT 1
#define E_PO 36
#define E_POB 30
#define Q_L1 9
#define Q_L1C 14
#define QW 1
#define HQ0 4
#define HQS 8
#define C0 0
#define CS 2
#define C1 44
#define E_PUB 11
#define E_FLD 40
#define QB 1
#define QSP 1
#define HB 3
constexpr int EM = 36, EQ = 44, EH = 62, NEV = 74;
constexpr int Q_SLAB = 2;
constexpr int e_fld(int br) { return br ? EQ + 12 : E_FLD; }
constexpr int e_fchk(int br) { return br ? EQ + 16 : EQ; }
constexpr int clr_at(int E) { return E >= C1 && E < C1 + 3 ? 4 + E - C1 : E >= C0 && (E - C0) % CS == 0 && (E - C0) / CS < 4 ? (E - C0) / CS : -1; }
constexpr bool is_clr(int E) { return clr_at(E) >= 0; }
static_assert((E_PUB & 3) == 3 && E_PUB > C0 + 3 * CS && E_PUB < 35 && E_FLD >= EM && E_FLD < EQ && C1 > C0 + 3 * CS && (C1 < 34 || C1 >= EQ) && C1 < EQ + 6, "marks");
struct Sched {
  int op[NEV][3];
  int nop[NEV];
  int pos_ev[NEV + 1];
  int pos_hook[NEV];
  int last[36];
  int total;
};
constexpr int ext_ops(int br, int E) {
  return (E == (br ? E_POB : E_PO)) + (br && is_clr(E)) + (E == e_fld(br)) +
         (br ? 0 : E == EQ + Q_SLAB ? 2 : E == EQ + Q_L1 ? 4 : E == EQ + Q_L1C ? 1 : 0);
}
constexpr int op_chunk(int op) { return op < 200 ? (op - 100) >> 2 : op < 236 ? ((op - 200) >> 1) % 9 : (op - 200) >> 1; }
constexpr int op_earliest(int br, int op) {
  const int n = op_chunk(op);
  if (n < 9) return 4 * n - 1 - LEAD;
  if (n < 27) return n < 21 ? 23 : n < 25 ? 27 : 31;
  const int m = n - 27;
  if (br == 0) return m < 6 ? EQ + 3 : EQ + 6;
  return m < 2 ? 31 : m == 2 ? EQ : EQ + 2 * (m - 3) + 3;
}
constexpr int op_deadline(int br, int op) {
  const int n = op_chunk(op);
  if (n < 9) return 4 * n - 1;
  if (n < 27) return br == 0 || n == 18 ? EQ : EQ + 2 * (n - 18) - 1;
  return NEV;
}
static_assert(LEAD <= 12, "ring");
constexpr Sched make_sched(int br) {
  Sched s{};
  const int nx = br ? 3 : 2;
  int list[128] = {}, when[128] = {}, n = 0;
  for (int c = 0; c < 9; ++c) {
    for (int i = 0; i < nx; ++i) list[n++] = 100 + 4 * c + i;
    list[n++] = 200 + 2 * c; list[n++] = 201 + 2 * c; list[n++] = 218 + 2 * c; list[n++] = 219 + 2 * c;
  }
  for (int c = 18; c < 36; ++c) { list[n++] = 200 + 2 * c; list[n++] = 201 + 2 * c; }
  for (int k = 0; k < n; ++k) when[k] = 1000;
  int pos = 0, done = 0;
  for (; done < PRO * (nx + 4); ++done) { s.last[op_chunk(list[done])] = pos++; when[done] = -1; if (done == nx + 3) pos += 1; }
  for (int E = 0; E < NEV; ++E) {
    s.pos_ev[E] = pos;
    int j = 0;
    for (int k = 0; k < n; ++k) {
      if (when[k] != 1000 || op_earliest(br, list[k]) > E) continue;
      const int c = op_chunk(list[k]);
      int q = E < EM ? (c < 9 ? QA : QT) : E < EQ && br ? 0 : ext_ops(br, E) ? 1 : 2;
      if (br == 0 && E >= EQ) q = ext_ops(br, E) || (E < EH && E % QSP) ? 0 : 1;
      if (br == 1 && E >= EQ && c >= 27) q = E >= EH + 4 ? 0 : E >= EH ? HB : ext_ops(br, E) ? QB - 1 : QB;
      if (j >= q) break;
      s.op[E][j++] = list[k]; when[k] = E; ++done;
      s.last[c] = pos++;
      if (E >= op_deadline(br, list[k])) s.total = -1000;
    }
    s.nop[E] = j;
    s.pos_hook[E] = pos;
    pos += ext_ops(br, E);
  }
  s.pos_ev[NEV] = pos;
  s.total += done == n ? pos : -1000;
  return s;
}
template <int BR> struct SC { static constexpr Sched s = make_sched(BR); };
static_assert(SC<0>::s.total > 0 && SC<1>::s.total > 0, "schedule incomplete");
constexpr int imin(int a, int b) { return a < b ? a : b; }
template <int BR> constexpr int wait_n(int E, int c) {
  int n = SC<BR>::s.pos_ev[E] - SC<BR>::s.last[c] - 1;
  if (BR == 1 && E == E_PUB) n = imin(n, SC<BR>::s.pos_ev[E] - SC<BR>::s.pos_ev[C0 + 3 * CS + 1]);
  if (BR == 1 && E == EQ + 9) n = imin(n, SC<BR>::s.pos_ev[E] - SC<BR>::s.pos_ev[C1 + 3]);
  if (BR == 0 && E == EQ) n = imin(n, SC<BR>::s.pos_ev[E] - SC<BR>::s.pos_ev[E_FLD + 1]);
  return n;
}
template <int BR> constexpr int wait_h(int E, int E0) { return SC<BR>::s.pos_hook[E] - SC<BR>::s.pos_ev[E0 + 1]; }

#define STAMP(i)
#define RSTAMP(i)
#define STAMP_FLUSH()
#define STAMP_DECL

DEVI u64 next_tag(u32x2 e) {
  const u64 E = ((u64)(u32)__builtin_amdgcn_readfirstlane(e[1]) << 32) | (u32)__builtin_amdgcn_readfirstlane(e[0]);
  return E * 6364136223846793005ull + 1442695040888963407ull;
}
DEVI void poll_flags(GLB_AS u64* f, u64 tag) {
  for (int spin = 0; spin < (1 << 20); ++spin) {
    u64 v = __hip_atomic_load(f, __ATOMIC_RELAXED, __HIP_MEMORY_SCOPE_AGENT);
    if (__all(v == tag)) break;
    __builtin_amdgcn_s_sleep(1);
  }
  asm volatile("" ::: "memory");
}

template <int BR>
DEVI void body(const char* __restrict__ X, const char* __restrict__ W, const char* __restrict__ PHI, const char* __restrict__ WO, char* ws, char* out,
               int rb, int hq, int jx, u32 lb) {
  int tid = threadIdx.x;
  asm volatile("" : "+v"(tid));
  int l = tid & 63;
  const int w = __builtin_amdgcn_readfirstlane(tid >> 6);
  const int rg = w & 3, hpl = w >> 2;
  int c = l & 15, g = l >> 4, q = c >> 2, p = c & 3, g0 = g & 1, g1 = g >> 1;
  int lr = (l * 57) >> 9, lu = l - lr * 9;
  auto refresh = [&]() {
    asm volatile("" : "+v"(tid));
    l = tid & 63; c = l & 15; g = l >> 4; q = c >> 2; p = c & 3; g0 = g & 1; g1 = g >> 1;
    lr = (l * 57) >> 9; lu = l - lr * 9;
  };
  STAMP_DECL
  STAMP(0);
  RSTAMP(30);
  __amdgpu_buffer_rsrc_t srdX = __builtin_amdgcn_make_buffer_rsrc((void*)X, (short)0, 2048 * XROW, 0x00020000);
  __amdgpu_buffer_rsrc_t srdW = __builtin_amdgcn_make_buffer_rsrc((void*)W, (short)0, 576 * WROW, 0x00020000);
#define ROT (rb + 2 * hq) % 9
  const int rot = ROT;
  constexpr u64 M36 = (1ull << 36) - 1;

  u32 vx[2], vq[2], vh = 0;
#pragma unroll
  for (int i = 0; i < 2; ++i) {
    int row = 16 * w + 8 * i + (l >> 3);
    vx[i] = (u32)((rb * 128 + row) * XROW + (((l & 7) ^ ((row >> 1) & 7)) << 4));
  }
#pragma unroll
  for (int i = 0; i < 2; ++i) vq[i] = (u32)((8 * w + 4 * (lr & 1) + 2 * i + ((lr >> 1) & 1)) * WROW + hq * 144 + lu * 16);
  if (BR) {
    int row = 2 * w + ((l >> 3) & 1);
    int grow = rb * 128 - 16 + row; grow = grow < 0 ? 0 : grow;
    vh = (u32)(grow * XROW + (((l & 7) ^ ((row >> 1) & 7)) << 4));
  }
  auto kc_of = [&](int cc) { int k = cc + rot; return k >= 9 ? k - 9 : k; };
  __amdgpu_buffer_rsrc_t srdO = __builtin_amdgcn_make_buffer_rsrc((void*)WO, (short)0, 576 * XROW, 0x00020000);
  auto dma_op = [&](auto OP) {
    constexpr int op = OP.value;
    if constexpr (op < 200) {
      constexpr int cc = (op - 100) >> 2, i = op & 3;
      const int kc = kc_of(cc);
      if constexpr (i < 2) dma_full(lb + S0 + (cc & 3) * SS + w * 2048 + i * 1024, vx[i], srdX, (u32)(kc * 128));
      else dma_mask(lb + LHALO + (cc & 3) * 2048 + w * 256, vh, srdX, (u32)(kc * 128), 0xffffull);
    } else {
      constexpr int n = (op - 200) >> 1, i = op & 1;
      constexpr int at = n < 9 ? S0 + (n & 3) * SS + SK : n < 18 ? S0 + ((n - 9) & 3) * SS + SV : n < 27 ? WQ + (n - 18) * WU : wo_at(BR, n - 27);
      const u32 dst = lb + at + (2 * w + i) * 576;
      if constexpr (n < 27) {
        const int kc = kc_of(n % 9), sec = n < 9 ? 1152 : (n < 18 ? 2304 : 0);
        dma_mask(dst, vq[i], srdW, (u32)(kc * 64 * WROW + sec), M36);
      } else {
        const int hi = lr & 1, qq = 2 * i + ((lr >> 1) & 1);
        if constexpr (n > 27)
          dma_mask(dst, (u32)((36 * hpl + 20 * hi + 4 * rg + qq) * XROW + hq * 144 + lu * 16), srdO, (u32)(((n - 28 + hq) & 7) * 72 * XROW), M36);
        else dma_mask(dst, (u32)((36 * (8 * hpl + 2 * rg + hi) + 16 + qq) * XROW + hq * 144 + lu * 16), srdO, 0u, M36);
      }
    }
  };
  auto ev_dma = [&](auto EE, auto II) {
    if constexpr (II.value < SC<BR>::s.nop[EE.value]) dma_op(IC<SC<BR>::s.op[EE.value][II.value]>{});
  };
  auto warm_wo = [&]() {
    const int idx = w * 21 + l;
    const u32 po = l < 21 && idx < 162 ? (u32)((162 * jx + idx) * 128) : OOB;
    asm volatile("s_mov_b32 m0, %0\n\ts_nop 0\n\tbuffer_load_dword %1, %2, 0 offen lds" ::"s"(lb + LSCR), "v"(po), "s"(srdO) : "memory");
  };
  __amdgpu_buffer_rsrc_t srdF = __builtin_amdgcn_make_buffer_rsrc((void*)(ws + WS_OF), (short)0, 256 * 18 * 1024, 0x00020000);
  __amdgpu_buffer_rsrc_t srdS = __builtin_amdgcn_make_buffer_rsrc((void*)(ws + WS_SLAB), (short)0, 136 * SLAB, 0x00020000);
  auto mark = [&](auto EE) {
    constexpr int e = EE.value, f = (e + 2) % 3;
    const u32x4 sv = {SENT, SENT, SENT, SENT};
    const u32 ob = (u32)(((e > 3 ? 128 : 0) + rb * 8 + w) * 18 * 1024);
    if constexpr (e > 0 && f < 2) __builtin_amdgcn_raw_buffer_store_b128(sv, srdF, l * 16, ob + hq * 2048 + f * 1024, 16);
    if constexpr (e > 0 && f == 2) __builtin_amdgcn_raw_buffer_store_b128(sv, srdF, g == 0 ? c * 16 : OOB, ob + (16 + (hq >> 2)) * 1024 + (hq & 3) * 256, 16);
    if constexpr (e == 0) {
      const u32 so = tid < 400 ? (u32)((hq * 16 + rb) * SLAB + tid * 16) : tid < 425 ? (u32)((128 + hq) * SLAB + rb * 400 + (tid - 400) * 16) : OOB;
      __builtin_amdgcn_raw_buffer_store_b128(sv, srdS, so, 0, 16);
    }
  };
  GLB_AS u64* kflags = (GLB_AS u64*)(ws + WS_FLAG + hq * 256);
  GLB_AS u64* oflags = (GLB_AS u64*)(ws + WS_OFLAG + (BR * 16 + rb) * 256);
  u32x2 ep, fv;
  asm volatile("" ::"s"(srdX), "s"(srdW));
  STAMP(16);
  static_assert(PRO == 2, "prologue");
  auto dma_chunk = [&](auto cc) {
    constexpr int c_ = cc.value;
    dma_op(IC<100 + 4 * c_>{}); dma_op(IC<101 + 4 * c_>{});
    if constexpr (BR) dma_op(IC<102 + 4 * c_>{});
    dma_op(IC<200 + 2 * c_>{}); dma_op(IC<201 + 2 * c_>{}); dma_op(IC<218 + 2 * c_>{}); dma_op(IC<219 + 2 * c_>{});
  };
  if constexpr (BR == 0) {
    __amdgpu_buffer_rsrc_t srdP = __builtin_amdgcn_make_buffer_rsrc((void*)PHI, (short)0, 32 * 648, 0x00020000);
    dma_mask(lb + LPK + w * 336, (u32)(hq * 2592 + (w * 21 + l) * 16), srdP, 0, (1ull << 21) - 1);
  }
  dma_chunk(IC<0>{});
  {
    const char* ea = ws + WS_EPOCH;
    asm volatile("global_load_dwordx2 %0, %1, off sc1" : "=v"(ep) : "v"(ea) : "memory");
  }
  STAMP(17);
  if constexpr (BR == 0) {
    wait_vm_bar<7>();
    STAMP(18);
    const int h = rg & 1, lf = rg >> 1;
    const int f = lf ? 16 + (q & 1) : c;
    const bool rowok = lf ? (p == h) : true;
    const u32 pb = lb + LPK + (hpl * 2 + h) * 648 + f * 2;
    const int d0 = 2 * h + 4 * g;
    u32 e0 = lds_r16(pb + (d0 + 0) * 36), e1 = lds_r16(pb + (d0 + 1) * 36), e2 = lds_r16(pb + (d0 + 2) * 36), e3 = lds_r16(pb + (d0 + 3) * 36);
    const int dl = h ? 0 : 16;
    u32 l0 = lds_r16(pb + dl * 36), l1 = lds_r16(pb + (dl + 1) * 36);
    SCHED_FENCE();
    dma_op(IC<104>{}); dma_op(IC<105>{});
    SCHED_FENCE();
    u32 lw = (g == 0) ? (l0 | (l1 << 16)) : 0u;
    u32x4 fr = {e0 | (e1 << 16), e2 | (e3 << 16), h ? 0u : lw, h ? lw : 0u};
    if (!rowok) fr = u32x4{0, 0, 0, 0};
    lds_w128(lb + LPHF + (hpl * 4 + rg) * 1024 + l * 16, fr);
    if (w < 3) {
      u32 a = (p == 2 * w) ? ONES : 0u, b = (p == 2 * w + 1) ? ONES : 0u;
      u32 x = ((p & 1) == g1) ? ((p >> 1) ? 0x3f800000u : 0x00003f80u) : 0u;
      lds_w128(lb + LSUM + w * 1024 + l * 16, w < 2 ? u32x4{a, a, b, b} : u32x4{x, 0u, 0u, 0u});
    }
    SCHED_FENCE();
    dma_op(IC<202>{}); dma_op(IC<203>{}); dma_op(IC<220>{}); dma_op(IC<221>{});
  } else {
    bar_only();
    STAMP(18);
    dma_chunk(IC<1>{});
  }
  STAMP(1);

  const u32 xa0 = lb + S0 + (rg * 32 + hpl * 16 + c) * 128 + ((g ^ ((c >> 1) & 7)) << 4);
  const u32 xa1 = xa0 ^ 64;
  const u32 xb0 = xa0 + 2048 - hpl * 4096, xb1 = xb0 ^ 64;
  const u32 xh0 = lb + LHALO + c * 128 + ((g ^ ((c >> 1) & 7)) << 4);
  const u32 xh1 = xh0 ^ 64;
  const u32 wrow = lb + (16 * g1 + 8 * g0 + 2 * q) * 144;
  const u32 wa = wrow + hpl * 72 + 8 * p;
  const u32 wl = wrow + 32 + (p & 1) * 72 + (p >> 1) * (SV - SK);
  const u32 wh = wa + (rg & 1) * 40 + (rg >> 1) * (SV - SK);
  float ninf = -__builtin_inff();
  asm volatile("" : "+s"(ninf));

  u32x4 xf[18][2];
  const f32x4 z4 = {0.f, 0.f, 0.f, 0.f};
  f32x4 k0[2] = {z4, z4}, k1[2] = {z4, z4}, a0[2] = {z4, z4}, a1[2] = {z4, z4}, aL = z4, aH = z4, aHL = z4;
  u32x4 f0, f1, fl, fh, xh;
  auto load_kv_frags = [&](auto HH_, auto JJ) {
    constexpr int H = HH_.value, j = JJ.value, T = H >> 1, n = T >> 1, ks = T & 1;
    constexpr u32 ok = S0 + (n & 3) * SS + SK + ks * 32 * 144, ov = ok + SV - SK;
    if constexpr (H < 36) {
      if constexpr ((H & 1) == 0) {
        if constexpr (j == 0) { xf[T][0] = lds_r128((ks ? xa1 : xa0) + (n & 3) * SS); f0 = frag_tr(wa + ok, wa + ok + 144); }
        if constexpr (j == 1) { xf[T][1] = lds_r128((ks ? xb1 : xb0) + (n & 3) * SS); f1 = frag_tr(wa + ok + 40, wa + ok + 184); }
      } else {
        if constexpr (j == 0) f0 = frag_tr(wa + ov, wa + ov + 144);
        if constexpr (j == 1) f1 = frag_tr(wa + ov + 40, wa + ov + 184);
        if constexpr (j == 2) fl = frag_tr(wl + ok, wl + ok + 144);
        if constexpr (j == 3 && BR == 1) { fh = frag_tr(wh + ok, wh + ok + 144); xh = lds_r128((ks ? xh1 : xh0) + (n & 3) * 2048); }
      }
    }
  };
  wait_bar<wait_n<BR>(0, 0)>();
  STAMP(2);
  load_kv_frags(IC<0>{}, IC<0>{}); load_kv_frags(IC<0>{}, IC<1>{});
  u64 tag = 0;
  static_for<0, 36>([&](auto hh_) {
    constexpr int H = hh_.value, T = H >> 1;
    if constexpr ((H & 3) == 3 && H < 35) {
      wait_bar<wait_n<BR>(H, (H + 1) >> 2)>();
    }
    u32x4 c0 = f0, c1 = f1, cl = fl, ch = fh, cx = xh;
    f32x4(&d0)[2] = (H & 1) ? a0 : k0;
    f32x4(&d1)[2] = (H & 1) ? a1 : k1;
    SCHED_FENCE();
    d0[0] = mfma32(c0, xf[T][0], d0[0]);
    load_kv_frags(IC<H + 1>{}, IC<0>{});
    SCHED_FENCE();
    d0[1] = mfma32(c0, xf[T][1], d0[1]);
    load_kv_frags(IC<H + 1>{}, IC<1>{});
    ev_dma(hh_, IC<0>{});
    SCHED_FENCE();
    d1[0] = mfma32(c1, xf[T][0], d1[0]);
    load_kv_frags(IC<H + 1>{}, IC<2>{});
    SCHED_FENCE();
    d1[1] = mfma32(c1, xf[T][1], d1[1]);
    load_kv_frags(IC<H + 1>{}, IC<3>{});
    ev_dma(hh_, IC<1>{});
    ev_dma(hh_, IC<2>{});
    SCHED_FENCE();
    if constexpr (H & 1) {
      aL = mfma32(cl, xf[T][0], aL);
      asm volatile("" : "+v"(aL));
      if constexpr (BR == 1) {
        aH = mfma32(ch, cx, aH); aHL = mfma32(cl, cx, aHL);
        asm volatile("" : "+v"(aH), "+v"(aHL));
      }
    }
    if constexpr (H == (BR ? E_POB : E_PO)) warm_wo();
    if constexpr (H == 3) {
      asm volatile("" : "+v"(ep));
      tag = next_tag(ep);
    }
    if constexpr (BR == 1 && is_clr(H)) mark(IC<clr_at(H)>{});
    if constexpr (BR == 1 && H == E_PUB) {
      if (tid < 2) __hip_atomic_store(tid == 0 ? kflags + rb : oflags + hq - 512, tag, __ATOMIC_RELAXED, __HIP_MEMORY_SCOPE_AGENT);
    }
    SCHED_FENCE();
  });
  STAMP(3);
  refresh();
  const u32 wqa = lb + WQ + (16 * g1 + 8 * g0 + 2 * q) * 144 + hpl * 72 + 8 * p;
  const u32 wql = lb + WQ + (16 * g1 + 8 * g0 + 2 * q) * 144 + 32 + (p & 1) * 72;
  auto load_q_frags = [&](auto TT, auto JJ) {
    constexpr int t = TT.value, j = JJ.value;
    constexpr u32 o = (t >> 1) * WU + (t & 1) * 32 * 144;
    if constexpr (t < 18) {
      if constexpr (j == 0) f0 = frag_tr(wqa + o, wqa + o + 144);
      if constexpr (j == 1) f1 = frag_tr(wqa + o + 40, wqa + o + 184);
      if constexpr (j == 2) fl = frag_tr(wql + o, wql + o + 144);
    }
  };
  if constexpr (BR == 1) {
    wait_bar<wait_n<BR>(EQ, 18)>();
    static_for<0, 3>([&](auto j) { load_q_frags(IC<0>{}, j); });
  }
  u32x2 kM[2][2];
#pragma unroll
  for (int sb = 0; sb < 2; ++sb) { kM[0][sb] = pk4(k0[sb]); kM[1][sb] = pk4(k1[sb]); }
  auto unswap = [&](u32x2& a, u32x2& b) {
    u32x2 x = a, y = b;
#pragma unroll
    for (int e = 0; e < 2; ++e) { a[e] = hpl ? y[e] : x[e]; b[e] = hpl ? x[e] : y[e]; }
  };
  {
    constexpr int VROW0 = BR ? 16 : 0;
    auto left = [&](f32x4 v, int blk) {
      u32x2 x = pk4(v);
      const u32 va = lb + lvi(BR) + (VROW0 - 16 + blk * 16 + c) * VIS + g0 * 80 + 64;
      lds_w64(g1 ? va : lb + KLX + (blk * 2 + g0) * 128 + c * 8, u32x2{x[0], g1 ? ONES : x[1]});
      lds_w64(g1 ? va + 8 : lb + LSCR + 256 + c * 8, u32x2{x[1], ONES});
    };
    left(aL, 1 + 2 * rg + hpl);
#pragma unroll
    for (int sb = 0; sb < 2; ++sb) {
      const u32 va = lb + lvi(BR) + (VROW0 + rg * 32 + (sb ^ hpl) * 16 + c) * VIS + hpl * 80;
      lds_w64(va + 8 * g, pk4(a0[sb]));
      lds_w64(va + 32 + 8 * g, pk4(a1[sb]));
    }
    if constexpr (BR == 1) {
      unswap(kM[0][0], kM[0][1]); unswap(kM[1][0], kM[1][1]);
      if (w == 2) left(aHL, 0);
      const u32 kx = lb + LKX + l * 8;
      lds_w64(rg < 2 ? kx + (hpl * 2 + rg) * 512 : lb + lvi(BR) + c * VIS + hpl * 80 + (rg & 1) * 32 + 8 * g, pk4(aH));
      if (rg < 3) {
        const u32 b = kx + (((rg + 1) * 2 + hpl) * 2) * 512;
        lds_w64(b, kM[0][1]); lds_w64(b + 512, kM[1][1]);
      }
    }
  }
  const u32 phf = lb + LPHF + hpl * 4096 + l * 16, sumf = lb + LSUM + l * 16;
  const bool lo = g < 2;
  auto load_flags = [&]() {
    GLB_AS u64* fa = (BR == 0 && g == 0) ? kflags + c : oflags + (l & 7);
    asm volatile("global_load_dwordx2 %0, %1, off sc1" : "=v"(fv) : "v"(fa) : "memory");
  };
  auto check_flags = [&]() {
    if (!__all((((u64)fv[1] << 32) | fv[0]) == tag)) poll_flags((BR == 0 && g == 0) ? kflags + c : oflags + (l & 7), tag);
  };
  if constexpr (BR == 0) {
    bar_lds();
    STAMP(4);
    HH h0, h1;
    h0.b0[0] = kM[0][0][0]; h0.b0[1] = kM[0][0][1]; h0.b1[0] = kM[1][0][0]; h0.b1[1] = kM[1][0][1];
    h1.b0[0] = kM[0][1][0]; h1.b0[1] = kM[0][1][1]; h1.b1[0] = kM[1][1][0]; h1.b1[1] = kM[1][1][1];
    const u32 kl0 = lb + KLX + ((1 + 2 * rg + hpl) * 2 + hpl) * 128 + c * 8, kl1 = lb + KLX + ((2 + 2 * rg - hpl) * 2 + hpl) * 128 + c * 8;
    static_for<0, HH_N>([&](auto st) {
      if constexpr (st.value >= 8 && (st.value - 8) % 12 == 0 && (st.value - 8) / 12 < EQ - EM) {
        constexpr int E = EM + (st.value - 8) / 12;
        SCHED_FENCE();
        ev_dma(IC<E>{}, IC<0>{}); ev_dma(IC<E>{}, IC<1>{});
        if constexpr (E == E_PO) warm_wo();
        if constexpr (E == E_FLD) load_flags();
        SCHED_FENCE();
      }
      hh_step<st.value, false>(h0, kl0, phf, sumf, lo, ninf);
      hh_step<st.value, false>(h1, kl1, phf, sumf, lo, ninf);
    });
#pragma unroll
    for (int sb = 0; sb < 2; ++sb) {
      HH& hs = sb ? h1 : h0;
      hh_left<false>(hs, lo);
      const u32 pa = lb + LPK + (rg * 32 + (sb ^ hpl) * 16 + c) * PKS + hpl * 144;
      lds_w64(pa + 8 * g, u32x2{hs.b0[0], hs.b0[1]});
      lds_w64(pa + 32 + 8 * g, u32x2{hs.b0[2], hs.b0[3]});
      lds_w64(pa + 64 + 8 * g, u32x2{hs.b1[0], hs.b1[1]});
      lds_w64(pa + 96 + 8 * g, u32x2{hs.b1[2], hs.b1[3]});
      lds_w32(pa + 128 + 4 * g, hs.bl);
    }
    STAMP(5);
    wait_bar<wait_n<BR>(EQ, 26)>();
    asm volatile("" : "+v"(fv));
    static_for<0, 3>([&](auto j) { load_q_frags(IC<0>{}, j); });
  }
  refresh();
  f32x4 dp = z4, dm = z4, df = z4;
  u32x4 ap[4], am[4], af[4], bv[4];
  u32 g0_, g1_, g2_, g3_;
  const int rr = 4 * (4 * g0 + q) + 2 * g1;
  const u32 pa = lb + LPK + rr * PKS + hpl * 144 + (rg & 1) * 64 + 8 * p;
  const u32 pf = lb + LPK + rr * PKS + hpl * 144 + 128 + 8 * (p >> 1);
  const u32 va = lb + lvi(BR) + rr * VIS + hpl * 80 + (rg < 2 ? rg * 32 + 8 * p : 64 + 8 * (p & 1));
  auto hook = [&](auto tt, auto jj) {
    constexpr int t = tt.value, E = EQ + t, j = jj.value;
    if constexpr (BR == 0 && j < 4) {
      if constexpr (t == Q_SLAB - 2) {
        ap[j] = frag_tr(pa + j * 32 * PKS, pa + j * 32 * PKS + PKS);
        am[j] = frag_tr(pa + 32 + j * 32 * PKS, pa + 32 + j * 32 * PKS + PKS);
        af[j] = frag_tr(pf + j * 32 * PKS, pf + j * 32 * PKS + PKS);
        bv[j] = frag_tr(va + j * 32 * VIS, va + j * 32 * VIS + VIS);
      }
      if constexpr (t == Q_SLAB - 1) {
        dp = mfma32(ap[j], bv[j], dp); dm = mfma32(am[j], bv[j], dm); df = mfma32(af[j], bv[j], df);
        asm volatile("" : "+v"(dp), "+v"(dm), "+v"(df));
      }
    }
    if constexpr (j != 4) return;
    if constexpr (BR == 1 && is_clr(E)) mark(IC<clr_at(E)>{});
    if constexpr (BR == 1 && t == 9) {
      if (tid == 0) __hip_atomic_store(oflags + hq, tag, __ATOMIC_RELAXED, __HIP_MEMORY_SCOPE_AGENT);
    }
    if constexpr (BR == 1 && E == e_fld(BR)) load_flags();
    if constexpr (E == e_fchk(BR)) {
      if constexpr (BR == 1) asm volatile("s_waitcnt vmcnt(%1)" : "+v"(fv) : "n"(wait_h<BR>(E, e_fld(BR))) : "memory");
      check_flags();
    }
    if constexpr (BR == 0) {
      if constexpr (t == Q_SLAB) {
        const u32 slab = (u32)((hq * 16 + rb) * SLAB);
        const bool h0set = (0x47 >> (c & 7)) & 1, h = rg & 1, own = (c >> 3) == hpl;
        u32x2 a = pk4(dp), b = pk4(dm);
        const u32 o1 = rg < 2 ? slab + (hpl * 2 + rg) * 1024 + l * 16 : (own && h0set != h) ? slab + 4096 + l * 16 : OOB;
        __builtin_amdgcn_raw_buffer_store_b128(u32x4{ns(a[0]), ns(a[1]), ns(b[0]), ns(b[1])}, srdS, o1, 0, 16);
        const float x0 = g0 ? df[2] : df[0], x1 = g0 ? df[3] : df[1];
        const bool first = rg < 2 ? !h : h0set;
        const u32 o2 = rg < 2 ? slab + 5120 + (hpl * 2 + rg) * 256 + l * 4 : own ? slab + 6144 + l * 4 : OOB;
        __builtin_amdgcn_raw_buffer_store_b32(ns(pk(first ? x0 : 0.f, first ? 0.f : x1)), srdS, o2, 0, 16);
        STAMP(6);
      }
      if constexpr (t == Q_L1 || t == Q_L1C) {
        const int dw = w * 16 + c;
        const u32 dof = (u32)((rb * 100 + (dw < 100 ? dw : 99)) * 4);
        const u32 go = (u32)((hq * 16 + 4 * g) * SLAB) + dof;
        if constexpr (t == Q_L1) {
          asm volatile("buffer_load_dword %0, %4, %5, 0 offen sc1\n\tbuffer_load_dword %1, %4, %5, %6 offen sc1\n\t"
                       "buffer_load_dword %2, %4, %5, %7 offen sc1\n\tbuffer_load_dword %3, %4, %5, %8 offen sc1"
                       : "=&v"(g0_), "=&v"(g1_), "=&v"(g2_), "=&v"(g3_)
                       : "v"(go), "s"(srdS), "s"(SLAB), "s"(2 * SLAB), "s"(3 * SLAB)
                       : "memory");
        } else {
          asm volatile("s_waitcnt vmcnt(%4)" : "+v"(g0_), "+v"(g1_), "+v"(g2_), "+v"(g3_) : "n"(wait_h<BR>(E, EQ + Q_L1)) : "memory");
          _Pragma("nounroll") for (int spin = 0; spin < (1 << 20); ++spin) {
            asm volatile("" ::: "memory");
            if (!__any(umax(umax3(g0_, g1_, g2_), g3_) == SENT)) break;
            g0_ = __builtin_amdgcn_raw_buffer_load_b32(srdS, go, 0, 16);
            g1_ = __builtin_amdgcn_raw_buffer_load_b32(srdS, go, SLAB, 16);
            g2_ = __builtin_amdgcn_raw_buffer_load_b32(srdS, go, 2 * SLAB, 16);
            g3_ = __builtin_amdgcn_raw_buffer_load_b32(srdS, go, 3 * SLAB, 16);
          }
          const u32 sel = c == 0 ? 0x00003f80u : c == 1 ? 0x3f800000u : 0u;
          f32x4 sum = mfma32(u32x4{sel, sel, sel, sel}, u32x4{g0_, g1_, g2_, g3_}, z4);
          __builtin_amdgcn_raw_buffer_store_b32(ns(pk(sum[0], sum[1])), srdS, g == 0 ? (u32)((128 + hq) * SLAB) + dof : OOB, 0, 16);
          STAMP(7);
        }
      }
    }
  };
  a0[0] = z4; a0[1] = z4; a1[0] = z4; a1[1] = z4; aL = z4;
  static_for<0, 18>([&](auto tt) {
    constexpr int t = tt.value, E = EQ + t;
    if constexpr (BR == 0) {
      if constexpr (t == 3 || t == 6) bar_only();
    } else if constexpr ((t & 1) && t < 17) {
      if constexpr (t == 1) wait_bar<wait_n<BR>(E, 18 + (t >> 1) + 1)>();
      else wait_vm_bar<wait_n<BR>(E, 18 + (t >> 1) + 1)>();
    }
    u32x4 c0 = f0, c1 = f1, cl = fl;
    SCHED_FENCE();
    a0[0] = mfma32(c0, xf[t][0], a0[0]);
    load_q_frags(IC<t + 1>{}, IC<0>{});
    hook(tt, IC<0>{});
    SCHED_FENCE();
    a0[1] = mfma32(c0, xf[t][1], a0[1]);
    load_q_frags(IC<t + 1>{}, IC<1>{});
    hook(tt, IC<1>{});
    ev_dma(IC<E>{}, IC<0>{});
    SCHED_FENCE();
    a1[0] = mfma32(c1, xf[t][0], a1[0]);
    load_q_frags(IC<t + 1>{}, IC<2>{});
    hook(tt, IC<2>{});
    SCHED_FENCE();
    a1[1] = mfma32(c1, xf[t][1], a1[1]);
    hook(tt, IC<3>{});
    ev_dma(IC<E>{}, IC<1>{});
    SCHED_FENCE();
    aL = mfma32(cl, xf[t][0], aL);
    asm volatile("" : "+v"(aL));
    hook(tt, IC<4>{});
    SCHED_FENCE();
  });
  STAMP(8);
  refresh();
  if (g < 2) lds_w64(lb + QLX + ((2 * rg + hpl) * 2 + g) * 128 + c * 8, pk4(aL));
  bar_lds();
  const u32 ofo = (u32)((BR * 128 + rb * 8 + rg * 2) * 18 * 1024);
  const int hp = hq * 2 + hpl;
  auto store_o = [&](int sb, f32x4 o0, f32x4 o1, float l0, float l1, float r0, float r1) {
    u32x2 a = pk4(o0 * r0), b = pk4(o1 * r1);
    __builtin_amdgcn_raw_buffer_store_b128(u32x4{ns(a[0]), ns(a[1]), ns(b[0]), ns(b[1])}, srdF, ofo + (sb * 18 + hp) * 1024 + l * 16, 0, 16);
    lds_w128(lb + LOWN + ((rg * 2 + sb) * 2 + hpl) * 1024 + l * 16, u32x4{a[0], a[1], b[0], b[1]});
    float rr = g0 ? r1 : r0;
    u32 lv = ns(pk(l0 * rr, l1 * rr));
    auto sw = __builtin_amdgcn_permlane16_swap(lv, lv, false, false);
    u32 s0 = sw[0], s1 = sw[1];
    __builtin_amdgcn_raw_buffer_store_b64(u32x2{s0, s1}, srdF, g == 0 ? ofo + (sb * 18 + 16 + (hq >> 2)) * 1024 + (c + 16 * (hq & 3)) * 16 + 8 * hpl : OOB, 0, 16);
  };

  if (BR == 0) {
    HH h0, h1;
    {
      u32x2 qa0 = pk4(a0[0]), qb0 = pk4(a1[0]), qa1 = pk4(a0[1]), qb1 = pk4(a1[1]);
      h0.b0[0] = qa0[0]; h0.b0[1] = qa0[1]; h0.b1[0] = qb0[0]; h0.b1[1] = qb0[1];
      h1.b0[0] = qa1[0]; h1.b0[1] = qa1[1]; h1.b1[0] = qb1[0]; h1.b1[1] = qb1[1];
    }
    const u32 ql0 = lb + QLX + ((2 * rg + hpl) * 2 + hpl) * 128 + c * 8, ql1 = lb + QLX + ((2 * rg + 1 - hpl) * 2 + hpl) * 128 + c * 8;
    const u32 kf = (u32)((128 + hq) * SLAB);
    const int l8 = g * 16 + (c & 7) + 8 * hpl;
    u32x4 AM0, AM1, AMLm;
    u32 AL0, AL1, ALL;
    auto load_kv = [&]() {
      AM0 = __builtin_amdgcn_raw_buffer_load_b128(srdS, kf + hpl * 2048 + l * 16, 0, 16);
      AM1 = __builtin_amdgcn_raw_buffer_load_b128(srdS, kf + hpl * 2048 + 1024 + l * 16, 0, 16);
      AMLm = __builtin_amdgcn_raw_buffer_load_b128(srdS, kf + 4096 + l8 * 16, 0, 16);
      AL0 = __builtin_amdgcn_raw_buffer_load_b32(srdS, kf + 5120 + hpl * 512 + l * 4, 0, 16);
      AL1 = __builtin_amdgcn_raw_buffer_load_b32(srdS, kf + 5120 + hpl * 512 + 256 + l * 4, 0, 16);
      ALL = __builtin_amdgcn_raw_buffer_load_b32(srdS, kf + 6144 + l8 * 4, 0, 16);
    };
    static_for<0, HH_N>([&](auto st) {
      if (st.value == L2ST) { SCHED_FENCE(); load_kv(); SCHED_FENCE(); }
      if constexpr (st.value >= HQ0 && (st.value - HQ0) % HQS == 0 && (st.value - HQ0) / HQS < NEV - EH) {
        constexpr int E = EH + (st.value - HQ0) / HQS;
        if constexpr (SC<BR>::s.nop[E] > 0) { SCHED_FENCE(); ev_dma(IC<E>{}, IC<0>{}); SCHED_FENCE(); }
      }
      hh_step<st.value, true>(h0, ql0, phf, sumf, lo, ninf);
      hh_step<st.value, true>(h1, ql1, phf, sumf, lo, ninf);
    });
    hh_left<true>(h0, lo);
    hh_left<true>(h1, lo);
    STAMP(9);
    _Pragma("nounroll") for (int spin = 0; spin < (1 << 20); ++spin) {
      asm volatile("" ::: "memory");
      if (!__any(umax4(umax4(umax4(umax3(AL0, AL1, ALL), AM0), AM1), AMLm) == SENT)) break;
      load_kv();
    }
    STAMP(10);
    const bool h0set = (0x47 >> (c & 7)) & 1;
    u32x4 AML0, AML1;
#pragma unroll
    for (int e = 0; e < 4; ++e) { AML0[e] = h0set ? AMLm[e] : 0u; AML1[e] = h0set ? 0u : AMLm[e]; }
#pragma unroll
    for (int sb = 0; sb < 2; ++sb) {
      const HH& h = sb ? h1 : h0;
      u32x4 bl = {h.bl, 0u, 0u, 0u};
      f32x4 O0 = mfma32(u32x4{AL0, 0u, 0u, 0u}, bl, mfma32(AM0, h.b0, z4));
      f32x4 O1 = mfma32(u32x4{AL1, 0u, 0u, 0u}, bl, mfma32(AM1, h.b1, z4));
      f32x4 OL = mfma32(u32x4{ALL, 0u, 0u, 0u}, bl, mfma32(AML1, h.b1, mfma32(AML0, h.b0, z4)));
      float r0 = __builtin_amdgcn_rcpf(fmaf(h.S[0], 1e-6f, OL[2])), r1 = __builtin_amdgcn_rcpf(fmaf(h.S[2], 1e-6f, OL[3]));
      store_o(sb ^ hpl, O0, O1, OL[0], OL[1], r0, r1);
    }
  } else {
    auto ev3 = [&](auto EE) { SCHED_FENCE(); ev_dma(EE, IC<0>{}); ev_dma(EE, IC<1>{}); ev_dma(EE, IC<2>{}); SCHED_FENCE(); };
    const u32 kx = lb + LKX + ((rg * 2 + hpl) * 2) * 512 + l * 8;
    u32x2 pM0 = lds_r64(kx), pM1 = lds_r64(kx + 512);
    u32x2 kL[3];
#pragma unroll
    for (int i = 0; i < 3; ++i) kL[i] = lds_r64(lb + KLX + ((2 * rg + i) * 2 + hpl) * 128 + c * 8);
    ev3(IC<EH>{});
    const int par = c & 1;
    const bool ina = (4 * g + par) > c, inb = (4 * g + par + 2) > c;
    const float sc = 0.23570226039551584f * LOG2E;
    float la[2][2], lbv[2][2], mx[2][2];
    u32x2 qM[2][2] = {{pk4(a0[0]), pk4(a0[1])}, {pk4(a1[0]), pk4(a1[1])}};
    unswap(qM[0][0], qM[0][1]); unswap(qM[1][0], qM[1][1]);
#pragma unroll
    for (int sb = 0; sb < 2; ++sb) {
      u32x2 qlv = lds_r64(lb + QLX + ((2 * rg + sb) * 2 + hpl) * 128 + c * 8);
      u32x2 q0 = qM[0][sb], q1 = qM[1][sb];
      u32x4 b0 = {q0[0], q0[1], g == 0 ? qlv[0] : 0u, 0u};
      u32x4 b1 = {q1[0], q1[1], 0u, g == 0 ? qlv[1] : 0u};
      u32x2 kp0 = sb ? kM[0][0] : pM0, kp1 = sb ? kM[1][0] : pM1, kpl = kL[sb], kcl = kL[sb + 1];
      f32x4 sp0 = mfma32(u32x4{kp0[0], kp0[1], kpl[0], kpl[1]}, b0, z4);
      f32x4 sp1 = mfma32(u32x4{kp1[0], kp1[1], kpl[0], kpl[1]}, b1, z4);
      f32x4 sc0 = mfma32(u32x4{kM[0][sb][0], kM[0][sb][1], kcl[0], kcl[1]}, b0, z4);
      f32x4 sc1 = mfma32(u32x4{kM[1][sb][0], kM[1][sb][1], kcl[0], kcl[1]}, b1, z4);
      if (sb) ev3(IC<EH + 2>{}); else ev3(IC<EH + 1>{});
      const bool dead = (sb == 0) && (rb == 0) && (rg == 0);
      float pa0 = par ? sp0[1] : sp0[0], pb0 = par ? sp0[3] : sp0[2], pa1 = par ? sp1[1] : sp1[0], pb1 = par ? sp1[3] : sp1[2];
      if (dead) { pa0 = pb0 = pa1 = pb1 = -1e30f; }
      la[0][sb] = (ina ? pa0 : (par ? sc0[1] : sc0[0])) * sc;
      lbv[0][sb] = (inb ? pb0 : (par ? sc0[3] : sc0[2])) * sc;
      la[1][sb] = (ina ? pa1 : (par ? sc1[1] : sc1[0])) * sc;
      lbv[1][sb] = (inb ? pb1 : (par ? sc1[3] : sc1[2])) * sc;
      mx[0][sb] = fmaxf(la[0][sb], lbv[0][sb]);
      mx[1][sb] = fmaxf(la[1][sb], lbv[1][sb]);
    }
    float tm = redmax4(mx[0][0], mx[0][1], mx[1][0], mx[1][1], ninf);
    bcast4(tm, mx[0][0], mx[0][1], mx[1][0], mx[1][1]);
    ev3(IC<EH + 3>{});
    const u32 vb = lb + lvi(BR) + (rg * 32 + 4 * g + q) * VIS + hpl * 80;
#pragma unroll
    for (int sb = 0; sb < 2; ++sb) {
      const u32 v0 = vb + sb * 16 * VIS, v1 = v0 + 16 * VIS;
      u32x4 A0 = frag_tr(v0 + 8 * p, v1 + 8 * p), A1 = frag_tr(v0 + 32 + 8 * p, v1 + 32 + 8 * p);
      u32x4 AL = frag_tr(v0 + 64 + 8 * (p & 1), v1 + 64 + 8 * (p & 1));
      u32x4 P[2];
#pragma unroll
      for (int h = 0; h < 2; ++h) {
        float ea = __builtin_amdgcn_exp2f(la[h][sb] - mx[h][sb]), eb = __builtin_amdgcn_exp2f(lbv[h][sb] - mx[h][sb]);
        u32 wa_ = par ? pk(0.f, ea) : pk(ea, 0.f), wb_ = par ? pk(0.f, eb) : pk(eb, 0.f);
        P[h] = u32x4{ina ? wa_ : 0u, inb ? wb_ : 0u, ina ? 0u : wa_, inb ? 0u : wb_};
      }
      f32x4 O0 = mfma32(A0, P[0], z4), O1 = mfma32(A1, P[1], z4);
      f32x4 L0 = mfma32(AL, P[0], z4), L1 = mfma32(AL, P[1], z4);
      float r0 = __builtin_amdgcn_rcpf(L0[2]), r1 = __builtin_amdgcn_rcpf(L1[2]);
      store_o(sb, O0, O1, g0 ? L1[0] : L0[0], g0 ? L1[1] : L0[1], r0, r1);
    }
  }
  STAMP(11);
  refresh();
  wait_bar<4>();
  STAMP(12);
  const u32 wrow2 = lb + (16 * g1 + 8 * g0 + 2 * q) * 144;
  u32 oa[3], oa4[3];
#pragma unroll
  for (int i = 0; i < 3; ++i) {
    oa[i] = wrow2 + 8 * p + i * 57344; oa4[i] = wrow2 + 128 + 8 * (p & 1) + i * 57344;
    asm volatile("" : "+v"(oa[i]), "+v"(oa4[i]));
  }
  u32x4 wf[18][5], of[18];
  auto load_wf = [&](auto TT, auto II) {
    constexpr int t = TT.value, i = II.value;
    if constexpr (t < 18) {
      constexpr u32 cb = wo_at(BR, t < 4 ? 1 - (t >> 1) : t >> 1) + (t & 1) * 32 * 144, s = cb / 57344, o = cb % 57344;
      if constexpr (i < 4) wf[t][i] = frag_tr(oa[s] + o + 32 * i, oa[s] + o + 144 + 32 * i);
      else wf[t][4] = frag_tr(oa4[s] + o, oa4[s] + o + 144);
    }
  };
  const u32 ofl = (u32)((BR * 128 + rb * 8 + w) * 18 * 1024 + l * 16);
  f32x4 acc[5];
  u32 seen;
  auto kstep = [&](auto tt) {
    constexpr int t = tt.value;
    static_for<0, 5>([&](auto ii) {
      constexpr int i = ii.value;
      acc[i] = mfma32(wf[t][i], of[t], t ? acc[i] : z4);
      if constexpr (i == 1 && t >= 4) seen = umax4(seen, of[t]);
      load_wf(IC<t + PRE>{}, ii);
      SCHED_FENCE();
    });
  };
  _Pragma("nounroll") for (int pass = 0; pass < (1 << 16); ++pass) {
    of[2] = __builtin_amdgcn_raw_buffer_load_b128(srdF, ofl, 16 * 1024, 16);
    of[3] = __builtin_amdgcn_raw_buffer_load_b128(srdF, ofl, 17 * 1024, 16);
    of[0] = lds_r128(lb + LOWN + w * 2048 + l * 16);
    of[1] = lds_r128(lb + LOWN + w * 2048 + 1024 + l * 16);
    SCHED_FENCE();
    static_for<0, PRE * 5>([&](auto t) { load_wf(IC<t.value / 5>{}, IC<t.value % 5>{}); });
    SCHED_FENCE();
    kstep(IC<0>{}); kstep(IC<1>{});
    _Pragma("nounroll") for (int spin = 0; spin < (1 << 20); ++spin) {
      asm volatile("" ::: "memory");
      if (!__any(umax4(umax4(0u, of[2]), of[3]) == SENT)) break;
      of[2] = __builtin_amdgcn_raw_buffer_load_b128(srdF, ofl, 16 * 1024, 16);
      of[3] = __builtin_amdgcn_raw_buffer_load_b128(srdF, ofl, 17 * 1024, 16);
    }
    STAMP(13);
#pragma unroll
    for (int t = 4; t < 18; ++t) of[t] = __builtin_amdgcn_raw_buffer_load_b128(srdF, ofl, ((t - 2 + 2 * hq) & 15) * 1024, 16);
    SCHED_FENCE();
    seen = 0;
    static_for<2, 18>(kstep);
    if (!__any(seen == SENT)) break;
  }
  STAMP(14);
  if (BR == 0 && rb == 0 && hq == 0 && tid == 0) __hip_atomic_store((GLB_AS u64*)(ws + WS_EPOCH), tag, __ATOMIC_RELAXED, __HIP_MEMORY_SCOPE_AGENT);
  char* op = out + (size_t)(rb * 128 + w * 16 + c) * 2304 + BR * 1152 + hq * 144;
  __amdgpu_buffer_rsrc_t srdU = __builtin_amdgcn_make_buffer_rsrc((void*)out, (short)0, 2048 * 2304, 0x00020000);
  const u32 oo = (u32)((rb * 128 + w * 16 + c) * 2304 + BR * 1152 + hq * 144);
#pragma unroll
  for (int i = 0; i < 2; ++i) {
    u32x2 a = pk4(acc[2 * i]), b = pk4(acc[2 * i + 1]);
    auto s0 = __builtin_amdgcn_permlane16_swap(a[0], b[0], false, false);
    auto s1 = __builtin_amdgcn_permlane16_swap(a[1], b[1], false, false);
    u32 s00 = s0[0], s01 = s0[1], s10 = s1[0], s11 = s1[1];
    *(u32x4*)(op + 64 * i + 32 * g0 + 16 * g1) = u32x4{s00, s10, s01, s11};
  }
  if (g < 2) *(u32x2*)(op + 128 + 8 * g) = pk4(acc[4]);
  STAMP(15);
  RSTAMP(31);
  STAMP_FLUSH();
  __builtin_amdgcn_endpgm();
}

__global__ __launch_bounds__(512, 2) void fused(const char* __restrict__ XA, const char* __restrict__ WA, const char* __restrict__ PHI,
                                                const char* __restrict__ WOA, const char* __restrict__ XB, const char* __restrict__ WB,
                                                const char* __restrict__ WOB, char* ws, char* out) {
  __shared__ __attribute__((aligned(16))) char smem[LDS_BYTES];
  const u32 lb = (u32)(uintptr_t)(LDS_AS char*)smem;
  const int b = blockIdx.x, x = b & 7, j = b >> 3, xx = x & 3;
  const int rb = xx * 4 + (j >> 3), hq = j & 7;
  if (x < 4) body<0>(XA, WA, PHI, WOA, ws, out, rb, hq, j, lb);
  else body<1>(XB, WB, PHI, WOB, ws, out, rb, hq, j, lb);
}
}

extern "C" void kernel_launch(void* const* d_in, const int* in_sizes, int n_in, void* d_out, int out_size, void* d_ws, size_t ws_size,
                              hipStream_t stream) {
  hk::fused<<<256, 512, 0, stream>>>((const char*)d_in[0], (const char*)d_in[1], (const char*)d_in[2], (const char*)d_in[3], (const char*)d_in[4],
                                     (const char*)d_in[5], (const char*)d_in[6], (char*)d_ws, (char*)d_out);
}
```

```cpp
#define PP 34
#define C1 44
#include <hip/hip_runtime.h>
#include <stdint.h>

namespace hk {
typedef unsigned int u32;
typedef unsigned long long u64;
typedef unsigned short u16;
typedef float f32x4 __attribute__((ext_vector_type(4)));
typedef float f32x2 __attribute__((ext_vector_type(2)));
typedef u32 u32x4 __attribute__((ext_vector_type(4)));
typedef u32 u32x2 __attribute__((ext_vector_type(2)));
typedef __bf16 bf16x8 __attribute__((ext_vector_type(8)));
typedef __bf16 bf16x2 __attribute__((ext_vector_type(2)));
typedef short s16x4 __attribute__((ext_vector_type(4)));
#define LDS_AS __attribute__((address_space(3)))
#define GLB_AS __attribute__((address_space(1)))
#define DEVI __device__ __forceinline__
#define SCHED_FENCE() __builtin_amdgcn_sched_barrier(0)

template <int V> struct IC { static constexpr int value = V; };
template <int I, int N, class F>
DEVI void static_for(F&& f) {
  if constexpr (I < N) { f(IC<I>{}); static_for<I + 1, N>(f); }
}

constexpr int XROW = 1152, WROW = 3456;
constexpr size_t WS_OF = 0;
constexpr int SLAB = 6400;
constexpr size_t WS_SLAB = 5u << 20;
constexpr size_t WS_FLAG = 6u << 20;
constexpr size_t WS_EPOCH = WS_FLAG + 65536;
constexpr size_t WS_OFLAG = WS_FLAG + 4096;
constexpr u32 OOB = 0x7fffffffu;
constexpr u32 SENT = 0xffffffffu;
constexpr int WU = 9216;
constexpr int LPK = 1024, PKS = 296;
constexpr int S0 = 3968, SS = 34944;
constexpr int SK = 16384, SV = SK + WU + 16;
constexpr int WQ = S0 + SS;
constexpr int LT = WQ + 9 * WU;
constexpr int LM = S0 + 4 * SS;
constexpr int LPHF = LM;
constexpr int LSUM = LPHF + 8192;
constexpr int LHALO = LM;
constexpr int KLX = LM + 11264;
constexpr int QLX = KLX + 2304;
constexpr int LSCR = QLX + 2048;
constexpr int VIS = 168;
constexpr int lvi(int br) { return br ? 1024 : LT; }
constexpr int LKX = 25600;
constexpr int wo_at(int br, int m) {
  if (br == 0) return m < 4 ? 1024 + m * WU : m < 6 ? LT + (m - 4) * WU : WQ + (m - 6) * WU;
  return m < 2 ? LT + m * WU : m == 2 ? LM : WQ + (m - 3) * WU;
}
constexpr int LOWN = WQ + 6 * WU;
constexpr int LDS_BYTES = 163840;
static_assert(LSCR + 512 <= LDS_BYTES && LPK + 2688 <= S0 && LPK + 128 * PKS <= WQ && LT + 128 * VIS <= LM && 1024 + 144 * VIS <= LKX && LKX + 8192 <= WQ &&
              SV + WU <= SS && 3 * WU <= SS && 7 * WU <= 2 * SS && S0 % 128 == 0 && SS % 128 == 0, "lds");

DEVI u32x2 tr_read(u32 a) { return __builtin_bit_cast(u32x2, __builtin_amdgcn_ds_read_tr16_b64_v4i16((LDS_AS s16x4*)a)); }
DEVI u32x4 frag_tr(u32 a0, u32 a1) { u32x2 x = tr_read(a0), y = tr_read(a1); return u32x4{x[0], x[1], y[0], y[1]}; }
DEVI u32x4 lds_r128(u32 a) { return *(LDS_AS u32x4*)a; }
DEVI u32x2 lds_r64(u32 a) { return *(LDS_AS u32x2*)a; }
DEVI u32 lds_r32(u32 a) { return *(LDS_AS u32*)a; }
DEVI u32 lds_r16(u32 a) { return *(LDS_AS u16*)a; }
DEVI void lds_w128(u32 a, u32x4 v) { *(LDS_AS u32x4*)a = v; }
DEVI void lds_w64(u32 a, u32x2 v) { *(LDS_AS u32x2*)a = v; }
DEVI void lds_w32(u32 a, u32 v) { *(LDS_AS u32*)a = v; }
DEVI f32x4 mfma32(u32x4 a, u32x4 b, f32x4 c) {
  return __builtin_amdgcn_mfma_f32_16x16x32_bf16(__builtin_bit_cast(bf16x8, a), __builtin_bit_cast(bf16x8, b), c, 0, 0, 0);
}
DEVI u32 pk(float a, float b) { f32x2 v = {a, b}; return __builtin_bit_cast(u32, __builtin_convertvector(v, bf16x2)); }
DEVI u32x2 pk4(f32x4 v) { return u32x2{pk(v[0], v[1]), pk(v[2], v[3])}; }
DEVI float max3(float a, float b, float c) { return fmaxf(fmaxf(a, b), c); }
DEVI u32 umax(u32 a, u32 b) { return a > b ? a : b; }
DEVI u32 umax3(u32 a, u32 b, u32 c) { return umax(umax(a, b), c); }
DEVI u32 umax4(u32 m, u32x4 v) { return umax3(umax3(m, v[0], v[1]), v[2], v[3]); }
DEVI u32 ns(u32 v) { return v < 0xfffffffeu ? v : 0xfffffffeu; }

DEVI void dma_full(u32 lds, u32 voff, __amdgpu_buffer_rsrc_t srd, u32 soff) {
  asm volatile("s_mov_b32 m0, %0\n\ts_nop 0\n\tbuffer_load_dwordx4 %1, %2, %3 offen lds" ::"s"(lds), "v"(voff), "s"(srd), "s"(soff) : "memory");
}
DEVI void dma_mask(u32 lds, u32 voff, __amdgpu_buffer_rsrc_t srd, u32 soff, u64 mask) {
  asm volatile("s_mov_b32 m0, %0\n\ts_mov_b64 exec, %4\n\tbuffer_load_dwordx4 %1, %2, %3 offen lds\n\ts_mov_b64 exec, -1" ::"s"(lds), "v"(voff), "s"(srd),
               "s"(soff), "s"(mask)
               : "memory");
}
template <int N>
DEVI void wait_bar() { asm volatile("s_waitcnt vmcnt(%0) lgkmcnt(0)\n\ts_barrier" ::"n"(N) : "memory"); }
template <int N>
DEVI void wait_vm_bar() { asm volatile("s_waitcnt vmcnt(%0)\n\ts_barrier" ::"n"(N) : "memory"); }
DEVI void bar_lds() { asm volatile("s_waitcnt lgkmcnt(0)\n\ts_barrier" ::: "memory"); }
DEVI void bar_only() { asm volatile("s_barrier" ::: "memory"); }

DEVI float redmax4(float a, float b, float c, float d, float ninf) {
  auto r1 = __builtin_amdgcn_permlane32_swap(__float_as_uint(a), __float_as_uint(b), false, false);
  auto r2 = __builtin_amdgcn_permlane32_swap(__float_as_uint(c), __float_as_uint(d), false, false);
  u32 r10 = r1[0], r11 = r1[1], r20 = r2[0], r21 = r2[1];
  float ab = max3(__uint_as_float(r10), __uint_as_float(r11), ninf);
  float cd = max3(__uint_as_float(r20), __uint_as_float(r21), ninf);
  auto r3 = __builtin_amdgcn_permlane16_swap(__float_as_uint(ab), __float_as_uint(cd), false, false);
  u32 r30 = r3[0], r31 = r3[1];
  return max3(__uint_as_float(r30), __uint_as_float(r31), ninf);
}
DEVI void bcast4(float t, float& a, float& b, float& c, float& d) {
  u32 tu = __float_as_uint(t);
  auto s1 = __builtin_amdgcn_permlane16_swap(tu, tu, false, false);
  u32 x = s1[0], y = s1[1];
  auto s2 = __builtin_amdgcn_permlane32_swap(x, x, false, false);
  auto s3 = __builtin_amdgcn_permlane32_swap(y, y, false, false);
  u32 s20 = s2[0], s21 = s2[1], s30 = s3[0], s31 = s3[1];
  a = __uint_as_float(s20); b = __uint_as_float(s21); c = __uint_as_float(s30); d = __uint_as_float(s31);
}

constexpr float LOG2E = 1.4426950408889634f;
constexpr u32 ONES = 0x3f803f80u;

struct HH {
  u32x4 f0, f1, f2, f3;
  u32x4 b0, b1;
  u32 bl;
  f32x4 T0, T1, TL, S;
  float a, b, c, d;
  f32x4 p0, q0, p1, q1;
  float l0, l1;
};
constexpr int HH_N = 106;
DEVI void swap32(float& a, float& b) {
  auto r = __builtin_amdgcn_permlane32_swap(__float_as_uint(a), __float_as_uint(b), false, false);
  u32 x = r[0], y = r[1]; a = __uint_as_float(x); b = __uint_as_float(y);
}
DEVI void swap16(float& a, float& b) {
  auto r = __builtin_amdgcn_permlane16_swap(__float_as_uint(a), __float_as_uint(b), false, false);
  u32 x = r[0], y = r[1]; a = __uint_as_float(x); b = __uint_as_float(y);
}
template <int I, bool QS>
DEVI void hh_step(HH& s, u32 lft, u32 phf, u32 sumf, bool lo, float ninf) {
  const f32x4 z = {0.f, 0.f, 0.f, 0.f};
  if constexpr (I == 0) {
    u32x2 x = lds_r64(lft); s.b0[2] = x[0]; s.b0[3] = x[1];
    asm volatile("" ::: "memory");
    u32x2 y = lds_r64(lft); s.b1[2] = y[0]; s.b1[3] = y[1];
    s.f0 = lds_r128(phf);
  } else if constexpr (I == 1) { s.f1 = lds_r128(phf + 1024); s.f2 = lds_r128(phf + 2048);
  } else if constexpr (I == 2) { s.f3 = lds_r128(phf + 3072);
  } else if constexpr (I == 3) { s.T0 = mfma32(s.f0, s.b0, z);
  } else if constexpr (I == 4) { s.TL = mfma32(s.f2, s.b0, z);
  } else if constexpr (I == 5) { s.T1 = mfma32(s.f1, s.b1, z);
  } else if constexpr (I == 6) { s.TL = mfma32(s.f3, s.b1, s.TL);
  } else if constexpr (I == 7) { s.f0 = lds_r128(sumf); s.f1 = lds_r128(sumf + 1024); s.f2 = lds_r128(sumf + 2048);
  } else if constexpr (I == 8) { s.a = max3(s.T0[0], s.T0[1], s.T0[2]);
  } else if constexpr (I == 9) { s.c = max3(-s.T0[0], -s.T0[1], -s.T0[2]);
  } else if constexpr (I == 10) { s.b = max3(s.T1[0], s.T1[1], s.T1[2]);
  } else if constexpr (I == 11) { s.d = max3(-s.T1[0], -s.T1[1], -s.T1[2]);
  } else if constexpr (I == 12) {
  } else if constexpr (I == 13) { s.a = max3(s.a, s.T0[3], s.TL[0]);
  } else if constexpr (I == 14) { s.b = max3(s.b, s.T1[3], s.TL[1]);
  } else if constexpr (I == 15) { s.c = max3(s.c, -s.T0[3], -s.TL[0]);
  } else if constexpr (I == 16) { s.d = max3(s.d, -s.T1[3], -s.TL[1]);
  } else if constexpr (I == 17) { swap32(s.a, s.b);
  } else if constexpr (I == 18) { swap32(s.c, s.d);
  } else if constexpr (I == 19) { s.a = max3(s.a, s.b, ninf);
  } else if constexpr (I == 20) { s.c = max3(s.c, s.d, ninf);
  } else if constexpr (I == 21) { swap16(s.a, s.c);
  } else if constexpr (I == 22) { s.a = max3(s.a, s.c, ninf);
  } else if constexpr (I == 23) { s.a *= -LOG2E;
  } else if constexpr (I == 24) { s.c = s.a; swap16(s.a, s.c);
  } else if constexpr (I == 25) { s.b = s.a; swap32(s.a, s.b);
  } else if constexpr (I == 26) { s.d = s.c; swap32(s.c, s.d);
  } else if constexpr (I == 27) { s.l0 = lo ? s.a : s.c;
  } else if constexpr (I == 28) { s.l1 = lo ? s.b : s.d;
  } else if constexpr (I == 29) { s.l0 = fmaf(s.TL[0], lo ? LOG2E : -LOG2E, s.l0);
  } else if constexpr (I == 30) { s.l1 = fmaf(s.TL[1], lo ? LOG2E : -LOG2E, s.l1);
  } else if constexpr (I == 31) { s.l0 = __builtin_amdgcn_exp2f(s.l0);
  } else if constexpr (I == 32) { s.l1 = __builtin_amdgcn_exp2f(s.l1);
  } else if constexpr (I == 33) { s.bl = pk(s.l0, s.l1);
  } else if constexpr (I == 34) { s.S = mfma32(s.f2, u32x4{s.bl, 0u, 0u, 0u}, z);
  } else if constexpr (I < 77) {
    constexpr int h = (I - 35) / 21, k = (I - 35) % 21;
    f32x4& T = h ? s.T1 : s.T0; f32x4& p = h ? s.p1 : s.p0; f32x4& q = h ? s.q1 : s.q0; u32x4& o = h ? s.b1 : s.b0;
    const float m = h ? s.b : s.a, n = h ? s.d : s.c;
    constexpr bool arg = k < 2 || ((k & 1) && k < 15);
    constexpr int e = arg ? (k < 2 ? k : (k + 1) / 2) : (k < 15 ? (k - 2) / 2 : 7);
    if constexpr (k < 16) {
      if constexpr (arg) { if constexpr (e < 4) p[e] = fmaf(T[e], LOG2E, m); else q[e - 4] = fmaf(T[e - 4], -LOG2E, n); }
      else { if constexpr (e < 4) p[e] = __builtin_amdgcn_exp2f(p[e]); else q[e - 4] = __builtin_amdgcn_exp2f(q[e - 4]); }
    } else if constexpr (k < 18) { o[k - 16] = pk(p[2 * (k - 16)], p[2 * (k - 16) + 1]);
    } else if constexpr (k < 20) { o[k - 16] = pk(q[2 * (k - 18)], q[2 * (k - 18) + 1]);
    } else { s.S = mfma32(h ? s.f1 : s.f0, o, s.S); }
  } else if constexpr (I == 77) {
  } else if constexpr (I == 78) { s.c = __builtin_amdgcn_rcpf(s.S[1]);
  } else if constexpr (I == 79) { s.d = __builtin_amdgcn_rcpf(s.S[3]);
  } else if constexpr (I == 80) { if constexpr (QS) s.c *= s.S[0]; else s.a = __builtin_amdgcn_rcpf(s.S[0]);
  } else if constexpr (I == 81) { if constexpr (QS) s.d *= s.S[2]; else s.b = __builtin_amdgcn_rcpf(s.S[2]);
  } else if constexpr (I < 106) {
    constexpr int v = (I - 82) / 6, k = (I - 82) % 6;
    if constexpr (!(QS && v >= 2)) {
      f32x4& x = v == 0 ? s.q0 : v == 1 ? s.q1 : v == 2 ? s.p0 : s.p1;
      u32x4& o = (v & 1) ? s.b1 : s.b0;
      const float r = v == 0 ? s.c : v == 1 ? s.d : v == 2 ? s.a : s.b;
      if constexpr (k < 4) x[k] *= r;
      else o[(v < 2 ? 2 : 0) + k - 4] = pk(x[2 * (k - 4)], x[2 * (k - 4) + 1]);
    }
  }
}
template <bool QS>
DEVI void hh_left(HH& s, bool lo) {
  if constexpr (QS) s.bl = pk(s.l0 * (lo ? 1.f : s.c), s.l1 * (lo ? 1.f : s.d));
  else s.bl = pk(s.l0 * (lo ? s.a : s.c), s.l1 * (lo ? s.b : s.d));
}

#define L2ST 40
#define L2ST2 72
#define PRE 7
#define PRO 2
#define LEAD 8
#define QA 2
#define QT 1
#define E_PO 43
#define E_POB 30
#define Q_L1 9
#define Q_L1C 14
#define QTB QT
#define NOPT 0
#define PCW 2
#define PCW0 0
#define QW 1
#define HQ0 4
#define HQS 8
#define C0 0
#define CS 2
#define E_PUB 11
#define E_FLD 36
#define QB 1
#define QSP 1
#define HB 3
constexpr int EM = 36, EQ = 44, EH = 62, NEV = 74;
constexpr int Q_SLAB = 2;
#define E_PUBB 15
#define E_FLDB 12
constexpr int E_PUB2 = C1 < 34 ? E_PUBB : EQ + 9;
static_assert(C1 >= 34 || ((E_PUBB & 3) == 3 && E_PUBB > C1 + 2 && E_PUBB < 35), "marks");
constexpr int e_fld(int br) { return br ? EQ + E_FLDB : E_FLD; }
constexpr int e_fchk(int br) { return br ? EQ + 16 : EQ; }
constexpr int clr_at(int E) { return E >= C1 && E < C1 + 3 ? 4 + E - C1 : E >= C0 && (E - C0) % CS == 0 && (E - C0) / CS < 4 ? (E - C0) / CS : -1; }
constexpr bool is_clr(int E) { return clr_at(E) >= 0; }
static_assert((E_PUB & 3) == 3 && E_PUB > C0 + 3 * CS && E_PUB < 35 && E_FLD >= EM && E_FLD < EQ && C1 > C0 + 3 * CS && (C1 < 34 || C1 >= EQ) && C1 < EQ + 6, "marks");
struct Sched {
  int op[NEV][3];
  int nop[NEV];
  int pos_ev[NEV + 1];
  int pos_hook[NEV];
  int last[36];
  int total;
};
constexpr int ext_ops(int br, int E) {
  return (E == (br ? E_POB : E_PO)) + (br && is_clr(E)) + (E == e_fld(br)) +
         (br ? 0 : E == EQ + Q_SLAB ? 2 : E == EQ + Q_L1 ? 4 : E == EQ + Q_L1C ? 1 : 0);
}
constexpr int op_chunk(int op) { return op < 200 ? (op - 100) >> 2 : op < 236 ? ((op - 200) >> 1) % 9 : (op - 200) >> 1; }
constexpr int op_earliest(int br, int op) {
  const int n = op_chunk(op);
  if (n < 9) return 4 * n - 1 - LEAD;
  if (n < 27) return n < 21 ? 23 : n < 25 ? 27 : 31;
  const int m = n - 27;
  if (br == 0) return m < 6 ? EQ + 3 : EQ + 6;
  return m < 2 ? 31 : m == 2 ? EQ : EQ + 2 * (m - 3) + 3;
}
constexpr int op_deadline(int br, int op) {
  const int n = op_chunk(op);
  if (n < 9) return 4 * n - 1;
  if (n < 27) return br == 0 || n == 18 ? EQ : EQ + 2 * (n - 18) - 1;
  return NEV;
}
static_assert(LEAD <= 12, "ring");
constexpr Sched make_sched(int br) {
  Sched s{};
  const int nx = br ? 3 : 2;
  int list[128] = {}, when[128] = {}, n = 0;
  for (int c = 0; c < 9; ++c) {
    for (int i = 0; i < nx; ++i) list[n++] = 100 + 4 * c + i;
    list[n++] = 200 + 2 * c; list[n++] = 201 + 2 * c; list[n++] = 218 + 2 * c; list[n++] = 219 + 2 * c;
  }
  for (int c = 18; c < 36; ++c) { list[n++] = 200 + 2 * c; list[n++] = 201 + 2 * c; }
  for (int k = 0; k < n; ++k) when[k] = 1000;
  int pos = 0, done = 0;
  for (; done < PRO * (nx + 4); ++done) { s.last[op_chunk(list[done])] = pos++; when[done] = -1; if (done == nx + 3) pos += 1 + (PCW == 2); }
  pos += PCW == 3;
  for (int E = 0; E < NEV; ++E) {
    s.pos_ev[E] = pos;
    int j = 0;
    for (int k = 0; k < n; ++k) {
      if (when[k] != 1000 || op_earliest(br, list[k]) > E) continue;
      const int c = op_chunk(list[k]);
      int q = E < EM ? (c < 9 ? QA : br ? QTB : QT) : E < EQ && br ? 0 : ext_ops(br, E) ? 1 : 2;
      if (br == 0 && E >= EQ) q = ext_ops(br, E) || (E < EH && E % QSP) ? 0 : 1;
      if (br == 1 && E >= EQ && c >= 27) q = E >= EH + 4 ? 0 : E >= EH ? HB : ext_ops(br, E) ? QB - 1 : QB;
      if (j >= q) break;
      s.op[E][j++] = list[k]; when[k] = E; ++done;
      s.last[c] = pos++;
      if (E >= op_deadline(br, list[k])) s.total = -1000;
    }
    s.nop[E] = j;
    s.pos_hook[E] = pos;
    pos += ext_ops(br, E);
  }
  s.pos_ev[NEV] = pos;
  s.total += done == n ? pos : -1000;
  return s;
}
template <int BR> struct SC { static constexpr Sched s = make_sched(BR); };
static_assert(SC<0>::s.total > 0 && SC<1>::s.total > 0, "schedule incomplete");
constexpr int imin(int a, int b) { return a < b ? a : b; }
template <int BR> constexpr int wait_n(int E, int c) {
  int n = SC<BR>::s.pos_ev[E] - SC<BR>::s.last[c] - 1;
  if (BR == 1 && E == E_PUB) n = imin(n, SC<BR>::s.pos_ev[E] - SC<BR>::s.pos_ev[C0 + 3 * CS + 1]);
  if (BR == 1 && E == E_PUB2) n = imin(n, SC<BR>::s.pos_ev[E] - SC<BR>::s.pos_ev[C1 + 3]);
  if (BR == 0 && E == EQ) n = imin(n, SC<BR>::s.pos_ev[E] - SC<BR>::s.pos_ev[E_FLD + 1]);
  return n;
}
template <int BR> constexpr int wait_h(int E, int E0) { return SC<BR>::s.pos_hook[E] - SC<BR>::s.pos_ev[E0 + 1]; }

#define STAMP(i)
#define RSTAMP(i)
#define STAMP_FLUSH()
#define STAMP_DECL

DEVI u64 next_tag(u32x2 e) {
  const u64 E = ((u64)(u32)__builtin_amdgcn_readfirstlane(e[1]) << 32) | (u32)__builtin_amdgcn_readfirstlane(e[0]);
  return E * 6364136223846793005ull + 1442695040888963407ull;
}
DEVI void poll_flags(GLB_AS u64* f, u64 tag) {
  for (int spin = 0; spin < (1 << 20); ++spin) {
    u64 v = __hip_atomic_load(f, __ATOMIC_RELAXED, __HIP_MEMORY_SCOPE_AGENT);
    if (__all(v == tag)) break;
    __builtin_amdgcn_s_sleep(1);
  }
  asm volatile("" ::: "memory");
}

template <int BR>
DEVI void body(const char* __restrict__ X, const char* __restrict__ W, const char* __restrict__ PHI, const char* __restrict__ WO, char* ws, char* out,
               int rb, int hq, int jx, u32 lb) {
  int tid = threadIdx.x;
  asm volatile("" : "+v"(tid));
  int l = tid & 63;
  const int w = __builtin_amdgcn_readfirstlane(tid >> 6);
  const int rg = w & 3, hpl = w >> 2;
  int c = l & 15, g = l >> 4, q = c >> 2, p = c & 3, g0 = g & 1, g1 = g >> 1;
  int lr = (l * 57) >> 9, lu = l - lr * 9;
  auto refresh = [&]() {
    asm volatile("" : "+v"(tid));
    l = tid & 63; c = l & 15; g = l >> 4; q = c >> 2; p = c & 3; g0 = g & 1; g1 = g >> 1;
    lr = (l * 57) >> 9; lu = l - lr * 9;
  };
  STAMP_DECL
  STAMP(0);
  RSTAMP(30);
  __amdgpu_buffer_rsrc_t srdX = __builtin_amdgcn_make_buffer_rsrc((void*)X, (short)0, 2048 * XROW, 0x00020000);
  __amdgpu_buffer_rsrc_t srdW = __builtin_amdgcn_make_buffer_rsrc((void*)W, (short)0, 576 * WROW, 0x00020000);
#define ROT (rb + 2 * hq) % 9
  const int rot = ROT;
  constexpr u64 M36 = (1ull << 36) - 1;

  u32 vx[2], vq[2], vh = 0;
#pragma unroll
  for (int i = 0; i < 2; ++i) {
    int row = 16 * w + 8 * i + (l >> 3);
    vx[i] = (u32)((rb * 128 + row) * XROW + (((l & 7) ^ ((row >> 1) & 7)) << 4));
  }
#pragma unroll
  for (int i = 0; i < 2; ++i) vq[i] = (u32)((8 * w + 4 * (lr & 1) + 2 * i + ((lr >> 1) & 1)) * WROW + hq * 144 + lu * 16);
  if (BR) {
    int row = 2 * w + ((l >> 3) & 1);
    int grow = rb * 128 - 16 + row; grow = grow < 0 ? 0 : grow;
    vh = (u32)(grow * XROW + (((l & 7) ^ ((row >> 1) & 7)) << 4));
  }
  auto kc_of = [&](int cc) { int k = cc + rot; return k >= 9 ? k - 9 : k; };
  __amdgpu_buffer_rsrc_t srdO = __builtin_amdgcn_make_buffer_rsrc((void*)WO, (short)0, 576 * XROW, 0x00020000);
  auto dma_op = [&](auto OP) {
    constexpr int op = OP.value;
    if constexpr (op < 200) {
      constexpr int cc = (op - 100) >> 2, i = op & 3;
      const int kc = kc_of(cc);
      if constexpr (i < 2) dma_full(lb + S0 + (cc & 3) * SS + w * 2048 + i * 1024, vx[i], srdX, (u32)(kc * 128));
      else dma_mask(lb + LHALO + (cc & 3) * 2048 + w * 256, vh, srdX, (u32)(kc * 128), 0xffffull);
    } else {
      constexpr int n = (op - 200) >> 1, i = op & 1;
      constexpr int at = n < 9 ? S0 + (n & 3) * SS + SK : n < 18 ? S0 + ((n - 9) & 3) * SS + SV : n < 27 ? WQ + (n - 18) * WU : wo_at(BR, n - 27);
      const u32 dst = lb + at + (2 * w + i) * 576;
      if constexpr (n < 27) {
        const int kc = kc_of(n % 9), sec = n < 9 ? 1152 : (n < 18 ? 2304 : 0);
        dma_mask(dst, vq[i], srdW, (u32)(kc * 64 * WROW + sec), M36);
      } else {
        const int hi = lr & 1, qq = 2 * i + ((lr >> 1) & 1);
        if constexpr (n > 27)
          dma_mask(dst, (u32)((36 * hpl + 20 * hi + 4 * rg + qq) * XROW + hq * 144 + lu * 16), srdO, (u32)(((n - 28 + hq) & 7) * 72 * XROW), M36);
        else dma_mask(dst, (u32)((36 * (8 * hpl + 2 * rg + hi) + 16 + qq) * XROW + hq * 144 + lu * 16), srdO, 0u, M36);
      }
    }
  };
  auto ev_dma = [&](auto EE, auto II) {
    if constexpr (II.value < SC<BR>::s.nop[EE.value]) dma_op(IC<SC<BR>::s.op[EE.value][II.value]>{});
  };
  auto warm_wo = [&]() {
    const int idx = w * 21 + l;
    const u32 po = l < 21 && idx < 162 ? (u32)((162 * jx + idx) * 128) : OOB;
    asm volatile("s_mov_b32 m0, %0\n\ts_nop 0\n\tbuffer_load_dword %1, %2, 0 offen lds" ::"s"(lb + LSCR), "v"(po), "s"(srdO) : "memory");
  };
  __amdgpu_buffer_rsrc_t srdF = __builtin_amdgcn_make_buffer_rsrc((void*)(ws + WS_OF), (short)0, 256 * 18 * 1024, 0x00020000);
  __amdgpu_buffer_rsrc_t srdS = __builtin_amdgcn_make_buffer_rsrc((void*)(ws + WS_SLAB), (short)0, 136 * SLAB, 0x00020000);
  auto mark = [&](auto EE) {
    constexpr int e = EE.value, f = (e + 2) % 3;
    const u32x4 sv = {SENT, SENT, SENT, SENT};
    const u32 ob = (u32)(((e > 3 ? 128 : 0) + rb * 8 + w) * 18 * 1024);
    if constexpr (e > 0 && f < 2) __builtin_amdgcn_raw_buffer_store_b128(sv, srdF, l * 16, ob + hq * 2048 + f * 1024, 16);
    if constexpr (e > 0 && f == 2) __builtin_amdgcn_raw_buffer_store_b128(sv, srdF, g == 0 ? c * 16 : OOB, ob + (16 + (hq >> 2)) * 1024 + (hq & 3) * 256, 16);
    if constexpr (e == 0) {
      const u32 so = tid < 400 ? (u32)((hq * 16 + rb) * SLAB + tid * 16) : tid < 425 ? (u32)((128 + hq) * SLAB + rb * 400 + (tid - 400) * 16) : OOB;
      __builtin_amdgcn_raw_buffer_store_b128(sv, srdS, so, 0, 16);
    }
  };
  GLB_AS u64* kflags = (GLB_AS u64*)(ws + WS_FLAG + hq * 256);
  GLB_AS u64* oflags = (GLB_AS u64*)(ws + WS_OFLAG + (BR * 16 + rb) * 256);
  u32x2 ep, fv;
  asm volatile("" ::"s"(srdX), "s"(srdW));
  auto warm_code = [&]() {
    u64 pc;
    int len;
    asm volatile("s_getpc_b64 %0\n.Lhk_from_%2:\n\ts_max_i32 %1, .Lhk_to_%2-.Lhk_from_%2, 0" : "=s"(pc), "=s"(len) : "n"(BR) : "scc");
    __amdgpu_buffer_rsrc_t srdC = __builtin_amdgcn_make_buffer_rsrc((void*)pc, (short)0, len & ~3, 0x00020000);
    const u32 po = l == 0 ? (u32)((PCW0 + 8 * jx + w) * 128) : OOB;
    asm volatile("s_mov_b32 m0, %0\n\ts_nop 0\n\tbuffer_load_dword %1, %2, 0 offen lds" ::"s"(lb + LSCR), "v"(po), "s"(srdC) : "memory");
  };
  STAMP(16);
  if constexpr (PCW == 1) warm_code();
  static_assert(PRO == 2, "prologue");
  auto dma_chunk = [&](auto cc) {
    constexpr int c_ = cc.value;
    dma_op(IC<100 + 4 * c_>{}); dma_op(IC<101 + 4 * c_>{});
    if constexpr (BR) dma_op(IC<102 + 4 * c_>{});
    dma_op(IC<200 + 2 * c_>{}); dma_op(IC<201 + 2 * c_>{}); dma_op(IC<218 + 2 * c_>{}); dma_op(IC<219 + 2 * c_>{});
  };
  if constexpr (BR == 0) {
    __amdgpu_buffer_rsrc_t srdP = __builtin_amdgcn_make_buffer_rsrc((void*)PHI, (short)0, 32 * 648, 0x00020000);
    dma_mask(lb + LPK + w * 336, (u32)(hq * 2592 + (w * 21 + l) * 16), srdP, 0, (1ull << 21) - 1);
  }
  dma_chunk(IC<0>{});
  {
    const char* ea = ws + WS_EPOCH;
    asm volatile("global_load_dwordx2 %0, %1, off sc1" : "=v"(ep) : "v"(ea) : "memory");
  }
  if constexpr (PCW == 2) warm_code();
  STAMP(17);
  if constexpr (BR == 0) {
    wait_vm_bar<7 + (PCW == 2)>();
    STAMP(18);
    const int h = rg & 1, lf = rg >> 1;
    const int f = lf ? 16 + (q & 1) : c;
    const bool rowok = lf ? (p == h) : true;
    const u32 pb = lb + LPK + (hpl * 2 + h) * 648 + f * 2;
    const int d0 = 2 * h + 4 * g;
    u32 e0 = lds_r16(pb + (d0 + 0) * 36), e1 = lds_r16(pb + (d0 + 1) * 36), e2 = lds_r16(pb + (d0 + 2) * 36), e3 = lds_r16(pb + (d0 + 3) * 36);
    const int dl = h ? 0 : 16;
    u32 l0 = lds_r16(pb + dl * 36), l1 = lds_r16(pb + (dl + 1) * 36);
    SCHED_FENCE();
    dma_op(IC<104>{}); dma_op(IC<105>{});
    SCHED_FENCE();
    u32 lw = (g == 0) ? (l0 | (l1 << 16)) : 0u;
    u32x4 fr = {e0 | (e1 << 16), e2 | (e3 << 16), h ? 0u : lw, h ? lw : 0u};
    if (!rowok) fr = u32x4{0, 0, 0, 0};
    lds_w128(lb + LPHF + (hpl * 4 + rg) * 1024 + l * 16, fr);
    if (w < 3) {
      u32 a = (p == 2 * w) ? ONES : 0u, b = (p == 2 * w + 1) ? ONES : 0u;
      u32 x = ((p & 1) == g1) ? ((p >> 1) ? 0x3f800000u : 0x00003f80u) : 0u;
      lds_w128(lb + LSUM + w * 1024 + l * 16, w < 2 ? u32x4{a, a, b, b} : u32x4{x, 0u, 0u, 0u});
    }
    SCHED_FENCE();
    dma_op(IC<202>{}); dma_op(IC<203>{}); dma_op(IC<220>{}); dma_op(IC<221>{});
  } else {
    bar_only();
    STAMP(18);
    dma_chunk(IC<1>{});
  }
  if constexpr (PCW == 3) warm_code();
  STAMP(1);

  const u32 xa0 = lb + S0 + (rg * 32 + hpl * 16 + c) * 128 + ((g ^ ((c >> 1) & 7)) << 4);
  const u32 xa1 = xa0 ^ 64;
  const u32 xb0 = xa0 + 2048 - hpl * 4096, xb1 = xb0 ^ 64;
  const u32 xh0 = lb + LHALO + c * 128 + ((g ^ ((c >> 1) & 7)) << 4);
  const u32 xh1 = xh0 ^ 64;
  const u32 wrow = lb + (16 * g1 + 8 * g0 + 2 * q) * 144;
  const u32 wa = wrow + hpl * 72 + 8 * p;
  const u32 wl = wrow + 32 + (p & 1) * 72 + (p >> 1) * (SV - SK);
  const u32 wh = wa + (rg & 1) * 40 + (rg >> 1) * (SV - SK);
  float ninf = -__builtin_inff();
  asm volatile("" : "+s"(ninf));

  u32x4 xf[18][2];
  const f32x4 z4 = {0.f, 0.f, 0.f, 0.f};
  f32x4 k0[2] = {z4, z4}, k1[2] = {z4, z4}, a0[2] = {z4, z4}, a1[2] = {z4, z4}, aL = z4, aH = z4, aHL = z4;
  u32x4 f0, f1, fl, fh, xh;
  auto load_kv_frags = [&](auto HH_, auto JJ) {
    constexpr int H = HH_.value, j = JJ.value, T = H >> 1, n = T >> 1, ks = T & 1;
    constexpr u32 ok = S0 + (n & 3) * SS + SK + ks * 32 * 144, ov = ok + SV - SK;
    if constexpr (H < 36) {
      if constexpr ((H & 1) == 0) {
        if constexpr (j == 0) { xf[T][0] = lds_r128((ks ? xa1 : xa0) + (n & 3) * SS); f0 = frag_tr(wa + ok, wa + ok + 144); }
        if constexpr (j == 1) { xf[T][1] = lds_r128((ks ? xb1 : xb0) + (n & 3) * SS); f1 = frag_tr(wa + ok + 40, wa + ok + 184); }
      } else {
        if constexpr (j == 0) f0 = frag_tr(wa + ov, wa + ov + 144);
        if constexpr (j == 1) f1 = frag_tr(wa + ov + 40, wa + ov + 184);
        if constexpr (j == 2) fl = frag_tr(wl + ok, wl + ok + 144);
        if constexpr (j == 3 && BR == 1) { fh = frag_tr(wh + ok, wh + ok + 144); xh = lds_r128((ks ? xh1 : xh0) + (n & 3) * 2048); }
      }
    }
  };
  wait_bar<wait_n<BR>(0, 0)>();
  STAMP(2);
  load_kv_frags(IC<0>{}, IC<0>{}); load_kv_frags(IC<0>{}, IC<1>{});
  u64 tag = 0;
  static_for<0, 36>([&](auto hh_) {
    constexpr int H = hh_.value, T = H >> 1;
    if constexpr ((H & 3) == 3 && H < 35) {
      wait_bar<wait_n<BR>(H, (H + 1) >> 2)>();
    }
    u32x4 c0 = f0, c1 = f1, cl = fl, ch = fh, cx = xh;
    f32x4(&d0)[2] = (H & 1) ? a0 : k0;
    f32x4(&d1)[2] = (H & 1) ? a1 : k1;
    SCHED_FENCE();
    d0[0] = mfma32(c0, xf[T][0], d0[0]);
    load_kv_frags(IC<H + 1>{}, IC<0>{});
    SCHED_FENCE();
    d0[1] = mfma32(c0, xf[T][1], d0[1]);
    load_kv_frags(IC<H + 1>{}, IC<1>{});
    ev_dma(hh_, IC<0>{});
    SCHED_FENCE();
    d1[0] = mfma32(c1, xf[T][0], d1[0]);
    load_kv_frags(IC<H + 1>{}, IC<2>{});
    SCHED_FENCE();
    d1[1] = mfma32(c1, xf[T][1], d1[1]);
    load_kv_frags(IC<H + 1>{}, IC<3>{});
    ev_dma(hh_, IC<1>{});
    ev_dma(hh_, IC<2>{});
    SCHED_FENCE();
    if constexpr (H & 1) {
      aL = mfma32(cl, xf[T][0], aL);
      asm volatile("" : "+v"(aL));
      if constexpr (BR == 1) {
        aH = mfma32(ch, cx, aH); aHL = mfma32(cl, cx, aHL);
        asm volatile("" : "+v"(aH), "+v"(aHL));
      }
    }
    if constexpr (H == (BR ? E_POB : E_PO)) warm_wo();
    if constexpr (H == 3) {
      asm volatile("" : "+v"(ep));
      tag = next_tag(ep);
    }
    if constexpr (BR == 1 && is_clr(H)) mark(IC<clr_at(H)>{});
    if constexpr (BR == 1 && H == E_PUB) {
      if (tid < 2) __hip_atomic_store(tid == 0 ? kflags + rb : oflags + hq - 512, tag, __ATOMIC_RELAXED, __HIP_MEMORY_SCOPE_AGENT);
    }
    if constexpr (BR == 1 && H == E_PUB2) {
      if (tid == 64) __hip_atomic_store(oflags + hq, tag, __ATOMIC_RELAXED, __HIP_MEMORY_SCOPE_AGENT);
    }
    SCHED_FENCE();
  });
  STAMP(3);
  refresh();
  const u32 wqa = lb + WQ + (16 * g1 + 8 * g0 + 2 * q) * 144 + hpl * 72 + 8 * p;
  const u32 wql = lb + WQ + (16 * g1 + 8 * g0 + 2 * q) * 144 + 32 + (p & 1) * 72;
  auto load_q_frags = [&](auto TT, auto JJ) {
    constexpr int t = TT.value, j = JJ.value;
    constexpr u32 o = (t >> 1) * WU + (t & 1) * 32 * 144;
    if constexpr (t < 18) {
      if constexpr (j == 0) f0 = frag_tr(wqa + o, wqa + o + 144);
      if constexpr (j == 1) f1 = frag_tr(wqa + o + 40, wqa + o + 184);
      if constexpr (j == 2) fl = frag_tr(wql + o, wql + o + 144);
    }
  };
  if constexpr (BR == 1) {
    wait_bar<wait_n<BR>(EQ, 18)>();
    static_for<0, 3>([&](auto j) { load_q_frags(IC<0>{}, j); });
  }
  u32x2 kM[2][2];
#pragma unroll
  for (int sb = 0; sb < 2; ++sb) { kM[0][sb] = pk4(k0[sb]); kM[1][sb] = pk4(k1[sb]); }
  auto unswap = [&](u32x2& a, u32x2& b) {
    u32x2 x = a, y = b;
#pragma unroll
    for (int e = 0; e < 2; ++e) { a[e] = hpl ? y[e] : x[e]; b[e] = hpl ? x[e] : y[e]; }
  };
  {
    constexpr int VROW0 = BR ? 16 : 0;
    auto left = [&](f32x4 v, int blk) {
      u32x2 x = pk4(v);
      const u32 va = lb + lvi(BR) + (VROW0 - 16 + blk * 16 + c) * VIS + g0 * 80 + 64;
      lds_w64(g1 ? va : lb + KLX + (blk * 2 + g0) * 128 + c * 8, u32x2{x[0], g1 ? ONES : x[1]});
      lds_w64(g1 ? va + 8 : lb + LSCR + 256 + c * 8, u32x2{x[1], ONES});
    };
    left(aL, 1 + 2 * rg + hpl);
#pragma unroll
    for (int sb = 0; sb < 2; ++sb) {
      const u32 va = lb + lvi(BR) + (VROW0 + rg * 32 + (sb ^ hpl) * 16 + c) * VIS + hpl * 80;
      lds_w64(va + 8 * g, pk4(a0[sb]));
      lds_w64(va + 32 + 8 * g, pk4(a1[sb]));
    }
    if constexpr (BR == 1) {
      unswap(kM[0][0], kM[0][1]); unswap(kM[1][0], kM[1][1]);
      if (w == 2) left(aHL, 0);
      const u32 kx = lb + LKX + l * 8;
      lds_w64(rg < 2 ? kx + (hpl * 2 + rg) * 512 : lb + lvi(BR) + c * VIS + hpl * 80 + (rg & 1) * 32 + 8 * g, pk4(aH));
      if (rg < 3) {
        const u32 b = kx + (((rg + 1) * 2 + hpl) * 2) * 512;
        lds_w64(b, kM[0][1]); lds_w64(b + 512, kM[1][1]);
      }
    }
  }
  const u32 phf = lb + LPHF + hpl * 4096 + l * 16, sumf = lb + LSUM + l * 16;
  const bool lo = g < 2;
  auto load_flags = [&]() {
    GLB_AS u64* fa = (BR == 0 && g == 0) ? kflags + c : oflags + (l & 7);
    asm volatile("global_load_dwordx2 %0, %1, off sc1" : "=v"(fv) : "v"(fa) : "memory");
  };
  auto check_flags = [&]() {
    if (!__all((((u64)fv[1] << 32) | fv[0]) == tag)) poll_flags((BR == 0 && g == 0) ? kflags + c : oflags + (l & 7), tag);
  };
  if constexpr (BR == 0) {
    bar_lds();
    STAMP(4);
    HH h0, h1;
    h0.b0[0] = kM[0][0][0]; h0.b0[1] = kM[0][0][1]; h0.b1[0] = kM[1][0][0]; h0.b1[1] = kM[1][0][1];
    h1.b0[0] = kM[0][1][0]; h1.b0[1] = kM[0][1][1]; h1.b1[0] = kM[1][1][0]; h1.b1[1] = kM[1][1][1];
    const u32 kl0 = lb + KLX + ((1 + 2 * rg + hpl) * 2 + hpl) * 128 + c * 8, kl1 = lb + KLX + ((2 + 2 * rg - hpl) * 2 + hpl) * 128 + c * 8;
    static_for<0, HH_N>([&](auto st) {
      if constexpr (st.value >= 8 && (st.value - 8) % 12 == 0 && (st.value - 8) / 12 < EQ - EM) {
        constexpr int E = EM + (st.value - 8) / 12;
        SCHED_FENCE();
        ev_dma(IC<E>{}, IC<0>{}); ev_dma(IC<E>{}, IC<1>{});
        if constexpr (E == E_PO) warm_wo();
        if constexpr (E == E_FLD) load_flags();
        SCHED_FENCE();
      }
      hh_step<st.value, false>(h0, kl0, phf, sumf, lo, ninf);
      hh_step<st.value, false>(h1, kl1, phf, sumf, lo, ninf);
    });
#pragma unroll
    for (int sb = 0; sb < 2; ++sb) {
      HH& hs = sb ? h1 : h0;
      hh_left<false>(hs, lo);
      const u32 pa = lb + LPK + (rg * 32 + (sb ^ hpl) * 16 + c) * PKS + hpl * 144;
      lds_w64(pa + 8 * g, u32x2{hs.b0[0], hs.b0[1]});
      lds_w64(pa + 32 + 8 * g, u32x2{hs.b0[2], hs.b0[3]});
      lds_w64(pa + 64 + 8 * g, u32x2{hs.b1[0], hs.b1[1]});
      lds_w64(pa + 96 + 8 * g, u32x2{hs.b1[2], hs.b1[3]});
      lds_w32(pa + 128 + 4 * g, hs.bl);
    }
    STAMP(5);
    wait_bar<wait_n<BR>(EQ, 26)>();
    asm volatile("" : "+v"(fv));
    static_for<0, 3>([&](auto j) { load_q_frags(IC<0>{}, j); });
  }
  refresh();
  f32x4 dp = z4, dm = z4, df = z4;
  u32x4 ap[4], am[4], af[4], bv[4];
  u32 g0_, g1_, g2_, g3_;
  const int rr = 4 * (4 * g0 + q) + 2 * g1;
  const u32 pa = lb + LPK + rr * PKS + hpl * 144 + (rg & 1) * 64 + 8 * p;
  const u32 pf = lb + LPK + rr * PKS + hpl * 144 + 128 + 8 * (p >> 1);
  const u32 va = lb + lvi(BR) + rr * VIS + hpl * 80 + (rg < 2 ? rg * 32 + 8 * p : 64 + 8 * (p & 1));
  auto hook = [&](auto tt, auto jj) {
    constexpr int t = tt.value, E = EQ + t, j = jj.value;
    if constexpr (BR == 0 && j < 4) {
      if constexpr (t == Q_SLAB - 2) {
        ap[j] = frag_tr(pa + j * 32 * PKS, pa + j * 32 * PKS + PKS);
        am[j] = frag_tr(pa + 32 + j * 32 * PKS, pa + 32 + j * 32 * PKS + PKS);
        af[j] = frag_tr(pf + j * 32 * PKS, pf + j * 32 * PKS + PKS);
        bv[j] = frag_tr(va + j * 32 * VIS, va + j * 32 * VIS + VIS);
      }
      if constexpr (t == Q_SLAB - 1) {
        dp = mfma32(ap[j], bv[j], dp); dm = mfma32(am[j], bv[j], dm); df = mfma32(af[j], bv[j], df);
        asm volatile("" : "+v"(dp), "+v"(dm), "+v"(df));
      }
    }
    if constexpr (j != 4) return;
    if constexpr (BR == 1 && is_clr(E)) mark(IC<clr_at(E)>{});
    if constexpr (BR == 1 && E == E_PUB2) {
      if (tid == 0) __hip_atomic_store(oflags + hq, tag, __ATOMIC_RELAXED, __HIP_MEMORY_SCOPE_AGENT);
    }
    if constexpr (BR == 1 && E == e_fld(BR)) load_flags();
    if constexpr (E == e_fchk(BR)) {
      if constexpr (BR == 1) asm volatile("s_waitcnt vmcnt(%1)" : "+v"(fv) : "n"(wait_h<BR>(E, e_fld(BR))) : "memory");
      check_flags();
    }
    if constexpr (BR == 0) {
      if constexpr (t == Q_SLAB) {
        const u32 slab = (u32)((hq * 16 + rb) * SLAB);
        const bool h0set = (0x47 >> (c & 7)) & 1, h = rg & 1, own = (c >> 3) == hpl;
        u32x2 a = pk4(dp), b = pk4(dm);
        const u32 o1 = rg < 2 ? slab + (hpl * 2 + rg) * 1024 + l * 16 : (own && h0set != h) ? slab + 4096 + l * 16 : OOB;
        __builtin_amdgcn_raw_buffer_store_b128(u32x4{ns(a[0]), ns(a[1]), ns(b[0]), ns(b[1])}, srdS, o1, 0, 16);
        const float x0 = g0 ? df[2] : df[0], x1 = g0 ? df[3] : df[1];
        const bool first = rg < 2 ? !h : h0set;
        const u32 o2 = rg < 2 ? slab + 5120 + (hpl * 2 + rg) * 256 + l * 4 : own ? slab + 6144 + l * 4 : OOB;
        __builtin_amdgcn_raw_buffer_store_b32(ns(pk(first ? x0 : 0.f, first ? 0.f : x1)), srdS, o2, 0, 16);
        STAMP(6);
      }
      if constexpr (t == Q_L1 || t == Q_L1C) {
        const int dw = w * 16 + c;
        const u32 dof = (u32)((rb * 100 + (dw < 100 ? dw : 99)) * 4);
        const u32 go = (u32)((hq * 16 + 4 * g) * SLAB) + dof;
        if constexpr (t == Q_L1) {
          asm volatile("buffer_load_dword %0, %4, %5, 0 offen sc1\n\tbuffer_load_dword %1, %4, %5, %6 offen sc1\n\t"
                       "buffer_load_dword %2, %4, %5, %7 offen sc1\n\tbuffer_load_dword %3, %4, %5, %8 offen sc1"
                       : "=&v"(g0_), "=&v"(g1_), "=&v"(g2_), "=&v"(g3_)
                       : "v"(go), "s"(srdS), "s"(SLAB), "s"(2 * SLAB), "s"(3 * SLAB)
                       : "memory");
        } else {
          asm volatile("s_waitcnt vmcnt(%4)" : "+v"(g0_), "+v"(g1_), "+v"(g2_), "+v"(g3_) : "n"(wait_h<BR>(E, EQ + Q_L1)) : "memory");
          _Pragma("nounroll") for (int spin = 0; spin < (1 << 20); ++spin) {
            asm volatile("" ::: "memory");
            if (!__any(umax(umax3(g0_, g1_, g2_), g3_) == SENT)) break;
            g0_ = __builtin_amdgcn_raw_buffer_load_b32(srdS, go, 0, 16);
            g1_ = __builtin_amdgcn_raw_buffer_load_b32(srdS, go, SLAB, 16);
            g2_ = __builtin_amdgcn_raw_buffer_load_b32(srdS, go, 2 * SLAB, 16);
            g3_ = __builtin_amdgcn_raw_buffer_load_b32(srdS, go, 3 * SLAB, 16);
          }
          const u32 sel = c == 0 ? 0x00003f80u : c == 1 ? 0x3f800000u : 0u;
          f32x4 sum = mfma32(u32x4{sel, sel, sel, sel}, u32x4{g0_, g1_, g2_, g3_}, z4);
          __builtin_amdgcn_raw_buffer_store_b32(ns(pk(sum[0], sum[1])), srdS, g == 0 ? (u32)((128 + hq) * SLAB) + dof : OOB, 0, 16);
          STAMP(7);
        }
      }
    }
  };
  a0[0] = z4; a0[1] = z4; a1[0] = z4; a1[1] = z4; aL = z4;
  static_for<0, 18>([&](auto tt) {
    constexpr int t = tt.value, E = EQ + t;
    if constexpr (BR == 0) {
      if constexpr (t == 3 || t == 6) bar_only();
    } else if constexpr ((t & 1) && t < 17) {
      if constexpr (t == 1) wait_bar<wait_n<BR>(E, 18 + (t >> 1) + 1)>();
      else wait_vm_bar<wait_n<BR>(E, 18 + (t >> 1) + 1)>();
    }
    u32x4 c0 = f0, c1 = f1, cl = fl;
    SCHED_FENCE();
    a0[0] = mfma32(c0, xf[t][0], a0[0]);
    load_q_frags(IC<t + 1>{}, IC<0>{});
    hook(tt, IC<0>{});
    SCHED_FENCE();
    a0[1] = mfma32(c0, xf[t][1], a0[1]);
    load_q_frags(IC<t + 1>{}, IC<1>{});
    hook(tt, IC<1>{});
    ev_dma(IC<E>{}, IC<0>{});
    SCHED_FENCE();
    a1[0] = mfma32(c1, xf[t][0], a1[0]);
    load_q_frags(IC<t + 1>{}, IC<2>{});
    hook(tt, IC<2>{});
    SCHED_FENCE();
    a1[1] = mfma32(c1, xf[t][1], a1[1]);
    hook(tt, IC<3>{});
    ev_dma(IC<E>{}, IC<1>{});
    SCHED_FENCE();
    aL = mfma32(cl, xf[t][0], aL);
    asm volatile("" : "+v"(aL));
    hook(tt, IC<4>{});
    SCHED_FENCE();
  });
  STAMP(8);
  refresh();
  if (g < 2) lds_w64(lb + QLX + ((2 * rg + hpl) * 2 + g) * 128 + c * 8, pk4(aL));
  bar_lds();
  const u32 ofo = (u32)((BR * 128 + rb * 8 + rg * 2) * 18 * 1024);
  const int hp = hq * 2 + hpl;
  auto store_o = [&](int sb, f32x4 o0, f32x4 o1, float l0, float l1, float r0, float r1) {
    u32x2 a = pk4(o0 * r0), b = pk4(o1 * r1);
    __builtin_amdgcn_raw_buffer_store_b128(u32x4{ns(a[0]), ns(a[1]), ns(b[0]), ns(b[1])}, srdF, ofo + (sb * 18 + hp) * 1024 + l * 16, 0, 16);
    lds_w128(lb + LOWN + ((rg * 2 + sb) * 2 + hpl) * 1024 + l * 16, u32x4{a[0], a[1], b[0], b[1]});
    float rr = g0 ? r1 : r0;
    u32 lv = ns(pk(l0 * rr, l1 * rr));
    auto sw = __builtin_amdgcn_permlane16_swap(lv, lv, false, false);
    u32 s0 = sw[0], s1 = sw[1];
    __builtin_amdgcn_raw_buffer_store_b64(u32x2{s0, s1}, srdF, g == 0 ? ofo + (sb * 18 + 16 + (hq >> 2)) * 1024 + (c + 16 * (hq & 3)) * 16 + 8 * hpl : OOB, 0, 16);
  };

  if (BR == 0) {
    HH h0, h1;
    {
      u32x2 qa0 = pk4(a0[0]), qb0 = pk4(a1[0]), qa1 = pk4(a0[1]), qb1 = pk4(a1[1]);
      h0.b0[0] = qa0[0]; h0.b0[1] = qa0[1]; h0.b1[0] = qb0[0]; h0.b1[1] = qb0[1];
      h1.b0[0] = qa1[0]; h1.b0[1] = qa1[1]; h1.b1[0] = qb1[0]; h1.b1[1] = qb1[1];
    }
    const u32 ql0 = lb + QLX + ((2 * rg + hpl) * 2 + hpl) * 128 + c * 8, ql1 = lb + QLX + ((2 * rg + 1 - hpl) * 2 + hpl) * 128 + c * 8;
    const u32 kf = (u32)((128 + hq) * SLAB);
    const int l8 = g * 16 + (c & 7) + 8 * hpl;
    u32x4 AM0, AM1, AMLm;
    u32 AL0, AL1, ALL;
    auto load_kv = [&]() {
      AM0 = __builtin_amdgcn_raw_buffer_load_b128(srdS, kf + hpl * 2048 + l * 16, 0, 16);
      AM1 = __builtin_amdgcn_raw_buffer_load_b128(srdS, kf + hpl * 2048 + 1024 + l * 16, 0, 16);
      AMLm = __builtin_amdgcn_raw_buffer_load_b128(srdS, kf + 4096 + l8 * 16, 0, 16);
      AL0 = __builtin_amdgcn_raw_buffer_load_b32(srdS, kf + 5120 + hpl * 512 + l * 4, 0, 16);
      AL1 = __builtin_amdgcn_raw_buffer_load_b32(srdS, kf + 5120 + hpl * 512 + 256 + l * 4, 0, 16);
      ALL = __builtin_amdgcn_raw_buffer_load_b32(srdS, kf + 6144 + l8 * 4, 0, 16);
    };
    static_for<0, HH_N>([&](auto st) {
      if (st.value == L2ST) { SCHED_FENCE(); load_kv(); SCHED_FENCE(); }
      if (st.value == L2ST2) {
        SCHED_FENCE();
        if (__any(umax4(umax4(umax4(umax3(AL0, AL1, ALL), AM0), AM1), AMLm) == SENT)) load_kv();
        SCHED_FENCE();
      }
      if constexpr (st.value >= HQ0 && (st.value - HQ0) % HQS == 0 && (st.value - HQ0) / HQS < NEV - EH) {
        constexpr int E = EH + (st.value - HQ0) / HQS;
        if constexpr (SC<BR>::s.nop[E] > 0) { SCHED_FENCE(); ev_dma(IC<E>{}, IC<0>{}); SCHED_FENCE(); }
      }
      hh_step<st.value, true>(h0, ql0, phf, sumf, lo, ninf);
      hh_step<st.value, true>(h1, ql1, phf, sumf, lo, ninf);
    });
    hh_left<true>(h0, lo);
    hh_left<true>(h1, lo);
    asm volatile("" : "+v"(h0.b0), "+v"(h0.b1), "+v"(h0.bl), "+v"(h1.b0), "+v"(h1.b1), "+v"(h1.bl));
    STAMP(9);
    _Pragma("nounroll") for (int spin = 0; spin < (1 << 20); ++spin) {
      asm volatile("" ::: "memory");
      if (!__any(umax4(umax4(umax4(umax3(AL0, AL1, ALL), AM0), AM1), AMLm) == SENT)) break;
      load_kv();
    }
    STAMP(10);
    const bool h0set = (0x47 >> (c & 7)) & 1;
    u32x4 AML0, AML1;
#pragma unroll
    for (int e = 0; e < 4; ++e) { AML0[e] = h0set ? AMLm[e] : 0u; AML1[e] = h0set ? 0u : AMLm[e]; }
    f32x4 O0[2], O1[2], OL[2];
#pragma unroll
    for (int sb = 0; sb < 2; ++sb) OL[sb] = mfma32(AML0, (sb ? h1 : h0).b0, z4);
#pragma unroll
    for (int sb = 0; sb < 2; ++sb) O0[sb] = mfma32(AM0, (sb ? h1 : h0).b0, z4);
#pragma unroll
    for (int sb = 0; sb < 2; ++sb) OL[sb] = mfma32(AML1, (sb ? h1 : h0).b1, OL[sb]);
#pragma unroll
    for (int sb = 0; sb < 2; ++sb) O1[sb] = mfma32(AM1, (sb ? h1 : h0).b1, z4);
#pragma unroll
    for (int sb = 0; sb < 2; ++sb) OL[sb] = mfma32(u32x4{ALL, 0u, 0u, 0u}, u32x4{(sb ? h1 : h0).bl, 0u, 0u, 0u}, OL[sb]);
#pragma unroll
    for (int sb = 0; sb < 2; ++sb) O0[sb] = mfma32(u32x4{AL0, 0u, 0u, 0u}, u32x4{(sb ? h1 : h0).bl, 0u, 0u, 0u}, O0[sb]);
#pragma unroll
    for (int sb = 0; sb < 2; ++sb) O1[sb] = mfma32(u32x4{AL1, 0u, 0u, 0u}, u32x4{(sb ? h1 : h0).bl, 0u, 0u, 0u}, O1[sb]);
#pragma unroll
    for (int sb = 0; sb < 2; ++sb) {
      const HH& h = sb ? h1 : h0;
      float r0 = __builtin_amdgcn_rcpf(fmaf(h.S[0], 1e-6f, OL[sb][2])), r1 = __builtin_amdgcn_rcpf(fmaf(h.S[2], 1e-6f, OL[sb][3]));
      store_o(sb ^ hpl, O0[sb], O1[sb], OL[sb][0], OL[sb][1], r0, r1);
    }
  } else {
    auto ev3 = [&](auto EE) { SCHED_FENCE(); ev_dma(EE, IC<0>{}); ev_dma(EE, IC<1>{}); ev_dma(EE, IC<2>{}); SCHED_FENCE(); };
    const u32 kx = lb + LKX + ((rg * 2 + hpl) * 2) * 512 + l * 8;
    u32x2 pM0 = lds_r64(kx), pM1 = lds_r64(kx + 512);
    u32x2 kL[3];
#pragma unroll
    for (int i = 0; i < 3; ++i) kL[i] = lds_r64(lb + KLX + ((2 * rg + i) * 2 + hpl) * 128 + c * 8);
    ev3(IC<EH>{});
    const int par = c & 1;
    const bool ina = (4 * g + par) > c, inb = (4 * g + par + 2) > c;
    const float sc = 0.23570226039551584f * LOG2E;
    float la[2][2], lbv[2][2], mx[2][2];
    u32x2 qM[2][2] = {{pk4(a0[0]), pk4(a0[1])}, {pk4(a1[0]), pk4(a1[1])}};
    unswap(qM[0][0], qM[0][1]); unswap(qM[1][0], qM[1][1]);
#pragma unroll
    for (int sb = 0; sb < 2; ++sb) {
      u32x2 qlv = lds_r64(lb + QLX + ((2 * rg + sb) * 2 + hpl) * 128 + c * 8);
      u32x2 q0 = qM[0][sb], q1 = qM[1][sb];
      u32x4 b0 = {q0[0], q0[1], g == 0 ? qlv[0] : 0u, 0u};
      u32x4 b1 = {q1[0], q1[1], 0u, g == 0 ? qlv[1] : 0u};
      u32x2 kp0 = sb ? kM[0][0] : pM0, kp1 = sb ? kM[1][0] : pM1, kpl = kL[sb], kcl = kL[sb + 1];
      f32x4 sp0 = mfma32(u32x4{kp0[0], kp0[1], kpl[0], kpl[1]}, b0, z4);
      f32x4 sp1 = mfma32(u32x4{kp1[0], kp1[1], kpl[0], kpl[1]}, b1, z4);
      f32x4 sc0 = mfma32(u32x4{kM[0][sb][0], kM[0][sb][1], kcl[0], kcl[1]}, b0, z4);
      f32x4 sc1 = mfma32(u32x4{kM[1][sb][0], kM[1][sb][1], kcl[0], kcl[1]}, b1, z4);
      if (sb) ev3(IC<EH + 2>{}); else ev3(IC<EH + 1>{});
      const bool dead = (sb == 0) && (rb == 0) && (rg == 0);
      float pa0 = par ? sp0[1] : sp0[0], pb0 = par ? sp0[3] : sp0[2], pa1 = par ? sp1[1] : sp1[0], pb1 = par ? sp1[3] : sp1[2];
      if (dead) { pa0 = pb0 = pa1 = pb1 = -1e30f; }
      la[0][sb] = (ina ? pa0 : (par ? sc0[1] : sc0[0])) * sc;
      lbv[0][sb] = (inb ? pb0 : (par ? sc0[3] : sc0[2])) * sc;
      la[1][sb] = (ina ? pa1 : (par ? sc1[1] : sc1[0])) * sc;
      lbv[1][sb] = (inb ? pb1 : (par ? sc1[3] : sc1[2])) * sc;
      mx[0][sb] = fmaxf(la[0][sb], lbv[0][sb]);
      mx[1][sb] = fmaxf(la[1][sb], lbv[1][sb]);
    }
    float tm = redmax4(mx[0][0], mx[0][1], mx[1][0], mx[1][1], ninf);
    bcast4(tm, mx[0][0], mx[0][1], mx[1][0], mx[1][1]);
    ev3(IC<EH + 3>{});
    const u32 vb = lb + lvi(BR) + (rg * 32 + 4 * g + q) * VIS + hpl * 80;
#pragma unroll
    for (int sb = 0; sb < 2; ++sb) {
      const u32 v0 = vb + sb * 16 * VIS, v1 = v0 + 16 * VIS;
      u32x4 A0 = frag_tr(v0 + 8 * p, v1 + 8 * p), A1 = frag_tr(v0 + 32 + 8 * p, v1 + 32 + 8 * p);
      u32x4 AL = frag_tr(v0 + 64 + 8 * (p & 1), v1 + 64 + 8 * (p & 1));
      u32x4 P[2];
#pragma unroll
      for (int h = 0; h < 2; ++h) {
        float ea = __builtin_amdgcn_exp2f(la[h][sb] - mx[h][sb]), eb = __builtin_amdgcn_exp2f(lbv[h][sb] - mx[h][sb]);
        u32 wa_ = par ? pk(0.f, ea) : pk(ea, 0.f), wb_ = par ? pk(0.f, eb) : pk(eb, 0.f);
        P[h] = u32x4{ina ? wa_ : 0u, inb ? wb_ : 0u, ina ? 0u : wa_, inb ? 0u : wb_};
      }
      f32x4 O0 = mfma32(A0, P[0], z4), O1 = mfma32(A1, P[1], z4);
      f32x4 L0 = mfma32(AL, P[0], z4), L1 = mfma32(AL, P[1], z4);
      float r0 = __builtin_amdgcn_rcpf(L0[2]), r1 = __builtin_amdgcn_rcpf(L1[2]);
      store_o(sb, O0, O1, g0 ? L1[0] : L0[0], g0 ? L1[1] : L0[1], r0, r1);
    }
  }
  STAMP(11);
  refresh();
  const u32 ofl = (u32)((BR * 128 + rb * 8 + w) * 18 * 1024 + l * 16);
  u32x4 wf[18][5], of[18];
  wait_bar<4>();
  STAMP(12);
  const u32 wrow2 = lb + (16 * g1 + 8 * g0 + 2 * q) * 144;
  u32 oa[3], oa4[3];
#pragma unroll
  for (int i = 0; i < 3; ++i) {
    oa[i] = wrow2 + 8 * p + i * 57344; oa4[i] = wrow2 + 128 + 8 * (p & 1) + i * 57344;
    asm volatile("" : "+v"(oa[i]), "+v"(oa4[i]));
  }
  auto load_wf = [&](auto TT, auto II) {
    constexpr int t = TT.value, i = II.value;
    if constexpr (t < 18) {
      constexpr u32 cb = wo_at(BR, t < 4 ? 1 - (t >> 1) : t >> 1) + (t & 1) * 32 * 144, s = cb / 57344, o = cb % 57344;
      if constexpr (i < 4) wf[t][i] = frag_tr(oa[s] + o + 32 * i, oa[s] + o + 144 + 32 * i);
      else wf[t][4] = frag_tr(oa4[s] + o, oa4[s] + o + 144);
    }
  };
  f32x4 acc[5];
  u32 seen;
  auto kstep = [&](auto tt) {
    constexpr int t = tt.value;
    static_for<0, 5>([&](auto ii) {
      constexpr int i = ii.value;
      acc[i] = mfma32(wf[t][i], of[t], t ? acc[i] : z4);
      if constexpr (i == 1 && t >= 4) seen = umax4(seen, of[t]);
      load_wf(IC<t + PRE>{}, ii);
      SCHED_FENCE();
    });
  };
  u32 so[14], own = lb + LOWN + w * 2048 + l * 16;
#pragma unroll
  for (int t = 0; t < 14; ++t) { so[t] = ((t + 2 + 2 * hq) & 15) * 1024; asm volatile("" : "+s"(so[t])); }
  asm volatile("" : "+v"(own));
  _Pragma("nounroll") for (int pass = 0; pass < (1 << 16); ++pass) {
#pragma unroll
    for (int t = 4; t < 4 + NOPT; ++t) of[t] = __builtin_amdgcn_raw_buffer_load_b128(srdF, ofl, so[t - 4], 16);
    of[0] = lds_r128(own);
    of[1] = lds_r128(own + 1024);
    SCHED_FENCE();
    static_for<0, PRE * 5>([&](auto t) {
      load_wf(IC<t.value / 5>{}, IC<t.value % 5>{});
      if constexpr (t.value == PP) {
        SCHED_FENCE();
        of[2] = __builtin_amdgcn_raw_buffer_load_b128(srdF, ofl, 16 * 1024, 16);
        of[3] = __builtin_amdgcn_raw_buffer_load_b128(srdF, ofl, 17 * 1024, 16);
        SCHED_FENCE();
      }
    });
    SCHED_FENCE();
    kstep(IC<0>{}); kstep(IC<1>{});
    _Pragma("nounroll") for (int spin = 0; spin < (1 << 20); ++spin) {
      asm volatile("" ::: "memory");
      if (!__any(umax4(umax4(0u, of[2]), of[3]) == SENT)) break;
      of[2] = __builtin_amdgcn_raw_buffer_load_b128(srdF, ofl, 16 * 1024, 16);
      of[3] = __builtin_amdgcn_raw_buffer_load_b128(srdF, ofl, 17 * 1024, 16);
#pragma unroll
      for (int t = 4; t < 4 + NOPT; ++t) of[t] = __builtin_amdgcn_raw_buffer_load_b128(srdF, ofl, so[t - 4], 16);
    }
    STAMP(13);
#pragma unroll
    for (int t = 4 + NOPT; t < 18; ++t) of[t] = __builtin_amdgcn_raw_buffer_load_b128(srdF, ofl, so[t - 4], 16);
    SCHED_FENCE();
    seen = 0;
    static_for<2, 18>(kstep);
    if (!__any(seen == SENT)) break;
  }
  STAMP(14);
  if (BR == 0 && rb == 0 && hq == 0 && tid == 0) __hip_atomic_store((GLB_AS u64*)(ws + WS_EPOCH), tag, __ATOMIC_RELAXED, __HIP_MEMORY_SCOPE_AGENT);
  char* op = out + (size_t)(rb * 128 + w * 16 + c) * 2304 + BR * 1152 + hq * 144;
  __amdgpu_buffer_rsrc_t srdU = __builtin_amdgcn_make_buffer_rsrc((void*)out, (short)0, 2048 * 2304, 0x00020000);
  const u32 oo = (u32)((rb * 128 + w * 16 + c) * 2304 + BR * 1152 + hq * 144);
#pragma unroll
  for (int i = 0; i < 2; ++i) {
    u32x2 a = pk4(acc[2 * i]), b = pk4(acc[2 * i + 1]);
    auto s0 = __builtin_amdgcn_permlane16_swap(a[0], b[0], false, false);
    auto s1 = __builtin_amdgcn_permlane16_swap(a[1], b[1], false, false);
    u32 s00 = s0[0], s01 = s0[1], s10 = s1[0], s11 = s1[1];
    *(u32x4*)(op + 64 * i + 32 * g0 + 16 * g1) = u32x4{s00, s10, s01, s11};
  }
  if (g < 2) *(u32x2*)(op + 128 + 8 * g) = pk4(acc[4]);
  STAMP(15);
  RSTAMP(31);
  STAMP_FLUSH();
  asm volatile(".Lhk_to_%0:" ::"n"(BR));
  __builtin_amdgcn_endpgm();
}

__global__ __launch_bounds__(512, 2) void fused(const char* __restrict__ XA, const char* __restrict__ WA, const char* __restrict__ PHI,
                                                const char* __restrict__ WOA, const char* __restrict__ XB, const char* __restrict__ WB,
                                                const char* __restrict__ WOB, char* ws, char* out) {
  __shared__ __attribute__((aligned(16))) char smem[LDS_BYTES];
  const u32 lb = (u32)(uintptr_t)(LDS_AS char*)smem;
  const int b = blockIdx.x, x = b & 7, j = b >> 3, xx = x & 3;
  const int rb = xx * 4 + (j >> 3), hq = j & 7;
  if (x < 4) body<0>(XA, WA, PHI, WOA, ws, out, rb, hq, j, lb);
  else body<1>(XB, WB, PHI, WOB, ws, out, rb, hq, j, lb);
}
}

extern "C" void kernel_launch(void* const* d_in, const int* in_sizes, int n_in, void* d_out, int out_size, void* d_ws, size_t ws_size,
                              hipStream_t stream) {
  hk::fused<<<256, 512, 0, stream>>>((const char*)d_in[0], (const char*)d_in[1], (const char*)d_in[2], (const char*)d_in[3], (const char*)d_in[4],
                                     (const char*)d_in[5], (const char*)d_in[6], (char*)d_ws, (char*)d_out);
}
```

```cpp
#define CPRO 1
#include <hip/hip_runtime.h>
#include <stdint.h>

namespace hk {
typedef unsigned int u32;
typedef unsigned long long u64;
typedef unsigned short u16;
typedef float f32x4 __attribute__((ext_vector_type(4)));
typedef float f32x2 __attribute__((ext_vector_type(2)));
typedef u32 u32x4 __attribute__((ext_vector_type(4)));
typedef u32 u32x2 __attribute__((ext_vector_type(2)));
typedef __bf16 bf16x8 __attribute__((ext_vector_type(8)));
typedef __bf16 bf16x2 __attribute__((ext_vector_type(2)));
typedef short s16x4 __attribute__((ext_vector_type(4)));
#define LDS_AS __attribute__((address_space(3)))
#define GLB_AS __attribute__((address_space(1)))
#define DEVI __device__ __forceinline__
#define SCHED_FENCE() __builtin_amdgcn_sched_barrier(0)

template <int V> struct IC { static constexpr int value = V; };
template <int I, int N, class F>
DEVI void static_for(F&& f) {
  if constexpr (I < N) { f(IC<I>{}); static_for<I + 1, N>(f); }
}

constexpr int XROW = 1152, WROW = 3456;
constexpr size_t WS_OF = 0;
constexpr int SLAB = 6400;
constexpr size_t WS_SLAB = 5u << 20;
constexpr size_t WS_FLAG = 6u << 20;
constexpr size_t WS_EPOCH = WS_FLAG + 65536;
constexpr size_t WS_OFLAG = WS_FLAG + 4096;
constexpr u32 OOB = 0x7fffffffu;
constexpr u32 SENT = 0xffffffffu;
constexpr int WU = 9216;
constexpr int LPK = 1024, PKS = 296;
constexpr int S0 = 3968, SS = 34944;
constexpr int SK = 16384, SV = SK + WU + 16;
constexpr int WQ = S0 + SS;
constexpr int LT = WQ + 9 * WU;
constexpr int LM = S0 + 4 * SS;
constexpr int LPHF = LM;
constexpr int LSUM = LPHF + 8192;
constexpr int LHALO = LM;
constexpr int KLX = LM + 11264;
constexpr int QLX = KLX + 2304;
constexpr int LSCR = QLX + 2048;
constexpr int VIS = 168;
constexpr int lvi(int br) { return br ? 1024 : LT; }
constexpr int LKX = 25600;
constexpr int wo_at(int br, int m) {
  if (br == 0) return m < 4 ? 1024 + m * WU : m < 6 ? LT + (m - 4) * WU : WQ + (m - 6) * WU;
  return m < 2 ? LT + m * WU : m == 2 ? LM : WQ + (m - 3) * WU;
}
constexpr int LOWN = WQ + 6 * WU;
constexpr int LDS_BYTES = 163840;
static_assert(LSCR + 512 <= LDS_BYTES && LPK + 2688 <= S0 && LPK + 128 * PKS <= WQ && LT + 128 * VIS <= LM && 1024 + 144 * VIS <= LKX && LKX + 8192 <= WQ &&
              SV + WU <= SS && 3 * WU <= SS && 7 * WU <= 2 * SS && S0 % 128 == 0 && SS % 128 == 0, "lds");

DEVI u32x2 tr_read(u32 a) { return __builtin_bit_cast(u32x2, __builtin_amdgcn_ds_read_tr16_b64_v4i16((LDS_AS s16x4*)a)); }
DEVI u32x4 frag_tr(u32 a0, u32 a1) { u32x2 x = tr_read(a0), y = tr_read(a1); return u32x4{x[0], x[1], y[0], y[1]}; }
DEVI u32x4 lds_r128(u32 a) { return *(LDS_AS u32x4*)a; }
DEVI u32x2 lds_r64(u32 a) { return *(LDS_AS u32x2*)a; }
DEVI u32 lds_r32(u32 a) { return *(LDS_AS u32*)a; }
DEVI u32 lds_r16(u32 a) { return *(LDS_AS u16*)a; }
DEVI void lds_w128(u32 a, u32x4 v) { *(LDS_AS u32x4*)a = v; }
DEVI void lds_w64(u32 a, u32x2 v) { *(LDS_AS u32x2*)a = v; }
DEVI void lds_w32(u32 a, u32 v) { *(LDS_AS u32*)a = v; }
DEVI f32x4 mfma32(u32x4 a, u32x4 b, f32x4 c) {
  return __builtin_amdgcn_mfma_f32_16x16x32_bf16(__builtin_bit_cast(bf16x8, a), __builtin_bit_cast(bf16x8, b), c, 0, 0, 0);
}
DEVI u32 pk(float a, float b) { f32x2 v = {a, b}; return __builtin_bit_cast(u32, __builtin_convertvector(v, bf16x2)); }
DEVI u32x2 pk4(f32x4 v) { return u32x2{pk(v[0], v[1]), pk(v[2], v[3])}; }
DEVI float max3(float a, float b, float c) { return fmaxf(fmaxf(a, b), c); }
DEVI u32 umax(u32 a, u32 b) { return a > b ? a : b; }
DEVI u32 umax3(u32 a, u32 b, u32 c) { return umax(umax(a, b), c); }
DEVI u32 umax4(u32 m, u32x4 v) { return umax3(umax3(m, v[0], v[1]), v[2], v[3]); }
DEVI u32 ns(u32 v) { return v < 0xfffffffeu ? v : 0xfffffffeu; }

DEVI void dma_full(u32 lds, u32 voff, __amdgpu_buffer_rsrc_t srd, u32 soff) {
  asm volatile("s_mov_b32 m0, %0\n\ts_nop 0\n\tbuffer_load_dwordx4 %1, %2, %3 offen lds" ::"s"(lds), "v"(voff), "s"(srd), "s"(soff) : "memory");
}
DEVI void dma_mask(u32 lds, u32 voff, __amdgpu_buffer_rsrc_t srd, u32 soff, u64 mask) {
  asm volatile("s_mov_b32 m0, %0\n\ts_mov_b64 exec, %4\n\tbuffer_load_dwordx4 %1, %2, %3 offen lds\n\ts_mov_b64 exec, -1" ::"s"(lds), "v"(voff), "s"(srd),
               "s"(soff), "s"(mask)
               : "memory");
}
template <int N>
DEVI void wait_bar() { asm volatile("s_waitcnt vmcnt(%0) lgkmcnt(0)\n\ts_barrier" ::"n"(N) : "memory"); }
template <int N>
DEVI void wait_vm_bar() { asm volatile("s_waitcnt vmcnt(%0)\n\ts_barrier" ::"n"(N) : "memory"); }
DEVI void bar_lds() { asm volatile("s_waitcnt lgkmcnt(0)\n\ts_barrier" ::: "memory"); }
DEVI void bar_only() { asm volatile("s_barrier" ::: "memory"); }

DEVI float redmax4(float a, float b, float c, float d, float ninf) {
  auto r1 = __builtin_amdgcn_permlane32_swap(__float_as_uint(a), __float_as_uint(b), false, false);
  auto r2 = __builtin_amdgcn_permlane32_swap(__float_as_uint(c), __float_as_uint(d), false, false);
  u32 r10 = r1[0], r11 = r1[1], r20 = r2[0], r21 = r2[1];
  float ab = max3(__uint_as_float(r10), __uint_as_float(r11), ninf);
  float cd = max3(__uint_as_float(r20), __uint_as_float(r21), ninf);
  auto r3 = __builtin_amdgcn_permlane16_swap(__float_as_uint(ab), __float_as_uint(cd), false, false);
  u32 r30 = r3[0], r31 = r3[1];
  return max3(__uint_as_float(r30), __uint_as_float(r31), ninf);
}
DEVI void bcast4(float t, float& a, float& b, float& c, float& d) {
  u32 tu = __float_as_uint(t);
  auto s1 = __builtin_amdgcn_permlane16_swap(tu, tu, false, false);
  u32 x = s1[0], y = s1[1];
  auto s2 = __builtin_amdgcn_permlane32_swap(x, x, false, false);
  auto s3 = __builtin_amdgcn_permlane32_swap(y, y, false, false);
  u32 s20 = s2[0], s21 = s2[1], s30 = s3[0], s31 = s3[1];
  a = __uint_as_float(s20); b = __uint_as_float(s21); c = __uint_as_float(s30); d = __uint_as_float(s31);
}

constexpr float LOG2E = 1.4426950408889634f;
constexpr u32 ONES = 0x3f803f80u;

struct HH {
  u32x4 f0, f1, f2, f3;
  u32x4 b0, b1;
  u32 bl;
  f32x4 T0, T1, TL, S;
  float a, b, c, d;
  f32x4 p0, q0, p1, q1;
  float l0, l1;
};
constexpr int HH_N = 106;
DEVI void swap32(float& a, float& b) {
  auto r = __builtin_amdgcn_permlane32_swap(__float_as_uint(a), __float_as_uint(b), false, false);
  u32 x = r[0], y = r[1]; a = __uint_as_float(x); b = __uint_as_float(y);
}
DEVI void swap16(float& a, float& b) {
  auto r = __builtin_amdgcn_permlane16_swap(__float_as_uint(a), __float_as_uint(b), false, false);
  u32 x = r[0], y = r[1]; a = __uint_as_float(x); b = __uint_as_float(y);
}
template <int I, bool QS>
DEVI void hh_step(HH& s, u32 lft, u32 phf, u32 sumf, bool lo, float ninf) {
  const f32x4 z = {0.f, 0.f, 0.f, 0.f};
  if constexpr (I == 0) {
    u32x2 x = lds_r64(lft); s.b0[2] = x[0]; s.b0[3] = x[1];
    asm volatile("" ::: "memory");
    u32x2 y = lds_r64(lft); s.b1[2] = y[0]; s.b1[3] = y[1];
    s.f0 = lds_r128(phf);
  } else if constexpr (I == 1) { s.f1 = lds_r128(phf + 1024); s.f2 = lds_r128(phf + 2048);
  } else if constexpr (I == 2) { s.f3 = lds_r128(phf + 3072);
  } else if constexpr (I == 3) { s.T0 = mfma32(s.f0, s.b0, z);
  } else if constexpr (I == 4) { s.TL = mfma32(s.f2, s.b0, z);
  } else if constexpr (I == 5) { s.T1 = mfma32(s.f1, s.b1, z);
  } else if constexpr (I == 6) { s.TL = mfma32(s.f3, s.b1, s.TL);
  } else if constexpr (I == 7) { s.f0 = lds_r128(sumf); s.f1 = lds_r128(sumf + 1024); s.f2 = lds_r128(sumf + 2048);
  } else if constexpr (I == 8) { s.a = max3(s.T0[0], s.T0[1], s.T0[2]);
  } else if constexpr (I == 9) { s.c = max3(-s.T0[0], -s.T0[1], -s.T0[2]);
  } else if constexpr (I == 10) { s.b = max3(s.T1[0], s.T1[1], s.T1[2]);
  } else if constexpr (I == 11) { s.d = max3(-s.T1[0], -s.T1[1], -s.T1[2]);
  } else if constexpr (I == 12) {
  } else if constexpr (I == 13) { s.a = max3(s.a, s.T0[3], s.TL[0]);
  } else if constexpr (I == 14) { s.b = max3(s.b, s.T1[3], s.TL[1]);
  } else if constexpr (I == 15) { s.c = max3(s.c, -s.T0[3], -s.TL[0]);
  } else if constexpr (I == 16) { s.d = max3(s.d, -s.T1[3], -s.TL[1]);
  } else if constexpr (I == 17) { swap32(s.a, s.b);
  } else if constexpr (I == 18) { swap32(s.c, s.d);
  } else if constexpr (I == 19) { s.a = max3(s.a, s.b, ninf);
  } else if constexpr (I == 20) { s.c = max3(s.c, s.d, ninf);
  } else if constexpr (I == 21) { swap16(s.a, s.c);
  } else if constexpr (I == 22) { s.a = max3(s.a, s.c, ninf);
  } else if constexpr (I == 23) { s.a *= -LOG2E;
  } else if constexpr (I == 24) { s.c = s.a; swap16(s.a, s.c);
  } else if constexpr (I == 25) { s.b = s.a; swap32(s.a, s.b);
  } else if constexpr (I == 26) { s.d = s.c; swap32(s.c, s.d);
  } else if constexpr (I == 27) { s.l0 = lo ? s.a : s.c;
  } else if constexpr (I == 28) { s.l1 = lo ? s.b : s.d;
  } else if constexpr (I == 29) { s.l0 = fmaf(s.TL[0], lo ? LOG2E : -LOG2E, s.l0);
  } else if constexpr (I == 30) { s.l1 = fmaf(s.TL[1], lo ? LOG2E : -LOG2E, s.l1);
  } else if constexpr (I == 31) { s.l0 = __builtin_amdgcn_exp2f(s.l0);
  } else if constexpr (I == 32) { s.l1 = __builtin_amdgcn_exp2f(s.l1);
  } else if constexpr (I == 33) { s.bl = pk(s.l0, s.l1);
  } else if constexpr (I == 34) { s.S = mfma32(s.f2, u32x4{s.bl, 0u, 0u, 0u}, z);
  } else if constexpr (I < 77) {
    constexpr int h = (I - 35) / 21, k = (I - 35) % 21;
    f32x4& T = h ? s.T1 : s.T0; f32x4& p = h ? s.p1 : s.p0; f32x4& q = h ? s.q1 : s.q0; u32x4& o = h ? s.b1 : s.b0;
    const float m = h ? s.b : s.a, n = h ? s.d : s.c;
    constexpr bool arg = k < 2 || ((k & 1) && k < 15);
    constexpr int e = arg ? (k < 2 ? k : (k + 1) / 2) : (k < 15 ? (k - 2) / 2 : 7);
    if constexpr (k < 16) {
      if constexpr (arg) { if constexpr (e < 4) p[e] = fmaf(T[e], LOG2E, m); else q[e - 4] = fmaf(T[e - 4], -LOG2E, n); }
      else { if constexpr (e < 4) p[e] = __builtin_amdgcn_exp2f(p[e]); else q[e - 4] = __builtin_amdgcn_exp2f(q[e - 4]); }
    } else if constexpr (k < 18) { o[k - 16] = pk(p[2 * (k - 16)], p[2 * (k - 16) + 1]);
    } else if constexpr (k < 20) { o[k - 16] = pk(q[2 * (k - 18)], q[2 * (k - 18) + 1]);
    } else { s.S = mfma32(h ? s.f1 : s.f0, o, s.S); }
  } else if constexpr (I == 77) {
  } else if constexpr (I == 78) { s.c = __builtin_amdgcn_rcpf(s.S[1]);
  } else if constexpr (I == 79) { s.d = __builtin_amdgcn_rcpf(s.S[3]);
  } else if constexpr (I == 80) { if constexpr (QS) s.c *= s.S[0]; else s.a = __builtin_amdgcn_rcpf(s.S[0]);
  } else if constexpr (I == 81) { if constexpr (QS) s.d *= s.S[2]; else s.b = __builtin_amdgcn_rcpf(s.S[2]);
  } else if constexpr (I < 106) {
    constexpr int v = (I - 82) / 6, k = (I - 82) % 6;
    if constexpr (!(QS && v >= 2)) {
      f32x4& x = v == 0 ? s.q0 : v == 1 ? s.q1 : v == 2 ? s.p0 : s.p1;
      u32x4& o = (v & 1) ? s.b1 : s.b0;
      const float r = v == 0 ? s.c : v == 1 ? s.d : v == 2 ? s.a : s.b;
      if constexpr (k < 4) x[k] *= r;
      else o[(v < 2 ? 2 : 0) + k - 4] = pk(x[2 * (k - 4)], x[2 * (k - 4) + 1]);
    }
  }
}
template <bool QS>
DEVI void hh_left(HH& s, bool lo) {
  if constexpr (QS) s.bl = pk(s.l0 * (lo ? 1.f : s.c), s.l1 * (lo ? 1.f : s.d));
  else s.bl = pk(s.l0 * (lo ? s.a : s.c), s.l1 * (lo ? s.b : s.d));
}

#define L2ST 40
#define L2ST2 72
#define PRE 7
#define PRO 2
#define LEAD 8
#define QA 2
#define QT 1
#define E_PO 36
#define E_POB 30
#define Q_L1 10
#define Q_L1C 16
#define QTB QT
#define NOPT 0
#define PCW 1
#define PCW0 0
#define PP 34
#define QW 1
#define HQ0 4
#define HQS 8
#define C0 0
#define CS 2
#define C1 44
#define E_PUB 11
#define E_FLD 40
#define QB 1
#define QSP 1
#define HB 3
constexpr int EM = 36, EQ = 44, EH = 62, NEV = 74;
constexpr int Q_SLAB = 2;
#define E_PUBQ 9
#define E_PUBB 15
#define E_FLDB 17
constexpr int E_PUB2 = C1 < 34 ? E_PUBB : EQ + E_PUBQ;
static_assert(C1 >= 34 || ((E_PUBB & 3) == 3 && E_PUBB > C1 + 2 && E_PUBB < 35), "marks");
constexpr int e_fld(int br) { return br ? EQ + E_FLDB : E_FLD; }
constexpr int e_fchk(int br) { return br ? (E_FLDB > 12 ? EH + 4 : EQ + 16) : EQ; }
constexpr int clr_at(int E) { return E >= C1 && E < C1 + 3 ? 4 + E - C1 : E >= C0 && (E - C0) % CS == 0 && (E - C0) / CS < 4 ? (E - C0) / CS : -1; }
constexpr bool is_clr(int E) { return clr_at(E) >= 0; }
static_assert((E_PUB & 3) == 3 && E_PUB > C0 + 3 * CS && E_PUB < 35 && E_FLD >= EM && E_FLD < EQ && C1 > C0 + 3 * CS && (C1 < 34 || C1 >= EQ) && C1 < EQ + 6, "marks");
struct Sched {
  int op[NEV][3];
  int nop[NEV];
  int pos_ev[NEV + 1];
  int pos_hook[NEV];
  int last[36];
  int total;
};
constexpr int ext_ops(int br, int E) {
  return (E == (br ? E_POB : E_PO)) + (br && is_clr(E)) + (E == e_fld(br)) +
         (br ? 0 : E == EQ + Q_SLAB ? 2 : E == EQ + Q_L1 ? 4 : E == EQ + Q_L1C ? 1 : 0);
}
constexpr int op_chunk(int op) { return op < 200 ? (op - 100) >> 2 : op < 236 ? ((op - 200) >> 1) % 9 : (op - 200) >> 1; }
constexpr int op_earliest(int br, int op) {
  const int n = op_chunk(op);
  if (n < 9) return 4 * n - 1 - LEAD;
  if (n < 27) return n < 21 ? 23 : n < 25 ? 27 : 31;
  const int m = n - 27;
  if (br == 0) return m < 6 ? EQ + 3 : EQ + 6;
  return m < 2 ? 31 : m == 2 ? EQ : EQ + 2 * (m - 3) + 3;
}
constexpr int op_deadline(int br, int op) {
  const int n = op_chunk(op);
  if (n < 9) return 4 * n - 1;
  if (n < 27) return br == 0 || n == 18 ? EQ : EQ + 2 * (n - 18) - 1;
  return NEV;
}
static_assert(LEAD <= 12, "ring");
constexpr Sched make_sched(int br) {
  Sched s{};
  const int nx = br ? 3 : 2;
  int list[128] = {}, when[128] = {}, n = 0;
  for (int c = 0; c < 9; ++c) {
    for (int i = 0; i < nx; ++i) list[n++] = 100 + 4 * c + i;
    list[n++] = 200 + 2 * c; list[n++] = 201 + 2 * c; list[n++] = 218 + 2 * c; list[n++] = 219 + 2 * c;
  }
  for (int c = 18; c < 36; ++c) { list[n++] = 200 + 2 * c; list[n++] = 201 + 2 * c; }
  for (int k = 0; k < n; ++k) when[k] = 1000;
  int pos = 0, done = 0;
  for (; done < PRO * (nx + 4); ++done) { s.last[op_chunk(list[done])] = pos++; when[done] = -1; if (done == nx + 3) pos += 1 + (PCW == 2); }
  pos += PCW == 3;
  for (int E = 0; E < NEV; ++E) {
    s.pos_ev[E] = pos;
    int j = 0;
    for (int k = 0; k < n; ++k) {
      if (when[k] != 1000 || op_earliest(br, list[k]) > E) continue;
      const int c = op_chunk(list[k]);
      int q = E < EM ? (c < 9 ? QA : br ? QTB : QT) : E < EQ && br ? 0 : ext_ops(br, E) ? 1 : 2;
      if (br == 0 && E >= EQ) q = ext_ops(br, E) || (E < EH && E % QSP) ? 0 : 1;
      if (br == 1 && E >= EQ && c >= 27) q = E >= EH + 4 ? 0 : E >= EH ? HB : ext_ops(br, E) ? QB - 1 : QB;
      if (j >= q) break;
      s.op[E][j++] = list[k]; when[k] = E; ++done;
      s.last[c] = pos++;
      if (E >= op_deadline(br, list[k])) s.total = -1000;
    }
    s.nop[E] = j;
    s.pos_hook[E] = pos;
    pos += ext_ops(br, E);
  }
  s.pos_ev[NEV] = pos;
  s.total += done == n ? pos : -1000;
  return s;
}
template <int BR> struct SC { static constexpr Sched s = make_sched(BR); };
static_assert(SC<0>::s.total > 0 && SC<1>::s.total > 0, "schedule incomplete");
constexpr int imin(int a, int b) { return a < b ? a : b; }
template <int BR> constexpr int wait_n(int E, int c) {
  int n = SC<BR>::s.pos_ev[E] - SC<BR>::s.last[c] - 1;
  if (BR == 1 && E == E_PUB) n = imin(n, SC<BR>::s.pos_ev[E] - SC<BR>::s.pos_ev[C0 + 3 * CS + 1]);
  if (BR == 1 && E == E_PUB2) n = imin(n, SC<BR>::s.pos_ev[E] - SC<BR>::s.pos_ev[C1 + 3]);
  if (BR == 0 && E == EQ) n = imin(n, SC<BR>::s.pos_ev[E] - SC<BR>::s.pos_ev[E_FLD + 1]);
  return n;
}
template <int BR> constexpr int wait_h(int E, int E0) { return SC<BR>::s.pos_hook[E] - SC<BR>::s.pos_ev[E0 + 1]; }

#define STAMP(i)
#define RSTAMP(i)
#define STAMP_FLUSH()
#define STAMP_DECL

DEVI u64 next_tag(u32x2 e) {
  const u64 E = ((u64)(u32)__builtin_amdgcn_readfirstlane(e[1]) << 32) | (u32)__builtin_amdgcn_readfirstlane(e[0]);
  return E * 6364136223846793005ull + 1442695040888963407ull;
}
DEVI void poll_flags(GLB_AS u64* f, u64 tag) {
  for (int spin = 0; spin < (1 << 20); ++spin) {
    u64 v = __hip_atomic_load(f, __ATOMIC_RELAXED, __HIP_MEMORY_SCOPE_AGENT);
    if (__all(v == tag)) break;
    __builtin_amdgcn_s_sleep(1);
  }
  asm volatile("" ::: "memory");
}

#define ROT (rb + 2 * hq) % 9
typedef u64 u64x4 __attribute__((ext_vector_type(4)));
DEVI void first(bool br, int rb, int hq, int jx, u32 lb, u64x4& ka, u64x4& kb, u64& kc_) {
  asm volatile("s_load_dwordx8 %0, %3, 0x0\n\ts_load_dwordx8 %1, %3, 0x20\n\ts_load_dwordx2 %2, %3, 0x40"
               : "=&s"(ka), "=&s"(kb), "=&s"(kc_)
               : "s"(__builtin_amdgcn_kernarg_segment_ptr()));
  const int tid = threadIdx.x, l = tid & 63, w = __builtin_amdgcn_readfirstlane(tid >> 6);
  u64 pc;
  int f0, t0, f1, t1;
  asm volatile("s_getpc_b64 %0\n.Lhk_pc:\n\ts_mov_b32 %1, .Lhk_from_0-.Lhk_pc\n\ts_max_i32 %2, .Lhk_to_0-.Lhk_pc, 0\n\t"
               "s_mov_b32 %3, .Lhk_from_1-.Lhk_pc\n\ts_max_i32 %4, .Lhk_to_1-.Lhk_pc, 0"
               : "=s"(pc), "=s"(f0), "=s"(t0), "=s"(f1), "=s"(t1)::"scc");
  __amdgpu_buffer_rsrc_t srdC = __builtin_amdgcn_make_buffer_rsrc((void*)pc, (short)0, (br ? t1 : t0) & ~3, 0x00020000);
  const u32 po = l == 0 ? (u32)((br ? f1 : f0) + (8 * jx + w) * 128) : OOB;
  asm volatile("s_mov_b32 m0, %0\n\ts_nop 0\n\tbuffer_load_dword %1, %2, 0 offen lds" ::"s"(lb + LSCR), "v"(po), "s"(srdC) : "memory");
  const int lr = (l * 57) >> 9, lu = l - lr * 9;
  int kc = ROT;
  u32 vx[2], vq[2], vh, vp = (u32)(hq * 2592 + (w * 21 + l) * 16), le = lb + (br ? LHALO : LPK) + w * (br ? 256 : 336);
  {
    const int row = 2 * w + ((l >> 3) & 1);
    int grow = rb * 128 - 16 + row; grow = grow < 0 ? 0 : grow;
    vh = (u32)(grow * XROW + (((l & 7) ^ ((row >> 1) & 7)) << 4) + kc * 128);
  }
#pragma unroll
  for (int i = 0; i < 2; ++i) {
    const int row = 16 * w + 8 * i + (l >> 3);
    vx[i] = (u32)((rb * 128 + row) * XROW + (((l & 7) ^ ((row >> 1) & 7)) << 4));
    vq[i] = (u32)((8 * w + 4 * (lr & 1) + 2 * i + ((lr >> 1) & 1)) * WROW + hq * 144 + lu * 16);
  }
  asm volatile("" : "+v"(vx[0]), "+v"(vx[1]), "+v"(vq[0]), "+v"(vq[1]), "+v"(vh), "+v"(vp), "+s"(le), "+s"(kc));
  asm volatile("s_waitcnt lgkmcnt(0)" : "+s"(ka), "+s"(kb), "+s"(kc_) : "s"(__builtin_amdgcn_kernarg_segment_ptr()));
  const u64 XA = ka[0], WA = ka[1], PHI = ka[2], XB = kb[0], WB = kb[1];
  __amdgpu_buffer_rsrc_t srdX = __builtin_amdgcn_make_buffer_rsrc((void*)(br ? XB : XA), (short)0, 2048 * XROW, 0x00020000);
  __amdgpu_buffer_rsrc_t srdW = __builtin_amdgcn_make_buffer_rsrc((void*)(br ? WB : WA), (short)0, 576 * WROW, 0x00020000);
  __amdgpu_buffer_rsrc_t srdE = __builtin_amdgcn_make_buffer_rsrc((void*)(br ? XB : PHI), (short)0, br ? 2048 * XROW : 32 * 648, 0x00020000);
  dma_mask(le, br ? vh : vp, srdE, 0u, br ? 0xffffull : (1ull << 21) - 1);
#pragma unroll
  for (int i = 0; i < 2; ++i) dma_full(lb + S0 + w * 2048 + i * 1024, vx[i], srdX, (u32)(kc * 128));
#pragma unroll
  for (int n = 0; n < 4; ++n)
    dma_mask(lb + S0 + (n < 2 ? SK : SV) + (2 * w + (n & 1)) * 576, vq[n & 1], srdW, (u32)(kc * 64 * WROW + (n < 2 ? 1152 : 2304)), (1ull << 36) - 1);
}

template <int BR>
DEVI void body(const char* __restrict__ X, const char* __restrict__ W, const char* __restrict__ PHI, const char* __restrict__ WO, char* ws, char* out,
               int rb, int hq, int jx, u32 lb, u64 tent, u64 rent) {
  asm volatile(".Lhk_from_%0:" ::"n"(BR));
  int tid = threadIdx.x;
  asm volatile("" : "+v"(tid));
  int l = tid & 63;
  const int w = __builtin_amdgcn_readfirstlane(tid >> 6);
  const int rg = w & 3, hpl = w >> 2;
  int c = l & 15, g = l >> 4, q = c >> 2, p = c & 3, g0 = g & 1, g1 = g >> 1;
  int lr = (l * 57) >> 9, lu = l - lr * 9;
  auto refresh = [&]() {
    asm volatile("" : "+v"(tid));
    l = tid & 63; c = l & 15; g = l >> 4; q = c >> 2; p = c & 3; g0 = g & 1; g1 = g >> 1;
    lr = (l * 57) >> 9; lu = l - lr * 9;
  };
  STAMP_DECL
  STAMP(0);
  RSTAMP(30);
  __amdgpu_buffer_rsrc_t srdX = __builtin_amdgcn_make_buffer_rsrc((void*)X, (short)0, 2048 * XROW, 0x00020000);
  __amdgpu_buffer_rsrc_t srdW = __builtin_amdgcn_make_buffer_rsrc((void*)W, (short)0, 576 * WROW, 0x00020000);
  const int rot = ROT;
  constexpr u64 M36 = (1ull << 36) - 1;

  u32 vx[2], vq[2], vh = 0;
#pragma unroll
  for (int i = 0; i < 2; ++i) {
    int row = 16 * w + 8 * i + (l >> 3);
    vx[i] = (u32)((rb * 128 + row) * XROW + (((l & 7) ^ ((row >> 1) & 7)) << 4));
  }
#pragma unroll
  for (int i = 0; i < 2; ++i) vq[i] = (u32)((8 * w + 4 * (lr & 1) + 2 * i + ((lr >> 1) & 1)) * WROW + hq * 144 + lu * 16);
  if (BR) {
    int row = 2 * w + ((l >> 3) & 1);
    int grow = rb * 128 - 16 + row; grow = grow < 0 ? 0 : grow;
    vh = (u32)(grow * XROW + (((l & 7) ^ ((row >> 1) & 7)) << 4));
  }
  auto kc_of = [&](int cc) { int k = cc + rot; return k >= 9 ? k - 9 : k; };
  __amdgpu_buffer_rsrc_t srdO = __builtin_amdgcn_make_buffer_rsrc((void*)WO, (short)0, 576 * XROW, 0x00020000);
  auto dma_op = [&](auto OP) {
    constexpr int op = OP.value;
    if constexpr (op < 200) {
      constexpr int cc = (op - 100) >> 2, i = op & 3;
      const int kc = kc_of(cc);
      if constexpr (i < 2) dma_full(lb + S0 + (cc & 3) * SS + w * 2048 + i * 1024, vx[i], srdX, (u32)(kc * 128));
      else dma_mask(lb + LHALO + (cc & 3) * 2048 + w * 256, vh, srdX, (u32)(kc * 128), 0xffffull);
    } else {
      constexpr int n = (op - 200) >> 1, i = op & 1;
      constexpr int at = n < 9 ? S0 + (n & 3) * SS + SK : n < 18 ? S0 + ((n - 9) & 3) * SS + SV : n < 27 ? WQ + (n - 18) * WU : wo_at(BR, n - 27);
      const u32 dst = lb + at + (2 * w + i) * 576;
      if constexpr (n < 27) {
        const int kc = kc_of(n % 9), sec = n < 9 ? 1152 : (n < 18 ? 2304 : 0);
        dma_mask(dst, vq[i], srdW, (u32)(kc * 64 * WROW + sec), M36);
      } else {
        const int hi = lr & 1, qq = 2 * i + ((lr >> 1) & 1);
        if constexpr (n > 27)
          dma_mask(dst, (u32)((36 * hpl + 20 * hi + 4 * rg + qq) * XROW + hq * 144 + lu * 16), srdO, (u32)(((n - 28 + hq) & 7) * 72 * XROW), M36);
        else dma_mask(dst, (u32)((36 * (8 * hpl + 2 * rg + hi) + 16 + qq) * XROW + hq * 144 + lu * 16), srdO, 0u, M36);
      }
    }
  };
  auto ev_dma = [&](auto EE, auto II) {
    if constexpr (II.value < SC<BR>::s.nop[EE.value]) dma_op(IC<SC<BR>::s.op[EE.value][II.value]>{});
  };
  auto warm_wo = [&]() {
    const int idx = w * 21 + l;
    const u32 po = l < 21 && idx < 162 ? (u32)((162 * jx + idx) * 128) : OOB;
    asm volatile("s_mov_b32 m0, %0\n\ts_nop 0\n\tbuffer_load_dword %1, %2, 0 offen lds" ::"s"(lb + LSCR), "v"(po), "s"(srdO) : "memory");
  };
  __amdgpu_buffer_rsrc_t srdF = __builtin_amdgcn_make_buffer_rsrc((void*)(ws + WS_OF), (short)0, 256 * 18 * 1024, 0x00020000);
  __amdgpu_buffer_rsrc_t srdS = __builtin_amdgcn_make_buffer_rsrc((void*)(ws + WS_SLAB), (short)0, 136 * SLAB, 0x00020000);
  auto mark = [&](auto EE) {
    constexpr int e = EE.value, f = (e + 2) % 3;
    const u32x4 sv = {SENT, SENT, SENT, SENT};
    const u32 ob = (u32)(((e > 3 ? 128 : 0) + rb * 8 + w) * 18 * 1024);
    if constexpr (e > 0 && f < 2) __builtin_amdgcn_raw_buffer_store_b128(sv, srdF, l * 16, ob + hq * 2048 + f * 1024, 16);
    if constexpr (e > 0 && f == 2) __builtin_amdgcn_raw_buffer_store_b128(sv, srdF, g == 0 ? c * 16 : OOB, ob + (16 + (hq >> 2)) * 1024 + (hq & 3) * 256, 16);
    if constexpr (e == 0) {
      const u32 so = tid < 400 ? (u32)((hq * 16 + rb) * SLAB + tid * 16) : tid < 425 ? (u32)((128 + hq) * SLAB + rb * 400 + (tid - 400) * 16) : OOB;
      __builtin_amdgcn_raw_buffer_store_b128(sv, srdS, so, 0, 16);
    }
  };
  GLB_AS u64* kflags = (GLB_AS u64*)(ws + WS_FLAG + hq * 256);
  GLB_AS u64* oflags = (GLB_AS u64*)(ws + WS_OFLAG + (BR * 16 + rb) * 256);
  u32x2 ep, fv;
  asm volatile("" ::"s"(srdX), "s"(srdW));
  STAMP(16);
  static_assert(PRO == 2, "prologue");
  auto dma_chunk = [&](auto cc) {
    constexpr int c_ = cc.value;
    dma_op(IC<100 + 4 * c_>{}); dma_op(IC<101 + 4 * c_>{});
    if constexpr (BR) dma_op(IC<102 + 4 * c_>{});
    dma_op(IC<200 + 2 * c_>{}); dma_op(IC<201 + 2 * c_>{}); dma_op(IC<218 + 2 * c_>{}); dma_op(IC<219 + 2 * c_>{});
  };
  {
    const char* ea = ws + WS_EPOCH;
    asm volatile("global_load_dwordx2 %0, %1, off sc1" : "=v"(ep) : "v"(ea) : "memory");
  }
  STAMP(17);
  if constexpr (BR == 0) {
    wait_vm_bar<7 + (PCW == 2)>();
    STAMP(18);
    const int h = rg & 1, lf = rg >> 1;
    const int f = lf ? 16 + (q & 1) : c;
    const bool rowok = lf ? (p == h) : true;
    const u32 pb = lb + LPK + (hpl * 2 + h) * 648 + f * 2;
    const int d0 = 2 * h + 4 * g;
    u32 e0 = lds_r16(pb + (d0 + 0) * 36), e1 = lds_r16(pb + (d0 + 1) * 36), e2 = lds_r16(pb + (d0 + 2) * 36), e3 = lds_r16(pb + (d0 + 3) * 36);
    const int dl = h ? 0 : 16;
    u32 l0 = lds_r16(pb + dl * 36), l1 = lds_r16(pb + (dl + 1) * 36);
    SCHED_FENCE();
    dma_op(IC<104>{}); dma_op(IC<105>{});
    SCHED_FENCE();
    u32 lw = (g == 0) ? (l0 | (l1 << 16)) : 0u;
    u32x4 fr = {e0 | (e1 << 16), e2 | (e3 << 16), h ? 0u : lw, h ? lw : 0u};
    if (!rowok) fr = u32x4{0, 0, 0, 0};
    lds_w128(lb + LPHF + (hpl * 4 + rg) * 1024 + l * 16, fr);
    if (w < 3) {
      u32 a = (p == 2 * w) ? ONES : 0u, b = (p == 2 * w + 1) ? ONES : 0u;
      u32 x = ((p & 1) == g1) ? ((p >> 1) ? 0x3f800000u : 0x00003f80u) : 0u;
      lds_w128(lb + LSUM + w * 1024 + l * 16, w < 2 ? u32x4{a, a, b, b} : u32x4{x, 0u, 0u, 0u});
    }
    SCHED_FENCE();
    dma_op(IC<202>{}); dma_op(IC<203>{}); dma_op(IC<220>{}); dma_op(IC<221>{});
  } else {
    bar_only();
    STAMP(18);
    dma_chunk(IC<1>{});
  }
  STAMP(1);

  const u32 xa0 = lb + S0 + (rg * 32 + hpl * 16 + c) * 128 + ((g ^ ((c >> 1) & 7)) << 4);
  const u32 xa1 = xa0 ^ 64;
  const u32 xb0 = xa0 + 2048 - hpl * 4096, xb1 = xb0 ^ 64;
  const u32 xh0 = lb + LHALO + c * 128 + ((g ^ ((c >> 1) & 7)) << 4);
  const u32 xh1 = xh0 ^ 64;
  const u32 wrow = lb + (16 * g1 + 8 * g0 + 2 * q) * 144;
  const u32 wa = wrow + hpl * 72 + 8 * p;
  const u32 wl = wrow + 32 + (p & 1) * 72 + (p >> 1) * (SV - SK);
  const u32 wh = wa + (rg & 1) * 40 + (rg >> 1) * (SV - SK);
  float ninf = -__builtin_inff();
  asm volatile("" : "+s"(ninf));

  u32x4 xf[18][2];
  const f32x4 z4 = {0.f, 0.f, 0.f, 0.f};
  f32x4 k0[2] = {z4, z4}, k1[2] = {z4, z4}, a0[2] = {z4, z4}, a1[2] = {z4, z4}, aL = z4, aH = z4, aHL = z4;
  u32x4 f0, f1, fl, fh, xh;
  auto load_kv_frags = [&](auto HH_, auto JJ) {
    constexpr int H = HH_.value, j = JJ.value, T = H >> 1, n = T >> 1, ks = T & 1;
    constexpr u32 ok = S0 + (n & 3) * SS + SK + ks * 32 * 144, ov = ok + SV - SK;
    if constexpr (H < 36) {
      if constexpr ((H & 1) == 0) {
        if constexpr (j == 0) { xf[T][0] = lds_r128((ks ? xa1 : xa0) + (n & 3) * SS); f0 = frag_tr(wa + ok, wa + ok + 144); }
        if constexpr (j == 1) { xf[T][1] = lds_r128((ks ? xb1 : xb0) + (n & 3) * SS); f1 = frag_tr(wa + ok + 40, wa + ok + 184); }
      } else {
        if constexpr (j == 0) f0 = frag_tr(wa + ov, wa + ov + 144);
        if constexpr (j == 1) f1 = frag_tr(wa + ov + 40, wa + ov + 184);
        if constexpr (j == 2) fl = frag_tr(wl + ok, wl + ok + 144);
        if constexpr (j == 3 && BR == 1) { fh = frag_tr(wh + ok, wh + ok + 144); xh = lds_r128((ks ? xh1 : xh0) + (n & 3) * 2048); }
      }
    }
  };
  wait_bar<wait_n<BR>(0, 0)>();
  STAMP(2);
  load_kv_frags(IC<0>{}, IC<0>{}); load_kv_frags(IC<0>{}, IC<1>{});
  u64 tag = 0;
  static_for<0, 36>([&](auto hh_) {
    constexpr int H = hh_.value, T = H >> 1;
    if constexpr ((H & 3) == 3 && H < 35) {
      wait_bar<wait_n<BR>(H, (H + 1) >> 2)>();
    }
    u32x4 c0 = f0, c1 = f1, cl = fl, ch = fh, cx = xh;
    f32x4(&d0)[2] = (H & 1) ? a0 : k0;
    f32x4(&d1)[2] = (H & 1) ? a1 : k1;
    SCHED_FENCE();
    d0[0] = mfma32(c0, xf[T][0], d0[0]);
    load_kv_frags(IC<H + 1>{}, IC<0>{});
    SCHED_FENCE();
    d0[1] = mfma32(c0, xf[T][1], d0[1]);
    load_kv_frags(IC<H + 1>{}, IC<1>{});
    ev_dma(hh_, IC<0>{});
    SCHED_FENCE();
    d1[0] = mfma32(c1, xf[T][0], d1[0]);
    load_kv_frags(IC<H + 1>{}, IC<2>{});
    SCHED_FENCE();
    d1[1] = mfma32(c1, xf[T][1], d1[1]);
    load_kv_frags(IC<H + 1>{}, IC<3>{});
    ev_dma(hh_, IC<1>{});
    ev_dma(hh_, IC<2>{});
    SCHED_FENCE();
    if constexpr (H & 1) {
      aL = mfma32(cl, xf[T][0], aL);
      asm volatile("" : "+v"(aL));
      if constexpr (BR == 1) {
        aH = mfma32(ch, cx, aH); aHL = mfma32(cl, cx, aHL);
        asm volatile("" : "+v"(aH), "+v"(aHL));
      }
    }
    if constexpr (H == (BR ? E_POB : E_PO)) warm_wo();
    if constexpr (H == 3) {
      asm volatile("" : "+v"(ep));
      tag = next_tag(ep);
    }
    if constexpr (BR == 1 && is_clr(H)) mark(IC<clr_at(H)>{});
    if constexpr (BR == 1 && H == E_PUB) {
      if (tid < 2) __hip_atomic_store(tid == 0 ? kflags + rb : oflags + hq - 512, tag, __ATOMIC_RELAXED, __HIP_MEMORY_SCOPE_AGENT);
    }
    if constexpr (BR == 1 && H == E_PUB2) {
      if (tid == 64) __hip_atomic_store(oflags + hq, tag, __ATOMIC_RELAXED, __HIP_MEMORY_SCOPE_AGENT);
    }
    SCHED_FENCE();
  });
  STAMP(3);
  refresh();
  const u32 wqa = lb + WQ + (16 * g1 + 8 * g0 + 2 * q) * 144 + hpl * 72 + 8 * p;
  const u32 wql = lb + WQ + (16 * g1 + 8 * g0 + 2 * q) * 144 + 32 + (p & 1) * 72;
  auto load_q_frags = [&](auto TT, auto JJ) {
    constexpr int t = TT.value, j = JJ.value;
    constexpr u32 o = (t >> 1) * WU + (t & 1) * 32 * 144;
    if constexpr (t < 18) {
      if constexpr (j == 0) f0 = frag_tr(wqa + o, wqa + o + 144);
      if constexpr (j == 1) f1 = frag_tr(wqa + o + 40, wqa + o + 184);
      if constexpr (j == 2) fl = frag_tr(wql + o, wql + o + 144);
    }
  };
  if constexpr (BR == 1) {
    wait_bar<wait_n<BR>(EQ, 18)>();
    static_for<0, 3>([&](auto j) { load_q_frags(IC<0>{}, j); });
  }
  u32x2 kM[2][2];
#pragma unroll
  for (int sb = 0; sb < 2; ++sb) { kM[0][sb] = pk4(k0[sb]); kM[1][sb] = pk4(k1[sb]); }
  auto unswap = [&](u32x2& a, u32x2& b) {
    u32x2 x = a, y = b;
#pragma unroll
    for (int e = 0; e < 2; ++e) { a[e] = hpl ? y[e] : x[e]; b[e] = hpl ? x[e] : y[e]; }
  };
  {
    constexpr int VROW0 = BR ? 16 : 0;
    auto left = [&](f32x4 v, int blk) {
      u32x2 x = pk4(v);
      const u32 va = lb + lvi(BR) + (VROW0 - 16 + blk * 16 + c) * VIS + g0 * 80 + 64;
      lds_w64(g1 ? va : lb + KLX + (blk * 2 + g0) * 128 + c * 8, u32x2{x[0], g1 ? ONES : x[1]});
      lds_w64(g1 ? va + 8 : lb + LSCR + 256 + c * 8, u32x2{x[1], ONES});
    };
    left(aL, 1 + 2 * rg + hpl);
#pragma unroll
    for (int sb = 0; sb < 2; ++sb) {
      const u32 va = lb + lvi(BR) + (VROW0 + rg * 32 + (sb ^ hpl) * 16 + c) * VIS + hpl * 80;
      lds_w64(va + 8 * g, pk4(a0[sb]));
      lds_w64(va + 32 + 8 * g, pk4(a1[sb]));
    }
    if constexpr (BR == 1) {
      unswap(kM[0][0], kM[0][1]); unswap(kM[1][0], kM[1][1]);
      if (w == 2) left(aHL, 0);
      const u32 kx = lb + LKX + l * 8;
      lds_w64(rg < 2 ? kx + (hpl * 2 + rg) * 512 : lb + lvi(BR) + c * VIS + hpl * 80 + (rg & 1) * 32 + 8 * g, pk4(aH));
      if (rg < 3) {
        const u32 b = kx + (((rg + 1) * 2 + hpl) * 2) * 512;
        lds_w64(b, kM[0][1]); lds_w64(b + 512, kM[1][1]);
      }
    }
  }
  const u32 phf = lb + LPHF + hpl * 4096 + l * 16, sumf = lb + LSUM + l * 16;
  const bool lo = g < 2;
  auto load_flags = [&]() {
    GLB_AS u64* fa = (BR == 0 && g == 0) ? kflags + c : oflags + (l & 7);
    asm volatile("global_load_dwordx2 %0, %1, off sc1" : "=v"(fv) : "v"(fa) : "memory");
  };
  auto check_flags = [&]() {
    if (!__all((((u64)fv[1] << 32) | fv[0]) == tag)) poll_flags((BR == 0 && g == 0) ? kflags + c : oflags + (l & 7), tag);
  };
  if constexpr (BR == 0) {
    bar_lds();
    STAMP(4);
    HH h0, h1;
    h0.b0[0] = kM[0][0][0]; h0.b0[1] = kM[0][0][1]; h0.b1[0] = kM[1][0][0]; h0.b1[1] = kM[1][0][1];
    h1.b0[0] = kM[0][1][0]; h1.b0[1] = kM[0][1][1]; h1.b1[0] = kM[1][1][0]; h1.b1[1] = kM[1][1][1];
    const u32 kl0 = lb + KLX + ((1 + 2 * rg + hpl) * 2 + hpl) * 128 + c * 8, kl1 = lb + KLX + ((2 + 2 * rg - hpl) * 2 + hpl) * 128 + c * 8;
    static_for<0, HH_N>([&](auto st) {
      if constexpr (st.value >= 8 && (st.value - 8) % 12 == 0 && (st.value - 8) / 12 < EQ - EM) {
        constexpr int E = EM + (st.value - 8) / 12;
        SCHED_FENCE();
        ev_dma(IC<E>{}, IC<0>{}); ev_dma(IC<E>{}, IC<1>{});
        if constexpr (E == E_PO) warm_wo();
        if constexpr (E == E_FLD) load_flags();
        SCHED_FENCE();
      }
      hh_step<st.value, false>(h0, kl0, phf, sumf, lo, ninf);
      hh_step<st.value, false>(h1, kl1, phf, sumf, lo, ninf);
    });
#pragma unroll
    for (int sb = 0; sb < 2; ++sb) {
      HH& hs = sb ? h1 : h0;
      hh_left<false>(hs, lo);
      const u32 pa = lb + LPK + (rg * 32 + (sb ^ hpl) * 16 + c) * PKS + hpl * 144;
      lds_w64(pa + 8 * g, u32x2{hs.b0[0], hs.b0[1]});
      lds_w64(pa + 32 + 8 * g, u32x2{hs.b0[2], hs.b0[3]});
      lds_w64(pa + 64 + 8 * g, u32x2{hs.b1[0], hs.b1[1]});
      lds_w64(pa + 96 + 8 * g, u32x2{hs.b1[2], hs.b1[3]});
      lds_w32(pa + 128 + 4 * g, hs.bl);
    }
    STAMP(5);
    wait_bar<wait_n<BR>(EQ, 26)>();
    asm volatile("" : "+v"(fv));
    static_for<0, 3>([&](auto j) { load_q_frags(IC<0>{}, j); });
  }
  refresh();
  f32x4 dp = z4, dm = z4, df = z4;
  u32x4 ap[4], am[4], af[4], bv[4];
  u32 g0_, g1_, g2_, g3_;
  const int rr = 4 * (4 * g0 + q) + 2 * g1;
  const u32 pa = lb + LPK + rr * PKS + hpl * 144 + (rg & 1) * 64 + 8 * p;
  const u32 pf = lb + LPK + rr * PKS + hpl * 144 + 128 + 8 * (p >> 1);
  const u32 va = lb + lvi(BR) + rr * VIS + hpl * 80 + (rg < 2 ? rg * 32 + 8 * p : 64 + 8 * (p & 1));
  auto hook = [&](auto tt, auto jj) {
    constexpr int t = tt.value, E = EQ + t, j = jj.value;
    if constexpr (BR == 0 && j < 4) {
      if constexpr (t == Q_SLAB - 2) {
        ap[j] = frag_tr(pa + j * 32 * PKS, pa + j * 32 * PKS + PKS);
        am[j] = frag_tr(pa + 32 + j * 32 * PKS, pa + 32 + j * 32 * PKS + PKS);
        af[j] = frag_tr(pf + j * 32 * PKS, pf + j * 32 * PKS + PKS);
        bv[j] = frag_tr(va + j * 32 * VIS, va + j * 32 * VIS + VIS);
      }
      if constexpr (t == Q_SLAB - 1) {
        dp = mfma32(ap[j], bv[j], dp); dm = mfma32(am[j], bv[j], dm); df = mfma32(af[j], bv[j], df);
        asm volatile("" : "+v"(dp), "+v"(dm), "+v"(df));
      }
    }
    if constexpr (j != 4) return;
    if constexpr (BR == 1 && is_clr(E)) mark(IC<clr_at(E)>{});
    if constexpr (BR == 1 && E == E_PUB2) {
      if (tid == 0) __hip_atomic_store(oflags + hq, tag, __ATOMIC_RELAXED, __HIP_MEMORY_SCOPE_AGENT);
    }
    if constexpr (BR == 1 && E == e_fld(BR)) load_flags();
    if constexpr (E == e_fchk(BR)) {
      if constexpr (BR == 1) asm volatile("s_waitcnt vmcnt(%1)" : "+v"(fv) : "n"(wait_h<BR>(E, e_fld(BR))) : "memory");
      check_flags();
    }
    if constexpr (BR == 0) {
      if constexpr (t == Q_SLAB) {
        const u32 slab = (u32)((hq * 16 + rb) * SLAB);
        const bool h0set = (0x47 >> (c & 7)) & 1, h = rg & 1, own = (c >> 3) == hpl;
        u32x2 a = pk4(dp), b = pk4(dm);
        const u32 o1 = rg < 2 ? slab + (hpl * 2 + rg) * 1024 + l * 16 : (own && h0set != h) ? slab + 4096 + l * 16 : OOB;
        __builtin_amdgcn_raw_buffer_store_b128(u32x4{ns(a[0]), ns(a[1]), ns(b[0]), ns(b[1])}, srdS, o1, 0, 16);
        const float x0 = g0 ? df[2] : df[0], x1 = g0 ? df[3] : df[1];
        const bool first = rg < 2 ? !h : h0set;
        const u32 o2 = rg < 2 ? slab + 5120 + (hpl * 2 + rg) * 256 + l * 4 : own ? slab + 6144 + l * 4 : OOB;
        __builtin_amdgcn_raw_buffer_store_b32(ns(pk(first ? x0 : 0.f, first ? 0.f : x1)), srdS, o2, 0, 16);
        STAMP(6);
      }
      if constexpr (t == Q_L1 || t == Q_L1C) {
        const int dw = w * 16 + c;
        const u32 dof = (u32)((rb * 100 + (dw < 100 ? dw : 99)) * 4);
        const u32 go = (u32)((hq * 16 + 4 * g) * SLAB) + dof;
        if constexpr (t == Q_L1) {
          asm volatile("buffer_load_dword %0, %4, %5, 0 offen sc1\n\tbuffer_load_dword %1, %4, %5, %6 offen sc1\n\t"
                       "buffer_load_dword %2, %4, %5, %7 offen sc1\n\tbuffer_load_dword %3, %4, %5, %8 offen sc1"
                       : "=&v"(g0_), "=&v"(g1_), "=&v"(g2_), "=&v"(g3_)
                       : "v"(go), "s"(srdS), "s"(SLAB), "s"(2 * SLAB), "s"(3 * SLAB)
                       : "memory");
        } else {
          asm volatile("s_waitcnt vmcnt(%4)" : "+v"(g0_), "+v"(g1_), "+v"(g2_), "+v"(g3_) : "n"(wait_h<BR>(E, EQ + Q_L1)) : "memory");
          _Pragma("nounroll") for (int spin = 0; spin < (1 << 20); ++spin) {
            asm volatile("" ::: "memory");
            if (!__any(umax(umax3(g0_, g1_, g2_), g3_) == SENT)) break;
            g0_ = __builtin_amdgcn_raw_buffer_load_b32(srdS, go, 0, 16);
            g1_ = __builtin_amdgcn_raw_buffer_load_b32(srdS, go, SLAB, 16);
            g2_ = __builtin_amdgcn_raw_buffer_load_b32(srdS, go, 2 * SLAB, 16);
            g3_ = __builtin_amdgcn_raw_buffer_load_b32(srdS, go, 3 * SLAB, 16);
          }
          const u32 sel = c == 0 ? 0x00003f80u : c == 1 ? 0x3f800000u : 0u;
          f32x4 sum = mfma32(u32x4{sel, sel, sel, sel}, u32x4{g0_, g1_, g2_, g3_}, z4);
          __builtin_amdgcn_raw_buffer_store_b32(ns(pk(sum[0], sum[1])), srdS, g == 0 ? (u32)((128 + hq) * SLAB) + dof : OOB, 0, 16);
          STAMP(7);
        }
      }
    }
  };
  a0[0] = z4; a0[1] = z4; a1[0] = z4; a1[1] = z4; aL = z4;
  static_for<0, 18>([&](auto tt) {
    constexpr int t = tt.value, E = EQ + t;
    if constexpr (BR == 0) {
      if constexpr (t == 3 || t == 6) bar_only();
    } else if constexpr ((t & 1) && t < 17) {
      if constexpr (t == 1) wait_bar<wait_n<BR>(E, 18 + (t >> 1) + 1)>();
      else wait_vm_bar<wait_n<BR>(E, 18 + (t >> 1) + 1)>();
    }
    u32x4 c0 = f0, c1 = f1, cl = fl;
    SCHED_FENCE();
    a0[0] = mfma32(c0, xf[t][0], a0[0]);
    load_q_frags(IC<t + 1>{}, IC<0>{});
    hook(tt, IC<0>{});
    SCHED_FENCE();
    a0[1] = mfma32(c0, xf[t][1], a0[1]);
    load_q_frags(IC<t + 1>{}, IC<1>{});
    hook(tt, IC<1>{});
    ev_dma(IC<E>{}, IC<0>{});
    SCHED_FENCE();
    a1[0] = mfma32(c1, xf[t][0], a1[0]);
    load_q_frags(IC<t + 1>{}, IC<2>{});
    hook(tt, IC<2>{});
    SCHED_FENCE();
    a1[1] = mfma32(c1, xf[t][1], a1[1]);
    hook(tt, IC<3>{});
    ev_dma(IC<E>{}, IC<1>{});
    SCHED_FENCE();
    aL = mfma32(cl, xf[t][0], aL);
    asm volatile("" : "+v"(aL));
    hook(tt, IC<4>{});
    SCHED_FENCE();
  });
  STAMP(8);
  refresh();
  if (g < 2) lds_w64(lb + QLX + ((2 * rg + hpl) * 2 + g) * 128 + c * 8, pk4(aL));
  bar_lds();
  const u32 ofo = (u32)((BR * 128 + rb * 8 + rg * 2) * 18 * 1024);
  const int hp = hq * 2 + hpl;
  auto store_o = [&](int sb, f32x4 o0, f32x4 o1, float l0, float l1, float r0, float r1) {
    u32x2 a = pk4(o0 * r0), b = pk4(o1 * r1);
    __builtin_amdgcn_raw_buffer_store_b128(u32x4{ns(a[0]), ns(a[1]), ns(b[0]), ns(b[1])}, srdF, ofo + (sb * 18 + hp) * 1024 + l * 16, 0, 16);
    lds_w128(lb + LOWN + ((rg * 2 + sb) * 2 + hpl) * 1024 + l * 16, u32x4{a[0], a[1], b[0], b[1]});
    float rr = g0 ? r1 : r0;
    u32 lv = ns(pk(l0 * rr, l1 * rr));
    auto sw = __builtin_amdgcn_permlane16_swap(lv, lv, false, false);
    u32 s0 = sw[0], s1 = sw[1];
    __builtin_amdgcn_raw_buffer_store_b64(u32x2{s0, s1}, srdF, g == 0 ? ofo + (sb * 18 + 16 + (hq >> 2)) * 1024 + (c + 16 * (hq & 3)) * 16 + 8 * hpl : OOB, 0, 16);
  };

  if (BR == 0) {
    HH h0, h1;
    {
      u32x2 qa0 = pk4(a0[0]), qb0 = pk4(a1[0]), qa1 = pk4(a0[1]), qb1 = pk4(a1[1]);
      h0.b0[0] = qa0[0]; h0.b0[1] = qa0[1]; h0.b1[0] = qb0[0]; h0.b1[1] = qb0[1];
      h1.b0[0] = qa1[0]; h1.b0[1] = qa1[1]; h1.b1[0] = qb1[0]; h1.b1[1] = qb1[1];
    }
    const u32 ql0 = lb + QLX + ((2 * rg + hpl) * 2 + hpl) * 128 + c * 8, ql1 = lb + QLX + ((2 * rg + 1 - hpl) * 2 + hpl) * 128 + c * 8;
    const u32 kf = (u32)((128 + hq) * SLAB);
    const int l8 = g * 16 + (c & 7) + 8 * hpl;
    u32x4 AM0, AM1, AMLm;
    u32 AL0, AL1, ALL;
    auto load_kv = [&]() {
      AM0 = __builtin_amdgcn_raw_buffer_load_b128(srdS, kf + hpl * 2048 + l * 16, 0, 16);
      AM1 = __builtin_amdgcn_raw_buffer_load_b128(srdS, kf + hpl * 2048 + 1024 + l * 16, 0, 16);
      AMLm = __builtin_amdgcn_raw_buffer_load_b128(srdS, kf + 4096 + l8 * 16, 0, 16);
      AL0 = __builtin_amdgcn_raw_buffer_load_b32(srdS, kf + 5120 + hpl * 512 + l * 4, 0, 16);
      AL1 = __builtin_amdgcn_raw_buffer_load_b32(srdS, kf + 5120 + hpl * 512 + 256 + l * 4, 0, 16);
      ALL = __builtin_amdgcn_raw_buffer_load_b32(srdS, kf + 6144 + l8 * 4, 0, 16);
    };
    static_for<0, HH_N>([&](auto st) {
      if (st.value == L2ST) { SCHED_FENCE(); load_kv(); SCHED_FENCE(); }
      if (st.value == L2ST2) {
        SCHED_FENCE();
        if (__any(umax4(umax4(umax4(umax3(AL0, AL1, ALL), AM0), AM1), AMLm) == SENT)) load_kv();
        SCHED_FENCE();
      }
      if constexpr (st.value >= HQ0 && (st.value - HQ0) % HQS == 0 && (st.value - HQ0) / HQS < NEV - EH) {
        constexpr int E = EH + (st.value - HQ0) / HQS;
        if constexpr (SC<BR>::s.nop[E] > 0) { SCHED_FENCE(); ev_dma(IC<E>{}, IC<0>{}); SCHED_FENCE(); }
      }
      hh_step<st.value, true>(h0, ql0, phf, sumf, lo, ninf);
      hh_step<st.value, true>(h1, ql1, phf, sumf, lo, ninf);
    });
    hh_left<true>(h0, lo);
    hh_left<true>(h1, lo);
    asm volatile("" : "+v"(h0.b0), "+v"(h0.b1), "+v"(h0.bl), "+v"(h1.b0), "+v"(h1.b1), "+v"(h1.bl));
    STAMP(9);
    _Pragma("nounroll") for (int spin = 0; spin < (1 << 20); ++spin) {
      asm volatile("" ::: "memory");
      if (!__any(umax4(umax4(umax4(umax3(AL0, AL1, ALL), AM0), AM1), AMLm) == SENT)) break;
      load_kv();
    }
    STAMP(10);
    const bool h0set = (0x47 >> (c & 7)) & 1;
    u32x4 AML0, AML1;
#pragma unroll
    for (int e = 0; e < 4; ++e) { AML0[e] = h0set ? AMLm[e] : 0u; AML1[e] = h0set ? 0u : AMLm[e]; }
    f32x4 O0[2], O1[2], OL[2];
#pragma unroll
    for (int sb = 0; sb < 2; ++sb) OL[sb] = mfma32(AML0, (sb ? h1 : h0).b0, z4);
#pragma unroll
    for (int sb = 0; sb < 2; ++sb) O0[sb] = mfma32(AM0, (sb ? h1 : h0).b0, z4);
#pragma unroll
    for (int sb = 0; sb < 2; ++sb) OL[sb] = mfma32(AML1, (sb ? h1 : h0).b1, OL[sb]);
#pragma unroll
    for (int sb = 0; sb < 2; ++sb) O1[sb] = mfma32(AM1, (sb ? h1 : h0).b1, z4);
#pragma unroll
    for (int sb = 0; sb < 2; ++sb) OL[sb] = mfma32(u32x4{ALL, 0u, 0u, 0u}, u32x4{(sb ? h1 : h0).bl, 0u, 0u, 0u}, OL[sb]);
#pragma unroll
    for (int sb = 0; sb < 2; ++sb) O0[sb] = mfma32(u32x4{AL0, 0u, 0u, 0u}, u32x4{(sb ? h1 : h0).bl, 0u, 0u, 0u}, O0[sb]);
#pragma unroll
    for (int sb = 0; sb < 2; ++sb) O1[sb] = mfma32(u32x4{AL1, 0u, 0u, 0u}, u32x4{(sb ? h1 : h0).bl, 0u, 0u, 0u}, O1[sb]);
#pragma unroll
    for (int sb = 0; sb < 2; ++sb) {
      const HH& h = sb ? h1 : h0;
      float r0 = __builtin_amdgcn_rcpf(fmaf(h.S[0], 1e-6f, OL[sb][2])), r1 = __builtin_amdgcn_rcpf(fmaf(h.S[2], 1e-6f, OL[sb][3]));
      store_o(sb ^ hpl, O0[sb], O1[sb], OL[sb][0], OL[sb][1], r0, r1);
    }
  } else {
    auto ev3 = [&](auto EE) {
      SCHED_FENCE();
      ev_dma(EE, IC<0>{}); ev_dma(EE, IC<1>{}); ev_dma(EE, IC<2>{});
      if constexpr (EE.value == e_fld(BR)) load_flags();
      SCHED_FENCE();
    };
    const u32 kx = lb + LKX + ((rg * 2 + hpl) * 2) * 512 + l * 8;
    u32x2 pM0 = lds_r64(kx), pM1 = lds_r64(kx + 512);
    u32x2 kL[3];
#pragma unroll
    for (int i = 0; i < 3; ++i) kL[i] = lds_r64(lb + KLX + ((2 * rg + i) * 2 + hpl) * 128 + c * 8);
    ev3(IC<EH>{});
    const int par = c & 1;
    const bool ina = (4 * g + par) > c, inb = (4 * g + par + 2) > c;
    const float sc = 0.23570226039551584f * LOG2E;
    float la[2][2], lbv[2][2], mx[2][2];
    u32x2 qM[2][2] = {{pk4(a0[0]), pk4(a0[1])}, {pk4(a1[0]), pk4(a1[1])}};
    unswap(qM[0][0], qM[0][1]); unswap(qM[1][0], qM[1][1]);
#pragma unroll
    for (int sb = 0; sb < 2; ++sb) {
      u32x2 qlv = lds_r64(lb + QLX + ((2 * rg + sb) * 2 + hpl) * 128 + c * 8);
      u32x2 q0 = qM[0][sb], q1 = qM[1][sb];
      u32x4 b0 = {q0[0], q0[1], g == 0 ? qlv[0] : 0u, 0u};
      u32x4 b1 = {q1[0], q1[1], 0u, g == 0 ? qlv[1] : 0u};
      u32x2 kp0 = sb ? kM[0][0] : pM0, kp1 = sb ? kM[1][0] : pM1, kpl = kL[sb], kcl = kL[sb + 1];
      f32x4 sp0 = mfma32(u32x4{kp0[0], kp0[1], kpl[0], kpl[1]}, b0, z4);
      f32x4 sp1 = mfma32(u32x4{kp1[0], kp1[1], kpl[0], kpl[1]}, b1, z4);
      f32x4 sc0 = mfma32(u32x4{kM[0][sb][0], kM[0][sb][1], kcl[0], kcl[1]}, b0, z4);
      f32x4 sc1 = mfma32(u32x4{kM[1][sb][0], kM[1][sb][1], kcl[0], kcl[1]}, b1, z4);
      if (sb) ev3(IC<EH + 2>{}); else ev3(IC<EH + 1>{});
      const bool dead = (sb == 0) && (rb == 0) && (rg == 0);
      float pa0 = par ? sp0[1] : sp0[0], pb0 = par ? sp0[3] : sp0[2], pa1 = par ? sp1[1] : sp1[0], pb1 = par ? sp1[3] : sp1[2];
      if (dead) { pa0 = pb0 = pa1 = pb1 = -1e30f; }
      la[0][sb] = (ina ? pa0 : (par ? sc0[1] : sc0[0])) * sc;
      lbv[0][sb] = (inb ? pb0 : (par ? sc0[3] : sc0[2])) * sc;
      la[1][sb] = (ina ? pa1 : (par ? sc1[1] : sc1[0])) * sc;
      lbv[1][sb] = (inb ? pb1 : (par ? sc1[3] : sc1[2])) * sc;
      mx[0][sb] = fmaxf(la[0][sb], lbv[0][sb]);
      mx[1][sb] = fmaxf(la[1][sb], lbv[1][sb]);
    }
    float tm = redmax4(mx[0][0], mx[0][1], mx[1][0], mx[1][1], ninf);
    bcast4(tm, mx[0][0], mx[0][1], mx[1][0], mx[1][1]);
    ev3(IC<EH + 3>{});
    const u32 vb = lb + lvi(BR) + (rg * 32 + 4 * g + q) * VIS + hpl * 80;
#pragma unroll
    for (int sb = 0; sb < 2; ++sb) {
      const u32 v0 = vb + sb * 16 * VIS, v1 = v0 + 16 * VIS;
      u32x4 A0 = frag_tr(v0 + 8 * p, v1 + 8 * p), A1 = frag_tr(v0 + 32 + 8 * p, v1 + 32 + 8 * p);
      u32x4 AL = frag_tr(v0 + 64 + 8 * (p & 1), v1 + 64 + 8 * (p & 1));
      u32x4 P[2];
#pragma unroll
      for (int h = 0; h < 2; ++h) {
        float ea = __builtin_amdgcn_exp2f(la[h][sb] - mx[h][sb]), eb = __builtin_amdgcn_exp2f(lbv[h][sb] - mx[h][sb]);
        u32 wa_ = par ? pk(0.f, ea) : pk(ea, 0.f), wb_ = par ? pk(0.f, eb) : pk(eb, 0.f);
        P[h] = u32x4{ina ? wa_ : 0u, inb ? wb_ : 0u, ina ? 0u : wa_, inb ? 0u : wb_};
      }
      f32x4 O0 = mfma32(A0, P[0], z4), O1 = mfma32(A1, P[1], z4);
      f32x4 L0 = mfma32(AL, P[0], z4), L1 = mfma32(AL, P[1], z4);
      float r0 = __builtin_amdgcn_rcpf(L0[2]), r1 = __builtin_amdgcn_rcpf(L1[2]);
      if (e_fchk(BR) == EH + 4 && sb == 0) {
        asm volatile("s_waitcnt vmcnt(%1)" : "+v"(fv) : "n"(wait_h<BR>(EH + 4, e_fld(BR))) : "memory");
        check_flags();
      }
      store_o(sb, O0, O1, g0 ? L1[0] : L0[0], g0 ? L1[1] : L0[1], r0, r1);
    }
  }
  STAMP(11);
  refresh();
  const u32 ofl = (u32)((BR * 128 + rb * 8 + w) * 18 * 1024 + l * 16);
  u32x4 wf[18][5], of[18];
  wait_bar<4>();
  STAMP(12);
  const u32 wrow2 = lb + (16 * g1 + 8 * g0 + 2 * q) * 144;
  u32 oa[3], oa4[3];
#pragma unroll
  for (int i = 0; i < 3; ++i) {
    oa[i] = wrow2 + 8 * p + i * 57344; oa4[i] = wrow2 + 128 + 8 * (p & 1) + i * 57344;
    asm volatile("" : "+v"(oa[i]), "+v"(oa4[i]));
  }
  auto load_wf = [&](auto TT, auto II) {
    constexpr int t = TT.value, i = II.value;
    if constexpr (t < 18) {
      constexpr u32 cb = wo_at(BR, t < 4 ? 1 - (t >> 1) : t >> 1) + (t & 1) * 32 * 144, s = cb / 57344, o = cb % 57344;
      if constexpr (i < 4) wf[t][i] = frag_tr(oa[s] + o + 32 * i, oa[s] + o + 144 + 32 * i);
      else wf[t][4] = frag_tr(oa4[s] + o, oa4[s] + o + 144);
    }
  };
  f32x4 acc[5];
  u32 seen;
  auto kstep = [&](auto tt) {
    constexpr int t = tt.value;
    static_for<0, 5>([&](auto ii) {
      constexpr int i = ii.value;
      acc[i] = mfma32(wf[t][i], of[t], t ? acc[i] : z4);
      if constexpr (i == 1 && t >= 4) seen = umax4(seen, of[t]);
      load_wf(IC<t + PRE>{}, ii);
      SCHED_FENCE();
    });
  };
  u32 so[14], own = lb + LOWN + w * 2048 + l * 16;
#pragma unroll
  for (int t = 0; t < 14; ++t) { so[t] = ((t + 2 + 2 * hq) & 15) * 1024; asm volatile("" : "+s"(so[t])); }
  asm volatile("" : "+v"(own));
  _Pragma("nounroll") for (int pass = 0; pass < (1 << 16); ++pass) {
#pragma unroll
    for (int t = 4; t < 4 + NOPT; ++t) of[t] = __builtin_amdgcn_raw_buffer_load_b128(srdF, ofl, so[t - 4], 16);
    of[0] = lds_r128(own);
    of[1] = lds_r128(own + 1024);
    SCHED_FENCE();
    static_for<0, PRE * 5>([&](auto t) {
      load_wf(IC<t.value / 5>{}, IC<t.value % 5>{});
      if constexpr (t.value == PP) {
        SCHED_FENCE();
        of[2] = __builtin_amdgcn_raw_buffer_load_b128(srdF, ofl, 16 * 1024, 16);
        of[3] = __builtin_amdgcn_raw_buffer_load_b128(srdF, ofl, 17 * 1024, 16);
        SCHED_FENCE();
      }
    });
    SCHED_FENCE();
    kstep(IC<0>{}); kstep(IC<1>{});
    _Pragma("nounroll") for (int spin = 0; spin < (1 << 20); ++spin) {
      asm volatile("" ::: "memory");
      if (!__any(umax4(umax4(0u, of[2]), of[3]) == SENT)) break;
      of[2] = __builtin_amdgcn_raw_buffer_load_b128(srdF, ofl, 16 * 1024, 16);
      of[3] = __builtin_amdgcn_raw_buffer_load_b128(srdF, ofl, 17 * 1024, 16);
#pragma unroll
      for (int t = 4; t < 4 + NOPT; ++t) of[t] = __builtin_amdgcn_raw_buffer_load_b128(srdF, ofl, so[t - 4], 16);
    }
    STAMP(13);
#pragma unroll
    for (int t = 4 + NOPT; t < 18; ++t) of[t] = __builtin_amdgcn_raw_buffer_load_b128(srdF, ofl, so[t - 4], 16);
    SCHED_FENCE();
    seen = 0;
    static_for<2, 18>(kstep);
    if (!__any(seen == SENT)) break;
  }
  STAMP(14);
  if (BR == 0 && rb == 0 && hq == 0 && tid == 0) __hip_atomic_store((GLB_AS u64*)(ws + WS_EPOCH), tag, __ATOMIC_RELAXED, __HIP_MEMORY_SCOPE_AGENT);
  char* op = out + (size_t)(rb * 128 + w * 16 + c) * 2304 + BR * 1152 + hq * 144;
  __amdgpu_buffer_rsrc_t srdU = __builtin_amdgcn_make_buffer_rsrc((void*)out, (short)0, 2048 * 2304, 0x00020000);
  const u32 oo = (u32)((rb * 128 + w * 16 + c) * 2304 + BR * 1152 + hq * 144);
#pragma unroll
  for (int i = 0; i < 2; ++i) {
    u32x2 a = pk4(acc[2 * i]), b = pk4(acc[2 * i + 1]);
    auto s0 = __builtin_amdgcn_permlane16_swap(a[0], b[0], false, false);
    auto s1 = __builtin_amdgcn_permlane16_swap(a[1], b[1], false, false);
    u32 s00 = s0[0], s01 = s0[1], s10 = s1[0], s11 = s1[1];
    *(u32x4*)(op + 64 * i + 32 * g0 + 16 * g1) = u32x4{s00, s10, s01, s11};
  }
  if (g < 2) *(u32x2*)(op + 128 + 8 * g) = pk4(acc[4]);
  STAMP(15);
  RSTAMP(31);
  STAMP_FLUSH();
  asm volatile(".Lhk_to_%0:" ::"n"(BR));
  __builtin_amdgcn_endpgm();
}

__global__ __launch_bounds__(512, 2) void fused(const char* __restrict__ XA, const char* __restrict__ WA, const char* __restrict__ PHI,
                                                const char* __restrict__ WOA, const char* __restrict__ XB, const char* __restrict__ WB,
                                                const char* __restrict__ WOB, char* ws, char* out) {
  __shared__ __attribute__((aligned(16))) char smem[LDS_BYTES];
  u64 tent = 0, rent = 0;
  const u32 lb = (u32)(uintptr_t)(LDS_AS char*)smem;
  const int b = blockIdx.x, x = b & 7, j = b >> 3, xx = x & 3;
  const int rb = xx * 4 + (j >> 3), hq = j & 7;
  u64x4 ka, kb;
  u64 kc;
  first(x >= 4, rb, hq, j, lb, ka, kb, kc);
  auto ptr = [](u64 a) { return (char*)(GLB_AS char*)a; };
  XA = ptr(ka[0]); WA = ptr(ka[1]); PHI = ptr(ka[2]); WOA = ptr(ka[3]);
  XB = ptr(kb[0]); WB = ptr(kb[1]); WOB = ptr(kb[2]); ws = ptr(kb[3]); out = ptr(kc);
  if (x < 4) body<0>(XA, WA, PHI, WOA, ws, out, rb, hq, j, lb, tent, rent);
  else body<1>(XB, WB, PHI, WOB, ws, out, rb, hq, j, lb, tent, rent);
}
}

extern "C" void kernel_launch(void* const* d_in, const int* in_sizes, int n_in, void* d_out, int out_size, void* d_ws, size_t ws_size,
                              hipStream_t stream) {
  hk::fused<<<256, 512, 0, stream>>>((const char*)d_in[0], (const char*)d_in[1], (const char*)d_in[2], (const char*)d_in[3], (const char*)d_in[4],
                                     (const char*)d_in[5], (const char*)d_in[6], (char*)d_ws, (char*)d_out);
}
```

```cpp
#define TCH 1
#define E_TCH 17
#define CPRO 1
#define E_PO 43
#define E_FLD 36
#include <hip/hip_runtime.h>
#include <stdint.h>

namespace hk {
typedef unsigned int u32;
typedef unsigned long long u64;
typedef unsigned short u16;
typedef float f32x4 __attribute__((ext_vector_type(4)));
typedef float f32x2 __attribute__((ext_vector_type(2)));
typedef u32 u32x4 __attribute__((ext_vector_type(4)));
typedef u32 u32x2 __attribute__((ext_vector_type(2)));
typedef __bf16 bf16x8 __attribute__((ext_vector_type(8)));
typedef __bf16 bf16x2 __attribute__((ext_vector_type(2)));
typedef short s16x4 __attribute__((ext_vector_type(4)));
#define LDS_AS __attribute__((address_space(3)))
#define GLB_AS __attribute__((address_space(1)))
#define DEVI __device__ __forceinline__
#define SCHED_FENCE() __builtin_amdgcn_sched_barrier(0)

template <int V> struct IC { static constexpr int value = V; };
template <int I, int N, class F>
DEVI void static_for(F&& f) {
  if constexpr (I < N) { f(IC<I>{}); static_for<I + 1, N>(f); }
}

constexpr int XROW = 1152, WROW = 3456;
constexpr size_t WS_OF = 0;
constexpr int SLAB = 6400;
constexpr size_t WS_SLAB = 5u << 20;
constexpr size_t WS_FLAG = 6u << 20;
constexpr size_t WS_EPOCH = WS_FLAG + 65536;
constexpr size_t WS_OFLAG = WS_FLAG + 4096;
constexpr u32 OOB = 0x7fffffffu;
constexpr u32 SENT = 0xffffffffu;
constexpr int WU = 9216;
constexpr int LPK = 1024, PKS = 296;
constexpr int S0 = 3968, SS = 34944;
constexpr int SK = 16384, SV = SK + WU + 16;
constexpr int WQ = S0 + SS;
constexpr int LT = WQ + 9 * WU;
constexpr int LM = S0 + 4 * SS;
constexpr int LPHF = LM;
constexpr int LSUM = LPHF + 8192;
constexpr int LHALO = LM;
constexpr int KLX = LM + 11264;
constexpr int QLX = KLX + 2304;
constexpr int LSCR = QLX + 2048;
constexpr int VIS = 168;
constexpr int lvi(int br) { return br ? 1024 : LT; }
constexpr int LKX = 25600;
constexpr int wo_at(int br, int m) {
  if (br == 0) return m < 4 ? 1024 + m * WU : m < 6 ? LT + (m - 4) * WU : WQ + (m - 6) * WU;
  return m < 2 ? LT + m * WU : m == 2 ? LM : WQ + (m - 3) * WU;
}
constexpr int LOWN = WQ + 6 * WU;
constexpr int LDS_BYTES = 163840;
static_assert(LSCR + 512 <= LDS_BYTES && LPK + 2688 <= S0 && LPK + 128 * PKS <= WQ && LT + 128 * VIS <= LM && 1024 + 144 * VIS <= LKX && LKX + 8192 <= WQ &&
              SV + WU <= SS && 3 * WU <= SS && 7 * WU <= 2 * SS && S0 % 128 == 0 && SS % 128 == 0, "lds");

DEVI u32x2 tr_read(u32 a) { return __builtin_bit_cast(u32x2, __builtin_amdgcn_ds_read_tr16_b64_v4i16((LDS_AS s16x4*)a)); }
DEVI u32x4 frag_tr(u32 a0, u32 a1) { u32x2 x = tr_read(a0), y = tr_read(a1); return u32x4{x[0], x[1], y[0], y[1]}; }
DEVI u32x4 lds_r128(u32 a) { return *(LDS_AS u32x4*)a; }
DEVI u32x2 lds_r64(u32 a) { return *(LDS_AS u32x2*)a; }
DEVI u32 lds_r32(u32 a) { return *(LDS_AS u32*)a; }
DEVI u32 lds_r16(u32 a) { return *(LDS_AS u16*)a; }
DEVI void lds_w128(u32 a, u32x4 v) { *(LDS_AS u32x4*)a = v; }
DEVI void lds_w64(u32 a, u32x2 v) { *(LDS_AS u32x2*)a = v; }
DEVI void lds_w32(u32 a, u32 v) { *(LDS_AS u32*)a = v; }
DEVI f32x4 mfma32(u32x4 a, u32x4 b, f32x4 c) {
  return __builtin_amdgcn_mfma_f32_16x16x32_bf16(__builtin_bit_cast(bf16x8, a), __builtin_bit_cast(bf16x8, b), c, 0, 0, 0);
}
DEVI u32 pk(float a, float b) { f32x2 v = {a, b}; return __builtin_bit_cast(u32, __builtin_convertvector(v, bf16x2)); }
DEVI u32x2 pk4(f32x4 v) { return u32x2{pk(v[0], v[1]), pk(v[2], v[3])}; }
DEVI float max3(float a, float b, float c) { return fmaxf(fmaxf(a, b), c); }
DEVI u32 umax(u32 a, u32 b) { return a > b ? a : b; }
DEVI u32 umax3(u32 a, u32 b, u32 c) { return umax(umax(a, b), c); }
DEVI u32 umax4(u32 m, u32x4 v) { return umax3(umax3(m, v[0], v[1]), v[2], v[3]); }
DEVI u32 ns(u32 v) { return v < 0xfffffffeu ? v : 0xfffffffeu; }

DEVI void dma_full(u32 lds, u32 voff, __amdgpu_buffer_rsrc_t srd, u32 soff) {
  asm volatile("s_mov_b32 m0, %0\n\ts_nop 0\n\tbuffer_load_dwordx4 %1, %2, %3 offen lds" ::"s"(lds), "v"(voff), "s"(srd), "s"(soff) : "memory");
}
DEVI void dma_mask(u32 lds, u32 voff, __amdgpu_buffer_rsrc_t srd, u32 soff, u64 mask) {
  asm volatile("s_mov_b32 m0, %0\n\ts_mov_b64 exec, %4\n\tbuffer_load_dwordx4 %1, %2, %3 offen lds\n\ts_mov_b64 exec, -1" ::"s"(lds), "v"(voff), "s"(srd),
               "s"(soff), "s"(mask)
               : "memory");
}
template <int N>
DEVI void wait_bar() { asm volatile("s_waitcnt vmcnt(%0) lgkmcnt(0)\n\ts_barrier" ::"n"(N) : "memory"); }
template <int N>
DEVI void wait_vm_bar() { asm volatile("s_waitcnt vmcnt(%0)\n\ts_barrier" ::"n"(N) : "memory"); }
DEVI void bar_lds() { asm volatile("s_waitcnt lgkmcnt(0)\n\ts_barrier" ::: "memory"); }
DEVI void bar_only() { asm volatile("s_barrier" ::: "memory"); }

DEVI float redmax4(float a, float b, float c, float d, float ninf) {
  auto r1 = __builtin_amdgcn_permlane32_swap(__float_as_uint(a), __float_as_uint(b), false, false);
  auto r2 = __builtin_amdgcn_permlane32_swap(__float_as_uint(c), __float_as_uint(d), false, false);
  u32 r10 = r1[0], r11 = r1[1], r20 = r2[0], r21 = r2[1];
  float ab = max3(__uint_as_float(r10), __uint_as_float(r11), ninf);
  float cd = max3(__uint_as_float(r20), __uint_as_float(r21), ninf);
  auto r3 = __builtin_amdgcn_permlane16_swap(__float_as_uint(ab), __float_as_uint(cd), false, false);
  u32 r30 = r3[0], r31 = r3[1];
  return max3(__uint_as_float(r30), __uint_as_float(r31), ninf);
}
DEVI void bcast4(float t, float& a, float& b, float& c, float& d) {
  u32 tu = __float_as_uint(t);
  auto s1 = __builtin_amdgcn_permlane16_swap(tu, tu, false, false);
  u32 x = s1[0], y = s1[1];
  auto s2 = __builtin_amdgcn_permlane32_swap(x, x, false, false);
  auto s3 = __builtin_amdgcn_permlane32_swap(y, y, false, false);
  u32 s20 = s2[0], s21 = s2[1], s30 = s3[0], s31 = s3[1];
  a = __uint_as_float(s20); b = __uint_as_float(s21); c = __uint_as_float(s30); d = __uint_as_float(s31);
}

constexpr float LOG2E = 1.4426950408889634f;
constexpr u32 ONES = 0x3f803f80u;

struct HH {
  u32x4 f0, f1, f2, f3;
  u32x4 b0, b1;
  u32 bl;
  f32x4 T0, T1, TL, S;
  float a, b, c, d;
  f32x4 p0, q0, p1, q1;
  float l0, l1;
};
constexpr int HH_N = 106;
DEVI void swap32(float& a, float& b) {
  auto r = __builtin_amdgcn_permlane32_swap(__float_as_uint(a), __float_as_uint(b), false, false);
  u32 x = r[0], y = r[1]; a = __uint_as_float(x); b = __uint_as_float(y);
}
DEVI void swap16(float& a, float& b) {
  auto r = __builtin_amdgcn_permlane16_swap(__float_as_uint(a), __float_as_uint(b), false, false);
  u32 x = r[0], y = r[1]; a = __uint_as_float(x); b = __uint_as_float(y);
}
template <int I, bool QS>
DEVI void hh_step(HH& s, u32 lft, u32 phf, u32 sumf, bool lo, float ninf) {
  const f32x4 z = {0.f, 0.f, 0.f, 0.f};
  if constexpr (I == 0) {
    u32x2 x = lds_r64(lft); s.b0[2] = x[0]; s.b0[3] = x[1];
    asm volatile("" ::: "memory");
    u32x2 y = lds_r64(lft); s.b1[2] = y[0]; s.b1[3] = y[1];
    s.f0 = lds_r128(phf);
  } else if constexpr (I == 1) { s.f1 = lds_r128(phf + 1024); s.f2 = lds_r128(phf + 2048);
  } else if constexpr (I == 2) { s.f3 = lds_r128(phf + 3072);
  } else if constexpr (I == 3) { s.T0 = mfma32(s.f0, s.b0, z);
  } else if constexpr (I == 4) { s.TL = mfma32(s.f2, s.b0, z);
  } else if constexpr (I == 5) { s.T1 = mfma32(s.f1, s.b1, z);
  } else if constexpr (I == 6) { s.TL = mfma32(s.f3, s.b1, s.TL);
  } else if constexpr (I == 7) { s.f0 = lds_r128(sumf); s.f1 = lds_r128(sumf + 1024); s.f2 = lds_r128(sumf + 2048);
  } else if constexpr (I == 8) { s.a = max3(s.T0[0], s.T0[1], s.T0[2]);
  } else if constexpr (I == 9) { s.c = max3(-s.T0[0], -s.T0[1], -s.T0[2]);
  } else if constexpr (I == 10) { s.b = max3(s.T1[0], s.T1[1], s.T1[2]);
  } else if constexpr (I == 11) { s.d = max3(-s.T1[0], -s.T1[1], -s.T1[2]);
  } else if constexpr (I == 12) {
  } else if constexpr (I == 13) { s.a = max3(s.a, s.T0[3], s.TL[0]);
  } else if constexpr (I == 14) { s.b = max3(s.b, s.T1[3], s.TL[1]);
  } else if constexpr (I == 15) { s.c = max3(s.c, -s.T0[3], -s.TL[0]);
  } else if constexpr (I == 16) { s.d = max3(s.d, -s.T1[3], -s.TL[1]);
  } else if constexpr (I == 17) { swap32(s.a, s.b);
  } else if constexpr (I == 18) { swap32(s.c, s.d);
  } else if constexpr (I == 19) { s.a = max3(s.a, s.b, ninf);
  } else if constexpr (I == 20) { s.c = max3(s.c, s.d, ninf);
  } else if constexpr (I == 21) { swap16(s.a, s.c);
  } else if constexpr (I == 22) { s.a = max3(s.a, s.c, ninf);
  } else if constexpr (I == 23) { s.a *= -LOG2E;
  } else if constexpr (I == 24) { s.c = s.a; swap16(s.a, s.c);
  } else if constexpr (I == 25) { s.b = s.a; swap32(s.a, s.b);
  } else if constexpr (I == 26) { s.d = s.c; swap32(s.c, s.d);
  } else if constexpr (I == 27) { s.l0 = lo ? s.a : s.c;
  } else if constexpr (I == 28) { s.l1 = lo ? s.b : s.d;
  } else if constexpr (I == 29) { s.l0 = fmaf(s.TL[0], lo ? LOG2E : -LOG2E, s.l0);
  } else if constexpr (I == 30) { s.l1 = fmaf(s.TL[1], lo ? LOG2E : -LOG2E, s.l1);
  } else if constexpr (I == 31) { s.l0 = __builtin_amdgcn_exp2f(s.l0);
  } else if constexpr (I == 32) { s.l1 = __builtin_amdgcn_exp2f(s.l1);
  } else if constexpr (I == 33) { s.bl = pk(s.l0, s.l1);
  } else if constexpr (I == 34) { s.S = mfma32(s.f2, u32x4{s.bl, 0u, 0u, 0u}, z);
  } else if constexpr (I < 77) {
    constexpr int h = (I - 35) / 21, k = (I - 35) % 21;
    f32x4& T = h ? s.T1 : s.T0; f32x4& p = h ? s.p1 : s.p0; f32x4& q = h ? s.q1 : s.q0; u32x4& o = h ? s.b1 : s.b0;
    const float m = h ? s.b : s.a, n = h ? s.d : s.c;
    constexpr bool arg = k < 2 || ((k & 1) && k < 15);
    constexpr int e = arg ? (k < 2 ? k : (k + 1) / 2) : (k < 15 ? (k - 2) / 2 : 7);
    if constexpr (k < 16) {
      if constexpr (arg) { if constexpr (e < 4) p[e] = fmaf(T[e], LOG2E, m); else q[e - 4] = fmaf(T[e - 4], -LOG2E, n); }
      else { if constexpr (e < 4) p[e] = __builtin_amdgcn_exp2f(p[e]); else q[e - 4] = __builtin_amdgcn_exp2f(q[e - 4]); }
    } else if constexpr (k < 18) { o[k - 16] = pk(p[2 * (k - 16)], p[2 * (k - 16) + 1]);
    } else if constexpr (k < 20) { o[k - 16] = pk(q[2 * (k - 18)], q[2 * (k - 18) + 1]);
    } else { s.S = mfma32(h ? s.f1 : s.f0, o, s.S); }
  } else if constexpr (I == 77) {
  } else if constexpr (I == 78) { s.c = __builtin_amdgcn_rcpf(s.S[1]);
  } else if constexpr (I == 79) { s.d = __builtin_amdgcn_rcpf(s.S[3]);
  } else if constexpr (I == 80) { if constexpr (QS) s.c *= s.S[0]; else s.a = __builtin_amdgcn_rcpf(s.S[0]);
  } else if constexpr (I == 81) { if constexpr (QS) s.d *= s.S[2]; else s.b = __builtin_amdgcn_rcpf(s.S[2]);
  } else if constexpr (I < 106) {
    constexpr int v = (I - 82) / 6, k = (I - 82) % 6;
    if constexpr (!(QS && v >= 2)) {
      f32x4& x = v == 0 ? s.q0 : v == 1 ? s.q1 : v == 2 ? s.p0 : s.p1;
      u32x4& o = (v & 1) ? s.b1 : s.b0;
      const float r = v == 0 ? s.c : v == 1 ? s.d : v == 2 ? s.a : s.b;
      if constexpr (k < 4) x[k] *= r;
      else o[(v < 2 ? 2 : 0) + k - 4] = pk(x[2 * (k - 4)], x[2 * (k - 4) + 1]);
    }
  }
}
template <bool QS>
DEVI void hh_left(HH& s, bool lo) {
  if constexpr (QS) s.bl = pk(s.l0 * (lo ? 1.f : s.c), s.l1 * (lo ? 1.f : s.d));
  else s.bl = pk(s.l0 * (lo ? s.a : s.c), s.l1 * (lo ? s.b : s.d));
}

#define L2ST 40
#define L2ST2 72
#define PRE 7
#define PRO 2
#define LEAD 8
#define QA 2
#define QT 1
#define E_POB 30
#define Q_L1 10
#define Q_L1C 16
#define QTB QT
#define NOPT 0
#define PCW 1
#define PCW0 0
#define PP 34
#define QW 1
#define HQ0 4
#define HQS 8
#define C0 0
#define CS 2
#define C1 44
#define E_PUB 11
#define QB 1
#define QSP 1
#define HB 3
constexpr int EM = 36, EQ = 44, EH = 62, NEV = 74;
constexpr int Q_SLAB = 2;
#define E_PUBQ 9
#define E_PUBB 15
#define E_FLDB 17
constexpr int E_PUB2 = C1 < 34 ? E_PUBB : EQ + E_PUBQ;
static_assert(C1 >= 34 || ((E_PUBB & 3) == 3 && E_PUBB > C1 + 2 && E_PUBB < 35), "marks");
constexpr int e_fld(int br) { return br ? EQ + E_FLDB : E_FLD; }
constexpr int e_fchk(int br) { return br ? (E_FLDB > 12 ? EH + 4 : EQ + 16) : EQ; }
constexpr int clr_at(int E) { return E >= C1 && E < C1 + 3 ? 4 + E - C1 : E >= C0 && (E - C0) % CS == 0 && (E - C0) / CS < 4 ? (E - C0) / CS : -1; }
constexpr bool is_clr(int E) { return clr_at(E) >= 0; }
static_assert((E_PUB & 3) == 3 && E_PUB > C0 + 3 * CS && E_PUB < 35 && E_FLD >= EM && E_FLD < EQ && C1 > C0 + 3 * CS && (C1 < 34 || C1 >= EQ) && C1 < EQ + 6, "marks");
struct Sched {
  int op[NEV][3];
  int nop[NEV];
  int pos_ev[NEV + 1];
  int pos_hook[NEV];
  int last[36];
  int total;
};
constexpr int ext_ops(int br, int E) {
  return (E == (br ? E_POB : E_PO)) + (br && is_clr(E)) + (E == e_fld(br)) + (E == E_TCH ? (TCH & 1) + (TCH >> 1) : 0) +
         (br ? 0 : E == EQ + Q_SLAB ? 2 : E == EQ + Q_L1 ? 4 : E == EQ + Q_L1C ? 1 : 0);
}
constexpr int op_chunk(int op) { return op < 200 ? (op - 100) >> 2 : op < 236 ? ((op - 200) >> 1) % 9 : (op - 200) >> 1; }
constexpr int op_earliest(int br, int op) {
  const int n = op_chunk(op);
  if (n < 9) return 4 * n - 1 - LEAD;
  if (n < 27) return n < 21 ? 23 : n < 25 ? 27 : 31;
  const int m = n - 27;
  if (br == 0) return m < 6 ? EQ + 3 : EQ + 6;
  return m < 2 ? 31 : m == 2 ? EQ : EQ + 2 * (m - 3) + 3;
}
constexpr int op_deadline(int br, int op) {
  const int n = op_chunk(op);
  if (n < 9) return 4 * n - 1;
  if (n < 27) return br == 0 || n == 18 ? EQ : EQ + 2 * (n - 18) - 1;
  return NEV;
}
static_assert(LEAD <= 12, "ring");
constexpr Sched make_sched(int br) {
  Sched s{};
  const int nx = br ? 3 : 2;
  int list[128] = {}, when[128] = {}, n = 0;
  for (int c = 0; c < 9; ++c) {
    for (int i = 0; i < nx; ++i) list[n++] = 100 + 4 * c + i;
    list[n++] = 200 + 2 * c; list[n++] = 201 + 2 * c; list[n++] = 218 + 2 * c; list[n++] = 219 + 2 * c;
  }
  for (int c = 18; c < 36; ++c) { list[n++] = 200 + 2 * c; list[n++] = 201 + 2 * c; }
  for (int k = 0; k < n; ++k) when[k] = 1000;
  int pos = 0, done = 0;
  for (; done < PRO * (nx + 4); ++done) { s.last[op_chunk(list[done])] = pos++; when[done] = -1; if (done == nx + 3) pos += 1 + (PCW == 2); }
  pos += PCW == 3;
  for (int E = 0; E < NEV; ++E) {
    s.pos_ev[E] = pos;
    int j = 0;
    for (int k = 0; k < n; ++k) {
      if (when[k] != 1000 || op_earliest(br, list[k]) > E) continue;
      const int c = op_chunk(list[k]);
      int q = E < EM ? (c < 9 ? QA : br ? QTB : QT) : E < EQ && br ? 0 : ext_ops(br, E) ? 1 : 2;
      if (br == 0 && E >= EQ) q = ext_ops(br, E) || (E < EH && E % QSP) ? 0 : 1;
      if (br == 1 && E >= EQ && c >= 27) q = E >= EH + 4 ? 0 : E >= EH ? HB : ext_ops(br, E) ? QB - 1 : QB;
      if (j >= q) break;
      s.op[E][j++] = list[k]; when[k] = E; ++done;
      s.last[c] = pos++;
      if (E >= op_deadline(br, list[k])) s.total = -1000;
    }
    s.nop[E] = j;
    s.pos_hook[E] = pos;
    pos += ext_ops(br, E);
  }
  s.pos_ev[NEV] = pos;
  s.total += done == n ? pos : -1000;
  return s;
}
template <int BR> struct SC { static constexpr Sched s = make_sched(BR); };
static_assert(SC<0>::s.total > 0 && SC<1>::s.total > 0, "schedule incomplete");
constexpr int imin(int a, int b) { return a < b ? a : b; }
template <int BR> constexpr int wait_n(int E, int c) {
  int n = SC<BR>::s.pos_ev[E] - SC<BR>::s.last[c] - 1;
  if (BR == 1 && E == E_PUB) n = imin(n, SC<BR>::s.pos_ev[E] - SC<BR>::s.pos_ev[C0 + 3 * CS + 1]);
  if (BR == 1 && E == E_PUB2) n = imin(n, SC<BR>::s.pos_ev[E] - SC<BR>::s.pos_ev[C1 + 3]);
  if (BR == 0 && E == EQ) n = imin(n, SC<BR>::s.pos_ev[E] - SC<BR>::s.pos_ev[E_FLD + 1]);
  return n;
}
template <int BR> constexpr int wait_h(int E, int E0) { return SC<BR>::s.pos_hook[E] - SC<BR>::s.pos_ev[E0 + 1]; }

#define STAMP(i)
#define RSTAMP(i)
#define STAMP_FLUSH()
#define STAMP_DECL

DEVI u64 next_tag(u32x2 e) {
  const u64 E = ((u64)(u32)__builtin_amdgcn_readfirstlane(e[1]) << 32) | (u32)__builtin_amdgcn_readfirstlane(e[0]);
  return E * 6364136223846793005ull + 1442695040888963407ull;
}
DEVI void poll_flags(GLB_AS u64* f, u64 tag) {
  for (int spin = 0; spin < (1 << 20); ++spin) {
    u64 v = __hip_atomic_load(f, __ATOMIC_RELAXED, __HIP_MEMORY_SCOPE_AGENT);
    if (__all(v == tag)) break;
    __builtin_amdgcn_s_sleep(1);
  }
  asm volatile("" ::: "memory");
}

#define ROT (rb + 2 * hq) % 9
typedef u64 u64x4 __attribute__((ext_vector_type(4)));
DEVI void first(bool br, int rb, int hq, int jx, u32 lb, u64x4& ka, u64x4& kb, u64& kc_) {
  asm volatile("s_load_dwordx8 %0, %3, 0x0\n\ts_load_dwordx8 %1, %3, 0x20\n\ts_load_dwordx2 %2, %3, 0x40"
               : "=&s"(ka), "=&s"(kb), "=&s"(kc_)
               : "s"(__builtin_amdgcn_kernarg_segment_ptr()));
  const int tid = threadIdx.x, l = tid & 63, w = __builtin_amdgcn_readfirstlane(tid >> 6);
  u64 pc;
  int f0, t0, f1, t1;
  asm volatile("s_getpc_b64 %0\n.Lhk_pc:\n\ts_mov_b32 %1, .Lhk_from_0-.Lhk_pc\n\ts_max_i32 %2, .Lhk_to_0-.Lhk_pc, 0\n\t"
               "s_mov_b32 %3, .Lhk_from_1-.Lhk_pc\n\ts_max_i32 %4, .Lhk_to_1-.Lhk_pc, 0"
               : "=s"(pc), "=s"(f0), "=s"(t0), "=s"(f1), "=s"(t1)::"scc");
  __amdgpu_buffer_rsrc_t srdC = __builtin_amdgcn_make_buffer_rsrc((void*)pc, (short)0, (br ? t1 : t0) & ~3, 0x00020000);
  const u32 po = l == 0 ? (u32)((br ? f1 : f0) + (8 * jx + w) * 128) : OOB;
  asm volatile("s_mov_b32 m0, %0\n\ts_nop 0\n\tbuffer_load_dword %1, %2, 0 offen lds" ::"s"(lb + LSCR), "v"(po), "s"(srdC) : "memory");
  const int lr = (l * 57) >> 9, lu = l - lr * 9;
  int kc = ROT;
  u32 vx[2], vq[2], vh, vp = (u32)(hq * 2592 + (w * 21 + l) * 16), le = lb + (br ? LHALO : LPK) + w * (br ? 256 : 336);
  {
    const int row = 2 * w + ((l >> 3) & 1);
    int grow = rb * 128 - 16 + row; grow = grow < 0 ? 0 : grow;
    vh = (u32)(grow * XROW + (((l & 7) ^ ((row >> 1) & 7)) << 4) + kc * 128);
  }
#pragma unroll
  for (int i = 0; i < 2; ++i) {
    const int row = 16 * w + 8 * i + (l >> 3);
    vx[i] = (u32)((rb * 128 + row) * XROW + (((l & 7) ^ ((row >> 1) & 7)) << 4));
    vq[i] = (u32)((8 * w + 4 * (lr & 1) + 2 * i + ((lr >> 1) & 1)) * WROW + hq * 144 + lu * 16);
  }
  asm volatile("" : "+v"(vx[0]), "+v"(vx[1]), "+v"(vq[0]), "+v"(vq[1]), "+v"(vh), "+v"(vp), "+s"(le), "+s"(kc));
  asm volatile("s_waitcnt lgkmcnt(0)" : "+s"(ka), "+s"(kb), "+s"(kc_) : "s"(__builtin_amdgcn_kernarg_segment_ptr()));
  const u64 XA = ka[0], WA = ka[1], PHI = ka[2], XB = kb[0], WB = kb[1];
  __amdgpu_buffer_rsrc_t srdX = __builtin_amdgcn_make_buffer_rsrc((void*)(br ? XB : XA), (short)0, 2048 * XROW, 0x00020000);
  __amdgpu_buffer_rsrc_t srdW = __builtin_amdgcn_make_buffer_rsrc((void*)(br ? WB : WA), (short)0, 576 * WROW, 0x00020000);
  __amdgpu_buffer_rsrc_t srdE = __builtin_amdgcn_make_buffer_rsrc((void*)(br ? XB : PHI), (short)0, br ? 2048 * XROW : 32 * 648, 0x00020000);
  dma_mask(le, br ? vh : vp, srdE, 0u, br ? 0xffffull : (1ull << 21) - 1);
#pragma unroll
  for (int i = 0; i < 2; ++i) dma_full(lb + S0 + w * 2048 + i * 1024, vx[i], srdX, (u32)(kc * 128));
#pragma unroll
  for (int n = 0; n < 4; ++n)
    dma_mask(lb + S0 + (n < 2 ? SK : SV) + (2 * w + (n & 1)) * 576, vq[n & 1], srdW, (u32)(kc * 64 * WROW + (n < 2 ? 1152 : 2304)), (1ull << 36) - 1);
}

template <int BR>
DEVI void body(const char* __restrict__ X, const char* __restrict__ W, const char* __restrict__ PHI, const char* __restrict__ WO, char* ws, char* out,
               int rb, int hq, int jx, u32 lb, u64 tent, u64 rent) {
  asm volatile(".Lhk_from_%0:" ::"n"(BR));
  int tid = threadIdx.x;
  asm volatile("" : "+v"(tid));
  int l = tid & 63;
  const int w = __builtin_amdgcn_readfirstlane(tid >> 6);
  const int rg = w & 3, hpl = w >> 2;
  int c = l & 15, g = l >> 4, q = c >> 2, p = c & 3, g0 = g & 1, g1 = g >> 1;
  int lr = (l * 57) >> 9, lu = l - lr * 9;
  auto refresh = [&]() {
    asm volatile("" : "+v"(tid));
    l = tid & 63; c = l & 15; g = l >> 4; q = c >> 2; p = c & 3; g0 = g & 1; g1 = g >> 1;
    lr = (l * 57) >> 9; lu = l - lr * 9;
  };
  STAMP_DECL
  STAMP(0);
  RSTAMP(30);
  __amdgpu_buffer_rsrc_t srdX = __builtin_amdgcn_make_buffer_rsrc((void*)X, (short)0, 2048 * XROW, 0x00020000);
  __amdgpu_buffer_rsrc_t srdW = __builtin_amdgcn_make_buffer_rsrc((void*)W, (short)0, 576 * WROW, 0x00020000);
  const int rot = ROT;
  constexpr u64 M36 = (1ull << 36) - 1;

  u32 vx[2], vq[2], vh = 0;
#pragma unroll
  for (int i = 0; i < 2; ++i) {
    int row = 16 * w + 8 * i + (l >> 3);
    vx[i] = (u32)((rb * 128 + row) * XROW + (((l & 7) ^ ((row >> 1) & 7)) << 4));
  }
#pragma unroll
  for (int i = 0; i < 2; ++i) vq[i] = (u32)((8 * w + 4 * (lr & 1) + 2 * i + ((lr >> 1) & 1)) * WROW + hq * 144 + lu * 16);
  if (BR) {
    int row = 2 * w + ((l >> 3) & 1);
    int grow = rb * 128 - 16 + row; grow = grow < 0 ? 0 : grow;
    vh = (u32)(grow * XROW + (((l & 7) ^ ((row >> 1) & 7)) << 4));
  }
  auto kc_of = [&](int cc) { int k = cc + rot; return k >= 9 ? k - 9 : k; };
  __amdgpu_buffer_rsrc_t srdO = __builtin_amdgcn_make_buffer_rsrc((void*)WO, (short)0, 576 * XROW, 0x00020000);
  auto dma_op = [&](auto OP) {
    constexpr int op = OP.value;
    if constexpr (op < 200) {
      constexpr int cc = (op - 100) >> 2, i = op & 3;
      const int kc = kc_of(cc);
      if constexpr (i < 2) dma_full(lb + S0 + (cc & 3) * SS + w * 2048 + i * 1024, vx[i], srdX, (u32)(kc * 128));
      else dma_mask(lb + LHALO + (cc & 3) * 2048 + w * 256, vh, srdX, (u32)(kc * 128), 0xffffull);
    } else {
      constexpr int n = (op - 200) >> 1, i = op & 1;
      constexpr int at = n < 9 ? S0 + (n & 3) * SS + SK : n < 18 ? S0 + ((n - 9) & 3) * SS + SV : n < 27 ? WQ + (n - 18) * WU : wo_at(BR, n - 27);
      const u32 dst = lb + at + (2 * w + i) * 576;
      if constexpr (n < 27) {
        const int kc = kc_of(n % 9), sec = n < 9 ? 1152 : (n < 18 ? 2304 : 0);
        dma_mask(dst, vq[i], srdW, (u32)(kc * 64 * WROW + sec), M36);
      } else {
        const int hi = lr & 1, qq = 2 * i + ((lr >> 1) & 1);
        if constexpr (n > 27)
          dma_mask(dst, (u32)((36 * hpl + 20 * hi + 4 * rg + qq) * XROW + hq * 144 + lu * 16), srdO, (u32)(((n - 28 + hq) & 7) * 72 * XROW), M36);
        else dma_mask(dst, (u32)((36 * (8 * hpl + 2 * rg + hi) + 16 + qq) * XROW + hq * 144 + lu * 16), srdO, 0u, M36);
      }
    }
  };
  auto ev_dma = [&](auto EE, auto II) {
    if constexpr (II.value < SC<BR>::s.nop[EE.value]) dma_op(IC<SC<BR>::s.op[EE.value][II.value]>{});
  };
  auto warm_wo = [&]() {
    const int idx = w * 21 + l;
    const u32 po = l < 21 && idx < 162 ? (u32)((162 * jx + idx) * 128) : OOB;
    asm volatile("s_mov_b32 m0, %0\n\ts_nop 0\n\tbuffer_load_dword %1, %2, 0 offen lds" ::"s"(lb + LSCR), "v"(po), "s"(srdO) : "memory");
  };
  __amdgpu_buffer_rsrc_t srdF = __builtin_amdgcn_make_buffer_rsrc((void*)(ws + WS_OF), (short)0, (int)WS_FLAG, 0x00020000);
  __amdgpu_buffer_rsrc_t srdS = __builtin_amdgcn_make_buffer_rsrc((void*)(ws + WS_SLAB), (short)0, 136 * SLAB, 0x00020000);
  auto touch = [&]() {
    if constexpr (TCH & 1) {
      __amdgpu_buffer_rsrc_t srdT = __builtin_amdgcn_make_buffer_rsrc((void*)out, (short)0, 2048 * 2304, 0x00020000);
      const u32 po = l == 0 ? (u32)((rb * 128 + w * 16) * 2304 + BR * 1152 + hq * 144) : OOB;
      asm volatile("s_mov_b32 m0, %0\n\ts_nop 0\n\tbuffer_load_dword %1, %2, 0 offen sc1 lds" ::"s"(lb + LSCR), "v"(po), "s"(srdT) : "memory");
    }
    if constexpr (TCH & 2) {
      const u32 po = l == 0 ? (u32)((BR * 128 + rb * 8 + w) * 18 * 1024) : BR == 0 && l == 1 ? (u32)(WS_SLAB + (w == 7 ? 128 + hq : hq * 16 + 2 * w) * SLAB) : OOB;
      asm volatile("s_mov_b32 m0, %0\n\ts_nop 0\n\tbuffer_load_dword %1, %2, 0 offen sc1 lds" ::"s"(lb + LSCR), "v"(po), "s"(srdF) : "memory");
    }
  };
  auto mark = [&](auto EE) {
    constexpr int e = EE.value, f = (e + 2) % 3;
    const u32x4 sv = {SENT, SENT, SENT, SENT};
    const u32 ob = (u32)(((e > 3 ? 128 : 0) + rb * 8 + w) * 18 * 1024);
    if constexpr (e > 0 && f < 2) __builtin_amdgcn_raw_buffer_store_b128(sv, srdF, l * 16, ob + hq * 2048 + f * 1024, 16);
    if constexpr (e > 0 && f == 2) __builtin_amdgcn_raw_buffer_store_b128(sv, srdF, g == 0 ? c * 16 : OOB, ob + (16 + (hq >> 2)) * 1024 + (hq & 3) * 256, 16);
    if constexpr (e == 0) {
      const u32 so = tid < 400 ? (u32)((hq * 16 + rb) * SLAB + tid * 16) : tid < 425 ? (u32)((128 + hq) * SLAB + rb * 400 + (tid - 400) * 16) : OOB;
      __builtin_amdgcn_raw_buffer_store_b128(sv, srdS, so, 0, 16);
    }
  };
  GLB_AS u64* kflags = (GLB_AS u64*)(ws + WS_FLAG + hq * 256);
  GLB_AS u64* oflags = (GLB_AS u64*)(ws + WS_OFLAG + (BR * 16 + rb) * 256);
  u32x2 ep, fv;
  asm volatile("" ::"s"(srdX), "s"(srdW));
  STAMP(16);
  static_assert(PRO == 2, "prologue");
  auto dma_chunk = [&](auto cc) {
    constexpr int c_ = cc.value;
    dma_op(IC<100 + 4 * c_>{}); dma_op(IC<101 + 4 * c_>{});
    if constexpr (BR) dma_op(IC<102 + 4 * c_>{});
    dma_op(IC<200 + 2 * c_>{}); dma_op(IC<201 + 2 * c_>{}); dma_op(IC<218 + 2 * c_>{}); dma_op(IC<219 + 2 * c_>{});
  };
  {
    const char* ea = ws + WS_EPOCH;
    asm volatile("global_load_dwordx2 %0, %1, off sc1" : "=v"(ep) : "v"(ea) : "memory");
  }
  STAMP(17);
  if constexpr (BR == 0) {
    wait_vm_bar<7 + (PCW == 2)>();
    STAMP(18);
    const int h = rg & 1, lf = rg >> 1;
    const int f = lf ? 16 + (q & 1) : c;
    const bool rowok = lf ? (p == h) : true;
    const u32 pb = lb + LPK + (hpl * 2 + h) * 648 + f * 2;
    const int d0 = 2 * h + 4 * g;
    u32 e0 = lds_r16(pb + (d0 + 0) * 36), e1 = lds_r16(pb + (d0 + 1) * 36), e2 = lds_r16(pb + (d0 + 2) * 36), e3 = lds_r16(pb + (d0 + 3) * 36);
    const int dl = h ? 0 : 16;
    u32 l0 = lds_r16(pb + dl * 36), l1 = lds_r16(pb + (dl + 1) * 36);
    SCHED_FENCE();
    dma_op(IC<104>{}); dma_op(IC<105>{});
    SCHED_FENCE();
    u32 lw = (g == 0) ? (l0 | (l1 << 16)) : 0u;
    u32x4 fr = {e0 | (e1 << 16), e2 | (e3 << 16), h ? 0u : lw, h ? lw : 0u};
    if (!rowok) fr = u32x4{0, 0, 0, 0};
    lds_w128(lb + LPHF + (hpl * 4 + rg) * 1024 + l * 16, fr);
    if (w < 3) {
      u32 a = (p == 2 * w) ? ONES : 0u, b = (p == 2 * w + 1) ? ONES : 0u;
      u32 x = ((p & 1) == g1) ? ((p >> 1) ? 0x3f800000u : 0x00003f80u) : 0u;
      lds_w128(lb + LSUM + w * 1024 + l * 16, w < 2 ? u32x4{a, a, b, b} : u32x4{x, 0u, 0u, 0u});
    }
    SCHED_FENCE();
    dma_op(IC<202>{}); dma_op(IC<203>{}); dma_op(IC<220>{}); dma_op(IC<221>{});
  } else {
    bar_only();
    STAMP(18);
    dma_chunk(IC<1>{});
  }
  STAMP(1);

  const u32 xa0 = lb + S0 + (rg * 32 + hpl * 16 + c) * 128 + ((g ^ ((c >> 1) & 7)) << 4);
  const u32 xa1 = xa0 ^ 64;
  const u32 xb0 = xa0 + 2048 - hpl * 4096, xb1 = xb0 ^ 64;
  const u32 xh0 = lb + LHALO + c * 128 + ((g ^ ((c >> 1) & 7)) << 4);
  const u32 xh1 = xh0 ^ 64;
  const u32 wrow = lb + (16 * g1 + 8 * g0 + 2 * q) * 144;
  const u32 wa = wrow + hpl * 72 + 8 * p;
  const u32 wl = wrow + 32 + (p & 1) * 72 + (p >> 1) * (SV - SK);
  const u32 wh = wa + (rg & 1) * 40 + (rg >> 1) * (SV - SK);
  float ninf = -__builtin_inff();
  asm volatile("" : "+s"(ninf));

  u32x4 xf[18][2];
  const f32x4 z4 = {0.f, 0.f, 0.f, 0.f};
  f32x4 k0[2] = {z4, z4}, k1[2] = {z4, z4}, a0[2] = {z4, z4}, a1[2] = {z4, z4}, aL = z4, aH = z4, aHL = z4;
  u32x4 f0, f1, fl, fh, xh;
  auto load_kv_frags = [&](auto HH_, auto JJ) {
    constexpr int H = HH_.value, j = JJ.value, T = H >> 1, n = T >> 1, ks = T & 1;
    constexpr u32 ok = S0 + (n & 3) * SS + SK + ks * 32 * 144, ov = ok + SV - SK;
    if constexpr (H < 36) {
      if constexpr ((H & 1) == 0) {
        if constexpr (j == 0) { xf[T][0] = lds_r128((ks ? xa1 : xa0) + (n & 3) * SS); f0 = frag_tr(wa + ok, wa + ok + 144); }
        if constexpr (j == 1) { xf[T][1] = lds_r128((ks ? xb1 : xb0) + (n & 3) * SS); f1 = frag_tr(wa + ok + 40, wa + ok + 184); }
      } else {
        if constexpr (j == 0) f0 = frag_tr(wa + ov, wa + ov + 144);
        if constexpr (j == 1) f1 = frag_tr(wa + ov + 40, wa + ov + 184);
        if constexpr (j == 2) fl = frag_tr(wl + ok, wl + ok + 144);
        if constexpr (j == 3 && BR == 1) { fh = frag_tr(wh + ok, wh + ok + 144); xh = lds_r128((ks ? xh1 : xh0) + (n & 3) * 2048); }
      }
    }
  };
  wait_bar<wait_n<BR>(0, 0)>();
  STAMP(2);
  load_kv_frags(IC<0>{}, IC<0>{}); load_kv_frags(IC<0>{}, IC<1>{});
  u64 tag = 0;
  static_for<0, 36>([&](auto hh_) {
    constexpr int H = hh_.value, T = H >> 1;
    if constexpr ((H & 3) == 3 && H < 35) {
      wait_bar<wait_n<BR>(H, (H + 1) >> 2)>();
    }
    u32x4 c0 = f0, c1 = f1, cl = fl, ch = fh, cx = xh;
    f32x4(&d0)[2] = (H & 1) ? a0 : k0;
    f32x4(&d1)[2] = (H & 1) ? a1 : k1;
    SCHED_FENCE();
    d0[0] = mfma32(c0, xf[T][0], d0[0]);
    load_kv_frags(IC<H + 1>{}, IC<0>{});
    SCHED_FENCE();
    d0[1] = mfma32(c0, xf[T][1], d0[1]);
    load_kv_frags(IC<H + 1>{}, IC<1>{});
    ev_dma(hh_, IC<0>{});
    SCHED_FENCE();
    d1[0] = mfma32(c1, xf[T][0], d1[0]);
    load_kv_frags(IC<H + 1>{}, IC<2>{});
    SCHED_FENCE();
    d1[1] = mfma32(c1, xf[T][1], d1[1]);
    load_kv_frags(IC<H + 1>{}, IC<3>{});
    ev_dma(hh_, IC<1>{});
    ev_dma(hh_, IC<2>{});
    SCHED_FENCE();
    if constexpr (H & 1) {
      aL = mfma32(cl, xf[T][0], aL);
      asm volatile("" : "+v"(aL));
      if constexpr (BR == 1) {
        aH = mfma32(ch, cx, aH); aHL = mfma32(cl, cx, aHL);
        asm volatile("" : "+v"(aH), "+v"(aHL));
      }
    }
    if constexpr (H == (BR ? E_POB : E_PO)) warm_wo();
    if constexpr (H == E_TCH) touch();
    if constexpr (H == 3) {
      asm volatile("" : "+v"(ep));
      tag = next_tag(ep);
    }
    if constexpr (BR == 1 && is_clr(H)) mark(IC<clr_at(H)>{});
    if constexpr (BR == 1 && H == E_PUB) {
      if (tid < 2) __hip_atomic_store(tid == 0 ? kflags + rb : oflags + hq - 512, tag, __ATOMIC_RELAXED, __HIP_MEMORY_SCOPE_AGENT);
    }
    if constexpr (BR == 1 && H == E_PUB2) {
      if (tid == 64) __hip_atomic_store(oflags + hq, tag, __ATOMIC_RELAXED, __HIP_MEMORY_SCOPE_AGENT);
    }
    SCHED_FENCE();
  });
  STAMP(3);
  refresh();
  const u32 wqa = lb + WQ + (16 * g1 + 8 * g0 + 2 * q) * 144 + hpl * 72 + 8 * p;
  const u32 wql = lb + WQ + (16 * g1 + 8 * g0 + 2 * q) * 144 + 32 + (p & 1) * 72;
  auto load_q_frags = [&](auto TT, auto JJ) {
    constexpr int t = TT.value, j = JJ.value;
    constexpr u32 o = (t >> 1) * WU + (t & 1) * 32 * 144;
    if constexpr (t < 18) {
      if constexpr (j == 0) f0 = frag_tr(wqa + o, wqa + o + 144);
      if constexpr (j == 1) f1 = frag_tr(wqa + o + 40, wqa + o + 184);
      if constexpr (j == 2) fl = frag_tr(wql + o, wql + o + 144);
    }
  };
  if constexpr (BR == 1) {
    wait_bar<wait_n<BR>(EQ, 18)>();
    static_for<0, 3>([&](auto j) { load_q_frags(IC<0>{}, j); });
  }
  u32x2 kM[2][2];
#pragma unroll
  for (int sb = 0; sb < 2; ++sb) { kM[0][sb] = pk4(k0[sb]); kM[1][sb] = pk4(k1[sb]); }
  auto unswap = [&](u32x2& a, u32x2& b) {
    u32x2 x = a, y = b;
#pragma unroll
    for (int e = 0; e < 2; ++e) { a[e] = hpl ? y[e] : x[e]; b[e] = hpl ? x[e] : y[e]; }
  };
  {
    constexpr int VROW0 = BR ? 16 : 0;
    auto left = [&](f32x4 v, int blk) {
      u32x2 x = pk4(v);
      const u32 va = lb + lvi(BR) + (VROW0 - 16 + blk * 16 + c) * VIS + g0 * 80 + 64;
      lds_w64(g1 ? va : lb + KLX + (blk * 2 + g0) * 128 + c * 8, u32x2{x[0], g1 ? ONES : x[1]});
      lds_w64(g1 ? va + 8 : lb + LSCR + 256 + c * 8, u32x2{x[1], ONES});
    };
    left(aL, 1 + 2 * rg + hpl);
#pragma unroll
    for (int sb = 0; sb < 2; ++sb) {
      const u32 va = lb + lvi(BR) + (VROW0 + rg * 32 + (sb ^ hpl) * 16 + c) * VIS + hpl * 80;
      lds_w64(va + 8 * g, pk4(a0[sb]));
      lds_w64(va + 32 + 8 * g, pk4(a1[sb]));
    }
    if constexpr (BR == 1) {
      unswap(kM[0][0], kM[0][1]); unswap(kM[1][0], kM[1][1]);
      if (w == 2) left(aHL, 0);
      const u32 kx = lb + LKX + l * 8;
      lds_w64(rg < 2 ? kx + (hpl * 2 + rg) * 512 : lb + lvi(BR) + c * VIS + hpl * 80 + (rg & 1) * 32 + 8 * g, pk4(aH));
      if (rg < 3) {
        const u32 b = kx + (((rg + 1) * 2 + hpl) * 2) * 512;
        lds_w64(b, kM[0][1]); lds_w64(b + 512, kM[1][1]);
      }
    }
  }
  const u32 phf = lb + LPHF + hpl * 4096 + l * 16, sumf = lb + LSUM + l * 16;
  const bool lo = g < 2;
  auto load_flags = [&]() {
    GLB_AS u64* fa = (BR == 0 && g == 0) ? kflags + c : oflags + (l & 7);
    asm volatile("global_load_dwordx2 %0, %1, off sc1" : "=v"(fv) : "v"(fa) : "memory");
  };
  auto check_flags = [&]() {
    if (!__all((((u64)fv[1] << 32) | fv[0]) == tag)) poll_flags((BR == 0 && g == 0) ? kflags + c : oflags + (l & 7), tag);
  };
  if constexpr (BR == 0) {
    bar_lds();
    STAMP(4);
    HH h0, h1;
    h0.b0[0] = kM[0][0][0]; h0.b0[1] = kM[0][0][1]; h0.b1[0] = kM[1][0][0]; h0.b1[1] = kM[1][0][1];
    h1.b0[0] = kM[0][1][0]; h1.b0[1] = kM[0][1][1]; h1.b1[0] = kM[1][1][0]; h1.b1[1] = kM[1][1][1];
    const u32 kl0 = lb + KLX + ((1 + 2 * rg + hpl) * 2 + hpl) * 128 + c * 8, kl1 = lb + KLX + ((2 + 2 * rg - hpl) * 2 + hpl) * 128 + c * 8;
    static_for<0, HH_N>([&](auto st) {
      if constexpr (st.value >= 8 && (st.value - 8) % 12 == 0 && (st.value - 8) / 12 < EQ - EM) {
        constexpr int E = EM + (st.value - 8) / 12;
        SCHED_FENCE();
        ev_dma(IC<E>{}, IC<0>{}); ev_dma(IC<E>{}, IC<1>{});
        if constexpr (E == E_PO) warm_wo();
        if constexpr (E == E_FLD) load_flags();
        SCHED_FENCE();
      }
      hh_step<st.value, false>(h0, kl0, phf, sumf, lo, ninf);
      hh_step<st.value, false>(h1, kl1, phf, sumf, lo, ninf);
    });
#pragma unroll
    for (int sb = 0; sb < 2; ++sb) {
      HH& hs = sb ? h1 : h0;
      hh_left<false>(hs, lo);
      const u32 pa = lb + LPK + (rg * 32 + (sb ^ hpl) * 16 + c) * PKS + hpl * 144;
      lds_w64(pa + 8 * g, u32x2{hs.b0[0], hs.b0[1]});
      lds_w64(pa + 32 + 8 * g, u32x2{hs.b0[2], hs.b0[3]});
      lds_w64(pa + 64 + 8 * g, u32x2{hs.b1[0], hs.b1[1]});
      lds_w64(pa + 96 + 8 * g, u32x2{hs.b1[2], hs.b1[3]});
      lds_w32(pa + 128 + 4 * g, hs.bl);
    }
    STAMP(5);
    wait_bar<wait_n<BR>(EQ, 26)>();
    asm volatile("" : "+v"(fv));
    static_for<0, 3>([&](auto j) { load_q_frags(IC<0>{}, j); });
  }
  refresh();
  f32x4 dp = z4, dm = z4, df = z4;
  u32x4 ap[4], am[4], af[4], bv[4];
  u32 g0_, g1_, g2_, g3_;
  const int rr = 4 * (4 * g0 + q) + 2 * g1;
  const u32 pa = lb + LPK + rr * PKS + hpl * 144 + (rg & 1) * 64 + 8 * p;
  const u32 pf = lb + LPK + rr * PKS + hpl * 144 + 128 + 8 * (p >> 1);
  const u32 va = lb + lvi(BR) + rr * VIS + hpl * 80 + (rg < 2 ? rg * 32 + 8 * p : 64 + 8 * (p & 1));
  auto hook = [&](auto tt, auto jj) {
    constexpr int t = tt.value, E = EQ + t, j = jj.value;
    if constexpr (BR == 0 && j < 4) {
      if constexpr (t == Q_SLAB - 2) {
        ap[j] = frag_tr(pa + j * 32 * PKS, pa + j * 32 * PKS + PKS);
        am[j] = frag_tr(pa + 32 + j * 32 * PKS, pa + 32 + j * 32 * PKS + PKS);
        af[j] = frag_tr(pf + j * 32 * PKS, pf + j * 32 * PKS + PKS);
        bv[j] = frag_tr(va + j * 32 * VIS, va + j * 32 * VIS + VIS);
      }
      if constexpr (t == Q_SLAB - 1) {
        dp = mfma32(ap[j], bv[j], dp); dm = mfma32(am[j], bv[j], dm); df = mfma32(af[j], bv[j], df);
        asm volatile("" : "+v"(dp), "+v"(dm), "+v"(df));
      }
    }
    if constexpr (j != 4) return;
    if constexpr (BR == 1 && is_clr(E)) mark(IC<clr_at(E)>{});
    if constexpr (BR == 1 && E == E_PUB2) {
      if (tid == 0) __hip_atomic_store(oflags + hq, tag, __ATOMIC_RELAXED, __HIP_MEMORY_SCOPE_AGENT);
    }
    if constexpr (BR == 1 && E == e_fld(BR)) load_flags();
    if constexpr (E == e_fchk(BR)) {
      if constexpr (BR == 1) asm volatile("s_waitcnt vmcnt(%1)" : "+v"(fv) : "n"(wait_h<BR>(E, e_fld(BR))) : "memory");
      check_flags();
    }
    if constexpr (BR == 0) {
      if constexpr (t == Q_SLAB) {
        const u32 slab = (u32)((hq * 16 + rb) * SLAB);
        const bool h0set = (0x47 >> (c & 7)) & 1, h = rg & 1, own = (c >> 3) == hpl;
        u32x2 a = pk4(dp), b = pk4(dm);
        const u32 o1 = rg < 2 ? slab + (hpl * 2 + rg) * 1024 + l * 16 : (own && h0set != h) ? slab + 4096 + l * 16 : OOB;
        __builtin_amdgcn_raw_buffer_store_b128(u32x4{ns(a[0]), ns(a[1]), ns(b[0]), ns(b[1])}, srdS, o1, 0, 16);
        const float x0 = g0 ? df[2] : df[0], x1 = g0 ? df[3] : df[1];
        const bool first = rg < 2 ? !h : h0set;
        const u32 o2 = rg < 2 ? slab + 5120 + (hpl * 2 + rg) * 256 + l * 4 : own ? slab + 6144 + l * 4 : OOB;
        __builtin_amdgcn_raw_buffer_store_b32(ns(pk(first ? x0 : 0.f, first ? 0.f : x1)), srdS, o2, 0, 16);
        STAMP(6);
      }
      if constexpr (t == Q_L1 || t == Q_L1C) {
        const int dw = w * 16 + c;
        const u32 dof = (u32)((rb * 100 + (dw < 100 ? dw : 99)) * 4);
        const u32 go = (u32)((hq * 16 + 4 * g) * SLAB) + dof;
        if constexpr (t == Q_L1) {
          asm volatile("buffer_load_dword %0, %4, %5, 0 offen sc1\n\tbuffer_load_dword %1, %4, %5, %6 offen sc1\n\t"
                       "buffer_load_dword %2, %4, %5, %7 offen sc1\n\tbuffer_load_dword %3, %4, %5, %8 offen sc1"
                       : "=&v"(g0_), "=&v"(g1_), "=&v"(g2_), "=&v"(g3_)
                       : "v"(go), "s"(srdS), "s"(SLAB), "s"(2 * SLAB), "s"(3 * SLAB)
                       : "memory");
        } else {
          asm volatile("s_waitcnt vmcnt(%4)" : "+v"(g0_), "+v"(g1_), "+v"(g2_), "+v"(g3_) : "n"(wait_h<BR>(E, EQ + Q_L1)) : "memory");
          _Pragma("nounroll") for (int spin = 0; spin < (1 << 20); ++spin) {
            asm volatile("" ::: "memory");
            if (!__any(umax(umax3(g0_, g1_, g2_), g3_) == SENT)) break;
            g0_ = __builtin_amdgcn_raw_buffer_load_b32(srdS, go, 0, 16);
            g1_ = __builtin_amdgcn_raw_buffer_load_b32(srdS, go, SLAB, 16);
            g2_ = __builtin_amdgcn_raw_buffer_load_b32(srdS, go, 2 * SLAB, 16);
            g3_ = __builtin_amdgcn_raw_buffer_load_b32(srdS, go, 3 * SLAB, 16);
          }
          const u32 sel = c == 0 ? 0x00003f80u : c == 1 ? 0x3f800000u : 0u;
          f32x4 sum = mfma32(u32x4{sel, sel, sel, sel}, u32x4{g0_, g1_, g2_, g3_}, z4);
          __builtin_amdgcn_raw_buffer_store_b32(ns(pk(sum[0], sum[1])), srdS, g == 0 ? (u32)((128 + hq) * SLAB) + dof : OOB, 0, 16);
          STAMP(7);
        }
      }
    }
  };
  a0[0] = z4; a0[1] = z4; a1[0] = z4; a1[1] = z4; aL = z4;
  static_for<0, 18>([&](auto tt) {
    constexpr int t = tt.value, E = EQ + t;
    if constexpr (BR == 0) {
      if constexpr (t == 3 || t == 6) bar_only();
    } else if constexpr ((t & 1) && t < 17) {
      if constexpr (t == 1) wait_bar<wait_n<BR>(E, 18 + (t >> 1) + 1)>();
      else wait_vm_bar<wait_n<BR>(E, 18 + (t >> 1) + 1)>();
    }
    u32x4 c0 = f0, c1 = f1, cl = fl;
    SCHED_FENCE();
    a0[0] = mfma32(c0, xf[t][0], a0[0]);
    load_q_frags(IC<t + 1>{}, IC<0>{});
    hook(tt, IC<0>{});
    SCHED_FENCE();
    a0[1] = mfma32(c0, xf[t][1], a0[1]);
    load_q_frags(IC<t + 1>{}, IC<1>{});
    hook(tt, IC<1>{});
    ev_dma(IC<E>{}, IC<0>{});
    SCHED_FENCE();
    a1[0] = mfma32(c1, xf[t][0], a1[0]);
    load_q_frags(IC<t + 1>{}, IC<2>{});
    hook(tt, IC<2>{});
    SCHED_FENCE();
    a1[1] = mfma32(c1, xf[t][1], a1[1]);
    hook(tt, IC<3>{});
    ev_dma(IC<E>{}, IC<1>{});
    SCHED_FENCE();
    aL = mfma32(cl, xf[t][0], aL);
    asm volatile("" : "+v"(aL));
    hook(tt, IC<4>{});
    SCHED_FENCE();
  });
  STAMP(8);
  refresh();
  if (g < 2) lds_w64(lb + QLX + ((2 * rg + hpl) * 2 + g) * 128 + c * 8, pk4(aL));
  bar_lds();
  const u32 ofo = (u32)((BR * 128 + rb * 8 + rg * 2) * 18 * 1024);
  const int hp = hq * 2 + hpl;
  auto store_o = [&](int sb, f32x4 o0, f32x4 o1, float l0, float l1, float r0, float r1) {
    u32x2 a = pk4(o0 * r0), b = pk4(o1 * r1);
    __builtin_amdgcn_raw_buffer_store_b128(u32x4{ns(a[0]), ns(a[1]), ns(b[0]), ns(b[1])}, srdF, ofo + (sb * 18 + hp) * 1024 + l * 16, 0, 16);
    lds_w128(lb + LOWN + ((rg * 2 + sb) * 2 + hpl) * 1024 + l * 16, u32x4{a[0], a[1], b[0], b[1]});
    float rr = g0 ? r1 : r0;
    u32 lv = ns(pk(l0 * rr, l1 * rr));
    auto sw = __builtin_amdgcn_permlane16_swap(lv, lv, false, false);
    u32 s0 = sw[0], s1 = sw[1];
    __builtin_amdgcn_raw_buffer_store_b64(u32x2{s0, s1}, srdF, g == 0 ? ofo + (sb * 18 + 16 + (hq >> 2)) * 1024 + (c + 16 * (hq & 3)) * 16 + 8 * hpl : OOB, 0, 16);
  };

  if (BR == 0) {
    HH h0, h1;
    {
      u32x2 qa0 = pk4(a0[0]), qb0 = pk4(a1[0]), qa1 = pk4(a0[1]), qb1 = pk4(a1[1]);
      h0.b0[0] = qa0[0]; h0.b0[1] = qa0[1]; h0.b1[0] = qb0[0]; h0.b1[1] = qb0[1];
      h1.b0[0] = qa1[0]; h1.b0[1] = qa1[1]; h1.b1[0] = qb1[0]; h1.b1[1] = qb1[1];
    }
    const u32 ql0 = lb + QLX + ((2 * rg + hpl) * 2 + hpl) * 128 + c * 8, ql1 = lb + QLX + ((2 * rg + 1 - hpl) * 2 + hpl) * 128 + c * 8;
    const u32 kf = (u32)((128 + hq) * SLAB);
    const int l8 = g * 16 + (c & 7) + 8 * hpl;
    u32x4 AM0, AM1, AMLm;
    u32 AL0, AL1, ALL;
    auto load_kv = [&]() {
      AM0 = __builtin_amdgcn_raw_buffer_load_b128(srdS, kf + hpl * 2048 + l * 16, 0, 16);
      AM1 = __builtin_amdgcn_raw_buffer_load_b128(srdS, kf + hpl * 2048 + 1024 + l * 16, 0, 16);
      AMLm = __builtin_amdgcn_raw_buffer_load_b128(srdS, kf + 4096 + l8 * 16, 0, 16);
      AL0 = __builtin_amdgcn_raw_buffer_load_b32(srdS, kf + 5120 + hpl * 512 + l * 4, 0, 16);
      AL1 = __builtin_amdgcn_raw_buffer_load_b32(srdS, kf + 5120 + hpl * 512 + 256 + l * 4, 0, 16);
      ALL = __builtin_amdgcn_raw_buffer_load_b32(srdS, kf + 6144 + l8 * 4, 0, 16);
    };
    static_for<0, HH_N>([&](auto st) {
      if (st.value == L2ST) { SCHED_FENCE(); load_kv(); SCHED_FENCE(); }
      if (st.value == L2ST2) {
        SCHED_FENCE();
        if (__any(umax4(umax4(umax4(umax3(AL0, AL1, ALL), AM0), AM1), AMLm) == SENT)) load_kv();
        SCHED_FENCE();
      }
      if constexpr (st.value >= HQ0 && (st.value - HQ0) % HQS == 0 && (st.value - HQ0) / HQS < NEV - EH) {
        constexpr int E = EH + (st.value - HQ0) / HQS;
        if constexpr (SC<BR>::s.nop[E] > 0) { SCHED_FENCE(); ev_dma(IC<E>{}, IC<0>{}); SCHED_FENCE(); }
      }
      hh_step<st.value, true>(h0, ql0, phf, sumf, lo, ninf);
      hh_step<st.value, true>(h1, ql1, phf, sumf, lo, ninf);
    });
    hh_left<true>(h0, lo);
    hh_left<true>(h1, lo);
    asm volatile("" : "+v"(h0.b0), "+v"(h0.b1), "+v"(h0.bl), "+v"(h1.b0), "+v"(h1.b1), "+v"(h1.bl));
    STAMP(9);
    _Pragma("nounroll") for (int spin = 0; spin < (1 << 20); ++spin) {
      asm volatile("" ::: "memory");
      if (!__any(umax4(umax4(umax4(umax3(AL0, AL1, ALL), AM0), AM1), AMLm) == SENT)) break;
      load_kv();
    }
    STAMP(10);
    const bool h0set = (0x47 >> (c & 7)) & 1;
    u32x4 AML0, AML1;
#pragma unroll
    for (int e = 0; e < 4; ++e) { AML0[e] = h0set ? AMLm[e] : 0u; AML1[e] = h0set ? 0u : AMLm[e]; }
    f32x4 O0[2], O1[2], OL[2];
#pragma unroll
    for (int sb = 0; sb < 2; ++sb) OL[sb] = mfma32(AML0, (sb ? h1 : h0).b0, z4);
#pragma unroll
    for (int sb = 0; sb < 2; ++sb) O0[sb] = mfma32(AM0, (sb ? h1 : h0).b0, z4);
#pragma unroll
    for (int sb = 0; sb < 2; ++sb) OL[sb] = mfma32(AML1, (sb ? h1 : h0).b1, OL[sb]);
#pragma unroll
    for (int sb = 0; sb < 2; ++sb) O1[sb] = mfma32(AM1, (sb ? h1 : h0).b1, z4);
#pragma unroll
    for (int sb = 0; sb < 2; ++sb) OL[sb] = mfma32(u32x4{ALL, 0u, 0u, 0u}, u32x4{(sb ? h1 : h0).bl, 0u, 0u, 0u}, OL[sb]);
#pragma unroll
    for (int sb = 0; sb < 2; ++sb) O0[sb] = mfma32(u32x4{AL0, 0u, 0u, 0u}, u32x4{(sb ? h1 : h0).bl, 0u, 0u, 0u}, O0[sb]);
#pragma unroll
    for (int sb = 0; sb < 2; ++sb) O1[sb] = mfma32(u32x4{AL1, 0u, 0u, 0u}, u32x4{(sb ? h1 : h0).bl, 0u, 0u, 0u}, O1[sb]);
#pragma unroll
    for (int sb = 0; sb < 2; ++sb) {
      const HH& h = sb ? h1 : h0;
      float r0 = __builtin_amdgcn_rcpf(fmaf(h.S[0], 1e-6f, OL[sb][2])), r1 = __builtin_amdgcn_rcpf(fmaf(h.S[2], 1e-6f, OL[sb][3]));
      store_o(sb ^ hpl, O0[sb], O1[sb], OL[sb][0], OL[sb][1], r0, r1);
    }
  } else {
    auto ev3 = [&](auto EE) {
      SCHED_FENCE();
      ev_dma(EE, IC<0>{}); ev_dma(EE, IC<1>{}); ev_dma(EE, IC<2>{});
      if constexpr (EE.value == e_fld(BR)) load_flags();
      SCHED_FENCE();
    };
    const u32 kx = lb + LKX + ((rg * 2 + hpl) * 2) * 512 + l * 8;
    u32x2 pM0 = lds_r64(kx), pM1 = lds_r64(kx + 512);
    u32x2 kL[3];
#pragma unroll
    for (int i = 0; i < 3; ++i) kL[i] = lds_r64(lb + KLX + ((2 * rg + i) * 2 + hpl) * 128 + c * 8);
    ev3(IC<EH>{});
    const int par = c & 1;
    const bool ina = (4 * g + par) > c, inb = (4 * g + par + 2) > c;
    const float sc = 0.23570226039551584f * LOG2E;
    float la[2][2], lbv[2][2], mx[2][2];
    u32x2 qM[2][2] = {{pk4(a0[0]), pk4(a0[1])}, {pk4(a1[0]), pk4(a1[1])}};
    unswap(qM[0][0], qM[0][1]); unswap(qM[1][0], qM[1][1]);
#pragma unroll
    for (int sb = 0; sb < 2; ++sb) {
      u32x2 qlv = lds_r64(lb + QLX + ((2 * rg + sb) * 2 + hpl) * 128 + c * 8);
      u32x2 q0 = qM[0][sb], q1 = qM[1][sb];
      u32x4 b0 = {q0[0], q0[1], g == 0 ? qlv[0] : 0u, 0u};
      u32x4 b1 = {q1[0], q1[1], 0u, g == 0 ? qlv[1] : 0u};
      u32x2 kp0 = sb ? kM[0][0] : pM0, kp1 = sb ? kM[1][0] : pM1, kpl = kL[sb], kcl = kL[sb + 1];
      f32x4 sp0 = mfma32(u32x4{kp0[0], kp0[1], kpl[0], kpl[1]}, b0, z4);
      f32x4 sp1 = mfma32(u32x4{kp1[0], kp1[1], kpl[0], kpl[1]}, b1, z4);
      f32x4 sc0 = mfma32(u32x4{kM[0][sb][0], kM[0][sb][1], kcl[0], kcl[1]}, b0, z4);
      f32x4 sc1 = mfma32(u32x4{kM[1][sb][0], kM[1][sb][1], kcl[0], kcl[1]}, b1, z4);
      if (sb) ev3(IC<EH + 2>{}); else ev3(IC<EH + 1>{});
      const bool dead = (sb == 0) && (rb == 0) && (rg == 0);
      float pa0 = par ? sp0[1] : sp0[0], pb0 = par ? sp0[3] : sp0[2], pa1 = par ? sp1[1] : sp1[0], pb1 = par ? sp1[3] : sp1[2];
      if (dead) { pa0 = pb0 = pa1 = pb1 = -1e30f; }
      la[0][sb] = (ina ? pa0 : (par ? sc0[1] : sc0[0])) * sc;
      lbv[0][sb] = (inb ? pb0 : (par ? sc0[3] : sc0[2])) * sc;
      la[1][sb] = (ina ? pa1 : (par ? sc1[1] : sc1[0])) * sc;
      lbv[1][sb] = (inb ? pb1 : (par ? sc1[3] : sc1[2])) * sc;
      mx[0][sb] = fmaxf(la[0][sb], lbv[0][sb]);
      mx[1][sb] = fmaxf(la[1][sb], lbv[1][sb]);
    }
    float tm = redmax4(mx[0][0], mx[0][1], mx[1][0], mx[1][1], ninf);
    bcast4(tm, mx[0][0], mx[0][1], mx[1][0], mx[1][1]);
    ev3(IC<EH + 3>{});
    const u32 vb = lb + lvi(BR) + (rg * 32 + 4 * g + q) * VIS + hpl * 80;
#pragma unroll
    for (int sb = 0; sb < 2; ++sb) {
      const u32 v0 = vb + sb * 16 * VIS, v1 = v0 + 16 * VIS;
      u32x4 A0 = frag_tr(v0 + 8 * p, v1 + 8 * p), A1 = frag_tr(v0 + 32 + 8 * p, v1 + 32 + 8 * p);
      u32x4 AL = frag_tr(v0 + 64 + 8 * (p & 1), v1 + 64 + 8 * (p & 1));
      u32x4 P[2];
#pragma unroll
      for (int h = 0; h < 2; ++h) {
        float ea = __builtin_amdgcn_exp2f(la[h][sb] - mx[h][sb]), eb = __builtin_amdgcn_exp2f(lbv[h][sb] - mx[h][sb]);
        u32 wa_ = par ? pk(0.f, ea) : pk(ea, 0.f), wb_ = par ? pk(0.f, eb) : pk(eb, 0.f);
        P[h] = u32x4{ina ? wa_ : 0u, inb ? wb_ : 0u, ina ? 0u : wa_, inb ? 0u : wb_};
      }
      f32x4 O0 = mfma32(A0, P[0], z4), O1 = mfma32(A1, P[1], z4);
      f32x4 L0 = mfma32(AL, P[0], z4), L1 = mfma32(AL, P[1], z4);
      float r0 = __builtin_amdgcn_rcpf(L0[2]), r1 = __builtin_amdgcn_rcpf(L1[2]);
      if (e_fchk(BR) == EH + 4 && sb == 0) {
        asm volatile("s_waitcnt vmcnt(%1)" : "+v"(fv) : "n"(wait_h<BR>(EH + 4, e_fld(BR))) : "memory");
        check_flags();
      }
      store_o(sb, O0, O1, g0 ? L1[0] : L0[0], g0 ? L1[1] : L0[1], r0, r1);
    }
  }
  STAMP(11);
  refresh();
  const u32 ofl = (u32)((BR * 128 + rb * 8 + w) * 18 * 1024 + l * 16);
  u32x4 wf[18][5], of[18];
  wait_bar<4>();
  STAMP(12);
  const u32 wrow2 = lb + (16 * g1 + 8 * g0 + 2 * q) * 144;
  u32 oa[3], oa4[3];
#pragma unroll
  for (int i = 0; i < 3; ++i) {
    oa[i] = wrow2 + 8 * p + i * 57344; oa4[i] = wrow2 + 128 + 8 * (p & 1) + i * 57344;
    asm volatile("" : "+v"(oa[i]), "+v"(oa4[i]));
  }
  auto load_wf = [&](auto TT, auto II) {
    constexpr int t = TT.value, i = II.value;
    if constexpr (t < 18) {
      constexpr u32 cb = wo_at(BR, t < 4 ? 1 - (t >> 1) : t >> 1) + (t & 1) * 32 * 144, s = cb / 57344, o = cb % 57344;
      if constexpr (i < 4) wf[t][i] = frag_tr(oa[s] + o + 32 * i, oa[s] + o + 144 + 32 * i);
      else wf[t][4] = frag_tr(oa4[s] + o, oa4[s] + o + 144);
    }
  };
  f32x4 acc[5];
  u32 seen;
  auto kstep = [&](auto tt) {
    constexpr int t = tt.value;
    static_for<0, 5>([&](auto ii) {
      constexpr int i = ii.value;
      acc[i] = mfma32(wf[t][i], of[t], t ? acc[i] : z4);
      if constexpr (i == 1 && t >= 4) seen = umax4(seen, of[t]);
      load_wf(IC<t + PRE>{}, ii);
      SCHED_FENCE();
    });
  };
  u32 so[14], own = lb + LOWN + w * 2048 + l * 16;
#pragma unroll
  for (int t = 0; t < 14; ++t) { so[t] = ((t + 2 + 2 * hq) & 15) * 1024; asm volatile("" : "+s"(so[t])); }
  asm volatile("" : "+v"(own));
  _Pragma("nounroll") for (int pass = 0; pass < (1 << 16); ++pass) {
#pragma unroll
    for (int t = 4; t < 4 + NOPT; ++t) of[t] = __builtin_amdgcn_raw_buffer_load_b128(srdF, ofl, so[t - 4], 16);
    of[0] = lds_r128(own);
    of[1] = lds_r128(own + 1024);
    SCHED_FENCE();
    static_for<0, PRE * 5>([&](auto t) {
      load_wf(IC<t.value / 5>{}, IC<t.value % 5>{});
      if constexpr (t.value == PP) {
        SCHED_FENCE();
        of[2] = __builtin_amdgcn_raw_buffer_load_b128(srdF, ofl, 16 * 1024, 16);
        of[3] = __builtin_amdgcn_raw_buffer_load_b128(srdF, ofl, 17 * 1024, 16);
        SCHED_FENCE();
      }
    });
    SCHED_FENCE();
    kstep(IC<0>{}); kstep(IC<1>{});
    _Pragma("nounroll") for (int spin = 0; spin < (1 << 20); ++spin) {
      asm volatile("" ::: "memory");
      if (!__any(umax4(umax4(0u, of[2]), of[3]) == SENT)) break;
      of[2] = __builtin_amdgcn_raw_buffer_load_b128(srdF, ofl, 16 * 1024, 16);
      of[3] = __builtin_amdgcn_raw_buffer_load_b128(srdF, ofl, 17 * 1024, 16);
#pragma unroll
      for (int t = 4; t < 4 + NOPT; ++t) of[t] = __builtin_amdgcn_raw_buffer_load_b128(srdF, ofl, so[t - 4], 16);
    }
    STAMP(13);
#pragma unroll
    for (int t = 4 + NOPT; t < 18; ++t) of[t] = __builtin_amdgcn_raw_buffer_load_b128(srdF, ofl, so[t - 4], 16);
    SCHED_FENCE();
    seen = 0;
    static_for<2, 18>(kstep);
    if (!__any(seen == SENT)) break;
  }
  STAMP(14);
  if (BR == 0 && rb == 0 && hq == 0 && tid == 0) __hip_atomic_store((GLB_AS u64*)(ws + WS_EPOCH), tag, __ATOMIC_RELAXED, __HIP_MEMORY_SCOPE_AGENT);
  char* op = out + (size_t)(rb * 128 + w * 16 + c) * 2304 + BR * 1152 + hq * 144;
  __amdgpu_buffer_rsrc_t srdU = __builtin_amdgcn_make_buffer_rsrc((void*)out, (short)0, 2048 * 2304, 0x00020000);
  const u32 oo = (u32)((rb * 128 + w * 16 + c) * 2304 + BR * 1152 + hq * 144);
#pragma unroll
  for (int i = 0; i < 2; ++i) {
    u32x2 a = pk4(acc[2 * i]), b = pk4(acc[2 * i + 1]);
    auto s0 = __builtin_amdgcn_permlane16_swap(a[0], b[0], false, false);
    auto s1 = __builtin_amdgcn_permlane16_swap(a[1], b[1], false, false);
    u32 s00 = s0[0], s01 = s0[1], s10 = s1[0], s11 = s1[1];
    *(u32x4*)(op + 64 * i + 32 * g0 + 16 * g1) = u32x4{s00, s10, s01, s11};
  }
  if (g < 2) *(u32x2*)(op + 128 + 8 * g) = pk4(acc[4]);
  STAMP(15);
  RSTAMP(31);
  STAMP_FLUSH();
  asm volatile(".Lhk_to_%0:" ::"n"(BR));
  __builtin_amdgcn_endpgm();
}

__global__ __launch_bounds__(512, 2) void fused(const char* __restrict__ XA, const char* __restrict__ WA, const char* __restrict__ PHI,
                                                const char* __restrict__ WOA, const char* __restrict__ XB, const char* __restrict__ WB,
                                                const char* __restrict__ WOB, char* ws, char* out) {
  __shared__ __attribute__((aligned(16))) char smem[LDS_BYTES];
  u64 tent = 0, rent = 0;
  const u32 lb = (u32)(uintptr_t)(LDS_AS char*)smem;
  const int b = blockIdx.x, x = b & 7, j = b >> 3, xx = x & 3;
  const int rb = xx * 4 + (j >> 3), hq = j & 7;
  u64x4 ka, kb;
  u64 kc;
  first(x >= 4, rb, hq, j, lb, ka, kb, kc);
  auto ptr = [](u64 a) { return (char*)(GLB_AS char*)a; };
  XA = ptr(ka[0]); WA = ptr(ka[1]); PHI = ptr(ka[2]); WOA = ptr(ka[3]);
  XB = ptr(kb[0]); WB = ptr(kb[1]); WOB = ptr(kb[2]); ws = ptr(kb[3]); out = ptr(kc);
  if (x < 4) body<0>(XA, WA, PHI, WOA, ws, out, rb, hq, j, lb, tent, rent);
  else body<1>(XB, WB, PHI, WOB, ws, out, rb, hq, j, lb, tent, rent);
}
}

extern "C" void kernel_launch(void* const* d_in, const int* in_sizes, int n_in, void* d_out, int out_size, void* d_ws, size_t ws_size,
                              hipStream_t stream) {
  hk::fused<<<256, 512, 0, stream>>>((const char*)d_in[0], (const char*)d_in[1], (const char*)d_in[2], (const char*)d_in[3], (const char*)d_in[4],
                                     (const char*)d_in[5], (const char*)d_in[6], (char*)d_ws, (char*)d_out);
}
```

```cpp
#define PP 24
#define HB 2
#define CPRO 1
#define E_PO 43
#define E_FLD 36
#include <hip/hip_runtime.h>
#include <stdint.h>

namespace hk {
typedef unsigned int u32;
typedef unsigned long long u64;
typedef unsigned short u16;
typedef float f32x4 __attribute__((ext_vector_type(4)));
typedef float f32x2 __attribute__((ext_vector_type(2)));
typedef u32 u32x4 __attribute__((ext_vector_type(4)));
typedef u32 u32x2 __attribute__((ext_vector_type(2)));
typedef __bf16 bf16x8 __attribute__((ext_vector_type(8)));
typedef __bf16 bf16x2 __attribute__((ext_vector_type(2)));
typedef short s16x4 __attribute__((ext_vector_type(4)));
#define LDS_AS __attribute__((address_space(3)))
#define GLB_AS __attribute__((address_space(1)))
#define DEVI __device__ __forceinline__
#define SCHED_FENCE() __builtin_amdgcn_sched_barrier(0)

template <int V> struct IC { static constexpr int value = V; };
template <int I, int N, class F>
DEVI void static_for(F&& f) {
  if constexpr (I < N) { f(IC<I>{}); static_for<I + 1, N>(f); }
}

constexpr int XROW = 1152, WROW = 3456;
constexpr size_t WS_OF = 0;
constexpr int SLAB = 6400;
constexpr size_t WS_SLAB = 5u << 20;
constexpr size_t WS_FLAG = 6u << 20;
constexpr size_t WS_EPOCH = WS_FLAG + 65536;
constexpr size_t WS_OFLAG = WS_FLAG + 4096;
constexpr u32 OOB = 0x7fffffffu;
constexpr u32 SENT = 0xffffffffu;
constexpr int WU = 9216;
constexpr int LPK = 1024, PKS = 296;
constexpr int S0 = 3968, SS = 34944;
constexpr int SK = 16384, SV = SK + WU + 16;
constexpr int WQ = S0 + SS;
constexpr int LT = WQ + 9 * WU;
constexpr int LM = S0 + 4 * SS;
constexpr int LPHF = LM;
constexpr int LSUM = LPHF + 8192;
constexpr int LHALO = LM;
constexpr int KLX = LM + 11264;
constexpr int QLX = KLX + 2304;
constexpr int LSCR = QLX + 2048;
constexpr int VIS = 168;
constexpr int lvi(int br) { return br ? 1024 : LT; }
constexpr int LKX = 25600;
constexpr int wo_at(int br, int m) {
  if (br == 0) return m < 4 ? 1024 + m * WU : m < 6 ? LT + (m - 4) * WU : WQ + (m - 6) * WU;
  return m < 2 ? LT + m * WU : m == 2 ? LM : WQ + (m - 3) * WU;
}
constexpr int LOWN = WQ + 6 * WU;
constexpr int LDS_BYTES = 163840;
static_assert(LSCR + 512 <= LDS_BYTES && LPK + 2688 <= S0 && LPK + 128 * PKS <= WQ && LT + 128 * VIS <= LM && 1024 + 144 * VIS <= LKX && LKX + 8192 <= WQ &&
              SV + WU <= SS && 3 * WU <= SS && 7 * WU <= 2 * SS && S0 % 128 == 0 && SS % 128 == 0, "lds");

DEVI u32x2 tr_read(u32 a) { return __builtin_bit_cast(u32x2, __builtin_amdgcn_ds_read_tr16_b64_v4i16((LDS_AS s16x4*)a)); }
DEVI u32x4 frag_tr(u32 a0, u32 a1) { u32x2 x = tr_read(a0), y = tr_read(a1); return u32x4{x[0], x[1], y[0], y[1]}; }
DEVI u32x4 lds_r128(u32 a) { return *(LDS_AS u32x4*)a; }
DEVI u32x2 lds_r64(u32 a) { return *(LDS_AS u32x2*)a; }
DEVI u32 lds_r32(u32 a) { return *(LDS_AS u32*)a; }
DEVI u32 lds_r16(u32 a) { return *(LDS_AS u16*)a; }
DEVI void lds_w128(u32 a, u32x4 v) { *(LDS_AS u32x4*)a = v; }
DEVI void lds_w64(u32 a, u32x2 v) { *(LDS_AS u32x2*)a = v; }
DEVI void lds_w32(u32 a, u32 v) { *(LDS_AS u32*)a = v; }
DEVI f32x4 mfma32(u32x4 a, u32x4 b, f32x4 c) {
  return __builtin_amdgcn_mfma_f32_16x16x32_bf16(__builtin_bit_cast(bf16x8, a), __builtin_bit_cast(bf16x8, b), c, 0, 0, 0);
}
DEVI u32 pk(float a, float b) { f32x2 v = {a, b}; return __builtin_bit_cast(u32, __builtin_convertvector(v, bf16x2)); }
DEVI u32x2 pk4(f32x4 v) { return u32x2{pk(v[0], v[1]), pk(v[2], v[3])}; }
DEVI float max3(float a, float b, float c) { return fmaxf(fmaxf(a, b), c); }
DEVI u32 umax(u32 a, u32 b) { return a > b ? a : b; }
DEVI u32 umax3(u32 a, u32 b, u32 c) { return umax(umax(a, b), c); }
DEVI u32 umax4(u32 m, u32x4 v) { return umax3(umax3(m, v[0], v[1]), v[2], v[3]); }
DEVI u32 ns(u32 v) { return v < 0xfffffffeu ? v : 0xfffffffeu; }

DEVI void dma_full(u32 lds, u32 voff, __amdgpu_buffer_rsrc_t srd, u32 soff) {
  asm volatile("s_mov_b32 m0, %0\n\ts_nop 0\n\tbuffer_load_dwordx4 %1, %2, %3 offen lds" ::"s"(lds), "v"(voff), "s"(srd), "s"(soff) : "memory");
}
DEVI void dma_mask(u32 lds, u32 voff, __amdgpu_buffer_rsrc_t srd, u32 soff, u64 mask) {
  asm volatile("s_mov_b32 m0, %0\n\ts_mov_b64 exec, %4\n\tbuffer_load_dwordx4 %1, %2, %3 offen lds\n\ts_mov_b64 exec, -1" ::"s"(lds), "v"(voff), "s"(srd),
               "s"(soff), "s"(mask)
               : "memory");
}
template <int N>
DEVI void wait_bar() { asm volatile("s_waitcnt vmcnt(%0) lgkmcnt(0)\n\ts_barrier" ::"n"(N) : "memory"); }
template <int N>
DEVI void wait_vm_bar() { asm volatile("s_waitcnt vmcnt(%0)\n\ts_barrier" ::"n"(N) : "memory"); }
DEVI void bar_lds() { asm volatile("s_waitcnt lgkmcnt(0)\n\ts_barrier" ::: "memory"); }
DEVI void bar_only() { asm volatile("s_barrier" ::: "memory"); }

DEVI float redmax4(float a, float b, float c, float d, float ninf) {
  auto r1 = __builtin_amdgcn_permlane32_swap(__float_as_uint(a), __float_as_uint(b), false, false);
  auto r2 = __builtin_amdgcn_permlane32_swap(__float_as_uint(c), __float_as_uint(d), false, false);
  u32 r10 = r1[0], r11 = r1[1], r20 = r2[0], r21 = r2[1];
  float ab = max3(__uint_as_float(r10), __uint_as_float(r11), ninf);
  float cd = max3(__uint_as_float(r20), __uint_as_float(r21), ninf);
  auto r3 = __builtin_amdgcn_permlane16_swap(__float_as_uint(ab), __float_as_uint(cd), false, false);
  u32 r30 = r3[0], r31 = r3[1];
  return max3(__uint_as_float(r30), __uint_as_float(r31), ninf);
}
DEVI void bcast4(float t, float& a, float& b, float& c, float& d) {
  u32 tu = __float_as_uint(t);
  auto s1 = __builtin_amdgcn_permlane16_swap(tu, tu, false, false);
  u32 x = s1[0], y = s1[1];
  auto s2 = __builtin_amdgcn_permlane32_swap(x, x, false, false);
  auto s3 = __builtin_amdgcn_permlane32_swap(y, y, false, false);
  u32 s20 = s2[0], s21 = s2[1], s30 = s3[0], s31 = s3[1];
  a = __uint_as_float(s20); b = __uint_as_float(s21); c = __uint_as_float(s30); d = __uint_as_float(s31);
}

constexpr float LOG2E = 1.4426950408889634f;
constexpr u32 ONES = 0x3f803f80u;

struct HH {
  u32x4 f0, f1, f2, f3;
  u32x4 b0, b1;
  u32 bl;
  f32x4 T0, T1, TL, S;
  float a, b, c, d;
  f32x4 p0, q0, p1, q1;
  float l0, l1;
};
constexpr int HH_N = 106;
DEVI void swap32(float& a, float& b) {
  auto r = __builtin_amdgcn_permlane32_swap(__float_as_uint(a), __float_as_uint(b), false, false);
  u32 x = r[0], y = r[1]; a = __uint_as_float(x); b = __uint_as_float(y);
}
DEVI void swap16(float& a, float& b) {
  auto r = __builtin_amdgcn_permlane16_swap(__float_as_uint(a), __float_as_uint(b), false, false);
  u32 x = r[0], y = r[1]; a = __uint_as_float(x); b = __uint_as_float(y);
}
template <int I, bool QS>
DEVI void hh_step(HH& s, u32 lft, u32 phf, u32 sumf, bool lo, float ninf) {
  const f32x4 z = {0.f, 0.f, 0.f, 0.f};
  if constexpr (I == 0) {
    u32x2 x = lds_r64(lft); s.b0[2] = x[0]; s.b0[3] = x[1];
    asm volatile("" ::: "memory");
    u32x2 y = lds_r64(lft); s.b1[2] = y[0]; s.b1[3] = y[1];
    s.f0 = lds_r128(phf);
  } else if constexpr (I == 1) { s.f1 = lds_r128(phf + 1024); s.f2 = lds_r128(phf + 2048);
  } else if constexpr (I == 2) { s.f3 = lds_r128(phf + 3072);
  } else if constexpr (I == 3) { s.T0 = mfma32(s.f0, s.b0, z);
  } else if constexpr (I == 4) { s.TL = mfma32(s.f2, s.b0, z);
  } else if constexpr (I == 5) { s.T1 = mfma32(s.f1, s.b1, z);
  } else if constexpr (I == 6) { s.TL = mfma32(s.f3, s.b1, s.TL);
  } else if constexpr (I == 7) { s.f0 = lds_r128(sumf); s.f1 = lds_r128(sumf + 1024); s.f2 = lds_r128(sumf + 2048);
  } else if constexpr (I == 8) { s.a = max3(s.T0[0], s.T0[1], s.T0[2]);
  } else if constexpr (I == 9) { s.c = max3(-s.T0[0], -s.T0[1], -s.T0[2]);
  } else if constexpr (I == 10) { s.b = max3(s.T1[0], s.T1[1], s.T1[2]);
  } else if constexpr (I == 11) { s.d = max3(-s.T1[0], -s.T1[1], -s.T1[2]);
  } else if constexpr (I == 12) {
  } else if constexpr (I == 13) { s.a = max3(s.a, s.T0[3], s.TL[0]);
  } else if constexpr (I == 14) { s.b = max3(s.b, s.T1[3], s.TL[1]);
  } else if constexpr (I == 15) { s.c = max3(s.c, -s.T0[3], -s.TL[0]);
  } else if constexpr (I == 16) { s.d = max3(s.d, -s.T1[3], -s.TL[1]);
  } else if constexpr (I == 17) { swap32(s.a, s.b);
  } else if constexpr (I == 18) { swap32(s.c, s.d);
  } else if constexpr (I == 19) { s.a = max3(s.a, s.b, ninf);
  } else if constexpr (I == 20) { s.c = max3(s.c, s.d, ninf);
  } else if constexpr (I == 21) { swap16(s.a, s.c);
  } else if constexpr (I == 22) { s.a = max3(s.a, s.c, ninf);
  } else if constexpr (I == 23) { s.a *= -LOG2E;
  } else if constexpr (I == 24) { s.c = s.a; swap16(s.a, s.c);
  } else if constexpr (I == 25) { s.b = s.a; swap32(s.a, s.b);
  } else if constexpr (I == 26) { s.d = s.c; swap32(s.c, s.d);
  } else if constexpr (I == 27) { s.l0 = lo ? s.a : s.c;
  } else if constexpr (I == 28) { s.l1 = lo ? s.b : s.d;
  } else if constexpr (I == 29) { s.l0 = fmaf(s.TL[0], lo ? LOG2E : -LOG2E, s.l0);
  } else if constexpr (I == 30) { s.l1 = fmaf(s.TL[1], lo ? LOG2E : -LOG2E, s.l1);
  } else if constexpr (I == 31) { s.l0 = __builtin_amdgcn_exp2f(s.l0);
  } else if constexpr (I == 32) { s.l1 = __builtin_amdgcn_exp2f(s.l1);
  } else if constexpr (I == 33) { s.bl = pk(s.l0, s.l1);
  } else if constexpr (I == 34) { s.S = mfma32(s.f2, u32x4{s.bl, 0u, 0u, 0u}, z);
  } else if constexpr (I < 77) {
    constexpr int h = (I - 35) / 21, k = (I - 35) % 21;
    f32x4& T = h ? s.T1 : s.T0; f32x4& p = h ? s.p1 : s.p0; f32x4& q = h ? s.q1 : s.q0; u32x4& o = h ? s.b1 : s.b0;
    const float m = h ? s.b : s.a, n = h ? s.d : s.c;
    constexpr bool arg = k < 2 || ((k & 1) && k < 15);
    constexpr int e = arg ? (k < 2 ? k : (k + 1) / 2) : (k < 15 ? (k - 2) / 2 : 7);
    if constexpr (k < 16) {
      if constexpr (arg) { if constexpr (e < 4) p[e] = fmaf(T[e], LOG2E, m); else q[e - 4] = fmaf(T[e - 4], -LOG2E, n); }
      else { if constexpr (e < 4) p[e] = __builtin_amdgcn_exp2f(p[e]); else q[e - 4] = __builtin_amdgcn_exp2f(q[e - 4]); }
    } else if constexpr (k < 18) { o[k - 16] = pk(p[2 * (k - 16)], p[2 * (k - 16) + 1]);
    } else if constexpr (k < 20) { o[k - 16] = pk(q[2 * (k - 18)], q[2 * (k - 18) + 1]);
    } else { s.S = mfma32(h ? s.f1 : s.f0, o, s.S); }
  } else if constexpr (I == 77) {
  } else if constexpr (I == 78) { s.c = __builtin_amdgcn_rcpf(s.S[1]);
  } else if constexpr (I == 79) { s.d = __builtin_amdgcn_rcpf(s.S[3]);
  } else if constexpr (I == 80) { if constexpr (QS) s.c *= s.S[0]; else s.a = __builtin_amdgcn_rcpf(s.S[0]);
  } else if constexpr (I == 81) { if constexpr (QS) s.d *= s.S[2]; else s.b = __builtin_amdgcn_rcpf(s.S[2]);
  } else if constexpr (I < 106) {
    constexpr int v = (I - 82) / 6, k = (I - 82) % 6;
    if constexpr (!(QS && v >= 2)) {
      f32x4& x = v == 0 ? s.q0 : v == 1 ? s.q1 : v == 2 ? s.p0 : s.p1;
      u32x4& o = (v & 1) ? s.b1 : s.b0;
      const float r = v == 0 ? s.c : v == 1 ? s.d : v == 2 ? s.a : s.b;
      if constexpr (k < 4) x[k] *= r;
      else o[(v < 2 ? 2 : 0) + k - 4] = pk(x[2 * (k - 4)], x[2 * (k - 4) + 1]);
    }
  }
}
template <bool QS>
DEVI void hh_left(HH& s, bool lo) {
  if constexpr (QS) s.bl = pk(s.l0 * (lo ? 1.f : s.c), s.l1 * (lo ? 1.f : s.d));
  else s.bl = pk(s.l0 * (lo ? s.a : s.c), s.l1 * (lo ? s.b : s.d));
}

#define L2ST 40
#define L2ST2 72
#define PRE 7
#define PRO 2
#define LEAD 8
#define QA 2
#define QT 1
#define E_POB 30
#define Q_L1 10
#define Q_L1C 16
#define QTB QT
#define NOPT 0
#define PCW 1
#define PCW0 0
#define QW 1
#define HQ0 4
#define HQS 8
#define C0 0
#define CS 2
#define C1 44
#define E_PUB 11
#define QB 1
#define QSP 1
constexpr int EM = 36, EQ = 44, EH = 62, NEV = 74;
constexpr int Q_SLAB = 2;
#define E_PUBQ 9
#define E_PUBB 15
#define E_FLDB 17
constexpr int E_PUB2 = C1 < 34 ? E_PUBB : EQ + E_PUBQ;
static_assert(C1 >= 34 || ((E_PUBB & 3) == 3 && E_PUBB > C1 + 2 && E_PUBB < 35), "marks");
constexpr int e_fld(int br) { return br ? EQ + E_FLDB : E_FLD; }
constexpr int e_fchk(int br) { return br ? (E_FLDB > 12 ? EH + 4 : EQ + 16) : EQ; }
constexpr int clr_at(int E) { return E >= C1 && E < C1 + 3 ? 4 + E - C1 : E >= C0 && (E - C0) % CS == 0 && (E - C0) / CS < 4 ? (E - C0) / CS : -1; }
constexpr bool is_clr(int E) { return clr_at(E) >= 0; }
static_assert((E_PUB & 3) == 3 && E_PUB > C0 + 3 * CS && E_PUB < 35 && E_FLD >= EM && E_FLD < EQ && C1 > C0 + 3 * CS && (C1 < 34 || C1 >= EQ) && C1 < EQ + 6, "marks");
struct Sched {
  int op[NEV][3];
  int nop[NEV];
  int pos_ev[NEV + 1];
  int pos_hook[NEV];
  int last[36];
  int total;
};
constexpr int ext_ops(int br, int E) {
  return (E == (br ? E_POB : E_PO)) + (br && is_clr(E)) + (E == e_fld(br)) +
         (br ? 0 : E == EQ + Q_SLAB ? 2 : E == EQ + Q_L1 ? 4 : E == EQ + Q_L1C ? 1 : 0);
}
constexpr int op_chunk(int op) { return op < 200 ? (op - 100) >> 2 : op < 236 ? ((op - 200) >> 1) % 9 : (op - 200) >> 1; }
constexpr int op_earliest(int br, int op) {
  const int n = op_chunk(op);
  if (n < 9) return 4 * n - 1 - LEAD;
  if (n < 27) return n < 21 ? 23 : n < 25 ? 27 : 31;
  const int m = n - 27;
  if (br == 0) return m < 6 ? EQ + 3 : EQ + 6;
  return m < 2 ? 31 : m == 2 ? EQ : EQ + 2 * (m - 3) + 3;
}
constexpr int op_deadline(int br, int op) {
  const int n = op_chunk(op);
  if (n < 9) return 4 * n - 1;
  if (n < 27) return br == 0 || n == 18 ? EQ : EQ + 2 * (n - 18) - 1;
  return NEV;
}
static_assert(LEAD <= 12, "ring");
constexpr Sched make_sched(int br) {
  Sched s{};
  const int nx = br ? 3 : 2;
  int list[128] = {}, when[128] = {}, n = 0;
  for (int c = 0; c < 9; ++c) {
    for (int i = 0; i < nx; ++i) list[n++] = 100 + 4 * c + i;
    list[n++] = 200 + 2 * c; list[n++] = 201 + 2 * c; list[n++] = 218 + 2 * c; list[n++] = 219 + 2 * c;
  }
  for (int c = 18; c < 36; ++c) { list[n++] = 200 + 2 * c; list[n++] = 201 + 2 * c; }
  for (int k = 0; k < n; ++k) when[k] = 1000;
  int pos = 0, done = 0;
  for (; done < PRO * (nx + 4); ++done) { s.last[op_chunk(list[done])] = pos++; when[done] = -1; if (done == nx + 3) pos += 1 + (PCW == 2); }
  pos += PCW == 3;
  for (int E = 0; E < NEV; ++E) {
    s.pos_ev[E] = pos;
    int j = 0;
    for (int k = 0; k < n; ++k) {
      if (when[k] != 1000 || op_earliest(br, list[k]) > E) continue;
      const int c = op_chunk(list[k]);
      int q = E < EM ? (c < 9 ? QA : br ? QTB : QT) : E < EQ && br ? 0 : ext_ops(br, E) ? 1 : 2;
      if (br == 0 && E >= EQ) q = ext_ops(br, E) || (E < EH && E % QSP) ? 0 : 1;
      if (br == 1 && E >= EQ && c >= 27) q = E >= EH + 4 ? 0 : E >= EH ? HB : ext_ops(br, E) ? QB - 1 : QB;
      if (j >= q) break;
      s.op[E][j++] = list[k]; when[k] = E; ++done;
      s.last[c] = pos++;
      if (E >= op_deadline(br, list[k])) s.total = -1000;
    }
    s.nop[E] = j;
    s.pos_hook[E] = pos;
    pos += ext_ops(br, E);
  }
  s.pos_ev[NEV] = pos;
  s.total += done == n ? pos : -1000;
  return s;
}
template <int BR> struct SC { static constexpr Sched s = make_sched(BR); };
static_assert(SC<0>::s.total > 0 && SC<1>::s.total > 0, "schedule incomplete");
constexpr int imin(int a, int b) { return a < b ? a : b; }
template <int BR> constexpr int wait_n(int E, int c) {
  int n = SC<BR>::s.pos_ev[E] - SC<BR>::s.last[c] - 1;
  if (BR == 1 && E == E_PUB) n = imin(n, SC<BR>::s.pos_ev[E] - SC<BR>::s.pos_ev[C0 + 3 * CS + 1]);
  if (BR == 1 && E == E_PUB2) n = imin(n, SC<BR>::s.pos_ev[E] - SC<BR>::s.pos_ev[C1 + 3]);
  if (BR == 0 && E == EQ) n = imin(n, SC<BR>::s.pos_ev[E] - SC<BR>::s.pos_ev[E_FLD + 1]);
  return n;
}
template <int BR> constexpr int wait_h(int E, int E0) { return SC<BR>::s.pos_hook[E] - SC<BR>::s.pos_ev[E0 + 1]; }

#define STAMP(i)
#define RSTAMP(i)
#define STAMP_FLUSH()
#define STAMP_DECL

DEVI u64 next_tag(u32x2 e) {
  const u64 E = ((u64)(u32)__builtin_amdgcn_readfirstlane(e[1]) << 32) | (u32)__builtin_amdgcn_readfirstlane(e[0]);
  return E * 6364136223846793005ull + 1442695040888963407ull;
}
DEVI void poll_flags(GLB_AS u64* f, u64 tag) {
  for (int spin = 0; spin < (1 << 20); ++spin) {
    u64 v = __hip_atomic_load(f, __ATOMIC_RELAXED, __HIP_MEMORY_SCOPE_AGENT);
    if (__all(v == tag)) break;
    __builtin_amdgcn_s_sleep(1);
  }
  asm volatile("" ::: "memory");
}

#define ROT (rb + 2 * hq) % 9
typedef u64 u64x4 __attribute__((ext_vector_type(4)));
DEVI void first(bool br, int rb, int hq, int jx, u32 lb, u64x4& ka, u64x4& kb, u64& kc_) {
  asm volatile("s_load_dwordx8 %0, %3, 0x0\n\ts_load_dwordx8 %1, %3, 0x20\n\ts_load_dwordx2 %2, %3, 0x40"
               : "=&s"(ka), "=&s"(kb), "=&s"(kc_)
               : "s"(__builtin_amdgcn_kernarg_segment_ptr()));
  const int tid = threadIdx.x, l = tid & 63, w = __builtin_amdgcn_readfirstlane(tid >> 6);
  u64 pc;
  int f0, t0, f1, t1;
  asm volatile("s_getpc_b64 %0\n.Lhk_pc:\n\ts_mov_b32 %1, .Lhk_from_0-.Lhk_pc\n\ts_max_i32 %2, .Lhk_to_0-.Lhk_pc, 0\n\t"
               "s_mov_b32 %3, .Lhk_from_1-.Lhk_pc\n\ts_max_i32 %4, .Lhk_to_1-.Lhk_pc, 0"
               : "=s"(pc), "=s"(f0), "=s"(t0), "=s"(f1), "=s"(t1)::"scc");
  __amdgpu_buffer_rsrc_t srdC = __builtin_amdgcn_make_buffer_rsrc((void*)pc, (short)0, (br ? t1 : t0) & ~3, 0x00020000);
  const u32 po = l == 0 ? (u32)((br ? f1 : f0) + (8 * jx + w) * 128) : OOB;
  asm volatile("s_mov_b32 m0, %0\n\ts_nop 0\n\tbuffer_load_dword %1, %2, 0 offen lds" ::"s"(lb + LSCR), "v"(po), "s"(srdC) : "memory");
  const int lr = (l * 57) >> 9, lu = l - lr * 9;
  int kc = ROT;
  u32 vx[2], vq[2], vh, vp = (u32)(hq * 2592 + (w * 21 + l) * 16), le = lb + (br ? LHALO : LPK) + w * (br ? 256 : 336);
  {
    const int row = 2 * w + ((l >> 3) & 1);
    int grow = rb * 128 - 16 + row; grow = grow < 0 ? 0 : grow;
    vh = (u32)(grow * XROW + (((l & 7) ^ ((row >> 1) & 7)) << 4) + kc * 128);
  }
#pragma unroll
  for (int i = 0; i < 2; ++i) {
    const int row = 16 * w + 8 * i + (l >> 3);
    vx[i] = (u32)((rb * 128 + row) * XROW + (((l & 7) ^ ((row >> 1) & 7)) << 4));
    vq[i] = (u32)((8 * w + 4 * (lr & 1) + 2 * i + ((lr >> 1) & 1)) * WROW + hq * 144 + lu * 16);
  }
  asm volatile("" : "+v"(vx[0]), "+v"(vx[1]), "+v"(vq[0]), "+v"(vq[1]), "+v"(vh), "+v"(vp), "+s"(le), "+s"(kc));
  asm volatile("s_waitcnt lgkmcnt(0)" : "+s"(ka), "+s"(kb), "+s"(kc_) : "s"(__builtin_amdgcn_kernarg_segment_ptr()));
  const u64 XA = ka[0], WA = ka[1], PHI = ka[2], XB = kb[0], WB = kb[1];
  __amdgpu_buffer_rsrc_t srdX = __builtin_amdgcn_make_buffer_rsrc((void*)(br ? XB : XA), (short)0, 2048 * XROW, 0x00020000);
  __amdgpu_buffer_rsrc_t srdW = __builtin_amdgcn_make_buffer_rsrc((void*)(br ? WB : WA), (short)0, 576 * WROW, 0x00020000);
  __amdgpu_buffer_rsrc_t srdE = __builtin_amdgcn_make_buffer_rsrc((void*)(br ? XB : PHI), (short)0, br ? 2048 * XROW : 32 * 648, 0x00020000);
  dma_mask(le, br ? vh : vp, srdE, 0u, br ? 0xffffull : (1ull << 21) - 1);
#pragma unroll
  for (int i = 0; i < 2; ++i) dma_full(lb + S0 + w * 2048 + i * 1024, vx[i], srdX, (u32)(kc * 128));
#pragma unroll
  for (int n = 0; n < 4; ++n)
    dma_mask(lb + S0 + (n < 2 ? SK : SV) + (2 * w + (n & 1)) * 576, vq[n & 1], srdW, (u32)(kc * 64 * WROW + (n < 2 ? 1152 : 2304)), (1ull << 36) - 1);
}

template <int BR>
DEVI void body(const char* __restrict__ X, const char* __restrict__ W, const char* __restrict__ PHI, const char* __restrict__ WO, char* ws, char* out,
               int rb, int hq, int jx, u32 lb, u64 tent, u64 rent) {
  asm volatile(".Lhk_from_%0:" ::"n"(BR));
  int tid = threadIdx.x;
  asm volatile("" : "+v"(tid));
  int l = tid & 63;
  const int w = __builtin_amdgcn_readfirstlane(tid >> 6);
  const int rg = w & 3, hpl = w >> 2;
  int c = l & 15, g = l >> 4, q = c >> 2, p = c & 3, g0 = g & 1, g1 = g >> 1;
  int lr = (l * 57) >> 9, lu = l - lr * 9;
  auto refresh = [&]() {
    asm volatile("" : "+v"(tid));
    l = tid & 63; c = l & 15; g = l >> 4; q = c >> 2; p = c & 3; g0 = g & 1; g1 = g >> 1;
    lr = (l * 57) >> 9; lu = l - lr * 9;
  };
  STAMP_DECL
  STAMP(0);
  RSTAMP(30);
  __amdgpu_buffer_rsrc_t srdX = __builtin_amdgcn_make_buffer_rsrc((void*)X, (short)0, 2048 * XROW, 0x00020000);
  __amdgpu_buffer_rsrc_t srdW = __builtin_amdgcn_make_buffer_rsrc((void*)W, (short)0, 576 * WROW, 0x00020000);
  const int rot = ROT;
  constexpr u64 M36 = (1ull << 36) - 1;

  u32 vx[2], vq[2], vh = 0;
#pragma unroll
  for (int i = 0; i < 2; ++i) {
    int row = 16 * w + 8 * i + (l >> 3);
    vx[i] = (u32)((rb * 128 + row) * XROW + (((l & 7) ^ ((row >> 1) & 7)) << 4));
  }
#pragma unroll
  for (int i = 0; i < 2; ++i) vq[i] = (u32)((8 * w + 4 * (lr & 1) + 2 * i + ((lr >> 1) & 1)) * WROW + hq * 144 + lu * 16);
  if (BR) {
    int row = 2 * w + ((l >> 3) & 1);
    int grow = rb * 128 - 16 + row; grow = grow < 0 ? 0 : grow;
    vh = (u32)(grow * XROW + (((l & 7) ^ ((row >> 1) & 7)) << 4));
  }
  auto kc_of = [&](int cc) { int k = cc + rot; return k >= 9 ? k - 9 : k; };
  __amdgpu_buffer_rsrc_t srdO = __builtin_amdgcn_make_buffer_rsrc((void*)WO, (short)0, 576 * XROW, 0x00020000);
  auto dma_op = [&](auto OP) {
    constexpr int op = OP.value;
    if constexpr (op < 200) {
      constexpr int cc = (op - 100) >> 2, i = op & 3;
      const int kc = kc_of(cc);
      if constexpr (i < 2) dma_full(lb + S0 + (cc & 3) * SS + w * 2048 + i * 1024, vx[i], srdX, (u32)(kc * 128));
      else dma_mask(lb + LHALO + (cc & 3) * 2048 + w * 256, vh, srdX, (u32)(kc * 128), 0xffffull);
    } else {
      constexpr int n = (op - 200) >> 1, i = op & 1;
      constexpr int at = n < 9 ? S0 + (n & 3) * SS + SK : n < 18 ? S0 + ((n - 9) & 3) * SS + SV : n < 27 ? WQ + (n - 18) * WU : wo_at(BR, n - 27);
      const u32 dst = lb + at + (2 * w + i) * 576;
      if constexpr (n < 27) {
        const int kc = kc_of(n % 9), sec = n < 9 ? 1152 : (n < 18 ? 2304 : 0);
        dma_mask(dst, vq[i], srdW, (u32)(kc * 64 * WROW + sec), M36);
      } else {
        const int hi = lr & 1, qq = 2 * i + ((lr >> 1) & 1);
        if constexpr (n > 27)
          dma_mask(dst, (u32)((36 * hpl + 20 * hi + 4 * rg + qq) * XROW + hq * 144 + lu * 16), srdO, (u32)(((n - 28 + hq) & 7) * 72 * XROW), M36);
        else dma_mask(dst, (u32)((36 * (8 * hpl + 2 * rg + hi) + 16 + qq) * XROW + hq * 144 + lu * 16), srdO, 0u, M36);
      }
    }
  };
  auto ev_dma = [&](auto EE, auto II) {
    if constexpr (II.value < SC<BR>::s.nop[EE.value]) dma_op(IC<SC<BR>::s.op[EE.value][II.value]>{});
  };
  auto warm_wo = [&]() {
    const int idx = w * 21 + l;
    const u32 po = l < 21 && idx < 162 ? (u32)((162 * jx + idx) * 128) : OOB;
    asm volatile("s_mov_b32 m0, %0\n\ts_nop 0\n\tbuffer_load_dword %1, %2, 0 offen lds" ::"s"(lb + LSCR), "v"(po), "s"(srdO) : "memory");
  };
  __amdgpu_buffer_rsrc_t srdF = __builtin_amdgcn_make_buffer_rsrc((void*)(ws + WS_OF), (short)0, 256 * 18 * 1024, 0x00020000);
  __amdgpu_buffer_rsrc_t srdS = __builtin_amdgcn_make_buffer_rsrc((void*)(ws + WS_SLAB), (short)0, 136 * SLAB, 0x00020000);
  auto mark = [&](auto EE) {
    constexpr int e = EE.value, f = (e + 2) % 3;
    const u32x4 sv = {SENT, SENT, SENT, SENT};
    const u32 ob = (u32)(((e > 3 ? 128 : 0) + rb * 8 + w) * 18 * 1024);
    if constexpr (e > 0 && f < 2) __builtin_amdgcn_raw_buffer_store_b128(sv, srdF, l * 16, ob + hq * 2048 + f * 1024, 16);
    if constexpr (e > 0 && f == 2) __builtin_amdgcn_raw_buffer_store_b128(sv, srdF, g == 0 ? c * 16 : OOB, ob + (16 + (hq >> 2)) * 1024 + (hq & 3) * 256, 16);
    if constexpr (e == 0) {
      const u32 so = tid < 400 ? (u32)((hq * 16 + rb) * SLAB + tid * 16) : tid < 425 ? (u32)((128 + hq) * SLAB + rb * 400 + (tid - 400) * 16) : OOB;
      __builtin_amdgcn_raw_buffer_store_b128(sv, srdS, so, 0, 16);
    }
  };
  GLB_AS u64* kflags = (GLB_AS u64*)(ws + WS_FLAG + hq * 256);
  GLB_AS u64* oflags = (GLB_AS u64*)(ws + WS_OFLAG + (BR * 16 + rb) * 256);
  u32x2 ep, fv;
  asm volatile("" ::"s"(srdX), "s"(srdW));
  STAMP(16);
  static_assert(PRO == 2, "prologue");
  auto dma_chunk = [&](auto cc) {
    constexpr int c_ = cc.value;
    dma_op(IC<100 + 4 * c_>{}); dma_op(IC<101 + 4 * c_>{});
    if constexpr (BR) dma_op(IC<102 + 4 * c_>{});
    dma_op(IC<200 + 2 * c_>{}); dma_op(IC<201 + 2 * c_>{}); dma_op(IC<218 + 2 * c_>{}); dma_op(IC<219 + 2 * c_>{});
  };
  {
    const char* ea = ws + WS_EPOCH;
    asm volatile("global_load_dwordx2 %0, %1, off sc1" : "=v"(ep) : "v"(ea) : "memory");
  }
  STAMP(17);
  if constexpr (BR == 0) {
    wait_vm_bar<7 + (PCW == 2)>();
    STAMP(18);
    const int h = rg & 1, lf = rg >> 1;
    const int f = lf ? 16 + (q & 1) : c;
    const bool rowok = lf ? (p == h) : true;
    const u32 pb = lb + LPK + (hpl * 2 + h) * 648 + f * 2;
    const int d0 = 2 * h + 4 * g;
    u32 e0 = lds_r16(pb + (d0 + 0) * 36), e1 = lds_r16(pb + (d0 + 1) * 36), e2 = lds_r16(pb + (d0 + 2) * 36), e3 = lds_r16(pb + (d0 + 3) * 36);
    const int dl = h ? 0 : 16;
    u32 l0 = lds_r16(pb + dl * 36), l1 = lds_r16(pb + (dl + 1) * 36);
    SCHED_FENCE();
    dma_op(IC<104>{}); dma_op(IC<105>{});
    SCHED_FENCE();
    u32 lw = (g == 0) ? (l0 | (l1 << 16)) : 0u;
    u32x4 fr = {e0 | (e1 << 16), e2 | (e3 << 16), h ? 0u : lw, h ? lw : 0u};
    if (!rowok) fr = u32x4{0, 0, 0, 0};
    lds_w128(lb + LPHF + (hpl * 4 + rg) * 1024 + l * 16, fr);
    if (w < 3) {
      u32 a = (p == 2 * w) ? ONES : 0u, b = (p == 2 * w + 1) ? ONES : 0u;
      u32 x = ((p & 1) == g1) ? ((p >> 1) ? 0x3f800000u : 0x00003f80u) : 0u;
      lds_w128(lb + LSUM + w * 1024 + l * 16, w < 2 ? u32x4{a, a, b, b} : u32x4{x, 0u, 0u, 0u});
    }
    SCHED_FENCE();
    dma_op(IC<202>{}); dma_op(IC<203>{}); dma_op(IC<220>{}); dma_op(IC<221>{});
  } else {
    bar_only();
    STAMP(18);
    dma_chunk(IC<1>{});
  }
  STAMP(1);

  const u32 xa0 = lb + S0 + (rg * 32 + hpl * 16 + c) * 128 + ((g ^ ((c >> 1) & 7)) << 4);
  const u32 xa1 = xa0 ^ 64;
  const u32 xb0 = xa0 + 2048 - hpl * 4096, xb1 = xb0 ^ 64;
  const u32 xh0 = lb + LHALO + c * 128 + ((g ^ ((c >> 1) & 7)) << 4);
  const u32 xh1 = xh0 ^ 64;
  const u32 wrow = lb + (16 * g1 + 8 * g0 + 2 * q) * 144;
  const u32 wa = wrow + hpl * 72 + 8 * p;
  const u32 wl = wrow + 32 + (p & 1) * 72 + (p >> 1) * (SV - SK);
  const u32 wh = wa + (rg & 1) * 40 + (rg >> 1) * (SV - SK);
  float ninf = -__builtin_inff();
  asm volatile("" : "+s"(ninf));

  u32x4 xf[18][2];
  const f32x4 z4 = {0.f, 0.f, 0.f, 0.f};
  f32x4 k0[2] = {z4, z4}, k1[2] = {z4, z4}, a0[2] = {z4, z4}, a1[2] = {z4, z4}, aL = z4, aH = z4, aHL = z4;
  u32x4 f0, f1, fl, fh, xh;
  auto load_kv_frags = [&](auto HH_, auto JJ) {
    constexpr int H = HH_.value, j = JJ.value, T = H >> 1, n = T >> 1, ks = T & 1;
    constexpr u32 ok = S0 + (n & 3) * SS + SK + ks * 32 * 144, ov = ok + SV - SK;
    if constexpr (H < 36) {
      if constexpr ((H & 1) == 0) {
        if constexpr (j == 0) { xf[T][0] = lds_r128((ks ? xa1 : xa0) + (n & 3) * SS); f0 = frag_tr(wa + ok, wa + ok + 144); }
        if constexpr (j == 1) { xf[T][1] = lds_r128((ks ? xb1 : xb0) + (n & 3) * SS); f1 = frag_tr(wa + ok + 40, wa + ok + 184); }
      } else {
        if constexpr (j == 0) f0 = frag_tr(wa + ov, wa + ov + 144);
        if constexpr (j == 1) f1 = frag_tr(wa + ov + 40, wa + ov + 184);
        if constexpr (j == 2) fl = frag_tr(wl + ok, wl + ok + 144);
        if constexpr (j == 3 && BR == 1) { fh = frag_tr(wh + ok, wh + ok + 144); xh = lds_r128((ks ? xh1 : xh0) + (n & 3) * 2048); }
      }
    }
  };
  wait_bar<wait_n<BR>(0, 0)>();
  STAMP(2);
  load_kv_frags(IC<0>{}, IC<0>{}); load_kv_frags(IC<0>{}, IC<1>{});
  u64 tag = 0;
  static_for<0, 36>([&](auto hh_) {
    constexpr int H = hh_.value, T = H >> 1;
    if constexpr ((H & 3) == 3 && H < 35) {
      wait_bar<wait_n<BR>(H, (H + 1) >> 2)>();
    }
    u32x4 c0 = f0, c1 = f1, cl = fl, ch = fh, cx = xh;
    f32x4(&d0)[2] = (H & 1) ? a0 : k0;
    f32x4(&d1)[2] = (H & 1) ? a1 : k1;
    SCHED_FENCE();
    d0[0] = mfma32(c0, xf[T][0], d0[0]);
    load_kv_frags(IC<H + 1>{}, IC<0>{});
    SCHED_FENCE();
    d0[1] = mfma32(c0, xf[T][1], d0[1]);
    load_kv_frags(IC<H + 1>{}, IC<1>{});
    ev_dma(hh_, IC<0>{});
    SCHED_FENCE();
    d1[0] = mfma32(c1, xf[T][0], d1[0]);
    load_kv_frags(IC<H + 1>{}, IC<2>{});
    SCHED_FENCE();
    d1[1] = mfma32(c1, xf[T][1], d1[1]);
    load_kv_frags(IC<H + 1>{}, IC<3>{});
    ev_dma(hh_, IC<1>{});
    ev_dma(hh_, IC<2>{});
    SCHED_FENCE();
    if constexpr (H & 1) {
      aL = mfma32(cl, xf[T][0], aL);
      asm volatile("" : "+v"(aL));
      if constexpr (BR == 1) {
        aH = mfma32(ch, cx, aH); aHL = mfma32(cl, cx, aHL);
        asm volatile("" : "+v"(aH), "+v"(aHL));
      }
    }
    if constexpr (H == (BR ? E_POB : E_PO)) warm_wo();
    if constexpr (H == 3) {
      asm volatile("" : "+v"(ep));
      tag = next_tag(ep);
    }
    if constexpr (BR == 1 && is_clr(H)) mark(IC<clr_at(H)>{});
    if constexpr (BR == 1 && H == E_PUB) {
      if (tid < 2) __hip_atomic_store(tid == 0 ? kflags + rb : oflags + hq - 512, tag, __ATOMIC_RELAXED, __HIP_MEMORY_SCOPE_AGENT);
    }
    if constexpr (BR == 1 && H == E_PUB2) {
      if (tid == 64) __hip_atomic_store(oflags + hq, tag, __ATOMIC_RELAXED, __HIP_MEMORY_SCOPE_AGENT);
    }
    SCHED_FENCE();
  });
  STAMP(3);
  refresh();
  const u32 wqa = lb + WQ + (16 * g1 + 8 * g0 + 2 * q) * 144 + hpl * 72 + 8 * p;
  const u32 wql = lb + WQ + (16 * g1 + 8 * g0 + 2 * q) * 144 + 32 + (p & 1) * 72;
  auto load_q_frags = [&](auto TT, auto JJ) {
    constexpr int t = TT.value, j = JJ.value;
    constexpr u32 o = (t >> 1) * WU + (t & 1) * 32 * 144;
    if constexpr (t < 18) {
      if constexpr (j == 0) f0 = frag_tr(wqa + o, wqa + o + 144);
      if constexpr (j == 1) f1 = frag_tr(wqa + o + 40, wqa + o + 184);
      if constexpr (j == 2) fl = frag_tr(wql + o, wql + o + 144);
    }
  };
  if constexpr (BR == 1) {
    wait_bar<wait_n<BR>(EQ, 18)>();
    static_for<0, 3>([&](auto j) { load_q_frags(IC<0>{}, j); });
  }
  u32x2 kM[2][2];
#pragma unroll
  for (int sb = 0; sb < 2; ++sb) { kM[0][sb] = pk4(k0[sb]); kM[1][sb] = pk4(k1[sb]); }
  auto unswap = [&](u32x2& a, u32x2& b) {
    u32x2 x = a, y = b;
#pragma unroll
    for (int e = 0; e < 2; ++e) { a[e] = hpl ? y[e] : x[e]; b[e] = hpl ? x[e] : y[e]; }
  };
  {
    constexpr int VROW0 = BR ? 16 : 0;
    auto left = [&](f32x4 v, int blk) {
      u32x2 x = pk4(v);
      const u32 va = lb + lvi(BR) + (VROW0 - 16 + blk * 16 + c) * VIS + g0 * 80 + 64;
      lds_w64(g1 ? va : lb + KLX + (blk * 2 + g0) * 128 + c * 8, u32x2{x[0], g1 ? ONES : x[1]});
      lds_w64(g1 ? va + 8 : lb + LSCR + 256 + c * 8, u32x2{x[1], ONES});
    };
    left(aL, 1 + 2 * rg + hpl);
#pragma unroll
    for (int sb = 0; sb < 2; ++sb) {
      const u32 va = lb + lvi(BR) + (VROW0 + rg * 32 + (sb ^ hpl) * 16 + c) * VIS + hpl * 80;
      lds_w64(va + 8 * g, pk4(a0[sb]));
      lds_w64(va + 32 + 8 * g, pk4(a1[sb]));
    }
    if constexpr (BR == 1) {
      unswap(kM[0][0], kM[0][1]); unswap(kM[1][0], kM[1][1]);
      if (w == 2) left(aHL, 0);
      const u32 kx = lb + LKX + l * 8;
      lds_w64(rg < 2 ? kx + (hpl * 2 + rg) * 512 : lb + lvi(BR) + c * VIS + hpl * 80 + (rg & 1) * 32 + 8 * g, pk4(aH));
      if (rg < 3) {
        const u32 b = kx + (((rg + 1) * 2 + hpl) * 2) * 512;
        lds_w64(b, kM[0][1]); lds_w64(b + 512, kM[1][1]);
      }
    }
  }
  const u32 phf = lb + LPHF + hpl * 4096 + l * 16, sumf = lb + LSUM + l * 16;
  const bool lo = g < 2;
  auto load_flags = [&]() {
    GLB_AS u64* fa = (BR == 0 && g == 0) ? kflags + c : oflags + (l & 7);
    asm volatile("global_load_dwordx2 %0, %1, off sc1" : "=v"(fv) : "v"(fa) : "memory");
  };
  auto check_flags = [&]() {
    if (!__all((((u64)fv[1] << 32) | fv[0]) == tag)) poll_flags((BR == 0 && g == 0) ? kflags + c : oflags + (l & 7), tag);
  };
  if constexpr (BR == 0) {
    bar_lds();
    STAMP(4);
    HH h0, h1;
    h0.b0[0] = kM[0][0][0]; h0.b0[1] = kM[0][0][1]; h0.b1[0] = kM[1][0][0]; h0.b1[1] = kM[1][0][1];
    h1.b0[0] = kM[0][1][0]; h1.b0[1] = kM[0][1][1]; h1.b1[0] = kM[1][1][0]; h1.b1[1] = kM[1][1][1];
    const u32 kl0 = lb + KLX + ((1 + 2 * rg + hpl) * 2 + hpl) * 128 + c * 8, kl1 = lb + KLX + ((2 + 2 * rg - hpl) * 2 + hpl) * 128 + c * 8;
    static_for<0, HH_N>([&](auto st) {
      if constexpr (st.value >= 8 && (st.value - 8) % 12 == 0 && (st.value - 8) / 12 < EQ - EM) {
        constexpr int E = EM + (st.value - 8) / 12;
        SCHED_FENCE();
        ev_dma(IC<E>{}, IC<0>{}); ev_dma(IC<E>{}, IC<1>{});
        if constexpr (E == E_PO) warm_wo();
        if constexpr (E == E_FLD) load_flags();
        SCHED_FENCE();
      }
      hh_step<st.value, false>(h0, kl0, phf, sumf, lo, ninf);
      hh_step<st.value, false>(h1, kl1, phf, sumf, lo, ninf);
    });
#pragma unroll
    for (int sb = 0; sb < 2; ++sb) {
      HH& hs = sb ? h1 : h0;
      hh_left<false>(hs, lo);
      const u32 pa = lb + LPK + (rg * 32 + (sb ^ hpl) * 16 + c) * PKS + hpl * 144;
      lds_w64(pa + 8 * g, u32x2{hs.b0[0], hs.b0[1]});
      lds_w64(pa + 32 + 8 * g, u32x2{hs.b0[2], hs.b0[3]});
      lds_w64(pa + 64 + 8 * g, u32x2{hs.b1[0], hs.b1[1]});
      lds_w64(pa + 96 + 8 * g, u32x2{hs.b1[2], hs.b1[3]});
      lds_w32(pa + 128 + 4 * g, hs.bl);
    }
    STAMP(5);
    wait_bar<wait_n<BR>(EQ, 26)>();
    asm volatile("" : "+v"(fv));
    static_for<0, 3>([&](auto j) { load_q_frags(IC<0>{}, j); });
  }
  refresh();
  f32x4 dp = z4, dm = z4, df = z4;
  u32x4 ap[4], am[4], af[4], bv[4];
  u32 g0_, g1_, g2_, g3_;
  const int rr = 4 * (4 * g0 + q) + 2 * g1;
  const u32 pa = lb + LPK + rr * PKS + hpl * 144 + (rg & 1) * 64 + 8 * p;
  const u32 pf = lb + LPK + rr * PKS + hpl * 144 + 128 + 8 * (p >> 1);
  const u32 va = lb + lvi(BR) + rr * VIS + hpl * 80 + (rg < 2 ? rg * 32 + 8 * p : 64 + 8 * (p & 1));
  auto hook = [&](auto tt, auto jj) {
    constexpr int t = tt.value, E = EQ + t, j = jj.value;
    if constexpr (BR == 0 && j < 4) {
      if constexpr (t == Q_SLAB - 2) {
        ap[j] = frag_tr(pa + j * 32 * PKS, pa + j * 32 * PKS + PKS);
        am[j] = frag_tr(pa + 32 + j * 32 * PKS, pa + 32 + j * 32 * PKS + PKS);
        af[j] = frag_tr(pf + j * 32 * PKS, pf + j * 32 * PKS + PKS);
        bv[j] = frag_tr(va + j * 32 * VIS, va + j * 32 * VIS + VIS);
      }
      if constexpr (t == Q_SLAB - 1) {
        dp = mfma32(ap[j], bv[j], dp); dm = mfma32(am[j], bv[j], dm); df = mfma32(af[j], bv[j], df);
        asm volatile("" : "+v"(dp), "+v"(dm), "+v"(df));
      }
    }
    if constexpr (j != 4) return;
    if constexpr (BR == 1 && is_clr(E)) mark(IC<clr_at(E)>{});
    if constexpr (BR == 1 && E == E_PUB2) {
      if (tid == 0) __hip_atomic_store(oflags + hq, tag, __ATOMIC_RELAXED, __HIP_MEMORY_SCOPE_AGENT);
    }
    if constexpr (BR == 1 && E == e_fld(BR)) load_flags();
    if constexpr (E == e_fchk(BR)) {
      if constexpr (BR == 1) asm volatile("s_waitcnt vmcnt(%1)" : "+v"(fv) : "n"(wait_h<BR>(E, e_fld(BR))) : "memory");
      check_flags();
    }
    if constexpr (BR == 0) {
      if constexpr (t == Q_SLAB) {
        const u32 slab = (u32)((hq * 16 + rb) * SLAB);
        const bool h0set = (0x47 >> (c & 7)) & 1, h = rg & 1, own = (c >> 3) == hpl;
        u32x2 a = pk4(dp), b = pk4(dm);
        const u32 o1 = rg < 2 ? slab + (hpl * 2 + rg) * 1024 + l * 16 : (own && h0set != h) ? slab + 4096 + l * 16 : OOB;
        __builtin_amdgcn_raw_buffer_store_b128(u32x4{ns(a[0]), ns(a[1]), ns(b[0]), ns(b[1])}, srdS, o1, 0, 16);
        const float x0 = g0 ? df[2] : df[0], x1 = g0 ? df[3] : df[1];
        const bool first = rg < 2 ? !h : h0set;
        const u32 o2 = rg < 2 ? slab + 5120 + (hpl * 2 + rg) * 256 + l * 4 : own ? slab + 6144 + l * 4 : OOB;
        __builtin_amdgcn_raw_buffer_store_b32(ns(pk(first ? x0 : 0.f, first ? 0.f : x1)), srdS, o2, 0, 16);
        STAMP(6);
      }
      if constexpr (t == Q_L1 || t == Q_L1C) {
        const int dw = w * 16 + c;
        const u32 dof = (u32)((rb * 100 + (dw < 100 ? dw : 99)) * 4);
        const u32 go = (u32)((hq * 16 + 4 * g) * SLAB) + dof;
        if constexpr (t == Q_L1) {
          asm volatile("buffer_load_dword %0, %4, %5, 0 offen sc1\n\tbuffer_load_dword %1, %4, %5, %6 offen sc1\n\t"
                       "buffer_load_dword %2, %4, %5, %7 offen sc1\n\tbuffer_load_dword %3, %4, %5, %8 offen sc1"
                       : "=&v"(g0_), "=&v"(g1_), "=&v"(g2_), "=&v"(g3_)
                       : "v"(go), "s"(srdS), "s"(SLAB), "s"(2 * SLAB), "s"(3 * SLAB)
                       : "memory");
        } else {
          asm volatile("s_waitcnt vmcnt(%4)" : "+v"(g0_), "+v"(g1_), "+v"(g2_), "+v"(g3_) : "n"(wait_h<BR>(E, EQ + Q_L1)) : "memory");
          _Pragma("nounroll") for (int spin = 0; spin < (1 << 20); ++spin) {
            asm volatile("" ::: "memory");
            if (!__any(umax(umax3(g0_, g1_, g2_), g3_) == SENT)) break;
            g0_ = __builtin_amdgcn_raw_buffer_load_b32(srdS, go, 0, 16);
            g1_ = __builtin_amdgcn_raw_buffer_load_b32(srdS, go, SLAB, 16);
            g2_ = __builtin_amdgcn_raw_buffer_load_b32(srdS, go, 2 * SLAB, 16);
            g3_ = __builtin_amdgcn_raw_buffer_load_b32(srdS, go, 3 * SLAB, 16);
          }
          const u32 sel = c == 0 ? 0x00003f80u : c == 1 ? 0x3f800000u : 0u;
          f32x4 sum = mfma32(u32x4{sel, sel, sel, sel}, u32x4{g0_, g1_, g2_, g3_}, z4);
          __builtin_amdgcn_raw_buffer_store_b32(ns(pk(sum[0], sum[1])), srdS, g == 0 ? (u32)((128 + hq) * SLAB) + dof : OOB, 0, 16);
          STAMP(7);
        }
      }
    }
  };
  a0[0] = z4; a0[1] = z4; a1[0] = z4; a1[1] = z4; aL = z4;
  static_for<0, 18>([&](auto tt) {
    constexpr int t = tt.value, E = EQ + t;
    if constexpr (BR == 0) {
      if constexpr (t == 3 || t == 6) bar_only();
    } else if constexpr ((t & 1) && t < 17) {
      if constexpr (t == 1) wait_bar<wait_n<BR>(E, 18 + (t >> 1) + 1)>();
      else wait_vm_bar<wait_n<BR>(E, 18 + (t >> 1) + 1)>();
    }
    u32x4 c0 = f0, c1 = f1, cl = fl;
    SCHED_FENCE();
    a0[0] = mfma32(c0, xf[t][0], a0[0]);
    load_q_frags(IC<t + 1>{}, IC<0>{});
    hook(tt, IC<0>{});
    SCHED_FENCE();
    a0[1] = mfma32(c0, xf[t][1], a0[1]);
    load_q_frags(IC<t + 1>{}, IC<1>{});
    hook(tt, IC<1>{});
    ev_dma(IC<E>{}, IC<0>{});
    SCHED_FENCE();
    a1[0] = mfma32(c1, xf[t][0], a1[0]);
    load_q_frags(IC<t + 1>{}, IC<2>{});
    hook(tt, IC<2>{});
    SCHED_FENCE();
    a1[1] = mfma32(c1, xf[t][1], a1[1]);
    hook(tt, IC<3>{});
    ev_dma(IC<E>{}, IC<1>{});
    SCHED_FENCE();
    aL = mfma32(cl, xf[t][0], aL);
    asm volatile("" : "+v"(aL));
    hook(tt, IC<4>{});
    SCHED_FENCE();
  });
  STAMP(8);
  refresh();
  if (g < 2) lds_w64(lb + QLX + ((2 * rg + hpl) * 2 + g) * 128 + c * 8, pk4(aL));
  bar_lds();
  const u32 ofo = (u32)((BR * 128 + rb * 8 + rg * 2) * 18 * 1024);
  const int hp = hq * 2 + hpl;
  auto store_o = [&](int sb, f32x4 o0, f32x4 o1, float l0, float l1, float r0, float r1) {
    u32x2 a = pk4(o0 * r0), b = pk4(o1 * r1);
    __builtin_amdgcn_raw_buffer_store_b128(u32x4{ns(a[0]), ns(a[1]), ns(b[0]), ns(b[1])}, srdF, ofo + (sb * 18 + hp) * 1024 + l * 16, 0, 16);
    lds_w128(lb + LOWN + ((rg * 2 + sb) * 2 + hpl) * 1024 + l * 16, u32x4{a[0], a[1], b[0], b[1]});
    float rr = g0 ? r1 : r0;
    u32 lv = ns(pk(l0 * rr, l1 * rr));
    auto sw = __builtin_amdgcn_permlane16_swap(lv, lv, false, false);
    u32 s0 = sw[0], s1 = sw[1];
    __builtin_amdgcn_raw_buffer_store_b64(u32x2{s0, s1}, srdF, g == 0 ? ofo + (sb * 18 + 16 + (hq >> 2)) * 1024 + (c + 16 * (hq & 3)) * 16 + 8 * hpl : OOB, 0, 16);
  };

  if (BR == 0) {
    HH h0, h1;
    {
      u32x2 qa0 = pk4(a0[0]), qb0 = pk4(a1[0]), qa1 = pk4(a0[1]), qb1 = pk4(a1[1]);
      h0.b0[0] = qa0[0]; h0.b0[1] = qa0[1]; h0.b1[0] = qb0[0]; h0.b1[1] = qb0[1];
      h1.b0[0] = qa1[0]; h1.b0[1] = qa1[1]; h1.b1[0] = qb1[0]; h1.b1[1] = qb1[1];
    }
    const u32 ql0 = lb + QLX + ((2 * rg + hpl) * 2 + hpl) * 128 + c * 8, ql1 = lb + QLX + ((2 * rg + 1 - hpl) * 2 + hpl) * 128 + c * 8;
    const u32 kf = (u32)((128 + hq) * SLAB);
    const int l8 = g * 16 + (c & 7) + 8 * hpl;
    u32x4 AM0, AM1, AMLm;
    u32 AL0, AL1, ALL;
    auto load_kv = [&]() {
      AM0 = __builtin_amdgcn_raw_buffer_load_b128(srdS, kf + hpl * 2048 + l * 16, 0, 16);
      AM1 = __builtin_amdgcn_raw_buffer_load_b128(srdS, kf + hpl * 2048 + 1024 + l * 16, 0, 16);
      AMLm = __builtin_amdgcn_raw_buffer_load_b128(srdS, kf + 4096 + l8 * 16, 0, 16);
      AL0 = __builtin_amdgcn_raw_buffer_load_b32(srdS, kf + 5120 + hpl * 512 + l * 4, 0, 16);
      AL1 = __builtin_amdgcn_raw_buffer_load_b32(srdS, kf + 5120 + hpl * 512 + 256 + l * 4, 0, 16);
      ALL = __builtin_amdgcn_raw_buffer_load_b32(srdS, kf + 6144 + l8 * 4, 0, 16);
    };
    static_for<0, HH_N>([&](auto st) {
      if (st.value == L2ST) { SCHED_FENCE(); load_kv(); SCHED_FENCE(); }
      if (st.value == L2ST2) {
        SCHED_FENCE();
        if (__any(umax4(umax4(umax4(umax3(AL0, AL1, ALL), AM0), AM1), AMLm) == SENT)) load_kv();
        SCHED_FENCE();
      }
      if constexpr (st.value >= HQ0 && (st.value - HQ0) % HQS == 0 && (st.value - HQ0) / HQS < NEV - EH) {
        constexpr int E = EH + (st.value - HQ0) / HQS;
        if constexpr (SC<BR>::s.nop[E] > 0) { SCHED_FENCE(); ev_dma(IC<E>{}, IC<0>{}); SCHED_FENCE(); }
      }
      hh_step<st.value, true>(h0, ql0, phf, sumf, lo, ninf);
      hh_step<st.value, true>(h1, ql1, phf, sumf, lo, ninf);
    });
    hh_left<true>(h0, lo);
    hh_left<true>(h1, lo);
    asm volatile("" : "+v"(h0.b0), "+v"(h0.b1), "+v"(h0.bl), "+v"(h1.b0), "+v"(h1.b1), "+v"(h1.bl));
    STAMP(9);
    _Pragma("nounroll") for (int spin = 0; spin < (1 << 20); ++spin) {
      asm volatile("" ::: "memory");
      if (!__any(umax4(umax4(umax4(umax3(AL0, AL1, ALL), AM0), AM1), AMLm) == SENT)) break;
      load_kv();
    }
    STAMP(10);
    const bool h0set = (0x47 >> (c & 7)) & 1;
    u32x4 AML0, AML1;
#pragma unroll
    for (int e = 0; e < 4; ++e) { AML0[e] = h0set ? AMLm[e] : 0u; AML1[e] = h0set ? 0u : AMLm[e]; }
    f32x4 O0[2], O1[2], OL[2];
#pragma unroll
    for (int sb = 0; sb < 2; ++sb) OL[sb] = mfma32(AML0, (sb ? h1 : h0).b0, z4);
#pragma unroll
    for (int sb = 0; sb < 2; ++sb) O0[sb] = mfma32(AM0, (sb ? h1 : h0).b0, z4);
#pragma unroll
    for (int sb = 0; sb < 2; ++sb) OL[sb] = mfma32(AML1, (sb ? h1 : h0).b1, OL[sb]);
#pragma unroll
    for (int sb = 0; sb < 2; ++sb) O1[sb] = mfma32(AM1, (sb ? h1 : h0).b1, z4);
#pragma unroll
    for (int sb = 0; sb < 2; ++sb) OL[sb] = mfma32(u32x4{ALL, 0u, 0u, 0u}, u32x4{(sb ? h1 : h0).bl, 0u, 0u, 0u}, OL[sb]);
#pragma unroll
    for (int sb = 0; sb < 2; ++sb) O0[sb] = mfma32(u32x4{AL0, 0u, 0u, 0u}, u32x4{(sb ? h1 : h0).bl, 0u, 0u, 0u}, O0[sb]);
#pragma unroll
    for (int sb = 0; sb < 2; ++sb) O1[sb] = mfma32(u32x4{AL1, 0u, 0u, 0u}, u32x4{(sb ? h1 : h0).bl, 0u, 0u, 0u}, O1[sb]);
#pragma unroll
    for (int sb = 0; sb < 2; ++sb) {
      const HH& h = sb ? h1 : h0;
      float r0 = __builtin_amdgcn_rcpf(fmaf(h.S[0], 1e-6f, OL[sb][2])), r1 = __builtin_amdgcn_rcpf(fmaf(h.S[2], 1e-6f, OL[sb][3]));
      store_o(sb ^ hpl, O0[sb], O1[sb], OL[sb][0], OL[sb][1], r0, r1);
    }
  } else {
    auto ev3 = [&](auto EE) {
      SCHED_FENCE();
      ev_dma(EE, IC<0>{}); ev_dma(EE, IC<1>{}); ev_dma(EE, IC<2>{});
      if constexpr (EE.value == e_fld(BR)) load_flags();
      SCHED_FENCE();
    };
    const u32 kx = lb + LKX + ((rg * 2 + hpl) * 2) * 512 + l * 8;
    u32x2 pM0 = lds_r64(kx), pM1 = lds_r64(kx + 512);
    u32x2 kL[3];
#pragma unroll
    for (int i = 0; i < 3; ++i) kL[i] = lds_r64(lb + KLX + ((2 * rg + i) * 2 + hpl) * 128 + c * 8);
    ev3(IC<EH>{});
    const int par = c & 1;
    const bool ina = (4 * g + par) > c, inb = (4 * g + par + 2) > c;
    const float sc = 0.23570226039551584f * LOG2E;
    float la[2][2], lbv[2][2], mx[2][2];
    u32x2 qM[2][2] = {{pk4(a0[0]), pk4(a0[1])}, {pk4(a1[0]), pk4(a1[1])}};
    unswap(qM[0][0], qM[0][1]); unswap(qM[1][0], qM[1][1]);
#pragma unroll
    for (int sb = 0; sb < 2; ++sb) {
      u32x2 qlv = lds_r64(lb + QLX + ((2 * rg + sb) * 2 + hpl) * 128 + c * 8);
      u32x2 q0 = qM[0][sb], q1 = qM[1][sb];
      u32x4 b0 = {q0[0], q0[1], g == 0 ? qlv[0] : 0u, 0u};
      u32x4 b1 = {q1[0], q1[1], 0u, g == 0 ? qlv[1] : 0u};
      u32x2 kp0 = sb ? kM[0][0] : pM0, kp1 = sb ? kM[1][0] : pM1, kpl = kL[sb], kcl = kL[sb + 1];
      f32x4 sp0 = mfma32(u32x4{kp0[0], kp0[1], kpl[0], kpl[1]}, b0, z4);
      f32x4 sp1 = mfma32(u32x4{kp1[0], kp1[1], kpl[0], kpl[1]}, b1, z4);
      f32x4 sc0 = mfma32(u32x4{kM[0][sb][0], kM[0][sb][1], kcl[0], kcl[1]}, b0, z4);
      f32x4 sc1 = mfma32(u32x4{kM[1][sb][0], kM[1][sb][1], kcl[0], kcl[1]}, b1, z4);
      if (sb) ev3(IC<EH + 2>{}); else ev3(IC<EH + 1>{});
      const bool dead = (sb == 0) && (rb == 0) && (rg == 0);
      float pa0 = par ? sp0[1] : sp0[0], pb0 = par ? sp0[3] : sp0[2], pa1 = par ? sp1[1] : sp1[0], pb1 = par ? sp1[3] : sp1[2];
      if (dead) { pa0 = pb0 = pa1 = pb1 = -1e30f; }
      la[0][sb] = (ina ? pa0 : (par ? sc0[1] : sc0[0])) * sc;
      lbv[0][sb] = (inb ? pb0 : (par ? sc0[3] : sc0[2])) * sc;
      la[1][sb] = (ina ? pa1 : (par ? sc1[1] : sc1[0])) * sc;
      lbv[1][sb] = (inb ? pb1 : (par ? sc1[3] : sc1[2])) * sc;
      mx[0][sb] = fmaxf(la[0][sb], lbv[0][sb]);
      mx[1][sb] = fmaxf(la[1][sb], lbv[1][sb]);
    }
    float tm = redmax4(mx[0][0], mx[0][1], mx[1][0], mx[1][1], ninf);
    bcast4(tm, mx[0][0], mx[0][1], mx[1][0], mx[1][1]);
    ev3(IC<EH + 3>{});
    const u32 vb = lb + lvi(BR) + (rg * 32 + 4 * g + q) * VIS + hpl * 80;
#pragma unroll
    for (int sb = 0; sb < 2; ++sb) {
      const u32 v0 = vb + sb * 16 * VIS, v1 = v0 + 16 * VIS;
      u32x4 A0 = frag_tr(v0 + 8 * p, v1 + 8 * p), A1 = frag_tr(v0 + 32 + 8 * p, v1 + 32 + 8 * p);
      u32x4 AL = frag_tr(v0 + 64 + 8 * (p & 1), v1 + 64 + 8 * (p & 1));
      u32x4 P[2];
#pragma unroll
      for (int h = 0; h < 2; ++h) {
        float ea = __builtin_amdgcn_exp2f(la[h][sb] - mx[h][sb]), eb = __builtin_amdgcn_exp2f(lbv[h][sb] - mx[h][sb]);
        u32 wa_ = par ? pk(0.f, ea) : pk(ea, 0.f), wb_ = par ? pk(0.f, eb) : pk(eb, 0.f);
        P[h] = u32x4{ina ? wa_ : 0u, inb ? wb_ : 0u, ina ? 0u : wa_, inb ? 0u : wb_};
      }
      f32x4 O0 = mfma32(A0, P[0], z4), O1 = mfma32(A1, P[1], z4);
      f32x4 L0 = mfma32(AL, P[0], z4), L1 = mfma32(AL, P[1], z4);
      float r0 = __builtin_amdgcn_rcpf(L0[2]), r1 = __builtin_amdgcn_rcpf(L1[2]);
      if (e_fchk(BR) == EH + 4 && sb == 0) {
        asm volatile("s_waitcnt vmcnt(%1)" : "+v"(fv) : "n"(wait_h<BR>(EH + 4, e_fld(BR))) : "memory");
        check_flags();
      }
      store_o(sb, O0, O1, g0 ? L1[0] : L0[0], g0 ? L1[1] : L0[1], r0, r1);
    }
  }
  STAMP(11);
  refresh();
  const u32 ofl = (u32)((BR * 128 + rb * 8 + w) * 18 * 1024 + l * 16);
  u32x4 wf[18][5], of[18];
  wait_bar<4>();
  STAMP(12);
  const u32 wrow2 = lb + (16 * g1 + 8 * g0 + 2 * q) * 144;
  u32 oa[3], oa4[3];
#pragma unroll
  for (int i = 0; i < 3; ++i) {
    oa[i] = wrow2 + 8 * p + i * 57344; oa4[i] = wrow2 + 128 + 8 * (p & 1) + i * 57344;
    asm volatile("" : "+v"(oa[i]), "+v"(oa4[i]));
  }
  auto load_wf = [&](auto TT, auto II) {
    constexpr int t = TT.value, i = II.value;
    if constexpr (t < 18) {
      constexpr u32 cb = wo_at(BR, t < 4 ? 1 - (t >> 1) : t >> 1) + (t & 1) * 32 * 144, s = cb / 57344, o = cb % 57344;
      if constexpr (i < 4) wf[t][i] = frag_tr(oa[s] + o + 32 * i, oa[s] + o + 144 + 32 * i);
      else wf[t][4] = frag_tr(oa4[s] + o, oa4[s] + o + 144);
    }
  };
  f32x4 acc[5];
  u32 seen;
  auto kstep = [&](auto tt) {
    constexpr int t = tt.value;
    static_for<0, 5>([&](auto ii) {
      constexpr int i = ii.value;
      acc[i] = mfma32(wf[t][i], of[t], t ? acc[i] : z4);
      if constexpr (i == 1 && t >= 4) seen = umax4(seen, of[t]);
      load_wf(IC<t + PRE>{}, ii);
      SCHED_FENCE();
    });
  };
  u32 so[14], own = lb + LOWN + w * 2048 + l * 16;
#pragma unroll
  for (int t = 0; t < 14; ++t) { so[t] = ((t + 2 + 2 * hq) & 15) * 1024; asm volatile("" : "+s"(so[t])); }
  asm volatile("" : "+v"(own));
  _Pragma("nounroll") for (int pass = 0; pass < (1 << 16); ++pass) {
#pragma unroll
    for (int t = 4; t < 4 + NOPT; ++t) of[t] = __builtin_amdgcn_raw_buffer_load_b128(srdF, ofl, so[t - 4], 16);
    of[0] = lds_r128(own);
    of[1] = lds_r128(own + 1024);
    SCHED_FENCE();
    static_for<0, PRE * 5>([&](auto t) {
      load_wf(IC<t.value / 5>{}, IC<t.value % 5>{});
      if constexpr (t.value == PP) {
        SCHED_FENCE();
        of[2] = __builtin_amdgcn_raw_buffer_load_b128(srdF, ofl, 16 * 1024, 16);
        of[3] = __builtin_amdgcn_raw_buffer_load_b128(srdF, ofl, 17 * 1024, 16);
        SCHED_FENCE();
      }
    });
    SCHED_FENCE();
    kstep(IC<0>{}); kstep(IC<1>{});
    _Pragma("nounroll") for (int spin = 0; spin < (1 << 20); ++spin) {
      asm volatile("" ::: "memory");
      if (!__any(umax4(umax4(0u, of[2]), of[3]) == SENT)) break;
      of[2] = __builtin_amdgcn_raw_buffer_load_b128(srdF, ofl, 16 * 1024, 16);
      of[3] = __builtin_amdgcn_raw_buffer_load_b128(srdF, ofl, 17 * 1024, 16);
#pragma unroll
      for (int t = 4; t < 4 + NOPT; ++t) of[t] = __builtin_amdgcn_raw_buffer_load_b128(srdF, ofl, so[t - 4], 16);
    }
    STAMP(13);
#pragma unroll
    for (int t = 4 + NOPT; t < 18; ++t) of[t] = __builtin_amdgcn_raw_buffer_load_b128(srdF, ofl, so[t - 4], 16);
    SCHED_FENCE();
    seen = 0;
    static_for<2, 18>(kstep);
    if (!__any(seen == SENT)) break;
  }
  STAMP(14);
  if (BR == 0 && rb == 0 && hq == 0 && tid == 0) __hip_atomic_store((GLB_AS u64*)(ws + WS_EPOCH), tag, __ATOMIC_RELAXED, __HIP_MEMORY_SCOPE_AGENT);
  char* op = out + (size_t)(rb * 128 + w * 16 + c) * 2304 + BR * 1152 + hq * 144;
  __amdgpu_buffer_rsrc_t srdU = __builtin_amdgcn_make_buffer_rsrc((void*)out, (short)0, 2048 * 2304, 0x00020000);
  const u32 oo = (u32)((rb * 128 + w * 16 + c) * 2304 + BR * 1152 + hq * 144);
#pragma unroll
  for (int i = 0; i < 2; ++i) {
    u32x2 a = pk4(acc[2 * i]), b = pk4(acc[2 * i + 1]);
    auto s0 = __builtin_amdgcn_permlane16_swap(a[0], b[0], false, false);
    auto s1 = __builtin_amdgcn_permlane16_swap(a[1], b[1], false, false);
    u32 s00 = s0[0], s01 = s0[1], s10 = s1[0], s11 = s1[1];
    *(u32x4*)(op + 64 * i + 32 * g0 + 16 * g1) = u32x4{s00, s10, s01, s11};
  }
  if (g < 2) *(u32x2*)(op + 128 + 8 * g) = pk4(acc[4]);
  STAMP(15);
  RSTAMP(31);
  STAMP_FLUSH();
  asm volatile(".Lhk_to_%0:" ::"n"(BR));
  __builtin_amdgcn_endpgm();
}

__global__ __launch_bounds__(512, 2) void fused(const char* __restrict__ XA, const char* __restrict__ WA, const char* __restrict__ PHI,
                                                const char* __restrict__ WOA, const char* __restrict__ XB, const char* __restrict__ WB,
                                                const char* __restrict__ WOB, char* ws, char* out) {
  __shared__ __attribute__((aligned(16))) char smem[LDS_BYTES];
  u64 tent = 0, rent = 0;
  const u32 lb = (u32)(uintptr_t)(LDS_AS char*)smem;
  const int b = blockIdx.x, x = b & 7, j = b >> 3, xx = x & 3;
  const int rb = xx * 4 + (j >> 3), hq = j & 7;
  u64x4 ka, kb;
  u64 kc;
  first(x >= 4, rb, hq, j, lb, ka, kb, kc);
  auto ptr = [](u64 a) { return (char*)(GLB_AS char*)a; };
  XA = ptr(ka[0]); WA = ptr(ka[1]); PHI = ptr(ka[2]); WOA = ptr(ka[3]);
  XB = ptr(kb[0]); WB = ptr(kb[1]); WOB = ptr(kb[2]); ws = ptr(kb[3]); out = ptr(kc);
  if (x < 4) body<0>(XA, WA, PHI, WOA, ws, out, rb, hq, j, lb, tent, rent);
  else body<1>(XB, WB, PHI, WOB, ws, out, rb, hq, j, lb, tent, rent);
}
}

extern "C" void kernel_launch(void* const* d_in, const int* in_sizes, int n_in, void* d_out, int out_size, void* d_ws, size_t ws_size,
                              hipStream_t stream) {
  hk::fused<<<256, 512, 0, stream>>>((const char*)d_in[0], (const char*)d_in[1], (const char*)d_in[2], (const char*)d_in[3], (const char*)d_in[4],
                                     (const char*)d_in[5], (const char*)d_in[6], (char*)d_ws, (char*)d_out);
}
```

```cpp
#define CPRO 1
#define E_PO 43
#define E_FLD 36
#include <hip/hip_runtime.h>
#include <stdint.h>

namespace hk {
typedef unsigned int u32;
typedef unsigned long long u64;
typedef unsigned short u16;
typedef float f32x4 __attribute__((ext_vector_type(4)));
typedef float f32x2 __attribute__((ext_vector_type(2)));
typedef u32 u32x4 __attribute__((ext_vector_type(4)));
typedef u32 u32x2 __attribute__((ext_vector_type(2)));
typedef __bf16 bf16x8 __attribute__((ext_vector_type(8)));
typedef __bf16 bf16x2 __attribute__((ext_vector_type(2)));
typedef short s16x4 __attribute__((ext_vector_type(4)));
#define LDS_AS __attribute__((address_space(3)))
#define GLB_AS __attribute__((address_space(1)))
#define DEVI __device__ __forceinline__
#define SCHED_FENCE() __builtin_amdgcn_sched_barrier(0)

template <int V> struct IC { static constexpr int value = V; };
template <int I, int N, class F>
DEVI void static_for(F&& f) {
  if constexpr (I < N) { f(IC<I>{}); static_for<I + 1, N>(f); }
}

constexpr int XROW = 1152, WROW = 3456;
constexpr size_t WS_OF = 0;
constexpr int SLAB = 6400;
constexpr size_t WS_SLAB = 5u << 20;
constexpr size_t WS_FLAG = 6u << 20;
constexpr size_t WS_EPOCH = WS_FLAG + 65536;
constexpr size_t WS_OFLAG = WS_FLAG + 4096;
constexpr u32 OOB = 0x7fffffffu;
constexpr u32 SENT = 0xffffffffu;
constexpr int WU = 9216;
constexpr int LPK = 1024, PKS = 296;
constexpr int S0 = 3968, SS = 34944;
constexpr int SK = 16384, SV = SK + WU + 16;
constexpr int WQ = S0 + SS;
constexpr int LT = WQ + 9 * WU;
constexpr int LM = S0 + 4 * SS;
constexpr int LPHF = LM;
constexpr int LSUM = LPHF + 8192;
constexpr int LHALO = LM;
constexpr int KLX = LM + 11264;
constexpr int QLX = KLX + 2304;
constexpr int LSCR = QLX + 2048;
constexpr int VIS = 168;
constexpr int lvi(int br) { return br ? 1024 : LT; }
constexpr int LKX = 25600;
constexpr int wo_at(int br, int m) {
  if (br == 0) return m < 4 ? 1024 + m * WU : m < 6 ? LT + (m - 4) * WU : WQ + (m - 6) * WU;
  return m < 2 ? LT + m * WU : m == 2 ? LM : WQ + (m - 3) * WU;
}
constexpr int LOWN = WQ + 6 * WU;
constexpr int LDS_BYTES = 163840;
static_assert(LSCR + 512 <= LDS_BYTES && LPK + 2688 <= S0 && LPK + 128 * PKS <= WQ && LT + 128 * VIS <= LM && 1024 + 144 * VIS <= LKX && LKX + 8192 <= WQ &&
              SV + WU <= SS && 3 * WU <= SS && 7 * WU <= 2 * SS && S0 % 128 == 0 && SS % 128 == 0, "lds");

DEVI u32x2 tr_read(u32 a) { return __builtin_bit_cast(u32x2, __builtin_amdgcn_ds_read_tr16_b64_v4i16((LDS_AS s16x4*)a)); }
DEVI u32x4 frag_tr(u32 a0, u32 a1) { u32x2 x = tr_read(a0), y = tr_read(a1); return u32x4{x[0], x[1], y[0], y[1]}; }
DEVI u32x4 lds_r128(u32 a) { return *(LDS_AS u32x4*)a; }
DEVI u32x2 lds_r64(u32 a) { return *(LDS_AS u32x2*)a; }
DEVI u32 lds_r32(u32 a) { return *(LDS_AS u32*)a; }
DEVI u32 lds_r16(u32 a) { return *(LDS_AS u16*)a; }
DEVI void lds_w128(u32 a, u32x4 v) { *(LDS_AS u32x4*)a = v; }
DEVI void lds_w64(u32 a, u32x2 v) { *(LDS_AS u32x2*)a = v; }
DEVI void lds_w32(u32 a, u32 v) { *(LDS_AS u32*)a = v; }
DEVI f32x4 mfma32(u32x4 a, u32x4 b, f32x4 c) {
  return __builtin_amdgcn_mfma_f32_16x16x32_bf16(__builtin_bit_cast(bf16x8, a), __builtin_bit_cast(bf16x8, b), c, 0, 0, 0);
}
DEVI u32 pk(float a, float b) { f32x2 v = {a, b}; return __builtin_bit_cast(u32, __builtin_convertvector(v, bf16x2)); }
DEVI u32x2 pk4(f32x4 v) { return u32x2{pk(v[0], v[1]), pk(v[2], v[3])}; }
DEVI float max3(float a, float b, float c) { return fmaxf(fmaxf(a, b), c); }
DEVI u32 umax(u32 a, u32 b) { return a > b ? a : b; }
DEVI u32 umax3(u32 a, u32 b, u32 c) { return umax(umax(a, b), c); }
DEVI u32 umax4(u32 m, u32x4 v) { return umax3(umax3(m, v[0], v[1]), v[2], v[3]); }
DEVI u32 ns(u32 v) { return v < 0xfffffffeu ? v : 0xfffffffeu; }

DEVI void dma_full(u32 lds, u32 voff, __amdgpu_buffer_rsrc_t srd, u32 soff) {
  asm volatile("s_mov_b32 m0, %0\n\ts_nop 0\n\tbuffer_load_dwordx4 %1, %2, %3 offen lds" ::"s"(lds), "v"(voff), "s"(srd), "s"(soff) : "memory");
}
DEVI void dma_mask(u32 lds, u32 voff, __amdgpu_buffer_rsrc_t srd, u32 soff, u64 mask) {
  asm volatile("s_mov_b32 m0, %0\n\ts_mov_b64 exec, %4\n\tbuffer_load_dwordx4 %1, %2, %3 offen lds\n\ts_mov_b64 exec, -1" ::"s"(lds), "v"(voff), "s"(srd),
               "s"(soff), "s"(mask)
               : "memory");
}
template <int N>
DEVI void wait_bar() { asm volatile("s_waitcnt vmcnt(%0) lgkmcnt(0)\n\ts_barrier" ::"n"(N) : "memory"); }
template <int N>
DEVI void wait_vm_bar() { asm volatile("s_waitcnt vmcnt(%0)\n\ts_barrier" ::"n"(N) : "memory"); }
DEVI void bar_lds() { asm volatile("s_waitcnt lgkmcnt(0)\n\ts_barrier" ::: "memory"); }
DEVI void bar_only() { asm volatile("s_barrier" ::: "memory"); }

DEVI float redmax4(float a, float b, float c, float d, float ninf) {
  auto r1 = __builtin_amdgcn_permlane32_swap(__float_as_uint(a), __float_as_uint(b), false, false);
  auto r2 = __builtin_amdgcn_permlane32_swap(__float_as_uint(c), __float_as_uint(d), false, false);
  u32 r10 = r1[0], r11 = r1[1], r20 = r2[0], r21 = r2[1];
  float ab = max3(__uint_as_float(r10), __uint_as_float(r11), ninf);
  float cd = max3(__uint_as_float(r20), __uint_as_float(r21), ninf);
  auto r3 = __builtin_amdgcn_permlane16_swap(__float_as_uint(ab), __float_as_uint(cd), false, false);
  u32 r30 = r3[0], r31 = r3[1];
  return max3(__uint_as_float(r30), __uint_as_float(r31), ninf);
}
DEVI void bcast4(float t, float& a, float& b, float& c, float& d) {
  u32 tu = __float_as_uint(t);
  auto s1 = __builtin_amdgcn_permlane16_swap(tu, tu, false, false);
  u32 x = s1[0], y = s1[1];
  auto s2 = __builtin_amdgcn_permlane32_swap(x, x, false, false);
  auto s3 = __builtin_amdgcn_permlane32_swap(y, y, false, false);
  u32 s20 = s2[0], s21 = s2[1], s30 = s3[0], s31 = s3[1];
  a = __uint_as_float(s20); b = __uint_as_float(s21); c = __uint_as_float(s30); d = __uint_as_float(s31);
}

constexpr float LOG2E = 1.4426950408889634f;
constexpr u32 ONES = 0x3f803f80u;

struct HH {
  u32x4 f0, f1, f2, f3;
  u32x4 b0, b1;
  u32 bl;
  f32x4 T0, T1, TL, S;
  float a, b, c, d;
  f32x4 p0, q0, p1, q1;
  float l0, l1;
};
constexpr int HH_N = 106;
DEVI void swap32(float& a, float& b) {
  auto r = __builtin_amdgcn_permlane32_swap(__float_as_uint(a), __float_as_uint(b), false, false);
  u32 x = r[0], y = r[1]; a = __uint_as_float(x); b = __uint_as_float(y);
}
DEVI void swap16(float& a, float& b) {
  auto r = __builtin_amdgcn_permlane16_swap(__float_as_uint(a), __float_as_uint(b), false, false);
  u32 x = r[0], y = r[1]; a = __uint_as_float(x); b = __uint_as_float(y);
}
template <int I, bool QS>
DEVI void hh_step(HH& s, u32 lft, u32 phf, u32 sumf, bool lo, float ninf) {
  const f32x4 z = {0.f, 0.f, 0.f, 0.f};
  if constexpr (I == 0) {
    u32x2 x = lds_r64(lft); s.b0[2] = x[0]; s.b0[3] = x[1];
    asm volatile("" ::: "memory");
    u32x2 y = lds_r64(lft); s.b1[2] = y[0]; s.b1[3] = y[1];
    s.f0 = lds_r128(phf);
  } else if constexpr (I == 1) { s.f1 = lds_r128(phf + 1024); s.f2 = lds_r128(phf + 2048);
  } else if constexpr (I == 2) { s.f3 = lds_r128(phf + 3072);
  } else if constexpr (I == 3) { s.T0 = mfma32(s.f0, s.b0, z);
  } else if constexpr (I == 4) { s.TL = mfma32(s.f2, s.b0, z);
  } else if constexpr (I == 5) { s.T1 = mfma32(s.f1, s.b1, z);
  } else if constexpr (I == 6) { s.TL = mfma32(s.f3, s.b1, s.TL);
  } else if constexpr (I == 7) { s.f0 = lds_r128(sumf); s.f1 = lds_r128(sumf + 1024); s.f2 = lds_r128(sumf + 2048);
  } else if constexpr (I == 8) { s.a = max3(s.T0[0], s.T0[1], s.T0[2]);
  } else if constexpr (I == 9) { s.c = max3(-s.T0[0], -s.T0[1], -s.T0[2]);
  } else if constexpr (I == 10) { s.b = max3(s.T1[0], s.T1[1], s.T1[2]);
  } else if constexpr (I == 11) { s.d = max3(-s.T1[0], -s.T1[1], -s.T1[2]);
  } else if constexpr (I == 12) {
  } else if constexpr (I == 13) { s.a = max3(s.a, s.T0[3], s.TL[0]);
  } else if constexpr (I == 14) { s.b = max3(s.b, s.T1[3], s.TL[1]);
  } else if constexpr (I == 15) { s.c = max3(s.c, -s.T0[3], -s.TL[0]);
  } else if constexpr (I == 16) { s.d = max3(s.d, -s.T1[3], -s.TL[1]);
  } else if constexpr (I == 17) { swap32(s.a, s.b);
  } else if constexpr (I == 18) { swap32(s.c, s.d);
  } else if constexpr (I == 19) { s.a = max3(s.a, s.b, ninf);
  } else if constexpr (I == 20) { s.c = max3(s.c, s.d, ninf);
  } else if constexpr (I == 21) { swap16(s.a, s.c);
  } else if constexpr (I == 22) { s.a = max3(s.a, s.c, ninf);
  } else if constexpr (I == 23) { s.a *= -LOG2E;
  } else if constexpr (I == 24) { s.c = s.a; swap16(s.a, s.c);
  } else if constexpr (I == 25) { s.b = s.a; swap32(s.a, s.b);
  } else if constexpr (I == 26) { s.d = s.c; swap32(s.c, s.d);
  } else if constexpr (I == 27) { s.l0 = lo ? s.a : s.c;
  } else if constexpr (I == 28) { s.l1 = lo ? s.b : s.d;
  } else if constexpr (I == 29) { s.l0 = fmaf(s.TL[0], lo ? LOG2E : -LOG2E, s.l0);
  } else if constexpr (I == 30) { s.l1 = fmaf(s.TL[1], lo ? LOG2E : -LOG2E, s.l1);
  } else if constexpr (I == 31) { s.l0 = __builtin_amdgcn_exp2f(s.l0);
  } else if constexpr (I == 32) { s.l1 = __builtin_amdgcn_exp2f(s.l1);
  } else if constexpr (I == 33) { s.bl = pk(s.l0, s.l1);
  } else if constexpr (I == 34) { s.S = mfma32(s.f2, u32x4{s.bl, 0u, 0u, 0u}, z);
  } else if constexpr (I < 77) {
    constexpr int h = (I - 35) / 21, k = (I - 35) % 21;
    f32x4& T = h ? s.T1 : s.T0; f32x4& p = h ? s.p1 : s.p0; f32x4& q = h ? s.q1 : s.q0; u32x4& o = h ? s.b1 : s.b0;
    const float m = h ? s.b : s.a, n = h ? s.d : s.c;
    constexpr bool arg = k < 2 || ((k & 1) && k < 15);
    constexpr int e = arg ? (k < 2 ? k : (k + 1) / 2) : (k < 15 ? (k - 2) / 2 : 7);
    if constexpr (k < 16) {
      if constexpr (arg) { if constexpr (e < 4) p[e] = fmaf(T[e], LOG2E, m); else q[e - 4] = fmaf(T[e - 4], -LOG2E, n); }
      else { if constexpr (e < 4) p[e] = __builtin_amdgcn_exp2f(p[e]); else q[e - 4] = __builtin_amdgcn_exp2f(q[e - 4]); }
    } else if constexpr (k < 18) { o[k - 16] = pk(p[2 * (k - 16)], p[2 * (k - 16) + 1]);
    } else if constexpr (k < 20) { o[k - 16] = pk(q[2 * (k - 18)], q[2 * (k - 18) + 1]);
    } else { s.S = mfma32(h ? s.f1 : s.f0, o, s.S); }
  } else if constexpr (I == 77) {
  } else if constexpr (I == 78) { s.c = __builtin_amdgcn_rcpf(s.S[1]);
  } else if constexpr (I == 79) { s.d = __builtin_amdgcn_rcpf(s.S[3]);
  } else if constexpr (I == 80) { if constexpr (QS) s.c *= s.S[0]; else s.a = __builtin_amdgcn_rcpf(s.S[0]);
  } else if constexpr (I == 81) { if constexpr (QS) s.d *= s.S[2]; else s.b = __builtin_amdgcn_rcpf(s.S[2]);
  } else if constexpr (I < 106) {
    constexpr int v = (I - 82) / 6, k = (I - 82) % 6;
    if constexpr (!(QS && v >= 2)) {
      f32x4& x = v == 0 ? s.q0 : v == 1 ? s.q1 : v == 2 ? s.p0 : s.p1;
      u32x4& o = (v & 1) ? s.b1 : s.b0;
      const float r = v == 0 ? s.c : v == 1 ? s.d : v == 2 ? s.a : s.b;
      if constexpr (k < 4) x[k] *= r;
      else o[(v < 2 ? 2 : 0) + k - 4] = pk(x[2 * (k - 4)], x[2 * (k - 4) + 1]);
    }
  }
}
template <bool QS>
DEVI void hh_left(HH& s, bool lo) {
  if constexpr (QS) s.bl = pk(s.l0 * (lo ? 1.f : s.c), s.l1 * (lo ? 1.f : s.d));
  else s.bl = pk(s.l0 * (lo ? s.a : s.c), s.l1 * (lo ? s.b : s.d));
}

#define L2ST 40
#define L2ST2 72
#define PRE 7
#define PRO 2
#define LEAD 8
#define QA 2
#define QT 1
#define E_POB 30
#define Q_L1 10
#define Q_L1C 16
#define QTB QT
#define NOPT 0
#define PCW 1
#define PCW0 0
#define PP 34
#define QW 1
#define HQ0 4
#define HQS 8
#define C0 0
#define CS 2
#define C1 44
#define E_PUB 11
#define QB 1
#define QSP 1
#define HB 3
constexpr int EM = 36, EQ = 44, EH = 62, NEV = 74;
constexpr int Q_SLAB = 2;
#define E_PUBQ 9
#define E_PUBB 15
#define E_FLDB 17
constexpr int E_PUB2 = C1 < 34 ? E_PUBB : EQ + E_PUBQ;
static_assert(C1 >= 34 || ((E_PUBB & 3) == 3 && E_PUBB > C1 + 2 && E_PUBB < 35), "marks");
constexpr int e_fld(int br) { return br ? EQ + E_FLDB : E_FLD; }
constexpr int e_fchk(int br) { return br ? (E_FLDB > 12 ? EH + 4 : EQ + 16) : EQ; }
constexpr int clr_at(int E) { return E >= C1 && E < C1 + 3 ? 4 + E - C1 : E >= C0 && (E - C0) % CS == 0 && (E - C0) / CS < 4 ? (E - C0) / CS : -1; }
constexpr bool is_clr(int E) { return clr_at(E) >= 0; }
static_assert((E_PUB & 3) == 3 && E_PUB > C0 + 3 * CS && E_PUB < 35 && E_FLD >= EM && E_FLD < EQ && C1 > C0 + 3 * CS && (C1 < 34 || C1 >= EQ) && C1 < EQ + 6, "marks");
struct Sched {
  int op[NEV][3];
  int nop[NEV];
  int pos_ev[NEV + 1];
  int pos_hook[NEV];
  int last[36];
  int total;
};
constexpr int ext_ops(int br, int E) {
  return (E == (br ? E_POB : E_PO)) + (br && is_clr(E)) + (E == e_fld(br)) +
         (br ? 0 : E == EQ + Q_SLAB ? 2 : E == EQ + Q_L1 ? 4 : E == EQ + Q_L1C ? 1 : 0);
}
constexpr int op_chunk(int op) { return op < 200 ? (op - 100) >> 2 : op < 236 ? ((op - 200) >> 1) % 9 : (op - 200) >> 1; }
constexpr int op_earliest(int br, int op) {
  const int n = op_chunk(op);
  if (n < 9) return 4 * n - 1 - LEAD;
  if (n < 27) return n < 21 ? 23 : n < 25 ? 27 : 31;
  const int m = n - 27;
  if (br == 0) return m < 6 ? EQ + 3 : EQ + 6;
  return m < 2 ? 31 : m == 2 ? EQ : EQ + 2 * (m - 3) + 3;
}
constexpr int op_deadline(int br, int op) {
  const int n = op_chunk(op);
  if (n < 9) return 4 * n - 1;
  if (n < 27) return br == 0 || n == 18 ? EQ : EQ + 2 * (n - 18) - 1;
  return NEV;
}
static_assert(LEAD <= 12, "ring");
constexpr Sched make_sched(int br) {
  Sched s{};
  const int nx = br ? 3 : 2;
  int list[128] = {}, when[128] = {}, n = 0;
  for (int c = 0; c < 9; ++c) {
    for (int i = 0; i < nx; ++i) list[n++] = 100 + 4 * c + i;
    list[n++] = 200 + 2 * c; list[n++] = 201 + 2 * c; list[n++] = 218 + 2 * c; list[n++] = 219 + 2 * c;
  }
  for (int c = 18; c < 36; ++c) { list[n++] = 200 + 2 * c; list[n++] = 201 + 2 * c; }
  for (int k = 0; k < n; ++k) when[k] = 1000;
  int pos = 0, done = 0;
  for (; done < PRO * (nx + 4); ++done) { s.last[op_chunk(list[done])] = pos++; when[done] = -1; if (done == nx + 3) pos += 1 + (PCW == 2); }
  pos += PCW == 3;
  for (int E = 0; E < NEV; ++E) {
    s.pos_ev[E] = pos;
    int j = 0;
    for (int k = 0; k < n; ++k) {
      if (when[k] != 1000 || op_earliest(br, list[k]) > E) continue;
      const int c = op_chunk(list[k]);
      int q = E < EM ? (c < 9 ? QA : br ? QTB : QT) : E < EQ && br ? 0 : ext_ops(br, E) ? 1 : 2;
      if (br == 0 && E >= EQ) q = ext_ops(br, E) || (E < EH && E % QSP) ? 0 : 1;
      if (br == 1 && E >= EQ && c >= 27) q = E >= EH + 4 ? 0 : E >= EH ? HB : ext_ops(br, E) ? QB - 1 : QB;
      if (j >= q) break;
      s.op[E][j++] = list[k]; when[k] = E; ++done;
      s.last[c] = pos++;
      if (E >= op_deadline(br, list[k])) s.total = -1000;
    }
    s.nop[E] = j;
    s.pos_hook[E] = pos;
    pos += ext_ops(br, E);
  }
  s.pos_ev[NEV] = pos;
  s.total += done == n ? pos : -1000;
  return s;
}
template <int BR> struct SC { static constexpr Sched s = make_sched(BR); };
static_assert(SC<0>::s.total > 0 && SC<1>::s.total > 0, "schedule incomplete");
constexpr int imin(int a, int b) { return a < b ? a : b; }
template <int BR> constexpr int wait_n(int E, int c) {
  int n = SC<BR>::s.pos_ev[E] - SC<BR>::s.last[c] - 1;
  if (BR == 1 && E == E_PUB) n = imin(n, SC<BR>::s.pos_ev[E] - SC<BR>::s.pos_ev[C0 + 3 * CS + 1]);
  if (BR == 1 && E == E_PUB2) n = imin(n, SC<BR>::s.pos_ev[E] - SC<BR>::s.pos_ev[C1 + 3]);
  if (BR == 0 && E == EQ) n = imin(n, SC<BR>::s.pos_ev[E] - SC<BR>::s.pos_ev[E_FLD + 1]);
  return n;
}
template <int BR> constexpr int wait_h(int E, int E0) { return SC<BR>::s.pos_hook[E] - SC<BR>::s.pos_ev[E0 + 1]; }

#define STAMP(i)
#define RSTAMP(i)
#define STAMP_FLUSH()
#define STAMP_DECL

DEVI u64 next_tag(u32x2 e) {
  const u64 E = ((u64)(u32)__builtin_amdgcn_readfirstlane(e[1]) << 32) | (u32)__builtin_amdgcn_readfirstlane(e[0]);
  return E * 6364136223846793005ull + 1442695040888963407ull;
}
DEVI void poll_flags(GLB_AS u64* f, u64 tag) {
  for (int spin = 0; spin < (1 << 20); ++spin) {
    u64 v = __hip_atomic_load(f, __ATOMIC_RELAXED, __HIP_MEMORY_SCOPE_AGENT);
    if (__all(v == tag)) break;
    __builtin_amdgcn_s_sleep(1);
  }
  asm volatile("" ::: "memory");
}

#define ROT (rb + 2 * hq) % 9
typedef u64 u64x4 __attribute__((ext_vector_type(4)));
DEVI void first(bool br, int rb, int hq, int jx, u32 lb, u64x4& ka, u64x4& kb, u64& kc_) {
  asm volatile("s_load_dwordx8 %0, %3, 0x0\n\ts_load_dwordx8 %1, %3, 0x20\n\ts_load_dwordx2 %2, %3, 0x40"
               : "=&s"(ka), "=&s"(kb), "=&s"(kc_)
               : "s"(__builtin_amdgcn_kernarg_segment_ptr()));
  const int tid = threadIdx.x, l = tid & 63, w = __builtin_amdgcn_readfirstlane(tid >> 6);
  u64 pc;
  int f0, t0, f1, t1;
  asm volatile("s_getpc_b64 %0\n.Lhk_pc:\n\ts_mov_b32 %1, .Lhk_from_0-.Lhk_pc\n\ts_max_i32 %2, .Lhk_to_0-.Lhk_pc, 0\n\t"
               "s_mov_b32 %3, .Lhk_from_1-.Lhk_pc\n\ts_max_i32 %4, .Lhk_to_1-.Lhk_pc, 0"
               : "=s"(pc), "=s"(f0), "=s"(t0), "=s"(f1), "=s"(t1)::"scc");
  __amdgpu_buffer_rsrc_t srdC = __builtin_amdgcn_make_buffer_rsrc((void*)pc, (short)0, (br ? t1 : t0) & ~3, 0x00020000);
  const u32 po = l == 0 ? (u32)((br ? f1 : f0) + (8 * jx + w) * 128) : OOB;
  asm volatile("s_mov_b32 m0, %0\n\ts_nop 0\n\tbuffer_load_dword %1, %2, 0 offen lds" ::"s"(lb + LSCR), "v"(po), "s"(srdC) : "memory");
  const int lr = (l * 57) >> 9, lu = l - lr * 9;
  int kc = ROT;
  u32 vx[2], vq[2], vh, vp = (u32)(hq * 2592 + (w * 21 + l) * 16), le = lb + (br ? LHALO : LPK) + w * (br ? 256 : 336);
  {
    const int row = 2 * w + ((l >> 3) & 1);
    int grow = rb * 128 - 16 + row; grow = grow < 0 ? 0 : grow;
    vh = (u32)(grow * XROW + (((l & 7) ^ ((row >> 1) & 7)) << 4) + kc * 128);
  }
#pragma unroll
  for (int i = 0; i < 2; ++i) {
    const int row = 16 * w + 8 * i + (l >> 3);
    vx[i] = (u32)((rb * 128 + row) * XROW + (((l & 7) ^ ((row >> 1) & 7)) << 4));
    vq[i] = (u32)((8 * w + 4 * (lr & 1) + 2 * i + ((lr >> 1) & 1)) * WROW + hq * 144 + lu * 16);
  }
  asm volatile("" : "+v"(vx[0]), "+v"(vx[1]), "+v"(vq[0]), "+v"(vq[1]), "+v"(vh), "+v"(vp), "+s"(le), "+s"(kc));
  asm volatile("s_waitcnt lgkmcnt(0)" : "+s"(ka), "+s"(kb), "+s"(kc_) : "s"(__builtin_amdgcn_kernarg_segment_ptr()));
  const u64 XA = ka[0], WA = ka[1], PHI = ka[2], XB = kb[0], WB = kb[1];
  __amdgpu_buffer_rsrc_t srdX = __builtin_amdgcn_make_buffer_rsrc((void*)(br ? XB : XA), (short)0, 2048 * XROW, 0x00020000);
  __amdgpu_buffer_rsrc_t srdW = __builtin_amdgcn_make_buffer_rsrc((void*)(br ? WB : WA), (short)0, 576 * WROW, 0x00020000);
  __amdgpu_buffer_rsrc_t srdE = __builtin_amdgcn_make_buffer_rsrc((void*)(br ? XB : PHI), (short)0, br ? 2048 * XROW : 32 * 648, 0x00020000);
  dma_mask(le, br ? vh : vp, srdE, 0u, br ? 0xffffull : (1ull << 21) - 1);
#pragma unroll
  for (int i = 0; i < 2; ++i) dma_full(lb + S0 + w * 2048 + i * 1024, vx[i], srdX, (u32)(kc * 128));
#pragma unroll
  for (int n = 0; n < 4; ++n)
    dma_mask(lb + S0 + (n < 2 ? SK : SV) + (2 * w + (n & 1)) * 576, vq[n & 1], srdW, (u32)(kc * 64 * WROW + (n < 2 ? 1152 : 2304)), (1ull << 36) - 1);
}

template <int BR>
DEVI void body(const char* __restrict__ X, const char* __restrict__ W, const char* __restrict__ PHI, const char* __restrict__ WO, char* ws, char* out,
               int rb, int hq, int jx, u32 lb, u64 tent, u64 rent) {
  asm volatile(".Lhk_from_%0:" ::"n"(BR));
  int tid = threadIdx.x;
  asm volatile("" : "+v"(tid));
  int l = tid & 63;
  const int w = __builtin_amdgcn_readfirstlane(tid >> 6);
  const int rg = w & 3, hpl = w >> 2;
  int c = l & 15, g = l >> 4, q = c >> 2, p = c & 3, g0 = g & 1, g1 = g >> 1;
  int lr = (l * 57) >> 9, lu = l - lr * 9;
  auto refresh = [&]() {
    asm volatile("" : "+v"(tid));
    l = tid & 63; c = l & 15; g = l >> 4; q = c >> 2; p = c & 3; g0 = g & 1; g1 = g >> 1;
    lr = (l * 57) >> 9; lu = l - lr * 9;
  };
  STAMP_DECL
  STAMP(0);
  RSTAMP(30);
  __amdgpu_buffer_rsrc_t srdX = __builtin_amdgcn_make_buffer_rsrc((void*)X, (short)0, 2048 * XROW, 0x00020000);
  __amdgpu_buffer_rsrc_t srdW = __builtin_amdgcn_make_buffer_rsrc((void*)W, (short)0, 576 * WROW, 0x00020000);
  const int rot = ROT;
  constexpr u64 M36 = (1ull << 36) - 1;

  u32 vx[2], vq[2], vh = 0;
#pragma unroll
  for (int i = 0; i < 2; ++i) {
    int row = 16 * w + 8 * i + (l >> 3);
    vx[i] = (u32)((rb * 128 + row) * XROW + (((l & 7) ^ ((row >> 1) & 7)) << 4));
  }
#pragma unroll
  for (int i = 0; i < 2; ++i) vq[i] = (u32)((8 * w + 4 * (lr & 1) + 2 * i + ((lr >> 1) & 1)) * WROW + hq * 144 + lu * 16);
  if (BR) {
    int row = 2 * w + ((l >> 3) & 1);
    int grow = rb * 128 - 16 + row; grow = grow < 0 ? 0 : grow;
    vh = (u32)(grow * XROW + (((l & 7) ^ ((row >> 1) & 7)) << 4));
  }
  auto kc_of = [&](int cc) { int k = cc + rot; return k >= 9 ? k - 9 : k; };
  __amdgpu_buffer_rsrc_t srdO = __builtin_amdgcn_make_buffer_rsrc((void*)WO, (short)0, 576 * XROW, 0x00020000);
  auto dma_op = [&](auto OP) {
    constexpr int op = OP.value;
    if constexpr (op < 200) {
      constexpr int cc = (op - 100) >> 2, i = op & 3;
      const int kc = kc_of(cc);
      if constexpr (i < 2) dma_full(lb + S0 + (cc & 3) * SS + w * 2048 + i * 1024, vx[i], srdX, (u32)(kc * 128));
      else dma_mask(lb + LHALO + (cc & 3) * 2048 + w * 256, vh, srdX, (u32)(kc * 128), 0xffffull);
    } else {
      constexpr int n = (op - 200) >> 1, i = op & 1;
      constexpr int at = n < 9 ? S0 + (n & 3) * SS + SK : n < 18 ? S0 + ((n - 9) & 3) * SS + SV : n < 27 ? WQ + (n - 18) * WU : wo_at(BR, n - 27);
      const u32 dst = lb + at + (2 * w + i) * 576;
      if constexpr (n < 27) {
        const int kc = kc_of(n % 9), sec = n < 9 ? 1152 : (n < 18 ? 2304 : 0);
        dma_mask(dst, vq[i], srdW, (u32)(kc * 64 * WROW + sec), M36);
      } else {
        const int hi = lr & 1, qq = 2 * i + ((lr >> 1) & 1);
        if constexpr (n > 27)
          dma_mask(dst, (u32)((36 * hpl + 20 * hi + 4 * rg + qq) * XROW + hq * 144 + lu * 16), srdO, (u32)(((n - 28 + hq) & 7) * 72 * XROW), M36);
        else dma_mask(dst, (u32)((36 * (8 * hpl + 2 * rg + hi) + 16 + qq) * XROW + hq * 144 + lu * 16), srdO, 0u, M36);
      }
    }
  };
  auto ev_dma = [&](auto EE, auto II) {
    if constexpr (II.value < SC<BR>::s.nop[EE.value]) dma_op(IC<SC<BR>::s.op[EE.value][II.value]>{});
  };
  auto warm_wo = [&]() {
    const int idx = w * 21 + l;
    const u32 po = l < 21 && idx < 162 ? (u32)((162 * jx + idx) * 128) : OOB;
    asm volatile("s_mov_b32 m0, %0\n\ts_nop 0\n\tbuffer_load_dword %1, %2, 0 offen lds" ::"s"(lb + LSCR), "v"(po), "s"(srdO) : "memory");
  };
  __amdgpu_buffer_rsrc_t srdF = __builtin_amdgcn_make_buffer_rsrc((void*)(ws + WS_OF), (short)0, 256 * 18 * 1024, 0x00020000);
  __amdgpu_buffer_rsrc_t srdS = __builtin_amdgcn_make_buffer_rsrc((void*)(ws + WS_SLAB), (short)0, 136 * SLAB, 0x00020000);
  auto mark = [&](auto EE) {
    constexpr int e = EE.value, f = (e + 2) % 3;
    const u32x4 sv = {SENT, SENT, SENT, SENT};
    const u32 ob = (u32)(((e > 3 ? 128 : 0) + rb * 8 + w) * 18 * 1024);
    if constexpr (e > 0 && f < 2) __builtin_amdgcn_raw_buffer_store_b128(sv, srdF, l * 16, ob + hq * 2048 + f * 1024, 16);
    if constexpr (e > 0 && f == 2) __builtin_amdgcn_raw_buffer_store_b128(sv, srdF, g == 0 ? c * 16 : OOB, ob + (16 + (hq >> 2)) * 1024 + (hq & 3) * 256, 16);
    if constexpr (e == 0) {
      const u32 so = tid < 400 ? (u32)((hq * 16 + rb) * SLAB + tid * 16) : tid < 425 ? (u32)((128 + hq) * SLAB + rb * 400 + (tid - 400) * 16) : OOB;
      __builtin_amdgcn_raw_buffer_store_b128(sv, srdS, so, 0, 16);
    }
  };
  GLB_AS u64* kflags = (GLB_AS u64*)(ws + WS_FLAG + hq * 256);
  GLB_AS u64* oflags = (GLB_AS u64*)(ws + WS_OFLAG + (BR * 16 + rb) * 256);
  u32x2 ep, fv;
  asm volatile("" ::"s"(srdX), "s"(srdW));
  STAMP(16);
  static_assert(PRO == 2, "prologue");
  auto dma_chunk = [&](auto cc) {
    constexpr int c_ = cc.value;
    dma_op(IC<100 + 4 * c_>{}); dma_op(IC<101 + 4 * c_>{});
    if constexpr (BR) dma_op(IC<102 + 4 * c_>{});
    dma_op(IC<200 + 2 * c_>{}); dma_op(IC<201 + 2 * c_>{}); dma_op(IC<218 + 2 * c_>{}); dma_op(IC<219 + 2 * c_>{});
  };
  {
    const char* ea = ws + WS_EPOCH;
    asm volatile("global_load_dwordx2 %0, %1, off sc1" : "=v"(ep) : "v"(ea) : "memory");
  }
  STAMP(17);
  if constexpr (BR == 0) {
    wait_vm_bar<7 + (PCW == 2)>();
    STAMP(18);
    const int h = rg & 1, lf = rg >> 1;
    const int f = lf ? 16 + (q & 1) : c;
    const bool rowok = lf ? (p == h) : true;
    const u32 pb = lb + LPK + (hpl * 2 + h) * 648 + f * 2;
    const int d0 = 2 * h + 4 * g;
    u32 e0 = lds_r16(pb + (d0 + 0) * 36), e1 = lds_r16(pb + (d0 + 1) * 36), e2 = lds_r16(pb + (d0 + 2) * 36), e3 = lds_r16(pb + (d0 + 3) * 36);
    const int dl = h ? 0 : 16;
    u32 l0 = lds_r16(pb + dl * 36), l1 = lds_r16(pb + (dl + 1) * 36);
    SCHED_FENCE();
    dma_op(IC<104>{}); dma_op(IC<105>{});
    SCHED_FENCE();
    u32 lw = (g == 0) ? (l0 | (l1 << 16)) : 0u;
    u32x4 fr = {e0 | (e1 << 16), e2 | (e3 << 16), h ? 0u : lw, h ? lw : 0u};
    if (!rowok) fr = u32x4{0, 0, 0, 0};
    lds_w128(lb + LPHF + (hpl * 4 + rg) * 1024 + l * 16, fr);
    if (w < 3) {
      u32 a = (p == 2 * w) ? ONES : 0u, b = (p == 2 * w + 1) ? ONES : 0u;
      u32 x = ((p & 1) == g1) ? ((p >> 1) ? 0x3f800000u : 0x00003f80u) : 0u;
      lds_w128(lb + LSUM + w * 1024 + l * 16, w < 2 ? u32x4{a, a, b, b} : u32x4{x, 0u, 0u, 0u});
    }
    SCHED_FENCE();
    dma_op(IC<202>{}); dma_op(IC<203>{}); dma_op(IC<220>{}); dma_op(IC<221>{});
  } else {
    bar_only();
    STAMP(18);
    dma_chunk(IC<1>{});
  }
  STAMP(1);

  const u32 xa0 = lb + S0 + (rg * 32 + hpl * 16 + c) * 128 + ((g ^ ((c >> 1) & 7)) << 4);
  const u32 xa1 = xa0 ^ 64;
  const u32 xb0 = xa0 + 2048 - hpl * 4096, xb1 = xb0 ^ 64;
  const u32 xh0 = lb + LHALO + c * 128 + ((g ^ ((c >> 1) & 7)) << 4);
  const u32 xh1 = xh0 ^ 64;
  const u32 wrow = lb + (16 * g1 + 8 * g0 + 2 * q) * 144;
  const u32 wa = wrow + hpl * 72 + 8 * p;
  const u32 wl = wrow + 32 + (p & 1) * 72 + (p >> 1) * (SV - SK);
  const u32 wh = wa + (rg & 1) * 40 + (rg >> 1) * (SV - SK);
  float ninf = -__builtin_inff();
  asm volatile("" : "+s"(ninf));

  u32x4 xf[18][2];
  const f32x4 z4 = {0.f, 0.f, 0.f, 0.f};
  f32x4 k0[2] = {z4, z4}, k1[2] = {z4, z4}, a0[2] = {z4, z4}, a1[2] = {z4, z4}, aL = z4, aH = z4, aHL = z4;
  u32x4 f0, f1, fl, fh, xh;
  auto load_kv_frags = [&](auto HH_, auto JJ) {
    constexpr int H = HH_.value, j = JJ.value, T = H >> 1, n = T >> 1, ks = T & 1;
    constexpr u32 ok = S0 + (n & 3) * SS + SK + ks * 32 * 144, ov = ok + SV - SK;
    if constexpr (H < 36) {
      if constexpr ((H & 1) == 0) {
        if constexpr (j == 0) { xf[T][0] = lds_r128((ks ? xa1 : xa0) + (n & 3) * SS); f0 = frag_tr(wa + ok, wa + ok + 144); }
        if constexpr (j == 1) { xf[T][1] = lds_r128((ks ? xb1 : xb0) + (n & 3) * SS); f1 = frag_tr(wa + ok + 40, wa + ok + 184); }
      } else {
        if constexpr (j == 0) f0 = frag_tr(wa + ov, wa + ov + 144);
        if constexpr (j == 1) f1 = frag_tr(wa + ov + 40, wa + ov + 184);
        if constexpr (j == 2) fl = frag_tr(wl + ok, wl + ok + 144);
        if constexpr (j == 3 && BR == 1) { fh = frag_tr(wh + ok, wh + ok + 144); xh = lds_r128((ks ? xh1 : xh0) + (n & 3) * 2048); }
      }
    }
  };
  wait_bar<wait_n<BR>(0, 0)>();
  STAMP(2);
  load_kv_frags(IC<0>{}, IC<0>{}); load_kv_frags(IC<0>{}, IC<1>{});
  u64 tag = 0;
  static_for<0, 36>([&](auto hh_) {
    constexpr int H = hh_.value, T = H >> 1;
    if constexpr ((H & 3) == 3 && H < 35) {
      wait_bar<wait_n<BR>(H, (H + 1) >> 2)>();
    }
    u32x4 c0 = f0, c1 = f1, cl = fl, ch = fh, cx = xh;
    f32x4(&d0)[2] = (H & 1) ? a0 : k0;
    f32x4(&d1)[2] = (H & 1) ? a1 : k1;
    SCHED_FENCE();
    d0[0] = mfma32(c0, xf[T][0], d0[0]);
    load_kv_frags(IC<H + 1>{}, IC<0>{});
    SCHED_FENCE();
    d0[1] = mfma32(c0, xf[T][1], d0[1]);
    load_kv_frags(IC<H + 1>{}, IC<1>{});
    ev_dma(hh_, IC<0>{});
    SCHED_FENCE();
    d1[0] = mfma32(c1, xf[T][0], d1[0]);
    load_kv_frags(IC<H + 1>{}, IC<2>{});
    SCHED_FENCE();
    d1[1] = mfma32(c1, xf[T][1], d1[1]);
    load_kv_frags(IC<H + 1>{}, IC<3>{});
    ev_dma(hh_, IC<1>{});
    ev_dma(hh_, IC<2>{});
    SCHED_FENCE();
    if constexpr (H & 1) {
      aL = mfma32(cl, xf[T][0], aL);
      asm volatile("" : "+v"(aL));
      if constexpr (BR == 1) {
        aH = mfma32(ch, cx, aH); aHL = mfma32(cl, cx, aHL);
        asm volatile("" : "+v"(aH), "+v"(aHL));
      }
    }
    if constexpr (H == (BR ? E_POB : E_PO)) warm_wo();
    if constexpr (H == 3) {
      asm volatile("" : "+v"(ep));
      tag = next_tag(ep);
    }
    if constexpr (BR == 1 && is_clr(H)) mark(IC<clr_at(H)>{});
    if constexpr (BR == 1 && H == E_PUB) {
      if (tid < 2) __hip_atomic_store(tid == 0 ? kflags + rb : oflags + hq - 512, tag, __ATOMIC_RELAXED, __HIP_MEMORY_SCOPE_AGENT);
    }
    if constexpr (BR == 1 && H == E_PUB2) {
      if (tid == 64) __hip_atomic_store(oflags + hq, tag, __ATOMIC_RELAXED, __HIP_MEMORY_SCOPE_AGENT);
    }
    SCHED_FENCE();
  });
  STAMP(3);
  refresh();
  const u32 wqa = lb + WQ + (16 * g1 + 8 * g0 + 2 * q) * 144 + hpl * 72 + 8 * p;
  const u32 wql = lb + WQ + (16 * g1 + 8 * g0 + 2 * q) * 144 + 32 + (p & 1) * 72;
  auto load_q_frags = [&](auto TT, auto JJ) {
    constexpr int t = TT.value, j = JJ.value;
    constexpr u32 o = (t >> 1) * WU + (t & 1) * 32 * 144;
    if constexpr (t < 18) {
      if constexpr (j == 0) f0 = frag_tr(wqa + o, wqa + o + 144);
      if constexpr (j == 1) f1 = frag_tr(wqa + o + 40, wqa + o + 184);
      if constexpr (j == 2) fl = frag_tr(wql + o, wql + o + 144);
    }
  };
  if constexpr (BR == 1) {
    wait_bar<wait_n<BR>(EQ, 18)>();
    static_for<0, 3>([&](auto j) { load_q_frags(IC<0>{}, j); });
  }
  u32x2 kM[2][2];
#pragma unroll
  for (int sb = 0; sb < 2; ++sb) { kM[0][sb] = pk4(k0[sb]); kM[1][sb] = pk4(k1[sb]); }
  auto unswap = [&](u32x2& a, u32x2& b) {
    u32x2 x = a, y = b;
#pragma unroll
    for (int e = 0; e < 2; ++e) { a[e] = hpl ? y[e] : x[e]; b[e] = hpl ? x[e] : y[e]; }
  };
  {
    constexpr int VROW0 = BR ? 16 : 0;
    auto left = [&](f32x4 v, int blk) {
      u32x2 x = pk4(v);
      const u32 va = lb + lvi(BR) + (VROW0 - 16 + blk * 16 + c) * VIS + g0 * 80 + 64;
      lds_w64(g1 ? va : lb + KLX + (blk * 2 + g0) * 128 + c * 8, u32x2{x[0], g1 ? ONES : x[1]});
      lds_w64(g1 ? va + 8 : lb + LSCR + 256 + c * 8, u32x2{x[1], ONES});
    };
    left(aL, 1 + 2 * rg + hpl);
#pragma unroll
    for (int sb = 0; sb < 2; ++sb) {
      const u32 va = lb + lvi(BR) + (VROW0 + rg * 32 + (sb ^ hpl) * 16 + c) * VIS + hpl * 80;
      lds_w64(va + 8 * g, pk4(a0[sb]));
      lds_w64(va + 32 + 8 * g, pk4(a1[sb]));
    }
    if constexpr (BR == 1) {
      unswap(kM[0][0], kM[0][1]); unswap(kM[1][0], kM[1][1]);
      if (w == 2) left(aHL, 0);
      const u32 kx = lb + LKX + l * 8;
      lds_w64(rg < 2 ? kx + (hpl * 2 + rg) * 512 : lb + lvi(BR) + c * VIS + hpl * 80 + (rg & 1) * 32 + 8 * g, pk4(aH));
      if (rg < 3) {
        const u32 b = kx + (((rg + 1) * 2 + hpl) * 2) * 512;
        lds_w64(b, kM[0][1]); lds_w64(b + 512, kM[1][1]);
      }
    }
  }
  const u32 phf = lb + LPHF + hpl * 4096 + l * 16, sumf = lb + LSUM + l * 16;
  const bool lo = g < 2;
  auto load_flags = [&]() {
    GLB_AS u64* fa = (BR == 0 && g == 0) ? kflags + c : oflags + (l & 7);
    asm volatile("global_load_dwordx2 %0, %1, off sc1" : "=v"(fv) : "v"(fa) : "memory");
  };
  auto check_flags = [&]() {
    if (!__all((((u64)fv[1] << 32) | fv[0]) == tag)) poll_flags((BR == 0 && g == 0) ? kflags + c : oflags + (l & 7), tag);
  };
  if constexpr (BR == 0) {
    bar_lds();
    STAMP(4);
    HH h0, h1;
    h0.b0[0] = kM[0][0][0]; h0.b0[1] = kM[0][0][1]; h0.b1[0] = kM[1][0][0]; h0.b1[1] = kM[1][0][1];
    h1.b0[0] = kM[0][1][0]; h1.b0[1] = kM[0][1][1]; h1.b1[0] = kM[1][1][0]; h1.b1[1] = kM[1][1][1];
    const u32 kl0 = lb + KLX + ((1 + 2 * rg + hpl) * 2 + hpl) * 128 + c * 8, kl1 = lb + KLX + ((2 + 2 * rg - hpl) * 2 + hpl) * 128 + c * 8;
    static_for<0, HH_N>([&](auto st) {
      if constexpr (st.value >= 8 && (st.value - 8) % 12 == 0 && (st.value - 8) / 12 < EQ - EM) {
        constexpr int E = EM + (st.value - 8) / 12;
        SCHED_FENCE();
        ev_dma(IC<E>{}, IC<0>{}); ev_dma(IC<E>{}, IC<1>{});
        if constexpr (E == E_PO) warm_wo();
        if constexpr (E == E_FLD) load_flags();
        SCHED_FENCE();
      }
      hh_step<st.value, false>(h0, kl0, phf, sumf, lo, ninf);
      hh_step<st.value, false>(h1, kl1, phf, sumf, lo, ninf);
    });
#pragma unroll
    for (int sb = 0; sb < 2; ++sb) {
      HH& hs = sb ? h1 : h0;
      hh_left<false>(hs, lo);
      const u32 pa = lb + LPK + (rg * 32 + (sb ^ hpl) * 16 + c) * PKS + hpl * 144;
      lds_w64(pa + 8 * g, u32x2{hs.b0[0], hs.b0[1]});
      lds_w64(pa + 32 + 8 * g, u32x2{hs.b0[2], hs.b0[3]});
      lds_w64(pa + 64 + 8 * g, u32x2{hs.b1[0], hs.b1[1]});
      lds_w64(pa + 96 + 8 * g, u32x2{hs.b1[2], hs.b1[3]});
      lds_w32(pa + 128 + 4 * g, hs.bl);
    }
    STAMP(5);
    wait_bar<wait_n<BR>(EQ, 26)>();
    asm volatile("" : "+v"(fv));
    static_for<0, 3>([&](auto j) { load_q_frags(IC<0>{}, j); });
  }
  refresh();
  f32x4 dp = z4, dm = z4, df = z4;
  u32x4 ap[4], am[4], af[4], bv[4];
  u32 g0_, g1_, g2_, g3_;
  const int rr = 4 * (4 * g0 + q) + 2 * g1;
  const u32 pa = lb + LPK + rr * PKS + hpl * 144 + (rg & 1) * 64 + 8 * p;
  const u32 pf = lb + LPK + rr * PKS + hpl * 144 + 128 + 8 * (p >> 1);
  const u32 va = lb + lvi(BR) + rr * VIS + hpl * 80 + (rg < 2 ? rg * 32 + 8 * p : 64 + 8 * (p & 1));
  auto hook = [&](auto tt, auto jj) {
    constexpr int t = tt.value, E = EQ + t, j = jj.value;
    if constexpr (BR == 0 && j < 4) {
      if constexpr (t == Q_SLAB - 2) {
        ap[j] = frag_tr(pa + j * 32 * PKS, pa + j * 32 * PKS + PKS);
        am[j] = frag_tr(pa + 32 + j * 32 * PKS, pa + 32 + j * 32 * PKS + PKS);
        af[j] = frag_tr(pf + j * 32 * PKS, pf + j * 32 * PKS + PKS);
        bv[j] = frag_tr(va + j * 32 * VIS, va + j * 32 * VIS + VIS);
      }
      if constexpr (t == Q_SLAB - 1) {
        dp = mfma32(ap[j], bv[j], dp); dm = mfma32(am[j], bv[j], dm); df = mfma32(af[j], bv[j], df);
        asm volatile("" : "+v"(dp), "+v"(dm), "+v"(df));
      }
    }
    if constexpr (j != 4) return;
    if constexpr (BR == 1 && is_clr(E)) mark(IC<clr_at(E)>{});
    if constexpr (BR == 1 && E == E_PUB2) {
      if (tid == 0) __hip_atomic_store(oflags + hq, tag, __ATOMIC_RELAXED, __HIP_MEMORY_SCOPE_AGENT);
    }
    if constexpr (BR == 1 && E == e_fld(BR)) load_flags();
    if constexpr (E == e_fchk(BR)) {
      if constexpr (BR == 1) asm volatile("s_waitcnt vmcnt(%1)" : "+v"(fv) : "n"(wait_h<BR>(E, e_fld(BR))) : "memory");
      check_flags();
    }
    if constexpr (BR == 0) {
      if constexpr (t == Q_SLAB) {
        const u32 slab = (u32)((hq * 16 + rb) * SLAB);
        const bool h0set = (0x47 >> (c & 7)) & 1, h = rg & 1, own = (c >> 3) == hpl;
        u32x2 a = pk4(dp), b = pk4(dm);
        const u32 o1 = rg < 2 ? slab + (hpl * 2 + rg) * 1024 + l * 16 : (own && h0set != h) ? slab + 4096 + l * 16 : OOB;
        __builtin_amdgcn_raw_buffer_store_b128(u32x4{ns(a[0]), ns(a[1]), ns(b[0]), ns(b[1])}, srdS, o1, 0, 16);
        const float x0 = g0 ? df[2] : df[0], x1 = g0 ? df[3] : df[1];
        const bool first = rg < 2 ? !h : h0set;
        const u32 o2 = rg < 2 ? slab + 5120 + (hpl * 2 + rg) * 256 + l * 4 : own ? slab + 6144 + l * 4 : OOB;
        __builtin_amdgcn_raw_buffer_store_b32(ns(pk(first ? x0 : 0.f, first ? 0.f : x1)), srdS, o2, 0, 16);
        STAMP(6);
      }
      if constexpr (t == Q_L1 || t == Q_L1C) {
        const int dw = w * 16 + c;
        const u32 dof = (u32)((rb * 100 + (dw < 100 ? dw : 99)) * 4);
        const u32 go = (u32)((hq * 16 + 4 * g) * SLAB) + dof;
        if constexpr (t == Q_L1) {
          asm volatile("buffer_load_dword %0, %4, %5, 0 offen sc1\n\tbuffer_load_dword %1, %4, %5, %6 offen sc1\n\t"
                       "buffer_load_dword %2, %4, %5, %7 offen sc1\n\tbuffer_load_dword %3, %4, %5, %8 offen sc1"
                       : "=&v"(g0_), "=&v"(g1_), "=&v"(g2_), "=&v"(g3_)
                       : "v"(go), "s"(srdS), "s"(SLAB), "s"(2 * SLAB), "s"(3 * SLAB)
                       : "memory");
        } else {
          asm volatile("s_waitcnt vmcnt(%4)" : "+v"(g0_), "+v"(g1_), "+v"(g2_), "+v"(g3_) : "n"(wait_h<BR>(E, EQ + Q_L1)) : "memory");
          _Pragma("nounroll") for (int spin = 0; spin < (1 << 20); ++spin) {
            asm volatile("" ::: "memory");
            if (!__any(umax(umax3(g0_, g1_, g2_), g3_) == SENT)) break;
            g0_ = __builtin_amdgcn_raw_buffer_load_b32(srdS, go, 0, 16);
            g1_ = __builtin_amdgcn_raw_buffer_load_b32(srdS, go, SLAB, 16);
            g2_ = __builtin_amdgcn_raw_buffer_load_b32(srdS, go, 2 * SLAB, 16);
            g3_ = __builtin_amdgcn_raw_buffer_load_b32(srdS, go, 3 * SLAB, 16);
          }
          const u32 sel = c == 0 ? 0x00003f80u : c == 1 ? 0x3f800000u : 0u;
          f32x4 sum = mfma32(u32x4{sel, sel, sel, sel}, u32x4{g0_, g1_, g2_, g3_}, z4);
          __builtin_amdgcn_raw_buffer_store_b32(ns(pk(sum[0], sum[1])), srdS, g == 0 ? (u32)((128 + hq) * SLAB) + dof : OOB, 0, 16);
          STAMP(7);
        }
      }
    }
  };
  a0[0] = z4; a0[1] = z4; a1[0] = z4; a1[1] = z4; aL = z4;
  static_for<0, 18>([&](auto tt) {
    constexpr int t = tt.value, E = EQ + t;
    if constexpr (BR == 0) {
      if constexpr (t == 3 || t == 6) bar_only();
    } else if constexpr ((t & 1) && t < 17) {
      if constexpr (t == 1) wait_bar<wait_n<BR>(E, 18 + (t >> 1) + 1)>();
      else wait_vm_bar<wait_n<BR>(E, 18 + (t >> 1) + 1)>();
    }
    u32x4 c0 = f0, c1 = f1, cl = fl;
    SCHED_FENCE();
    a0[0] = mfma32(c0, xf[t][0], a0[0]);
    load_q_frags(IC<t + 1>{}, IC<0>{});
    hook(tt, IC<0>{});
    SCHED_FENCE();
    a0[1] = mfma32(c0, xf[t][1], a0[1]);
    load_q_frags(IC<t + 1>{}, IC<1>{});
    hook(tt, IC<1>{});
    ev_dma(IC<E>{}, IC<0>{});
    SCHED_FENCE();
    a1[0] = mfma32(c1, xf[t][0], a1[0]);
    load_q_frags(IC<t + 1>{}, IC<2>{});
    hook(tt, IC<2>{});
    SCHED_FENCE();
    a1[1] = mfma32(c1, xf[t][1], a1[1]);
    hook(tt, IC<3>{});
    ev_dma(IC<E>{}, IC<1>{});
    SCHED_FENCE();
    aL = mfma32(cl, xf[t][0], aL);
    asm volatile("" : "+v"(aL));
    hook(tt, IC<4>{});
    SCHED_FENCE();
  });
  STAMP(8);
  refresh();
  if (g < 2) lds_w64(lb + QLX + ((2 * rg + hpl) * 2 + g) * 128 + c * 8, pk4(aL));
  bar_lds();
  const u32 ofo = (u32)((BR * 128 + rb * 8 + rg * 2) * 18 * 1024);
  const int hp = hq * 2 + hpl;
  auto store_o = [&](int sb, f32x4 o0, f32x4 o1, float l0, float l1, float r0, float r1) {
    u32x2 a = pk4(o0 * r0), b = pk4(o1 * r1);
    __builtin_amdgcn_raw_buffer_store_b128(u32x4{ns(a[0]), ns(a[1]), ns(b[0]), ns(b[1])}, srdF, ofo + (sb * 18 + hp) * 1024 + l * 16, 0, 16);
    lds_w128(lb + LOWN + ((rg * 2 + sb) * 2 + hpl) * 1024 + l * 16, u32x4{a[0], a[1], b[0], b[1]});
    float rr = g0 ? r1 : r0;
    u32 lv = ns(pk(l0 * rr, l1 * rr));
    auto sw = __builtin_amdgcn_permlane16_swap(lv, lv, false, false);
    u32 s0 = sw[0], s1 = sw[1];
    __builtin_amdgcn_raw_buffer_store_b64(u32x2{s0, s1}, srdF, g == 0 ? ofo + (sb * 18 + 16 + (hq >> 2)) * 1024 + (hq & 3) * 256 + hpl * 128 + c * 8 : OOB, 0, 16);
  };

  if (BR == 0) {
    HH h0, h1;
    {
      u32x2 qa0 = pk4(a0[0]), qb0 = pk4(a1[0]), qa1 = pk4(a0[1]), qb1 = pk4(a1[1]);
      h0.b0[0] = qa0[0]; h0.b0[1] = qa0[1]; h0.b1[0] = qb0[0]; h0.b1[1] = qb0[1];
      h1.b0[0] = qa1[0]; h1.b0[1] = qa1[1]; h1.b1[0] = qb1[0]; h1.b1[1] = qb1[1];
    }
    const u32 ql0 = lb + QLX + ((2 * rg + hpl) * 2 + hpl) * 128 + c * 8, ql1 = lb + QLX + ((2 * rg + 1 - hpl) * 2 + hpl) * 128 + c * 8;
    const u32 kf = (u32)((128 + hq) * SLAB);
    const int l8 = g * 16 + (c & 7) + 8 * hpl;
    u32x4 AM0, AM1, AMLm;
    u32 AL0, AL1, ALL;
    auto load_kv = [&]() {
      AM0 = __builtin_amdgcn_raw_buffer_load_b128(srdS, kf + hpl * 2048 + l * 16, 0, 16);
      AM1 = __builtin_amdgcn_raw_buffer_load_b128(srdS, kf + hpl * 2048 + 1024 + l * 16, 0, 16);
      AMLm = __builtin_amdgcn_raw_buffer_load_b128(srdS, kf + 4096 + l8 * 16, 0, 16);
      AL0 = __builtin_amdgcn_raw_buffer_load_b32(srdS, kf + 5120 + hpl * 512 + l * 4, 0, 16);
      AL1 = __builtin_amdgcn_raw_buffer_load_b32(srdS, kf + 5120 + hpl * 512 + 256 + l * 4, 0, 16);
      ALL = __builtin_amdgcn_raw_buffer_load_b32(srdS, kf + 6144 + l8 * 4, 0, 16);
    };
    static_for<0, HH_N>([&](auto st) {
      if (st.value == L2ST) { SCHED_FENCE(); load_kv(); SCHED_FENCE(); }
      if (st.value == L2ST2) {
        SCHED_FENCE();
        if (__any(umax4(umax4(umax4(umax3(AL0, AL1, ALL), AM0), AM1), AMLm) == SENT)) load_kv();
        SCHED_FENCE();
      }
      if constexpr (st.value >= HQ0 && (st.value - HQ0) % HQS == 0 && (st.value - HQ0) / HQS < NEV - EH) {
        constexpr int E = EH + (st.value - HQ0) / HQS;
        if constexpr (SC<BR>::s.nop[E] > 0) { SCHED_FENCE(); ev_dma(IC<E>{}, IC<0>{}); SCHED_FENCE(); }
      }
      hh_step<st.value, true>(h0, ql0, phf, sumf, lo, ninf);
      hh_step<st.value, true>(h1, ql1, phf, sumf, lo, ninf);
    });
    hh_left<true>(h0, lo);
    hh_left<true>(h1, lo);
    asm volatile("" : "+v"(h0.b0), "+v"(h0.b1), "+v"(h0.bl), "+v"(h1.b0), "+v"(h1.b1), "+v"(h1.bl));
    STAMP(9);
    _Pragma("nounroll") for (int spin = 0; spin < (1 << 20); ++spin) {
      asm volatile("" ::: "memory");
      if (!__any(umax4(umax4(umax4(umax3(AL0, AL1, ALL), AM0), AM1), AMLm) == SENT)) break;
      load_kv();
    }
    STAMP(10);
    const bool h0set = (0x47 >> (c & 7)) & 1;
    u32x4 AML0, AML1;
#pragma unroll
    for (int e = 0; e < 4; ++e) { AML0[e] = h0set ? AMLm[e] : 0u; AML1[e] = h0set ? 0u : AMLm[e]; }
    f32x4 O0[2], O1[2], OL[2];
#pragma unroll
    for (int sb = 0; sb < 2; ++sb) OL[sb] = mfma32(AML0, (sb ? h1 : h0).b0, z4);
#pragma unroll
    for (int sb = 0; sb < 2; ++sb) O0[sb] = mfma32(AM0, (sb ? h1 : h0).b0, z4);
#pragma unroll
    for (int sb = 0; sb < 2; ++sb) OL[sb] = mfma32(AML1, (sb ? h1 : h0).b1, OL[sb]);
#pragma unroll
    for (int sb = 0; sb < 2; ++sb) O1[sb] = mfma32(AM1, (sb ? h1 : h0).b1, z4);
#pragma unroll
    for (int sb = 0; sb < 2; ++sb) OL[sb] = mfma32(u32x4{ALL, 0u, 0u, 0u}, u32x4{(sb ? h1 : h0).bl, 0u, 0u, 0u}, OL[sb]);
#pragma unroll
    for (int sb = 0; sb < 2; ++sb) O0[sb] = mfma32(u32x4{AL0, 0u, 0u, 0u}, u32x4{(sb ? h1 : h0).bl, 0u, 0u, 0u}, O0[sb]);
#pragma unroll
    for (int sb = 0; sb < 2; ++sb) O1[sb] = mfma32(u32x4{AL1, 0u, 0u, 0u}, u32x4{(sb ? h1 : h0).bl, 0u, 0u, 0u}, O1[sb]);
#pragma unroll
    for (int sb = 0; sb < 2; ++sb) {
      const HH& h = sb ? h1 : h0;
      float r0 = __builtin_amdgcn_rcpf(fmaf(h.S[0], 1e-6f, OL[sb][2])), r1 = __builtin_amdgcn_rcpf(fmaf(h.S[2], 1e-6f, OL[sb][3]));
      store_o(sb ^ hpl, O0[sb], O1[sb], OL[sb][0], OL[sb][1], r0, r1);
    }
  } else {
    auto ev3 = [&](auto EE) {
      SCHED_FENCE();
      ev_dma(EE, IC<0>{}); ev_dma(EE, IC<1>{}); ev_dma(EE, IC<2>{});
      if constexpr (EE.value == e_fld(BR)) load_flags();
      SCHED_FENCE();
    };
    const u32 kx = lb + LKX + ((rg * 2 + hpl) * 2) * 512 + l * 8;
    u32x2 pM0 = lds_r64(kx), pM1 = lds_r64(kx + 512);
    u32x2 kL[3];
#pragma unroll
    for (int i = 0; i < 3; ++i) kL[i] = lds_r64(lb + KLX + ((2 * rg + i) * 2 + hpl) * 128 + c * 8);
    ev3(IC<EH>{});
    const int par = c & 1;
    const bool ina = (4 * g + par) > c, inb = (4 * g + par + 2) > c;
    const float sc = 0.23570226039551584f * LOG2E;
    float la[2][2], lbv[2][2], mx[2][2];
    u32x2 qM[2][2] = {{pk4(a0[0]), pk4(a0[1])}, {pk4(a1[0]), pk4(a1[1])}};
    unswap(qM[0][0], qM[0][1]); unswap(qM[1][0], qM[1][1]);
#pragma unroll
    for (int sb = 0; sb < 2; ++sb) {
      u32x2 qlv = lds_r64(lb + QLX + ((2 * rg + sb) * 2 + hpl) * 128 + c * 8);
      u32x2 q0 = qM[0][sb], q1 = qM[1][sb];
      u32x4 b0 = {q0[0], q0[1], g == 0 ? qlv[0] : 0u, 0u};
      u32x4 b1 = {q1[0], q1[1], 0u, g == 0 ? qlv[1] : 0u};
      u32x2 kp0 = sb ? kM[0][0] : pM0, kp1 = sb ? kM[1][0] : pM1, kpl = kL[sb], kcl = kL[sb + 1];
      f32x4 sp0 = mfma32(u32x4{kp0[0], kp0[1], kpl[0], kpl[1]}, b0, z4);
      f32x4 sp1 = mfma32(u32x4{kp1[0], kp1[1], kpl[0], kpl[1]}, b1, z4);
      f32x4 sc0 = mfma32(u32x4{kM[0][sb][0], kM[0][sb][1], kcl[0], kcl[1]}, b0, z4);
      f32x4 sc1 = mfma32(u32x4{kM[1][sb][0], kM[1][sb][1], kcl[0], kcl[1]}, b1, z4);
      if (sb) ev3(IC<EH + 2>{}); else ev3(IC<EH + 1>{});
      const bool dead = (sb == 0) && (rb == 0) && (rg == 0);
      float pa0 = par ? sp0[1] : sp0[0], pb0 = par ? sp0[3] : sp0[2], pa1 = par ? sp1[1] : sp1[0], pb1 = par ? sp1[3] : sp1[2];
      if (dead) { pa0 = pb0 = pa1 = pb1 = -1e30f; }
      la[0][sb] = (ina ? pa0 : (par ? sc0[1] : sc0[0])) * sc;
      lbv[0][sb] = (inb ? pb0 : (par ? sc0[3] : sc0[2])) * sc;
      la[1][sb] = (ina ? pa1 : (par ? sc1[1] : sc1[0])) * sc;
      lbv[1][sb] = (inb ? pb1 : (par ? sc1[3] : sc1[2])) * sc;
      mx[0][sb] = fmaxf(la[0][sb], lbv[0][sb]);
      mx[1][sb] = fmaxf(la[1][sb], lbv[1][sb]);
    }
    float tm = redmax4(mx[0][0], mx[0][1], mx[1][0], mx[1][1], ninf);
    bcast4(tm, mx[0][0], mx[0][1], mx[1][0], mx[1][1]);
    ev3(IC<EH + 3>{});
    const u32 vb = lb + lvi(BR) + (rg * 32 + 4 * g + q) * VIS + hpl * 80;
#pragma unroll
    for (int sb = 0; sb < 2; ++sb) {
      const u32 v0 = vb + sb * 16 * VIS, v1 = v0 + 16 * VIS;
      u32x4 A0 = frag_tr(v0 + 8 * p, v1 + 8 * p), A1 = frag_tr(v0 + 32 + 8 * p, v1 + 32 + 8 * p);
      u32x4 AL = frag_tr(v0 + 64 + 8 * (p & 1), v1 + 64 + 8 * (p & 1));
      u32x4 P[2];
#pragma unroll
      for (int h = 0; h < 2; ++h) {
        float ea = __builtin_amdgcn_exp2f(la[h][sb] - mx[h][sb]), eb = __builtin_amdgcn_exp2f(lbv[h][sb] - mx[h][sb]);
        u32 wa_ = par ? pk(0.f, ea) : pk(ea, 0.f), wb_ = par ? pk(0.f, eb) : pk(eb, 0.f);
        P[h] = u32x4{ina ? wa_ : 0u, inb ? wb_ : 0u, ina ? 0u : wa_, inb ? 0u : wb_};
      }
      f32x4 O0 = mfma32(A0, P[0], z4), O1 = mfma32(A1, P[1], z4);
      f32x4 L0 = mfma32(AL, P[0], z4), L1 = mfma32(AL, P[1], z4);
      float r0 = __builtin_amdgcn_rcpf(L0[2]), r1 = __builtin_amdgcn_rcpf(L1[2]);
      if (e_fchk(BR) == EH + 4 && sb == 0) {
        asm volatile("s_waitcnt vmcnt(%1)" : "+v"(fv) : "n"(wait_h<BR>(EH + 4, e_fld(BR))) : "memory");
        check_flags();
      }
      store_o(sb, O0, O1, g0 ? L1[0] : L0[0], g0 ? L1[1] : L0[1], r0, r1);
    }
  }
  STAMP(11);
  refresh();
  const u32 ofl = (u32)((BR * 128 + rb * 8 + w) * 18 * 1024 + l * 16);
  auto ld_left = [&](int so_) {
    const u32 o2 = ofl - c * 8;
    u32x2 p = __builtin_amdgcn_raw_buffer_load_b64(srdF, o2, so_, 16), q = __builtin_amdgcn_raw_buffer_load_b64(srdF, o2 + 128, so_, 16);
    return u32x4{p[0], p[1], q[0], q[1]};
  };
  u32x4 wf[18][5], of[18];
  wait_bar<4>();
  STAMP(12);
  const u32 wrow2 = lb + (16 * g1 + 8 * g0 + 2 * q) * 144;
  u32 oa[3], oa4[3];
#pragma unroll
  for (int i = 0; i < 3; ++i) {
    oa[i] = wrow2 + 8 * p + i * 57344; oa4[i] = wrow2 + 128 + 8 * (p & 1) + i * 57344;
    asm volatile("" : "+v"(oa[i]), "+v"(oa4[i]));
  }
  auto load_wf = [&](auto TT, auto II) {
    constexpr int t = TT.value, i = II.value;
    if constexpr (t < 18) {
      constexpr u32 cb = wo_at(BR, t < 4 ? 1 - (t >> 1) : t >> 1) + (t & 1) * 32 * 144, s = cb / 57344, o = cb % 57344;
      if constexpr (i < 4) wf[t][i] = frag_tr(oa[s] + o + 32 * i, oa[s] + o + 144 + 32 * i);
      else wf[t][4] = frag_tr(oa4[s] + o, oa4[s] + o + 144);
    }
  };
  f32x4 acc[5];
  u32 seen;
  auto kstep = [&](auto tt) {
    constexpr int t = tt.value;
    static_for<0, 5>([&](auto ii) {
      constexpr int i = ii.value;
      acc[i] = mfma32(wf[t][i], of[t], t ? acc[i] : z4);
      if constexpr (i == 1 && t >= 4) seen = umax4(seen, of[t]);
      load_wf(IC<t + PRE>{}, ii);
      SCHED_FENCE();
    });
  };
  u32 so[14], own = lb + LOWN + w * 2048 + l * 16;
#pragma unroll
  for (int t = 0; t < 14; ++t) { so[t] = ((t + 2 + 2 * hq) & 15) * 1024; asm volatile("" : "+s"(so[t])); }
  asm volatile("" : "+v"(own));
  _Pragma("nounroll") for (int pass = 0; pass < (1 << 16); ++pass) {
#pragma unroll
    for (int t = 4; t < 4 + NOPT; ++t) of[t] = __builtin_amdgcn_raw_buffer_load_b128(srdF, ofl, so[t - 4], 16);
    of[0] = lds_r128(own);
    of[1] = lds_r128(own + 1024);
    SCHED_FENCE();
    static_for<0, PRE * 5>([&](auto t) {
      load_wf(IC<t.value / 5>{}, IC<t.value % 5>{});
      if constexpr (t.value == PP) {
        SCHED_FENCE();
        of[2] = ld_left(16 * 1024);
        of[3] = ld_left(17 * 1024);
        SCHED_FENCE();
      }
    });
    SCHED_FENCE();
    kstep(IC<0>{}); kstep(IC<1>{});
    _Pragma("nounroll") for (int spin = 0; spin < (1 << 20); ++spin) {
      asm volatile("" ::: "memory");
      if (!__any(umax4(umax4(0u, of[2]), of[3]) == SENT)) break;
      of[2] = ld_left(16 * 1024);
      of[3] = ld_left(17 * 1024);
#pragma unroll
      for (int t = 4; t < 4 + NOPT; ++t) of[t] = __builtin_amdgcn_raw_buffer_load_b128(srdF, ofl, so[t - 4], 16);
    }
    STAMP(13);
#pragma unroll
    for (int t = 4 + NOPT; t < 18; ++t) of[t] = __builtin_amdgcn_raw_buffer_load_b128(srdF, ofl, so[t - 4], 16);
    SCHED_FENCE();
    seen = 0;
    static_for<2, 18>(kstep);
    if (!__any(seen == SENT)) break;
  }
  STAMP(14);
  if (BR == 0 && rb == 0 && hq == 0 && tid == 0) __hip_atomic_store((GLB_AS u64*)(ws + WS_EPOCH), tag, __ATOMIC_RELAXED, __HIP_MEMORY_SCOPE_AGENT);
  char* op = out + (size_t)(rb * 128 + w * 16 + c) * 2304 + BR * 1152 + hq * 144;
  __amdgpu_buffer_rsrc_t srdU = __builtin_amdgcn_make_buffer_rsrc((void*)out, (short)0, 2048 * 2304, 0x00020000);
  const u32 oo = (u32)((rb * 128 + w * 16 + c) * 2304 + BR * 1152 + hq * 144);
#pragma unroll
  for (int i = 0; i < 2; ++i) {
    u32x2 a = pk4(acc[2 * i]), b = pk4(acc[2 * i + 1]);
    auto s0 = __builtin_amdgcn_permlane16_swap(a[0], b[0], false, false);
    auto s1 = __builtin_amdgcn_permlane16_swap(a[1], b[1], false, false);
    u32 s00 = s0[0], s01 = s0[1], s10 = s1[0], s11 = s1[1];
    *(u32x4*)(op + 64 * i + 32 * g0 + 16 * g1) = u32x4{s00, s10, s01, s11};
  }
  if (g < 2) *(u32x2*)(op + 128 + 8 * g) = pk4(acc[4]);
  STAMP(15);
  RSTAMP(31);
  STAMP_FLUSH();
  asm volatile(".Lhk_to_%0:" ::"n"(BR));
  __builtin_amdgcn_endpgm();
}

__global__ __launch_bounds__(512, 2) void fused(const char* __restrict__ XA, const char* __restrict__ WA, const char* __restrict__ PHI,
                                                const char* __restrict__ WOA, const char* __restrict__ XB, const char* __restrict__ WB,
                                                const char* __restrict__ WOB, char* ws, char* out) {
  __shared__ __attribute__((aligned(16))) char smem[LDS_BYTES];
  u64 tent = 0, rent = 0;
  const u32 lb = (u32)(uintptr_t)(LDS_AS char*)smem;
  const int b = blockIdx.x, x = b & 7, j = b >> 3, xx = x & 3;
  const int rb = xx * 4 + (j >> 3), hq = j & 7;
  u64x4 ka, kb;
  u64 kc;
  first(x >= 4, rb, hq, j, lb, ka, kb, kc);
  auto ptr = [](u64 a) { return (char*)(GLB_AS char*)a; };
  XA = ptr(ka[0]); WA = ptr(ka[1]); PHI = ptr(ka[2]); WOA = ptr(ka[3]);
  XB = ptr(kb[0]); WB = ptr(kb[1]); WOB = ptr(kb[2]); ws = ptr(kb[3]); out = ptr(kc);
  if (x < 4) body<0>(XA, WA, PHI, WOA, ws, out, rb, hq, j, lb, tent, rent);
  else body<1>(XB, WB, PHI, WOB, ws, out, rb, hq, j, lb, tent, rent);
}
}

extern "C" void kernel_launch(void* const* d_in, const int* in_sizes, int n_in, void* d_out, int out_size, void* d_ws, size_t ws_size,
                              hipStream_t stream) {
  hk::fused<<<256, 512, 0, stream>>>((const char*)d_in[0], (const char*)d_in[1], (const char*)d_in[2], (const char*)d_in[3], (const char*)d_in[4],
                                     (const char*)d_in[5], (const char*)d_in[6], (char*)d_ws, (char*)d_out);
}
```

```cpp
#define L2ST 32
#define L2ST2 64
#define CPRO 1
#define E_PO 43
#define E_FLD 36
#include <hip/hip_runtime.h>
#include <stdint.h>

namespace hk {
typedef unsigned int u32;
typedef unsigned long long u64;
typedef unsigned short u16;
typedef float f32x4 __attribute__((ext_vector_type(4)));
typedef float f32x2 __attribute__((ext_vector_type(2)));
typedef u32 u32x4 __attribute__((ext_vector_type(4)));
typedef u32 u32x2 __attribute__((ext_vector_type(2)));
typedef __bf16 bf16x8 __attribute__((ext_vector_type(8)));
typedef __bf16 bf16x2 __attribute__((ext_vector_type(2)));
typedef short s16x4 __attribute__((ext_vector_type(4)));
#define LDS_AS __attribute__((address_space(3)))
#define GLB_AS __attribute__((address_space(1)))
#define DEVI __device__ __forceinline__
#define SCHED_FENCE() __builtin_amdgcn_sched_barrier(0)

template <int V> struct IC { static constexpr int value = V; };
template <int I, int N, class F>
DEVI void static_for(F&& f) {
  if constexpr (I < N) { f(IC<I>{}); static_for<I + 1, N>(f); }
}

constexpr int XROW = 1152, WROW = 3456;
constexpr size_t WS_OF = 0;
constexpr int SLAB = 6400;
constexpr size_t WS_SLAB = 5u << 20;
constexpr size_t WS_FLAG = 6u << 20;
constexpr size_t WS_EPOCH = WS_FLAG + 65536;
constexpr size_t WS_OFLAG = WS_FLAG + 4096;
constexpr u32 OOB = 0x7fffffffu;
constexpr u32 SENT = 0xffffffffu;
constexpr int WU = 9216;
constexpr int LPK = 1024, PKS = 296;
constexpr int S0 = 3968, SS = 34944;
constexpr int SK = 16384, SV = SK + WU + 16;
constexpr int WQ = S0 + SS;
constexpr int LT = WQ + 9 * WU;
constexpr int LM = S0 + 4 * SS;
constexpr int LPHF = LM;
constexpr int LSUM = LPHF + 8192;
constexpr int LHALO = LM;
constexpr int KLX = LM + 11264;
constexpr int QLX = KLX + 2304;
constexpr int LSCR = QLX + 2048;
constexpr int VIS = 168;
constexpr int lvi(int br) { return br ? 1024 : LT; }
constexpr int LKX = 25600;
constexpr int wo_at(int br, int m) {
  if (br == 0) return m < 4 ? 1024 + m * WU : m < 6 ? LT + (m - 4) * WU : WQ + (m - 6) * WU;
  return m < 2 ? LT + m * WU : m == 2 ? LM : WQ + (m - 3) * WU;
}
constexpr int LOWN = WQ + 6 * WU;
constexpr int LDS_BYTES = 163840;
static_assert(LSCR + 512 <= LDS_BYTES && LPK + 2688 <= S0 && LPK + 128 * PKS <= WQ && LT + 128 * VIS <= LM && 1024 + 144 * VIS <= LKX && LKX + 8192 <= WQ &&
              SV + WU <= SS && 3 * WU <= SS && 7 * WU <= 2 * SS && S0 % 128 == 0 && SS % 128 == 0, "lds");

DEVI u32x2 tr_read(u32 a) { return __builtin_bit_cast(u32x2, __builtin_amdgcn_ds_read_tr16_b64_v4i16((LDS_AS s16x4*)a)); }
DEVI u32x4 frag_tr(u32 a0, u32 a1) { u32x2 x = tr_read(a0), y = tr_read(a1); return u32x4{x[0], x[1], y[0], y[1]}; }
DEVI u32x4 lds_r128(u32 a) { return *(LDS_AS u32x4*)a; }
DEVI u32x2 lds_r64(u32 a) { return *(LDS_AS u32x2*)a; }
DEVI u32 lds_r32(u32 a) { return *(LDS_AS u32*)a; }
DEVI u32 lds_r16(u32 a) { return *(LDS_AS u16*)a; }
DEVI void lds_w128(u32 a, u32x4 v) { *(LDS_AS u32x4*)a = v; }
DEVI void lds_w64(u32 a, u32x2 v) { *(LDS_AS u32x2*)a = v; }
DEVI void lds_w32(u32 a, u32 v) { *(LDS_AS u32*)a = v; }
DEVI f32x4 mfma32(u32x4 a, u32x4 b, f32x4 c) {
  return __builtin_amdgcn_mfma_f32_16x16x32_bf16(__builtin_bit_cast(bf16x8, a), __builtin_bit_cast(bf16x8, b), c, 0, 0, 0);
}
DEVI u32 pk(float a, float b) { f32x2 v = {a, b}; return __builtin_bit_cast(u32, __builtin_convertvector(v, bf16x2)); }
DEVI u32x2 pk4(f32x4 v) { return u32x2{pk(v[0], v[1]), pk(v[2], v[3])}; }
DEVI float max3(float a, float b, float c) { return fmaxf(fmaxf(a, b), c); }
DEVI u32 umax(u32 a, u32 b) { return a > b ? a : b; }
DEVI u32 umax3(u32 a, u32 b, u32 c) { return umax(umax(a, b), c); }
DEVI u32 umax4(u32 m, u32x4 v) { return umax3(umax3(m, v[0], v[1]), v[2], v[3]); }
DEVI u32 ns(u32 v) { return v < 0xfffffffeu ? v : 0xfffffffeu; }

DEVI void dma_full(u32 lds, u32 voff, __amdgpu_buffer_rsrc_t srd, u32 soff) {
  asm volatile("s_mov_b32 m0, %0\n\ts_nop 0\n\tbuffer_load_dwordx4 %1, %2, %3 offen lds" ::"s"(lds), "v"(voff), "s"(srd), "s"(soff) : "memory");
}
DEVI void dma_mask(u32 lds, u32 voff, __amdgpu_buffer_rsrc_t srd, u32 soff, u64 mask) {
  asm volatile("s_mov_b32 m0, %0\n\ts_mov_b64 exec, %4\n\tbuffer_load_dwordx4 %1, %2, %3 offen lds\n\ts_mov_b64 exec, -1" ::"s"(lds), "v"(voff), "s"(srd),
               "s"(soff), "s"(mask)
               : "memory");
}
template <int N>
DEVI void wait_bar() { asm volatile("s_waitcnt vmcnt(%0) lgkmcnt(0)\n\ts_barrier" ::"n"(N) : "memory"); }
template <int N>
DEVI void wait_vm_bar() { asm volatile("s_waitcnt vmcnt(%0)\n\ts_barrier" ::"n"(N) : "memory"); }
DEVI void bar_lds() { asm volatile("s_waitcnt lgkmcnt(0)\n\ts_barrier" ::: "memory"); }
DEVI void bar_only() { asm volatile("s_barrier" ::: "memory"); }

DEVI float redmax4(float a, float b, float c, float d, float ninf) {
  auto r1 = __builtin_amdgcn_permlane32_swap(__float_as_uint(a), __float_as_uint(b), false, false);
  auto r2 = __builtin_amdgcn_permlane32_swap(__float_as_uint(c), __float_as_uint(d), false, false);
  u32 r10 = r1[0], r11 = r1[1], r20 = r2[0], r21 = r2[1];
  float ab = max3(__uint_as_float(r10), __uint_as_float(r11), ninf);
  float cd = max3(__uint_as_float(r20), __uint_as_float(r21), ninf);
  auto r3 = __builtin_amdgcn_permlane16_swap(__float_as_uint(ab), __float_as_uint(cd), false, false);
  u32 r30 = r3[0], r31 = r3[1];
  return max3(__uint_as_float(r30), __uint_as_float(r31), ninf);
}
DEVI void bcast4(float t, float& a, float& b, float& c, float& d) {
  u32 tu = __float_as_uint(t);
  auto s1 = __builtin_amdgcn_permlane16_swap(tu, tu, false, false);
  u32 x = s1[0], y = s1[1];
  auto s2 = __builtin_amdgcn_permlane32_swap(x, x, false, false);
  auto s3 = __builtin_amdgcn_permlane32_swap(y, y, false, false);
  u32 s20 = s2[0], s21 = s2[1], s30 = s3[0], s31 = s3[1];
  a = __uint_as_float(s20); b = __uint_as_float(s21); c = __uint_as_float(s30); d = __uint_as_float(s31);
}

constexpr float LOG2E = 1.4426950408889634f;
constexpr u32 ONES = 0x3f803f80u;

struct HH {
  u32x4 f0, f1, f2, f3;
  u32x4 b0, b1;
  u32 bl;
  f32x4 T0, T1, TL, S;
  float a, b, c, d;
  f32x4 p0, q0, p1, q1;
  float l0, l1;
};
constexpr int HH_N = 106;
DEVI void swap32(float& a, float& b) {
  auto r = __builtin_amdgcn_permlane32_swap(__float_as_uint(a), __float_as_uint(b), false, false);
  u32 x = r[0], y = r[1]; a = __uint_as_float(x); b = __uint_as_float(y);
}
DEVI void swap16(float& a, float& b) {
  auto r = __builtin_amdgcn_permlane16_swap(__float_as_uint(a), __float_as_uint(b), false, false);
  u32 x = r[0], y = r[1]; a = __uint_as_float(x); b = __uint_as_float(y);
}
template <int I, bool QS>
DEVI void hh_step(HH& s, u32 lft, u32 phf, u32 sumf, bool lo, float ninf) {
  const f32x4 z = {0.f, 0.f, 0.f, 0.f};
  if constexpr (I == 0) {
    u32x2 x = lds_r64(lft); s.b0[2] = x[0]; s.b0[3] = x[1];
    asm volatile("" ::: "memory");
    u32x2 y = lds_r64(lft); s.b1[2] = y[0]; s.b1[3] = y[1];
    s.f0 = lds_r128(phf);
  } else if constexpr (I == 1) { s.f1 = lds_r128(phf + 1024); s.f2 = lds_r128(phf + 2048);
  } else if constexpr (I == 2) { s.f3 = lds_r128(phf + 3072);
  } else if constexpr (I == 3) { s.T0 = mfma32(s.f0, s.b0, z);
  } else if constexpr (I == 4) { s.TL = mfma32(s.f2, s.b0, z);
  } else if constexpr (I == 5) { s.T1 = mfma32(s.f1, s.b1, z);
  } else if constexpr (I == 6) { s.TL = mfma32(s.f3, s.b1, s.TL);
  } else if constexpr (I == 7) { s.f0 = lds_r128(sumf); s.f1 = lds_r128(sumf + 1024); s.f2 = lds_r128(sumf + 2048);
  } else if constexpr (I == 8) { s.a = max3(s.T0[0], s.T0[1], s.T0[2]);
  } else if constexpr (I == 9) { s.c = max3(-s.T0[0], -s.T0[1], -s.T0[2]);
  } else if constexpr (I == 10) { s.b = max3(s.T1[0], s.T1[1], s.T1[2]);
  } else if constexpr (I == 11) { s.d = max3(-s.T1[0], -s.T1[1], -s.T1[2]);
  } else if constexpr (I == 12) {
  } else if constexpr (I == 13) { s.a = max3(s.a, s.T0[3], s.TL[0]);
  } else if constexpr (I == 14) { s.b = max3(s.b, s.T1[3], s.TL[1]);
  } else if constexpr (I == 15) { s.c = max3(s.c, -s.T0[3], -s.TL[0]);
  } else if constexpr (I == 16) { s.d = max3(s.d, -s.T1[3], -s.TL[1]);
  } else if constexpr (I == 17) { swap32(s.a, s.b);
  } else if constexpr (I == 18) { swap32(s.c, s.d);
  } else if constexpr (I == 19) { s.a = max3(s.a, s.b, ninf);
  } else if constexpr (I == 20) { s.c = max3(s.c, s.d, ninf);
  } else if constexpr (I == 21) { swap16(s.a, s.c);
  } else if constexpr (I == 22) { s.a = max3(s.a, s.c, ninf);
  } else if constexpr (I == 23) { s.a *= -LOG2E;
  } else if constexpr (I == 24) { s.c = s.a; swap16(s.a, s.c);
  } else if constexpr (I == 25) { s.b = s.a; swap32(s.a, s.b);
  } else if constexpr (I == 26) { s.d = s.c; swap32(s.c, s.d);
  } else if constexpr (I == 27) { s.l0 = lo ? s.a : s.c;
  } else if constexpr (I == 28) { s.l1 = lo ? s.b : s.d;
  } else if constexpr (I == 29) { s.l0 = fmaf(s.TL[0], lo ? LOG2E : -LOG2E, s.l0);
  } else if constexpr (I == 30) { s.l1 = fmaf(s.TL[1], lo ? LOG2E : -LOG2E, s.l1);
  } else if constexpr (I == 31) { s.l0 = __builtin_amdgcn_exp2f(s.l0);
  } else if constexpr (I == 32) { s.l1 = __builtin_amdgcn_exp2f(s.l1);
  } else if constexpr (I == 33) { s.bl = pk(s.l0, s.l1);
  } else if constexpr (I == 34) { s.S = mfma32(s.f2, u32x4{s.bl, 0u, 0u, 0u}, z);
  } else if constexpr (I < 77) {
    constexpr int h = (I - 35) / 21, k = (I - 35) % 21;
    f32x4& T = h ? s.T1 : s.T0; f32x4& p = h ? s.p1 : s.p0; f32x4& q = h ? s.q1 : s.q0; u32x4& o = h ? s.b1 : s.b0;
    const float m = h ? s.b : s.a, n = h ? s.d : s.c;
    constexpr bool arg = k < 2 || ((k & 1) && k < 15);
    constexpr int e = arg ? (k < 2 ? k : (k + 1) / 2) : (k < 15 ? (k - 2) / 2 : 7);
    if constexpr (k < 16) {
      if constexpr (arg) { if constexpr (e < 4) p[e] = fmaf(T[e], LOG2E, m); else q[e - 4] = fmaf(T[e - 4], -LOG2E, n); }
      else { if constexpr (e < 4) p[e] = __builtin_amdgcn_exp2f(p[e]); else q[e - 4] = __builtin_amdgcn_exp2f(q[e - 4]); }
    } else if constexpr (k < 18) { o[k - 16] = pk(p[2 * (k - 16)], p[2 * (k - 16) + 1]);
    } else if constexpr (k < 20) { o[k - 16] = pk(q[2 * (k - 18)], q[2 * (k - 18) + 1]);
    } else { s.S = mfma32(h ? s.f1 : s.f0, o, s.S); }
  } else if constexpr (I == 77) {
  } else if constexpr (I == 78) { s.c = __builtin_amdgcn_rcpf(s.S[1]);
  } else if constexpr (I == 79) { s.d = __builtin_amdgcn_rcpf(s.S[3]);
  } else if constexpr (I == 80) { if constexpr (QS) s.c *= s.S[0]; else s.a = __builtin_amdgcn_rcpf(s.S[0]);
  } else if constexpr (I == 81) { if constexpr (QS) s.d *= s.S[2]; else s.b = __builtin_amdgcn_rcpf(s.S[2]);
  } else if constexpr (I < 106) {
    constexpr int v = (I - 82) / 6, k = (I - 82) % 6;
    if constexpr (!(QS && v >= 2)) {
      f32x4& x = v == 0 ? s.q0 : v == 1 ? s.q1 : v == 2 ? s.p0 : s.p1;
      u32x4& o = (v & 1) ? s.b1 : s.b0;
      const float r = v == 0 ? s.c : v == 1 ? s.d : v == 2 ? s.a : s.b;
      if constexpr (k < 4) x[k] *= r;
      else o[(v < 2 ? 2 : 0) + k - 4] = pk(x[2 * (k - 4)], x[2 * (k - 4) + 1]);
    }
  }
}
template <bool QS>
DEVI void hh_left(HH& s, bool lo) {
  if constexpr (QS) s.bl = pk(s.l0 * (lo ? 1.f : s.c), s.l1 * (lo ? 1.f : s.d));
  else s.bl = pk(s.l0 * (lo ? s.a : s.c), s.l1 * (lo ? s.b : s.d));
}

#define PRE 7
#define PRO 2
#define LEAD 8
#define QA 2
#define QT 1
#define E_POB 30
#define Q_L1 10
#define Q_L1C 16
#define QTB QT
#define NOPT 0
#define PCW 1
#define PCW0 0
#define PP 34
#define QW 1
#define HQ0 4
#define HQS 8
#define C0 0
#define CS 2
#define C1 44
#define E_PUB 11
#define QB 1
#define QSP 1
#define HB 3
constexpr int EM = 36, EQ = 44, EH = 62, NEV = 74;
constexpr int Q_SLAB = 2;
#define E_PUBQ 9
#define E_PUBB 15
#define E_FLDB 17
constexpr int E_PUB2 = C1 < 34 ? E_PUBB : EQ + E_PUBQ;
static_assert(C1 >= 34 || ((E_PUBB & 3) == 3 && E_PUBB > C1 + 2 && E_PUBB < 35), "marks");
constexpr int e_fld(int br) { return br ? EQ + E_FLDB : E_FLD; }
constexpr int e_fchk(int br) { return br ? (E_FLDB > 12 ? EH + 4 : EQ + 16) : EQ; }
constexpr int clr_at(int E) { return E >= C1 && E < C1 + 3 ? 4 + E - C1 : E >= C0 && (E - C0) % CS == 0 && (E - C0) / CS < 4 ? (E - C0) / CS : -1; }
constexpr bool is_clr(int E) { return clr_at(E) >= 0; }
static_assert((E_PUB & 3) == 3 && E_PUB > C0 + 3 * CS && E_PUB < 35 && E_FLD >= EM && E_FLD < EQ && C1 > C0 + 3 * CS && (C1 < 34 || C1 >= EQ) && C1 < EQ + 6, "marks");
struct Sched {
  int op[NEV][3];
  int nop[NEV];
  int pos_ev[NEV + 1];
  int pos_hook[NEV];
  int last[36];
  int total;
};
constexpr int ext_ops(int br, int E) {
  return (E == (br ? E_POB : E_PO)) + (br && is_clr(E)) + (E == e_fld(br)) +
         (br ? 0 : E == EQ + Q_SLAB ? 2 : E == EQ + Q_L1 ? 4 : E == EQ + Q_L1C ? 1 : 0);
}
constexpr int op_chunk(int op) { return op < 200 ? (op - 100) >> 2 : op < 236 ? ((op - 200) >> 1) % 9 : (op - 200) >> 1; }
constexpr int op_earliest(int br, int op) {
  const int n = op_chunk(op);
  if (n < 9) return 4 * n - 1 - LEAD;
  if (n < 27) return n < 21 ? 23 : n < 25 ? 27 : 31;
  const int m = n - 27;
  if (br == 0) return m < 6 ? EQ + 3 : EQ + 6;
  return m < 2 ? 31 : m == 2 ? EQ : EQ + 2 * (m - 3) + 3;
}
constexpr int op_deadline(int br, int op) {
  const int n = op_chunk(op);
  if (n < 9) return 4 * n - 1;
  if (n < 27) return br == 0 || n == 18 ? EQ : EQ + 2 * (n - 18) - 1;
  return NEV;
}
static_assert(LEAD <= 12, "ring");
constexpr Sched make_sched(int br) {
  Sched s{};
  const int nx = br ? 3 : 2;
  int list[128] = {}, when[128] = {}, n = 0;
  for (int c = 0; c < 9; ++c) {
    for (int i = 0; i < nx; ++i) list[n++] = 100 + 4 * c + i;
    list[n++] = 200 + 2 * c; list[n++] = 201 + 2 * c; list[n++] = 218 + 2 * c; list[n++] = 219 + 2 * c;
  }
  for (int c = 18; c < 36; ++c) { list[n++] = 200 + 2 * c; list[n++] = 201 + 2 * c; }
  for (int k = 0; k < n; ++k) when[k] = 1000;
  int pos = 0, done = 0;
  for (; done < PRO * (nx + 4); ++done) { s.last[op_chunk(list[done])] = pos++; when[done] = -1; if (done == nx + 3) pos += 1 + (PCW == 2); }
  pos += PCW == 3;
  for (int E = 0; E < NEV; ++E) {
    s.pos_ev[E] = pos;
    int j = 0;
    for (int k = 0; k < n; ++k) {
      if (when[k] != 1000 || op_earliest(br, list[k]) > E) continue;
      const int c = op_chunk(list[k]);
      int q = E < EM ? (c < 9 ? QA : br ? QTB : QT) : E < EQ && br ? 0 : ext_ops(br, E) ? 1 : 2;
      if (br == 0 && E >= EQ) q = ext_ops(br, E) || (E < EH && E % QSP) ? 0 : 1;
      if (br == 1 && E >= EQ && c >= 27) q = E >= EH + 4 ? 0 : E >= EH ? HB : ext_ops(br, E) ? QB - 1 : QB;
      if (j >= q) break;
      s.op[E][j++] = list[k]; when[k] = E; ++done;
      s.last[c] = pos++;
      if (E >= op_deadline(br, list[k])) s.total = -1000;
    }
    s.nop[E] = j;
    s.pos_hook[E] = pos;
    pos += ext_ops(br, E);
  }
  s.pos_ev[NEV] = pos;
  s.total += done == n ? pos : -1000;
  return s;
}
template <int BR> struct SC { static constexpr Sched s = make_sched(BR); };
static_assert(SC<0>::s.total > 0 && SC<1>::s.total > 0, "schedule incomplete");
constexpr int imin(int a, int b) { return a < b ? a : b; }
template <int BR> constexpr int wait_n(int E, int c) {
  int n = SC<BR>::s.pos_ev[E] - SC<BR>::s.last[c] - 1;
  if (BR == 1 && E == E_PUB) n = imin(n, SC<BR>::s.pos_ev[E] - SC<BR>::s.pos_ev[C0 + 3 * CS + 1]);
  if (BR == 1 && E == E_PUB2) n = imin(n, SC<BR>::s.pos_ev[E] - SC<BR>::s.pos_ev[C1 + 3]);
  if (BR == 0 && E == EQ) n = imin(n, SC<BR>::s.pos_ev[E] - SC<BR>::s.pos_ev[E_FLD + 1]);
  return n;
}
template <int BR> constexpr int wait_h(int E, int E0) { return SC<BR>::s.pos_hook[E] - SC<BR>::s.pos_ev[E0 + 1]; }

#define STAMP(i)
#define RSTAMP(i)
#define STAMP_FLUSH()
#define STAMP_DECL

DEVI u64 next_tag(u32x2 e) {
  const u64 E = ((u64)(u32)__builtin_amdgcn_readfirstlane(e[1]) << 32) | (u32)__builtin_amdgcn_readfirstlane(e[0]);
  return E * 6364136223846793005ull + 1442695040888963407ull;
}
DEVI void poll_flags(GLB_AS u64* f, u64 tag) {
  for (int spin = 0; spin < (1 << 20); ++spin) {
    u64 v = __hip_atomic_load(f, __ATOMIC_RELAXED, __HIP_MEMORY_SCOPE_AGENT);
    if (__all(v == tag)) break;
    __builtin_amdgcn_s_sleep(1);
  }
  asm volatile("" ::: "memory");
}

#define ROT (rb + 2 * hq) % 9
typedef u64 u64x4 __attribute__((ext_vector_type(4)));
DEVI void first(bool br, int rb, int hq, int jx, u32 lb, u64x4& ka, u64x4& kb, u64& kc_) {
  asm volatile("s_load_dwordx8 %0, %3, 0x0\n\ts_load_dwordx8 %1, %3, 0x20\n\ts_load_dwordx2 %2, %3, 0x40"
               : "=&s"(ka), "=&s"(kb), "=&s"(kc_)
               : "s"(__builtin_amdgcn_kernarg_segment_ptr()));
  const int tid = threadIdx.x, l = tid & 63, w = __builtin_amdgcn_readfirstlane(tid >> 6);
  u64 pc;
  int f0, t0, f1, t1;
  asm volatile("s_getpc_b64 %0\n.Lhk_pc:\n\ts_mov_b32 %1, .Lhk_from_0-.Lhk_pc\n\ts_max_i32 %2, .Lhk_to_0-.Lhk_pc, 0\n\t"
               "s_mov_b32 %3, .Lhk_from_1-.Lhk_pc\n\ts_max_i32 %4, .Lhk_to_1-.Lhk_pc, 0"
               : "=s"(pc), "=s"(f0), "=s"(t0), "=s"(f1), "=s"(t1)::"scc");
  __amdgpu_buffer_rsrc_t srdC = __builtin_amdgcn_make_buffer_rsrc((void*)pc, (short)0, (br ? t1 : t0) & ~3, 0x00020000);
  const u32 po = l == 0 ? (u32)((br ? f1 : f0) + (8 * jx + w) * 128) : OOB;
  asm volatile("s_mov_b32 m0, %0\n\ts_nop 0\n\tbuffer_load_dword %1, %2, 0 offen lds" ::"s"(lb + LSCR), "v"(po), "s"(srdC) : "memory");
  const int lr = (l * 57) >> 9, lu = l - lr * 9;
  int kc = ROT;
  u32 vx[2], vq[2], vh, vp = (u32)(hq * 2592 + (w * 21 + l) * 16), le = lb + (br ? LHALO : LPK) + w * (br ? 256 : 336);
  {
    const int row = 2 * w + ((l >> 3) & 1);
    int grow = rb * 128 - 16 + row; grow = grow < 0 ? 0 : grow;
    vh = (u32)(grow * XROW + (((l & 7) ^ ((row >> 1) & 7)) << 4) + kc * 128);
  }
#pragma unroll
  for (int i = 0; i < 2; ++i) {
    const int row = 16 * w + 8 * i + (l >> 3);
    vx[i] = (u32)((rb * 128 + row) * XROW + (((l & 7) ^ ((row >> 1) & 7)) << 4));
    vq[i] = (u32)((8 * w + 4 * (lr & 1) + 2 * i + ((lr >> 1) & 1)) * WROW + hq * 144 + lu * 16);
  }
  asm volatile("" : "+v"(vx[0]), "+v"(vx[1]), "+v"(vq[0]), "+v"(vq[1]), "+v"(vh), "+v"(vp), "+s"(le), "+s"(kc));
  asm volatile("s_waitcnt lgkmcnt(0)" : "+s"(ka), "+s"(kb), "+s"(kc_) : "s"(__builtin_amdgcn_kernarg_segment_ptr()));
  const u64 XA = ka[0], WA = ka[1], PHI = ka[2], XB = kb[0], WB = kb[1];
  __amdgpu_buffer_rsrc_t srdX = __builtin_amdgcn_make_buffer_rsrc((void*)(br ? XB : XA), (short)0, 2048 * XROW, 0x00020000);
  __amdgpu_buffer_rsrc_t srdW = __builtin_amdgcn_make_buffer_rsrc((void*)(br ? WB : WA), (short)0, 576 * WROW, 0x00020000);
  __amdgpu_buffer_rsrc_t srdE = __builtin_amdgcn_make_buffer_rsrc((void*)(br ? XB : PHI), (short)0, br ? 2048 * XROW : 32 * 648, 0x00020000);
  dma_mask(le, br ? vh : vp, srdE, 0u, br ? 0xffffull : (1ull << 21) - 1);
#pragma unroll
  for (int i = 0; i < 2; ++i) dma_full(lb + S0 + w * 2048 + i * 1024, vx[i], srdX, (u32)(kc * 128));
#pragma unroll
  for (int n = 0; n < 4; ++n)
    dma_mask(lb + S0 + (n < 2 ? SK : SV) + (2 * w + (n & 1)) * 576, vq[n & 1], srdW, (u32)(kc * 64 * WROW + (n < 2 ? 1152 : 2304)), (1ull << 36) - 1);
}

template <int BR>
DEVI void body(const char* __restrict__ X, const char* __restrict__ W, const char* __restrict__ PHI, const char* __restrict__ WO, char* ws, char* out,
               int rb, int hq, int jx, u32 lb, u64 tent, u64 rent) {
  asm volatile(".Lhk_from_%0:" ::"n"(BR));
  int tid = threadIdx.x;
  asm volatile("" : "+v"(tid));
  int l = tid & 63;
  const int w = __builtin_amdgcn_readfirstlane(tid >> 6);
  const int rg = w & 3, hpl = w >> 2;
  int c = l & 15, g = l >> 4, q = c >> 2, p = c & 3, g0 = g & 1, g1 = g >> 1;
  int lr = (l * 57) >> 9, lu = l - lr * 9;
  auto refresh = [&]() {
    asm volatile("" : "+v"(tid));
    l = tid & 63; c = l & 15; g = l >> 4; q = c >> 2; p = c & 3; g0 = g & 1; g1 = g >> 1;
    lr = (l * 57) >> 9; lu = l - lr * 9;
  };
  STAMP_DECL
  STAMP(0);
  RSTAMP(30);
  __amdgpu_buffer_rsrc_t srdX = __builtin_amdgcn_make_buffer_rsrc((void*)X, (short)0, 2048 * XROW, 0x00020000);
  __amdgpu_buffer_rsrc_t srdW = __builtin_amdgcn_make_buffer_rsrc((void*)W, (short)0, 576 * WROW, 0x00020000);
  const int rot = ROT;
  constexpr u64 M36 = (1ull << 36) - 1;

  u32 vx[2], vq[2], vh = 0;
#pragma unroll
  for (int i = 0; i < 2; ++i) {
    int row = 16 * w + 8 * i + (l >> 3);
    vx[i] = (u32)((rb * 128 + row) * XROW + (((l & 7) ^ ((row >> 1) & 7)) << 4));
  }
#pragma unroll
  for (int i = 0; i < 2; ++i) vq[i] = (u32)((8 * w + 4 * (lr & 1) + 2 * i + ((lr >> 1) & 1)) * WROW + hq * 144 + lu * 16);
  if (BR) {
    int row = 2 * w + ((l >> 3) & 1);
    int grow = rb * 128 - 16 + row; grow = grow < 0 ? 0 : grow;
    vh = (u32)(grow * XROW + (((l & 7) ^ ((row >> 1) & 7)) << 4));
  }
  auto kc_of = [&](int cc) { int k = cc + rot; return k >= 9 ? k - 9 : k; };
  __amdgpu_buffer_rsrc_t srdO = __builtin_amdgcn_make_buffer_rsrc((void*)WO, (short)0, 576 * XROW, 0x00020000);
  auto dma_op = [&](auto OP) {
    constexpr int op = OP.value;
    if constexpr (op < 200) {
      constexpr int cc = (op - 100) >> 2, i = op & 3;
      const int kc = kc_of(cc);
      if constexpr (i < 2) dma_full(lb + S0 + (cc & 3) * SS + w * 2048 + i * 1024, vx[i], srdX, (u32)(kc * 128));
      else dma_mask(lb + LHALO + (cc & 3) * 2048 + w * 256, vh, srdX, (u32)(kc * 128), 0xffffull);
    } else {
      constexpr int n = (op - 200) >> 1, i = op & 1;
      constexpr int at = n < 9 ? S0 + (n & 3) * SS + SK : n < 18 ? S0 + ((n - 9) & 3) * SS + SV : n < 27 ? WQ + (n - 18) * WU : wo_at(BR, n - 27);
      const u32 dst = lb + at + (2 * w + i) * 576;
      if constexpr (n < 27) {
        const int kc = kc_of(n % 9), sec = n < 9 ? 1152 : (n < 18 ? 2304 : 0);
        dma_mask(dst, vq[i], srdW, (u32)(kc * 64 * WROW + sec), M36);
      } else {
        const int hi = lr & 1, qq = 2 * i + ((lr >> 1) & 1);
        if constexpr (n > 27)
          dma_mask(dst, (u32)((36 * hpl + 20 * hi + 4 * rg + qq) * XROW + hq * 144 + lu * 16), srdO, (u32)(((n - 28 + hq) & 7) * 72 * XROW), M36);
        else dma_mask(dst, (u32)((36 * (8 * hpl + 2 * rg + hi) + 16 + qq) * XROW + hq * 144 + lu * 16), srdO, 0u, M36);
      }
    }
  };
  auto ev_dma = [&](auto EE, auto II) {
    if constexpr (II.value < SC<BR>::s.nop[EE.value]) dma_op(IC<SC<BR>::s.op[EE.value][II.value]>{});
  };
  auto warm_wo = [&]() {
    const int idx = w * 21 + l;
    const u32 po = l < 21 && idx < 162 ? (u32)((162 * jx + idx) * 128) : OOB;
    asm volatile("s_mov_b32 m0, %0\n\ts_nop 0\n\tbuffer_load_dword %1, %2, 0 offen lds" ::"s"(lb + LSCR), "v"(po), "s"(srdO) : "memory");
  };
  __amdgpu_buffer_rsrc_t srdF = __builtin_amdgcn_make_buffer_rsrc((void*)(ws + WS_OF), (short)0, 256 * 18 * 1024, 0x00020000);
  __amdgpu_buffer_rsrc_t srdS = __builtin_amdgcn_make_buffer_rsrc((void*)(ws + WS_SLAB), (short)0, 136 * SLAB, 0x00020000);
  auto mark = [&](auto EE) {
    constexpr int e = EE.value, f = (e + 2) % 3;
    const u32x4 sv = {SENT, SENT, SENT, SENT};
    const u32 ob = (u32)(((e > 3 ? 128 : 0) + rb * 8 + w) * 18 * 1024);
    if constexpr (e > 0 && f < 2) __builtin_amdgcn_raw_buffer_store_b128(sv, srdF, l * 16, ob + hq * 2048 + f * 1024, 16);
    if constexpr (e > 0 && f == 2) __builtin_amdgcn_raw_buffer_store_b128(sv, srdF, g == 0 ? c * 16 : OOB, ob + (16 + (hq >> 2)) * 1024 + (hq & 3) * 256, 16);
    if constexpr (e == 0) {
      const u32 so = tid < 400 ? (u32)((hq * 16 + rb) * SLAB + tid * 16) : tid < 425 ? (u32)((128 + hq) * SLAB + rb * 400 + (tid - 400) * 16) : OOB;
      __builtin_amdgcn_raw_buffer_store_b128(sv, srdS, so, 0, 16);
    }
  };
  GLB_AS u64* kflags = (GLB_AS u64*)(ws + WS_FLAG + hq * 256);
  GLB_AS u64* oflags = (GLB_AS u64*)(ws + WS_OFLAG + (BR * 16 + rb) * 256);
  u32x2 ep, fv;
  asm volatile("" ::"s"(srdX), "s"(srdW));
  STAMP(16);
  static_assert(PRO == 2, "prologue");
  auto dma_chunk = [&](auto cc) {
    constexpr int c_ = cc.value;
    dma_op(IC<100 + 4 * c_>{}); dma_op(IC<101 + 4 * c_>{});
    if constexpr (BR) dma_op(IC<102 + 4 * c_>{});
    dma_op(IC<200 + 2 * c_>{}); dma_op(IC<201 + 2 * c_>{}); dma_op(IC<218 + 2 * c_>{}); dma_op(IC<219 + 2 * c_>{});
  };
  {
    const char* ea = ws + WS_EPOCH;
    asm volatile("global_load_dwordx2 %0, %1, off sc1" : "=v"(ep) : "v"(ea) : "memory");
  }
  STAMP(17);
  if constexpr (BR == 0) {
    wait_vm_bar<7 + (PCW == 2)>();
    STAMP(18);
    const int h = rg & 1, lf = rg >> 1;
    const int f = lf ? 16 + (q & 1) : c;
    const bool rowok = lf ? (p == h) : true;
    const u32 pb = lb + LPK + (hpl * 2 + h) * 648 + f * 2;
    const int d0 = 2 * h + 4 * g;
    u32 e0 = lds_r16(pb + (d0 + 0) * 36), e1 = lds_r16(pb + (d0 + 1) * 36), e2 = lds_r16(pb + (d0 + 2) * 36), e3 = lds_r16(pb + (d0 + 3) * 36);
    const int dl = h ? 0 : 16;
    u32 l0 = lds_r16(pb + dl * 36), l1 = lds_r16(pb + (dl + 1) * 36);
    SCHED_FENCE();
    dma_op(IC<104>{}); dma_op(IC<105>{});
    SCHED_FENCE();
    u32 lw = (g == 0) ? (l0 | (l1 << 16)) : 0u;
    u32x4 fr = {e0 | (e1 << 16), e2 | (e3 << 16), h ? 0u : lw, h ? lw : 0u};
    if (!rowok) fr = u32x4{0, 0, 0, 0};
    lds_w128(lb + LPHF + (hpl * 4 + rg) * 1024 + l * 16, fr);
    if (w < 3) {
      u32 a = (p == 2 * w) ? ONES : 0u, b = (p == 2 * w + 1) ? ONES : 0u;
      u32 x = ((p & 1) == g1) ? ((p >> 1) ? 0x3f800000u : 0x00003f80u) : 0u;
      lds_w128(lb + LSUM + w * 1024 + l * 16, w < 2 ? u32x4{a, a, b, b} : u32x4{x, 0u, 0u, 0u});
    }
    SCHED_FENCE();
    dma_op(IC<202>{}); dma_op(IC<203>{}); dma_op(IC<220>{}); dma_op(IC<221>{});
  } else {
    bar_only();
    STAMP(18);
    dma_chunk(IC<1>{});
  }
  STAMP(1);

  const u32 xa0 = lb + S0 + (rg * 32 + hpl * 16 + c) * 128 + ((g ^ ((c >> 1) & 7)) << 4);
  const u32 xa1 = xa0 ^ 64;
  const u32 xb0 = xa0 + 2048 - hpl * 4096, xb1 = xb0 ^ 64;
  const u32 xh0 = lb + LHALO + c * 128 + ((g ^ ((c >> 1) & 7)) << 4);
  const u32 xh1 = xh0 ^ 64;
  const u32 wrow = lb + (16 * g1 + 8 * g0 + 2 * q) * 144;
  const u32 wa = wrow + hpl * 72 + 8 * p;
  const u32 wl = wrow + 32 + (p & 1) * 72 + (p >> 1) * (SV - SK);
  const u32 wh = wa + (rg & 1) * 40 + (rg >> 1) * (SV - SK);
  float ninf = -__builtin_inff();
  asm volatile("" : "+s"(ninf));

  u32x4 xf[18][2];
  const f32x4 z4 = {0.f, 0.f, 0.f, 0.f};
  f32x4 k0[2] = {z4, z4}, k1[2] = {z4, z4}, a0[2] = {z4, z4}, a1[2] = {z4, z4}, aL = z4, aH = z4, aHL = z4;
  u32x4 f0, f1, fl, fh, xh;
  auto load_kv_frags = [&](auto HH_, auto JJ) {
    constexpr int H = HH_.value, j = JJ.value, T = H >> 1, n = T >> 1, ks = T & 1;
    constexpr u32 ok = S0 + (n & 3) * SS + SK + ks * 32 * 144, ov = ok + SV - SK;
    if constexpr (H < 36) {
      if constexpr ((H & 1) == 0) {
        if constexpr (j == 0) { xf[T][0] = lds_r128((ks ? xa1 : xa0) + (n & 3) * SS); f0 = frag_tr(wa + ok, wa + ok + 144); }
        if constexpr (j == 1) { xf[T][1] = lds_r128((ks ? xb1 : xb0) + (n & 3) * SS); f1 = frag_tr(wa + ok + 40, wa + ok + 184); }
      } else {
        if constexpr (j == 0) f0 = frag_tr(wa + ov, wa + ov + 144);
        if constexpr (j == 1) f1 = frag_tr(wa + ov + 40, wa + ov + 184);
        if constexpr (j == 2) fl = frag_tr(wl + ok, wl + ok + 144);
        if constexpr (j == 3 && BR == 1) { fh = frag_tr(wh + ok, wh + ok + 144); xh = lds_r128((ks ? xh1 : xh0) + (n & 3) * 2048); }
      }
    }
  };
  wait_bar<wait_n<BR>(0, 0)>();
  STAMP(2);
  load_kv_frags(IC<0>{}, IC<0>{}); load_kv_frags(IC<0>{}, IC<1>{});
  u64 tag = 0;
  static_for<0, 36>([&](auto hh_) {
    constexpr int H = hh_.value, T = H >> 1;
    if constexpr ((H & 3) == 3 && H < 35) {
      wait_bar<wait_n<BR>(H, (H + 1) >> 2)>();
    }
    u32x4 c0 = f0, c1 = f1, cl = fl, ch = fh, cx = xh;
    f32x4(&d0)[2] = (H & 1) ? a0 : k0;
    f32x4(&d1)[2] = (H & 1) ? a1 : k1;
    SCHED_FENCE();
    d0[0] = mfma32(c0, xf[T][0], d0[0]);
    load_kv_frags(IC<H + 1>{}, IC<0>{});
    SCHED_FENCE();
    d0[1] = mfma32(c0, xf[T][1], d0[1]);
    load_kv_frags(IC<H + 1>{}, IC<1>{});
    ev_dma(hh_, IC<0>{});
    SCHED_FENCE();
    d1[0] = mfma32(c1, xf[T][0], d1[0]);
    load_kv_frags(IC<H + 1>{}, IC<2>{});
    SCHED_FENCE();
    d1[1] = mfma32(c1, xf[T][1], d1[1]);
    load_kv_frags(IC<H + 1>{}, IC<3>{});
    ev_dma(hh_, IC<1>{});
    ev_dma(hh_, IC<2>{});
    SCHED_FENCE();
    if constexpr (H & 1) {
      aL = mfma32(cl, xf[T][0], aL);
      asm volatile("" : "+v"(aL));
      if constexpr (BR == 1) {
        aH = mfma32(ch, cx, aH); aHL = mfma32(cl, cx, aHL);
        asm volatile("" : "+v"(aH), "+v"(aHL));
      }
    }
    if constexpr (H == (BR ? E_POB : E_PO)) warm_wo();
    if constexpr (H == 3) {
      asm volatile("" : "+v"(ep));
      tag = next_tag(ep);
    }
    if constexpr (BR == 1 && is_clr(H)) mark(IC<clr_at(H)>{});
    if constexpr (BR == 1 && H == E_PUB) {
      if (tid < 2) __hip_atomic_store(tid == 0 ? kflags + rb : oflags + hq - 512, tag, __ATOMIC_RELAXED, __HIP_MEMORY_SCOPE_AGENT);
    }
    if constexpr (BR == 1 && H == E_PUB2) {
      if (tid == 64) __hip_atomic_store(oflags + hq, tag, __ATOMIC_RELAXED, __HIP_MEMORY_SCOPE_AGENT);
    }
    SCHED_FENCE();
  });
  STAMP(3);
  refresh();
  const u32 wqa = lb + WQ + (16 * g1 + 8 * g0 + 2 * q) * 144 + hpl * 72 + 8 * p;
  const u32 wql = lb + WQ + (16 * g1 + 8 * g0 + 2 * q) * 144 + 32 + (p & 1) * 72;
  auto load_q_frags = [&](auto TT, auto JJ) {
    constexpr int t = TT.value, j = JJ.value;
    constexpr u32 o = (t >> 1) * WU + (t & 1) * 32 * 144;
    if constexpr (t < 18) {
      if constexpr (j == 0) f0 = frag_tr(wqa + o, wqa + o + 144);
      if constexpr (j == 1) f1 = frag_tr(wqa + o + 40, wqa + o + 184);
      if constexpr (j == 2) fl = frag_tr(wql + o, wql + o + 144);
    }
  };
  if constexpr (BR == 1) {
    wait_bar<wait_n<BR>(EQ, 18)>();
    static_for<0, 3>([&](auto j) { load_q_frags(IC<0>{}, j); });
  }
  u32x2 kM[2][2];
#pragma unroll
  for (int sb = 0; sb < 2; ++sb) { kM[0][sb] = pk4(k0[sb]); kM[1][sb] = pk4(k1[sb]); }
  auto unswap = [&](u32x2& a, u32x2& b) {
    u32x2 x = a, y = b;
#pragma unroll
    for (int e = 0; e < 2; ++e) { a[e] = hpl ? y[e] : x[e]; b[e] = hpl ? x[e] : y[e]; }
  };
  {
    constexpr int VROW0 = BR ? 16 : 0;
    auto left = [&](f32x4 v, int blk) {
      u32x2 x = pk4(v);
      const u32 va = lb + lvi(BR) + (VROW0 - 16 + blk * 16 + c) * VIS + g0 * 80 + 64;
      lds_w64(g1 ? va : lb + KLX + (blk * 2 + g0) * 128 + c * 8, u32x2{x[0], g1 ? ONES : x[1]});
      lds_w64(g1 ? va + 8 : lb + LSCR + 256 + c * 8, u32x2{x[1], ONES});
    };
    left(aL, 1 + 2 * rg + hpl);
#pragma unroll
    for (int sb = 0; sb < 2; ++sb) {
      const u32 va = lb + lvi(BR) + (VROW0 + rg * 32 + (sb ^ hpl) * 16 + c) * VIS + hpl * 80;
      lds_w64(va + 8 * g, pk4(a0[sb]));
      lds_w64(va + 32 + 8 * g, pk4(a1[sb]));
    }
    if constexpr (BR == 1) {
      unswap(kM[0][0], kM[0][1]); unswap(kM[1][0], kM[1][1]);
      if (w == 2) left(aHL, 0);
      const u32 kx = lb + LKX + l * 8;
      lds_w64(rg < 2 ? kx + (hpl * 2 + rg) * 512 : lb + lvi(BR) + c * VIS + hpl * 80 + (rg & 1) * 32 + 8 * g, pk4(aH));
      if (rg < 3) {
        const u32 b = kx + (((rg + 1) * 2 + hpl) * 2) * 512;
        lds_w64(b, kM[0][1]); lds_w64(b + 512, kM[1][1]);
      }
    }
  }
  const u32 phf = lb + LPHF + hpl * 4096 + l * 16, sumf = lb + LSUM + l * 16;
  const bool lo = g < 2;
  auto load_flags = [&]() {
    GLB_AS u64* fa = (BR == 0 && g == 0) ? kflags + c : oflags + (l & 7);
    asm volatile("global_load_dwordx2 %0, %1, off sc1" : "=v"(fv) : "v"(fa) : "memory");
  };
  auto check_flags = [&]() {
    if (!__all((((u64)fv[1] << 32) | fv[0]) == tag)) poll_flags((BR == 0 && g == 0) ? kflags + c : oflags + (l & 7), tag);
  };
  if constexpr (BR == 0) {
    bar_lds();
    STAMP(4);
    HH h0, h1;
    h0.b0[0] = kM[0][0][0]; h0.b0[1] = kM[0][0][1]; h0.b1[0] = kM[1][0][0]; h0.b1[1] = kM[1][0][1];
    h1.b0[0] = kM[0][1][0]; h1.b0[1] = kM[0][1][1]; h1.b1[0] = kM[1][1][0]; h1.b1[1] = kM[1][1][1];
    const u32 kl0 = lb + KLX + ((1 + 2 * rg + hpl) * 2 + hpl) * 128 + c * 8, kl1 = lb + KLX + ((2 + 2 * rg - hpl) * 2 + hpl) * 128 + c * 8;
    static_for<0, HH_N>([&](auto st) {
      if constexpr (st.value >= 8 && (st.value - 8) % 12 == 0 && (st.value - 8) / 12 < EQ - EM) {
        constexpr int E = EM + (st.value - 8) / 12;
        SCHED_FENCE();
        ev_dma(IC<E>{}, IC<0>{}); ev_dma(IC<E>{}, IC<1>{});
        if constexpr (E == E_PO) warm_wo();
        if constexpr (E == E_FLD) load_flags();
        SCHED_FENCE();
      }
      hh_step<st.value, false>(h0, kl0, phf, sumf, lo, ninf);
      hh_step<st.value, false>(h1, kl1, phf, sumf, lo, ninf);
    });
#pragma unroll
    for (int sb = 0; sb < 2; ++sb) {
      HH& hs = sb ? h1 : h0;
      hh_left<false>(hs, lo);
      const u32 pa = lb + LPK + (rg * 32 + (sb ^ hpl) * 16 + c) * PKS + hpl * 144;
      lds_w64(pa + 8 * g, u32x2{hs.b0[0], hs.b0[1]});
      lds_w64(pa + 32 + 8 * g, u32x2{hs.b0[2], hs.b0[3]});
      lds_w64(pa + 64 + 8 * g, u32x2{hs.b1[0], hs.b1[1]});
      lds_w64(pa + 96 + 8 * g, u32x2{hs.b1[2], hs.b1[3]});
      lds_w32(pa + 128 + 4 * g, hs.bl);
    }
    STAMP(5);
    wait_bar<wait_n<BR>(EQ, 26)>();
    asm volatile("" : "+v"(fv));
    static_for<0, 3>([&](auto j) { load_q_frags(IC<0>{}, j); });
  }
  refresh();
  f32x4 dp = z4, dm = z4, df = z4;
  u32x4 ap[4], am[4], af[4], bv[4];
  u32 g0_, g1_, g2_, g3_;
  const int rr = 4 * (4 * g0 + q) + 2 * g1;
  const u32 pa = lb + LPK + rr * PKS + hpl * 144 + (rg & 1) * 64 + 8 * p;
  const u32 pf = lb + LPK + rr * PKS + hpl * 144 + 128 + 8 * (p >> 1);
  const u32 va = lb + lvi(BR) + rr * VIS + hpl * 80 + (rg < 2 ? rg * 32 + 8 * p : 64 + 8 * (p & 1));
  auto hook = [&](auto tt, auto jj) {
    constexpr int t = tt.value, E = EQ + t, j = jj.value;
    if constexpr (BR == 0 && j < 4) {
      if constexpr (t == Q_SLAB - 2) {
        ap[j] = frag_tr(pa + j * 32 * PKS, pa + j * 32 * PKS + PKS);
        am[j] = frag_tr(pa + 32 + j * 32 * PKS, pa + 32 + j * 32 * PKS + PKS);
        af[j] = frag_tr(pf + j * 32 * PKS, pf + j * 32 * PKS + PKS);
        bv[j] = frag_tr(va + j * 32 * VIS, va + j * 32 * VIS + VIS);
      }
      if constexpr (t == Q_SLAB - 1) {
        dp = mfma32(ap[j], bv[j], dp); dm = mfma32(am[j], bv[j], dm); df = mfma32(af[j], bv[j], df);
        asm volatile("" : "+v"(dp), "+v"(dm), "+v"(df));
      }
    }
    if constexpr (j != 4) return;
    if constexpr (BR == 1 && is_clr(E)) mark(IC<clr_at(E)>{});
    if constexpr (BR == 1 && E == E_PUB2) {
      if (tid == 0) __hip_atomic_store(oflags + hq, tag, __ATOMIC_RELAXED, __HIP_MEMORY_SCOPE_AGENT);
    }
    if constexpr (BR == 1 && E == e_fld(BR)) load_flags();
    if constexpr (E == e_fchk(BR)) {
      if constexpr (BR == 1) asm volatile("s_waitcnt vmcnt(%1)" : "+v"(fv) : "n"(wait_h<BR>(E, e_fld(BR))) : "memory");
      check_flags();
    }
    if constexpr (BR == 0) {
      if constexpr (t == Q_SLAB) {
        const u32 slab = (u32)((hq * 16 + rb) * SLAB);
        const bool h0set = (0x47 >> (c & 7)) & 1, h = rg & 1, own = (c >> 3) == hpl;
        u32x2 a = pk4(dp), b = pk4(dm);
        const u32 o1 = rg < 2 ? slab + (hpl * 2 + rg) * 1024 + l * 16 : (own && h0set != h) ? slab + 4096 + ((hpl * 2 + (rg & 1)) * 16 + g * 4 + ((0xf924 >> (2 * (c & 7))) & 3)) * 16 : OOB;
        __builtin_amdgcn_raw_buffer_store_b128(u32x4{ns(a[0]), ns(a[1]), ns(b[0]), ns(b[1])}, srdS, o1, 0, 16);
        const float x0 = g0 ? df[2] : df[0], x1 = g0 ? df[3] : df[1];
        const bool first = rg < 2 ? !h : h0set;
        const u32 o2 = rg < 2 ? slab + 5120 + (hpl * 2 + rg) * 256 + l * 4 : own ? slab + 6144 + (hpl * 32 + g * 8 + (c & 7)) * 4 : OOB;
        __builtin_amdgcn_raw_buffer_store_b32(ns(pk(first ? x0 : 0.f, first ? 0.f : x1)), srdS, o2, 0, 16);
        STAMP(6);
      }
      if constexpr (t == Q_L1 || t == Q_L1C) {
        const int un = rb * 8 + w;
        const u32 dof = (u32)(un * 64 + c * 4);
        const u32 go = un < 100 ? (u32)((hq * 16 + 4 * g) * SLAB) + dof : OOB;
        if constexpr (t == Q_L1) {
          asm volatile("buffer_load_dword %0, %4, %5, 0 offen sc1\n\tbuffer_load_dword %1, %4, %5, %6 offen sc1\n\t"
                       "buffer_load_dword %2, %4, %5, %7 offen sc1\n\tbuffer_load_dword %3, %4, %5, %8 offen sc1"
                       : "=&v"(g0_), "=&v"(g1_), "=&v"(g2_), "=&v"(g3_)
                       : "v"(go), "s"(srdS), "s"(SLAB), "s"(2 * SLAB), "s"(3 * SLAB)
                       : "memory");
        } else {
          asm volatile("s_waitcnt vmcnt(%4)" : "+v"(g0_), "+v"(g1_), "+v"(g2_), "+v"(g3_) : "n"(wait_h<BR>(E, EQ + Q_L1)) : "memory");
          _Pragma("nounroll") for (int spin = 0; spin < (1 << 20); ++spin) {
            asm volatile("" ::: "memory");
            if (!__any(umax(umax3(g0_, g1_, g2_), g3_) == SENT)) break;
            g0_ = __builtin_amdgcn_raw_buffer_load_b32(srdS, go, 0, 16);
            g1_ = __builtin_amdgcn_raw_buffer_load_b32(srdS, go, SLAB, 16);
            g2_ = __builtin_amdgcn_raw_buffer_load_b32(srdS, go, 2 * SLAB, 16);
            g3_ = __builtin_amdgcn_raw_buffer_load_b32(srdS, go, 3 * SLAB, 16);
          }
          const u32 sel = c == 0 ? 0x00003f80u : c == 1 ? 0x3f800000u : 0u;
          f32x4 sum = mfma32(u32x4{sel, sel, sel, sel}, u32x4{g0_, g1_, g2_, g3_}, z4);
          __builtin_amdgcn_raw_buffer_store_b32(ns(pk(sum[0], sum[1])), srdS, g == 0 && un < 100 ? (u32)((128 + hq) * SLAB) + dof : OOB, 0, 16);
          STAMP(7);
        }
      }
    }
  };
  a0[0] = z4; a0[1] = z4; a1[0] = z4; a1[1] = z4; aL = z4;
  static_for<0, 18>([&](auto tt) {
    constexpr int t = tt.value, E = EQ + t;
    if constexpr (BR == 0) {
      if constexpr (t == 3 || t == 6) bar_only();
    } else if constexpr ((t & 1) && t < 17) {
      if constexpr (t == 1) wait_bar<wait_n<BR>(E, 18 + (t >> 1) + 1)>();
      else wait_vm_bar<wait_n<BR>(E, 18 + (t >> 1) + 1)>();
    }
    u32x4 c0 = f0, c1 = f1, cl = fl;
    SCHED_FENCE();
    a0[0] = mfma32(c0, xf[t][0], a0[0]);
    load_q_frags(IC<t + 1>{}, IC<0>{});
    hook(tt, IC<0>{});
    SCHED_FENCE();
    a0[1] = mfma32(c0, xf[t][1], a0[1]);
    load_q_frags(IC<t + 1>{}, IC<1>{});
    hook(tt, IC<1>{});
    ev_dma(IC<E>{}, IC<0>{});
    SCHED_FENCE();
    a1[0] = mfma32(c1, xf[t][0], a1[0]);
    load_q_frags(IC<t + 1>{}, IC<2>{});
    hook(tt, IC<2>{});
    SCHED_FENCE();
    a1[1] = mfma32(c1, xf[t][1], a1[1]);
    hook(tt, IC<3>{});
    ev_dma(IC<E>{}, IC<1>{});
    SCHED_FENCE();
    aL = mfma32(cl, xf[t][0], aL);
    asm volatile("" : "+v"(aL));
    hook(tt, IC<4>{});
    SCHED_FENCE();
  });
  STAMP(8);
  refresh();
  if (g < 2) lds_w64(lb + QLX + ((2 * rg + hpl) * 2 + g) * 128 + c * 8, pk4(aL));
  bar_lds();
  const u32 ofo = (u32)((BR * 128 + rb * 8 + rg * 2) * 18 * 1024);
  const int hp = hq * 2 + hpl;
  auto store_o = [&](int sb, f32x4 o0, f32x4 o1, float l0, float l1, float r0, float r1) {
    u32x2 a = pk4(o0 * r0), b = pk4(o1 * r1);
    __builtin_amdgcn_raw_buffer_store_b128(u32x4{ns(a[0]), ns(a[1]), ns(b[0]), ns(b[1])}, srdF, ofo + (sb * 18 + hp) * 1024 + l * 16, 0, 16);
    lds_w128(lb + LOWN + ((rg * 2 + sb) * 2 + hpl) * 1024 + l * 16, u32x4{a[0], a[1], b[0], b[1]});
    float rr = g0 ? r1 : r0;
    u32 lv = ns(pk(l0 * rr, l1 * rr));
    auto sw = __builtin_amdgcn_permlane16_swap(lv, lv, false, false);
    u32 s0 = sw[0], s1 = sw[1];
    __builtin_amdgcn_raw_buffer_store_b64(u32x2{s0, s1}, srdF, g == 0 ? ofo + (sb * 18 + 16 + (hq >> 2)) * 1024 + (hq & 3) * 256 + hpl * 128 + c * 8 : OOB, 0, 16);
  };

  if (BR == 0) {
    HH h0, h1;
    {
      u32x2 qa0 = pk4(a0[0]), qb0 = pk4(a1[0]), qa1 = pk4(a0[1]), qb1 = pk4(a1[1]);
      h0.b0[0] = qa0[0]; h0.b0[1] = qa0[1]; h0.b1[0] = qb0[0]; h0.b1[1] = qb0[1];
      h1.b0[0] = qa1[0]; h1.b0[1] = qa1[1]; h1.b1[0] = qb1[0]; h1.b1[1] = qb1[1];
    }
    const u32 ql0 = lb + QLX + ((2 * rg + hpl) * 2 + hpl) * 128 + c * 8, ql1 = lb + QLX + ((2 * rg + 1 - hpl) * 2 + hpl) * 128 + c * 8;
    const u32 kf = (u32)((128 + hq) * SLAB);
    const int l8 = (hpl * 2 + 1 - ((0x47 >> (c & 7)) & 1)) * 16 + g * 4 + ((0xf924 >> (2 * (c & 7))) & 3), l9 = hpl * 32 + g * 8 + (c & 7);
    u32x4 AM0, AM1, AMLm;
    u32 AL0, AL1, ALL;
    auto load_kv = [&]() {
      AM0 = __builtin_amdgcn_raw_buffer_load_b128(srdS, kf + hpl * 2048 + l * 16, 0, 16);
      AM1 = __builtin_amdgcn_raw_buffer_load_b128(srdS, kf + hpl * 2048 + 1024 + l * 16, 0, 16);
      AMLm = __builtin_amdgcn_raw_buffer_load_b128(srdS, kf + 4096 + l8 * 16, 0, 16);
      AL0 = __builtin_amdgcn_raw_buffer_load_b32(srdS, kf + 5120 + hpl * 512 + l * 4, 0, 16);
      AL1 = __builtin_amdgcn_raw_buffer_load_b32(srdS, kf + 5120 + hpl * 512 + 256 + l * 4, 0, 16);
      ALL = __builtin_amdgcn_raw_buffer_load_b32(srdS, kf + 6144 + l9 * 4, 0, 16);
    };
    static_for<0, HH_N>([&](auto st) {
      if (st.value == L2ST) { SCHED_FENCE(); load_kv(); SCHED_FENCE(); }
      if (st.value == L2ST2) {
        SCHED_FENCE();
        if (__any(umax4(umax4(umax4(umax3(AL0, AL1, ALL), AM0), AM1), AMLm) == SENT)) load_kv();
        SCHED_FENCE();
      }
      if constexpr (st.value >= HQ0 && (st.value - HQ0) % HQS == 0 && (st.value - HQ0) / HQS < NEV - EH) {
        constexpr int E = EH + (st.value - HQ0) / HQS;
        if constexpr (SC<BR>::s.nop[E] > 0) { SCHED_FENCE(); ev_dma(IC<E>{}, IC<0>{}); SCHED_FENCE(); }
      }
      hh_step<st.value, true>(h0, ql0, phf, sumf, lo, ninf);
      hh_step<st.value, true>(h1, ql1, phf, sumf, lo, ninf);
    });
    hh_left<true>(h0, lo);
    hh_left<true>(h1, lo);
    asm volatile("" : "+v"(h0.b0), "+v"(h0.b1), "+v"(h0.bl), "+v"(h1.b0), "+v"(h1.b1), "+v"(h1.bl));
    STAMP(9);
    _Pragma("nounroll") for (int spin = 0; spin < (1 << 20); ++spin) {
      asm volatile("" ::: "memory");
      if (!__any(umax4(umax4(umax4(umax3(AL0, AL1, ALL), AM0), AM1), AMLm) == SENT)) break;
      load_kv();
    }
    STAMP(10);
    const bool h0set = (0x47 >> (c & 7)) & 1;
    u32x4 AML0, AML1;
#pragma unroll
    for (int e = 0; e < 4; ++e) { AML0[e] = h0set ? AMLm[e] : 0u; AML1[e] = h0set ? 0u : AMLm[e]; }
    f32x4 O0[2], O1[2], OL[2];
#pragma unroll
    for (int sb = 0; sb < 2; ++sb) OL[sb] = mfma32(AML0, (sb ? h1 : h0).b0, z4);
#pragma unroll
    for (int sb = 0; sb < 2; ++sb) O0[sb] = mfma32(AM0, (sb ? h1 : h0).b0, z4);
#pragma unroll
    for (int sb = 0; sb < 2; ++sb) OL[sb] = mfma32(AML1, (sb ? h1 : h0).b1, OL[sb]);
#pragma unroll
    for (int sb = 0; sb < 2; ++sb) O1[sb] = mfma32(AM1, (sb ? h1 : h0).b1, z4);
#pragma unroll
    for (int sb = 0; sb < 2; ++sb) OL[sb] = mfma32(u32x4{ALL, 0u, 0u, 0u}, u32x4{(sb ? h1 : h0).bl, 0u, 0u, 0u}, OL[sb]);
#pragma unroll
    for (int sb = 0; sb < 2; ++sb) O0[sb] = mfma32(u32x4{AL0, 0u, 0u, 0u}, u32x4{(sb ? h1 : h0).bl, 0u, 0u, 0u}, O0[sb]);
#pragma unroll
    for (int sb = 0; sb < 2; ++sb) O1[sb] = mfma32(u32x4{AL1, 0u, 0u, 0u}, u32x4{(sb ? h1 : h0).bl, 0u, 0u, 0u}, O1[sb]);
#pragma unroll
    for (int sb = 0; sb < 2; ++sb) {
      const HH& h = sb ? h1 : h0;
      float r0 = __builtin_amdgcn_rcpf(fmaf(h.S[0], 1e-6f, OL[sb][2])), r1 = __builtin_amdgcn_rcpf(fmaf(h.S[2], 1e-6f, OL[sb][3]));
      store_o(sb ^ hpl, O0[sb], O1[sb], OL[sb][0], OL[sb][1], r0, r1);
    }
  } else {
    auto ev3 = [&](auto EE) {
      SCHED_FENCE();
      ev_dma(EE, IC<0>{}); ev_dma(EE, IC<1>{}); ev_dma(EE, IC<2>{});
      if constexpr (EE.value == e_fld(BR)) load_flags();
      SCHED_FENCE();
    };
    const u32 kx = lb + LKX + ((rg * 2 + hpl) * 2) * 512 + l * 8;
    u32x2 pM0 = lds_r64(kx), pM1 = lds_r64(kx + 512);
    u32x2 kL[3];
#pragma unroll
    for (int i = 0; i < 3; ++i) kL[i] = lds_r64(lb + KLX + ((2 * rg + i) * 2 + hpl) * 128 + c * 8);
    ev3(IC<EH>{});
    const int par = c & 1;
    const bool ina = (4 * g + par) > c, inb = (4 * g + par + 2) > c;
    const float sc = 0.23570226039551584f * LOG2E;
    float la[2][2], lbv[2][2], mx[2][2];
    u32x2 qM[2][2] = {{pk4(a0[0]), pk4(a0[1])}, {pk4(a1[0]), pk4(a1[1])}};
    unswap(qM[0][0], qM[0][1]); unswap(qM[1][0], qM[1][1]);
#pragma unroll
    for (int sb = 0; sb < 2; ++sb) {
      u32x2 qlv = lds_r64(lb + QLX + ((2 * rg + sb) * 2 + hpl) * 128 + c * 8);
      u32x2 q0 = qM[0][sb], q1 = qM[1][sb];
      u32x4 b0 = {q0[0], q0[1], g == 0 ? qlv[0] : 0u, 0u};
      u32x4 b1 = {q1[0], q1[1], 0u, g == 0 ? qlv[1] : 0u};
      u32x2 kp0 = sb ? kM[0][0] : pM0, kp1 = sb ? kM[1][0] : pM1, kpl = kL[sb], kcl = kL[sb + 1];
      f32x4 sp0 = mfma32(u32x4{kp0[0], kp0[1], kpl[0], kpl[1]}, b0, z4);
      f32x4 sp1 = mfma32(u32x4{kp1[0], kp1[1], kpl[0], kpl[1]}, b1, z4);
      f32x4 sc0 = mfma32(u32x4{kM[0][sb][0], kM[0][sb][1], kcl[0], kcl[1]}, b0, z4);
      f32x4 sc1 = mfma32(u32x4{kM[1][sb][0], kM[1][sb][1], kcl[0], kcl[1]}, b1, z4);
      if (sb) ev3(IC<EH + 2>{}); else ev3(IC<EH + 1>{});
      const bool dead = (sb == 0) && (rb == 0) && (rg == 0);
      float pa0 = par ? sp0[1] : sp0[0], pb0 = par ? sp0[3] : sp0[2], pa1 = par ? sp1[1] : sp1[0], pb1 = par ? sp1[3] : sp1[2];
      if (dead) { pa0 = pb0 = pa1 = pb1 = -1e30f; }
      la[0][sb] = (ina ? pa0 : (par ? sc0[1] : sc0[0])) * sc;
      lbv[0][sb] = (inb ? pb0 : (par ? sc0[3] : sc0[2])) * sc;
      la[1][sb] = (ina ? pa1 : (par ? sc1[1] : sc1[0])) * sc;
      lbv[1][sb] = (inb ? pb1 : (par ? sc1[3] : sc1[2])) * sc;
      mx[0][sb] = fmaxf(la[0][sb], lbv[0][sb]);
      mx[1][sb] = fmaxf(la[1][sb], lbv[1][sb]);
    }
    float tm = redmax4(mx[0][0], mx[0][1], mx[1][0], mx[1][1], ninf);
    bcast4(tm, mx[0][0], mx[0][1], mx[1][0], mx[1][1]);
    ev3(IC<EH + 3>{});
    const u32 vb = lb + lvi(BR) + (rg * 32 + 4 * g + q) * VIS + hpl * 80;
#pragma unroll
    for (int sb = 0; sb < 2; ++sb) {
      const u32 v0 = vb + sb * 16 * VIS, v1 = v0 + 16 * VIS;
      u32x4 A0 = frag_tr(v0 + 8 * p, v1 + 8 * p), A1 = frag_tr(v0 + 32 + 8 * p, v1 + 32 + 8 * p);
      u32x4 AL = frag_tr(v0 + 64 + 8 * (p & 1), v1 + 64 + 8 * (p & 1));
      u32x4 P[2];
#pragma unroll
      for (int h = 0; h < 2; ++h) {
        float ea = __builtin_amdgcn_exp2f(la[h][sb] - mx[h][sb]), eb = __builtin_amdgcn_exp2f(lbv[h][sb] - mx[h][sb]);
        u32 wa_ = par ? pk(0.f, ea) : pk(ea, 0.f), wb_ = par ? pk(0.f, eb) : pk(eb, 0.f);
        P[h] = u32x4{ina ? wa_ : 0u, inb ? wb_ : 0u, ina ? 0u : wa_, inb ? 0u : wb_};
      }
      f32x4 O0 = mfma32(A0, P[0], z4), O1 = mfma32(A1, P[1], z4);
      f32x4 L0 = mfma32(AL, P[0], z4), L1 = mfma32(AL, P[1], z4);
      float r0 = __builtin_amdgcn_rcpf(L0[2]), r1 = __builtin_amdgcn_rcpf(L1[2]);
      if (e_fchk(BR) == EH + 4 && sb == 0) {
        asm volatile("s_waitcnt vmcnt(%1)" : "+v"(fv) : "n"(wait_h<BR>(EH + 4, e_fld(BR))) : "memory");
        check_flags();
      }
      store_o(sb, O0, O1, g0 ? L1[0] : L0[0], g0 ? L1[1] : L0[1], r0, r1);
    }
  }
  STAMP(11);
  refresh();
  const u32 ofl = (u32)((BR * 128 + rb * 8 + w) * 18 * 1024 + l * 16);
  auto ld_left = [&](int so_) {
    const u32 o2 = ofl - c * 8;
    u32x2 p = __builtin_amdgcn_raw_buffer_load_b64(srdF, o2, so_, 16), q = __builtin_amdgcn_raw_buffer_load_b64(srdF, o2 + 128, so_, 16);
    return u32x4{p[0], p[1], q[0], q[1]};
  };
  u32x4 wf[18][5], of[18];
  wait_bar<4>();
  STAMP(12);
  const u32 wrow2 = lb + (16 * g1 + 8 * g0 + 2 * q) * 144;
  u32 oa[3], oa4[3];
#pragma unroll
  for (int i = 0; i < 3; ++i) {
    oa[i] = wrow2 + 8 * p + i * 57344; oa4[i] = wrow2 + 128 + 8 * (p & 1) + i * 57344;
    asm volatile("" : "+v"(oa[i]), "+v"(oa4[i]));
  }
  auto load_wf = [&](auto TT, auto II) {
    constexpr int t = TT.value, i = II.value;
    if constexpr (t < 18) {
      constexpr u32 cb = wo_at(BR, t < 4 ? 1 - (t >> 1) : t >> 1) + (t & 1) * 32 * 144, s = cb / 57344, o = cb % 57344;
      if constexpr (i < 4) wf[t][i] = frag_tr(oa[s] + o + 32 * i, oa[s] + o + 144 + 32 * i);
      else wf[t][4] = frag_tr(oa4[s] + o, oa4[s] + o + 144);
    }
  };
  f32x4 acc[5];
  u32 seen;
  auto kstep = [&](auto tt) {
    constexpr int t = tt.value;
    static_for<0, 5>([&](auto ii) {
      constexpr int i = ii.value;
      acc[i] = mfma32(wf[t][i], of[t], t ? acc[i] : z4);
      if constexpr (i == 1 && t >= 4) seen = umax4(seen, of[t]);
      load_wf(IC<t + PRE>{}, ii);
      SCHED_FENCE();
    });
  };
  u32 so[14], own = lb + LOWN + w * 2048 + l * 16;
#pragma unroll
  for (int t = 0; t < 14; ++t) { so[t] = ((t + 2 + 2 * hq) & 15) * 1024; asm volatile("" : "+s"(so[t])); }
  asm volatile("" : "+v"(own));
  _Pragma("nounroll") for (int pass = 0; pass < (1 << 16); ++pass) {
#pragma unroll
    for (int t = 4; t < 4 + NOPT; ++t) of[t] = __builtin_amdgcn_raw_buffer_load_b128(srdF, ofl, so[t - 4], 16);
    of[0] = lds_r128(own);
    of[1] = lds_r128(own + 1024);
    SCHED_FENCE();
    static_for<0, PRE * 5>([&](auto t) {
      load_wf(IC<t.value / 5>{}, IC<t.value % 5>{});
      if constexpr (t.value == PP) {
        SCHED_FENCE();
        of[2] = ld_left(16 * 1024);
        of[3] = ld_left(17 * 1024);
        SCHED_FENCE();
      }
    });
    SCHED_FENCE();
    kstep(IC<0>{}); kstep(IC<1>{});
    _Pragma("nounroll") for (int spin = 0; spin < (1 << 20); ++spin) {
      asm volatile("" ::: "memory");
      if (!__any(umax4(umax4(0u, of[2]), of[3]) == SENT)) break;
      of[2] = ld_left(16 * 1024);
      of[3] = ld_left(17 * 1024);
#pragma unroll
      for (int t = 4; t < 4 + NOPT; ++t) of[t] = __builtin_amdgcn_raw_buffer_load_b128(srdF, ofl, so[t - 4], 16);
    }
    STAMP(13);
#pragma unroll
    for (int t = 4 + NOPT; t < 18; ++t) of[t] = __builtin_amdgcn_raw_buffer_load_b128(srdF, ofl, so[t - 4], 16);
    SCHED_FENCE();
    seen = 0;
    static_for<2, 18>(kstep);
    if (!__any(seen == SENT)) break;
  }
  STAMP(14);
  if (BR == 0 && rb == 0 && hq == 0 && tid == 0) __hip_atomic_store((GLB_AS u64*)(ws + WS_EPOCH), tag, __ATOMIC_RELAXED, __HIP_MEMORY_SCOPE_AGENT);
  char* op = out + (size_t)(rb * 128 + w * 16 + c) * 2304 + BR * 1152 + hq * 144;
  __amdgpu_buffer_rsrc_t srdU = __builtin_amdgcn_make_buffer_rsrc((void*)out, (short)0, 2048 * 2304, 0x00020000);
  const u32 oo = (u32)((rb * 128 + w * 16 + c) * 2304 + BR * 1152 + hq * 144);
#pragma unroll
  for (int i = 0; i < 2; ++i) {
    u32x2 a = pk4(acc[2 * i]), b = pk4(acc[2 * i + 1]);
    auto s0 = __builtin_amdgcn_permlane16_swap(a[0], b[0], false, false);
    auto s1 = __builtin_amdgcn_permlane16_swap(a[1], b[1], false, false);
    u32 s00 = s0[0], s01 = s0[1], s10 = s1[0], s11 = s1[1];
    *(u32x4*)(op + 64 * i + 32 * g0 + 16 * g1) = u32x4{s00, s10, s01, s11};
  }
  if (g < 2) *(u32x2*)(op + 128 + 8 * g) = pk4(acc[4]);
  STAMP(15);
  RSTAMP(31);
  STAMP_FLUSH();
  asm volatile(".Lhk_to_%0:" ::"n"(BR));
  __builtin_amdgcn_endpgm();
}

__global__ __launch_bounds__(512, 2) void fused(const char* __restrict__ XA, const char* __restrict__ WA, const char* __restrict__ PHI,
                                                const char* __restrict__ WOA, const char* __restrict__ XB, const char* __restrict__ WB,
                                                const char* __restrict__ WOB, char* ws, char* out) {
  __shared__ __attribute__((aligned(16))) char smem[LDS_BYTES];
  u64 tent = 0, rent = 0;
  const u32 lb = (u32)(uintptr_t)(LDS_AS char*)smem;
  const int b = blockIdx.x, x = b & 7, j = b >> 3, xx = x & 3;
  const int rb = xx * 4 + (j >> 3), hq = j & 7;
  u64x4 ka, kb;
  u64 kc;
  first(x >= 4, rb, hq, j, lb, ka, kb, kc);
  auto ptr = [](u64 a) { return (char*)(GLB_AS char*)a; };
  XA = ptr(ka[0]); WA = ptr(ka[1]); PHI = ptr(ka[2]); WOA = ptr(ka[3]);
  XB = ptr(kb[0]); WB = ptr(kb[1]); WOB = ptr(kb[2]); ws = ptr(kb[3]); out = ptr(kc);
  if (x < 4) body<0>(XA, WA, PHI, WOA, ws, out, rb, hq, j, lb, tent, rent);
  else body<1>(XB, WB, PHI, WOB, ws, out, rb, hq, j, lb, tent, rent);
}
}

extern "C" void kernel_launch(void* const* d_in, const int* in_sizes, int n_in, void* d_out, int out_size, void* d_ws, size_t ws_size,
                              hipStream_t stream) {
  hk::fused<<<256, 512, 0, stream>>>((const char*)d_in[0], (const char*)d_in[1], (const char*)d_in[2], (const char*)d_in[3], (const char*)d_in[4],
                                     (const char*)d_in[5], (const char*)d_in[6], (char*)d_ws, (char*)d_out);
}
```
